```python
import torch

def _softmax(x, axis=-1):
    return torch.softmax(x, dim=axis)
def _gelu(x):
    return torch.nn.functional.gelu(x, approximate="tanh")
def _silu(x):
    return torch.nn.functional.silu(x)
def _sigmoid(x):
    return torch.sigmoid(x)
def _layernorm(x, axis=-1, eps=1e-5):
    m = x.mean(dim=axis, keepdim=True)
    v = x.var(dim=axis, keepdim=True, unbiased=False)
    return (x - m) / torch.sqrt(v + eps)
def _scatter_add(tgt, idx, src, axis=0):
    return tgt.clone().index_add_(axis, idx, src)
def _segment_sum(src, seg, n):
    out = torch.zeros((n,) + src.shape[1:], dtype=src.dtype)
    return out.index_add_(0, seg, src)
def _pad(x, pw):
    flat = tuple(v for pair in reversed(pw) for v in pair)
    return torch.nn.functional.pad(x, flat)

kernel_name = 'sequential_preLN__spectral_fnet__dense_activation_squared_relu__prompt'

def setup_inputs():

    _w = (lambda a: (lambda t: t.to(torch.bfloat16) if t.is_floating_point() else t)(torch.as_tensor(a)))
    def _A_setup():
        B, S, D = 1, 2048, 1024
        ns = torch.arange(S)[:, None] * torch.arange(S)[None, :]
        nd = torch.arange(D)[:, None] * torch.arange(D)[None, :]
        Ws_re = torch.cos(-2.0 * torch.pi * ns / S).to(float)
        Ws_im = torch.sin(-2.0 * torch.pi * ns / S).to(float)
        Wd_re = torch.cos(-2.0 * torch.pi * nd / D).to(float)
        Wd_im = torch.sin(-2.0 * torch.pi * nd / D).to(float)
        return {
            "x": _w((lambda *s: torch.randn(*s, dtype=torch.bfloat16) * 0.05)(B, S, D)),
            "dft_seq_re": _w(Ws_re),
            "dft_seq_im": _w(Ws_im),
            "dft_hid_re": _w(Wd_re),
            "dft_hid_im": _w(Wd_im),
        }
    _A_in = _A_setup()

    def _B_setup():
        B, S, D, F = 1, 2048, 1024, 3584
        return {
            "x": _w((lambda *s: torch.randn(*s, dtype=torch.bfloat16) * 0.05)(B, S, D)),
            "w1": _w((lambda *s: torch.randn(*s, dtype=torch.bfloat16) * 0.2)(D, F)),
            "b1": _w((lambda *s: torch.randn(*s, dtype=torch.bfloat16) * 0.2)(F)),
            "w2": _w((lambda *s: torch.randn(*s, dtype=torch.bfloat16) * 0.2)(F, D)),
            "b2": _w((lambda *s: torch.randn(*s, dtype=torch.bfloat16) * 0.2)(D)),
        }
    _B_in = _B_setup()

    return {
        "A_x": _A_in["x"],
        "A_dft_seq_re": _A_in["dft_seq_re"],
        "A_dft_seq_im": _A_in["dft_seq_im"],
        "A_dft_hid_re": _A_in["dft_hid_re"],
        "A_dft_hid_im": _A_in["dft_hid_im"],
        "B_x": _B_in["x"],
        "B_w1": _B_in["w1"],
        "B_b1": _B_in["b1"],
        "B_w2": _B_in["w2"],
        "B_b2": _B_in["b2"],
    }

def reference(A_x, A_dft_seq_re, A_dft_seq_im, A_dft_hid_re, A_dft_hid_im, B_x, B_w1, B_b1, B_w2, B_b2):
    def _A_ref(x, dft_seq_re, dft_seq_im, dft_hid_re, dft_hid_im):
        a = torch.einsum("bsd,ed->bse", x, dft_hid_re)
        b = torch.einsum("bsd,ed->bse", x, dft_hid_im)
        y = torch.einsum("ts,bse->bte", dft_seq_re, a) - torch.einsum("ts,bse->bte", dft_seq_im, b)
        return y

    def _B_ref(x, w1, b1, w2, b2):
        h = torch.einsum("bsd,df->bsf", x, w1) + b1
        zero = torch.zeros_like(h)
        r = torch.where(h > zero, h, zero)
        h = r * r
        out = torch.einsum("bsf,fd->bsd", h, w2) + b2
        return out

    x = A_x
    h = x + _A_ref(_layernorm(x, axis=-1), A_dft_seq_re, A_dft_seq_im, A_dft_hid_re, A_dft_hid_im)
    h2 = h + _B_ref(_layernorm(h, axis=-1), B_w1, B_b1, B_w2, B_b2)
    return h2
```

```cpp
#include <hip/hip_runtime.h>
#include <hip/hip_bf16.h>
#include <stdint.h>

typedef unsigned short u16;
typedef short bf16x8 __attribute__((ext_vector_type(8)));
typedef float f32x4 __attribute__((ext_vector_type(4)));

constexpr int S = 2048, D = 1024, F = 3584;

__device__ __forceinline__ float bf2f(u16 v) { return __uint_as_float(((unsigned)v) << 16); }
__device__ __forceinline__ u16 f2bf(float f) { __hip_bfloat16 b = __float2bfloat16(f); return *(u16*)&b; }

__global__ void ln_kernel(const u16* __restrict__ x, u16* __restrict__ y) {
  int row = blockIdx.x * 4 + (threadIdx.x >> 6), lane = threadIdx.x & 63;
  const u16* p = x + (size_t)row * D;
  float v[16]; float s = 0;
  for (int i = 0; i < 16; ++i) { v[i] = bf2f(p[lane + 64 * i]); s += v[i]; }
  for (int o = 32; o; o >>= 1) s += __shfl_xor(s, o);
  float m = s / D, q = 0;
  for (int i = 0; i < 16; ++i) { float d = v[i] - m; q += d * d; }
  for (int o = 32; o; o >>= 1) q += __shfl_xor(q, o);
  float r = rsqrtf(q / D + 1e-5f);
  for (int i = 0; i < 16; ++i) y[(size_t)row * D + lane + 64 * i] = f2bf((v[i] - m) * r);
}

template <bool BT, int MODE>
__global__ __launch_bounds__(256) void gemm(const u16* __restrict__ A, int lda, const u16* __restrict__ B, int ldb,
                     const u16* __restrict__ A2, const u16* __restrict__ B2, int K, u16* __restrict__ C, int ldc,
                     const u16* __restrict__ bias, const u16* __restrict__ res) {
  __shared__ u16 As[64][40], Bs[64][40];
  int tid = threadIdx.x, w = tid >> 6, lane = tid & 63, wr = w >> 1, wc = w & 1;
  int bm = blockIdx.y * 64, bn = blockIdx.x * 64;
  f32x4 acc[2][2] = {};
  int np = A2 ? 2 : 1;
  for (int p = 0; p < np; ++p) {
    const u16* a = p ? A2 : A; const u16* b = p ? B2 : B;
    for (int k0 = 0; k0 < K; k0 += 32) {
      { int r = tid >> 2, c = (tid & 3) * 8;
        *(bf16x8*)&As[r][c] = *(const bf16x8*)&a[(size_t)(bm + r) * lda + k0 + c];
        if (BT) *(bf16x8*)&Bs[r][c] = *(const bf16x8*)&b[(size_t)(bn + r) * ldb + k0 + c];
        else { int kk = tid >> 3, n = (tid & 7) * 8; bf16x8 v = *(const bf16x8*)&b[(size_t)(k0 + kk) * ldb + bn + n];
          for (int j = 0; j < 8; ++j) Bs[n + j][kk] = v[j]; } }
      __syncthreads();
      for (int m = 0; m < 2; ++m) for (int n = 0; n < 2; ++n) {
        bf16x8 fa = *(const bf16x8*)&As[wr * 32 + m * 16 + (lane & 15)][(lane >> 4) * 8];
        bf16x8 fb = *(const bf16x8*)&Bs[wc * 32 + n * 16 + (lane & 15)][(lane >> 4) * 8];
        acc[m][n] = __builtin_amdgcn_mfma_f32_16x16x32_bf16(fa, fb, acc[m][n], 0, 0, 0);
      }
      __syncthreads();
    }
  }
  for (int m = 0; m < 2; ++m) for (int n = 0; n < 2; ++n) for (int j = 0; j < 4; ++j) {
    int r = bm + wr * 32 + m * 16 + (lane >> 4) * 4 + j, c = bn + wc * 32 + n * 16 + (lane & 15);
    float v = acc[m][n][j];
    if (MODE == 1) v = -v;
    if (MODE == 2) v = v + bf2f(res[(size_t)r * ldc + c]);
    if (MODE == 3) { v += bf2f(bias[c]); v = v > 0 ? v * v : 0; }
    if (MODE == 4) v = v + bf2f(bias[c]) + bf2f(res[(size_t)r * ldc + c]);
    C[(size_t)r * ldc + c] = f2bf(v);
  }
}

extern "C" void kernel_launch(void* const* d_in, const int* in_sizes, int n_in,
                              void* d_out, int out_size, void* d_ws, size_t ws_size,
                              hipStream_t stream) {
  const u16* x = (const u16*)d_in[0];
  const u16* Cs = (const u16*)d_in[1]; const u16* Ss = (const u16*)d_in[2];
  const u16* Cd = (const u16*)d_in[3]; const u16* Sd = (const u16*)d_in[4];
  const u16* w1 = (const u16*)d_in[6]; const u16* b1 = (const u16*)d_in[7];
  const u16* w2 = (const u16*)d_in[8]; const u16* b2 = (const u16*)d_in[9];
  u16* out = (u16*)d_out;
  u16* ws = (u16*)d_ws;
  u16* xn = ws; u16* aT = xn + S * D; u16* bT = aT + S * D; u16* h = bT + S * D; u16* hn = h + S * D; u16* g = hn + S * D;
  ln_kernel<<<S / 4, 256, 0, stream>>>(x, xn);
  gemm<true, 0><<<dim3(S / 64, D / 64), 256, 0, stream>>>(Cd, D, xn, D, nullptr, nullptr, D, aT, S, nullptr, nullptr);
  gemm<true, 1><<<dim3(S / 64, D / 64), 256, 0, stream>>>(Sd, D, xn, D, nullptr, nullptr, D, bT, S, nullptr, nullptr);
  gemm<true, 2><<<dim3(D / 64, S / 64), 256, 0, stream>>>(Cs, S, aT, S, Ss, bT, S, h, D, nullptr, x);
  ln_kernel<<<S / 4, 256, 0, stream>>>(h, hn);
  gemm<false, 3><<<dim3(F / 64, S / 64), 256, 0, stream>>>(hn, D, w1, F, nullptr, nullptr, D, g, F, b1, nullptr);
  gemm<false, 4><<<dim3(D / 64, S / 64), 256, 0, stream>>>(g, F, w2, D, nullptr, nullptr, F, out, D, b2, h);
}
```

```cpp
#include <hip/hip_runtime.h>
#include <hip/hip_bf16.h>
#include <stdint.h>

typedef unsigned short u16;
typedef short bf16x8 __attribute__((ext_vector_type(8)));
typedef float f32x4 __attribute__((ext_vector_type(4)));

constexpr int S = 2048, D = 1024, F = 3584;

__device__ __forceinline__ float bf2f(u16 v) { return __uint_as_float(((unsigned)v) << 16); }
__device__ __forceinline__ u16 f2bf(float f) { __hip_bfloat16 b = __float2bfloat16(f); return *(u16*)&b; }

__global__ void ln_kernel(const u16* __restrict__ x, u16* __restrict__ y) {
  int row = blockIdx.x * 4 + (threadIdx.x >> 6), lane = threadIdx.x & 63;
  const u16* p = x + (size_t)row * D;
  float v[16]; float s = 0;
  for (int i = 0; i < 16; ++i) { v[i] = bf2f(p[lane + 64 * i]); s += v[i]; }
  for (int o = 32; o; o >>= 1) s += __shfl_xor(s, o);
  float m = s / D, q = 0;
  for (int i = 0; i < 16; ++i) { float d = v[i] - m; q += d * d; }
  for (int o = 32; o; o >>= 1) q += __shfl_xor(q, o);
  float r = rsqrtf(q / D + 1e-5f);
  for (int i = 0; i < 16; ++i) y[(size_t)row * D + lane + 64 * i] = f2bf((v[i] - m) * r);
}

template <bool BT, int MODE>
__global__ __launch_bounds__(256) void gemm(const u16* __restrict__ A, int lda, const u16* __restrict__ B, int ldb,
                     const u16* __restrict__ A2, const u16* __restrict__ B2, int K, u16* __restrict__ C, int ldc,
                     const u16* __restrict__ bias, const u16* __restrict__ res) {
  __shared__ u16 As[64][40], Bs[64][40];
  int tid = threadIdx.x, w = tid >> 6, lane = tid & 63, wr = w >> 1, wc = w & 1;
  int bm = blockIdx.y * 64, bn = blockIdx.x * 64;
  f32x4 acc[2][2] = {};
  int np = A2 ? 2 : 1;
  for (int p = 0; p < np; ++p) {
    const u16* a = p ? A2 : A; const u16* b = p ? B2 : B;
    for (int k0 = 0; k0 < K; k0 += 32) {
      { int r = tid >> 2, c = (tid & 3) * 8;
        *(bf16x8*)&As[r][c] = *(const bf16x8*)&a[(size_t)(bm + r) * lda + k0 + c];
        if (BT) *(bf16x8*)&Bs[r][c] = *(const bf16x8*)&b[(size_t)(bn + r) * ldb + k0 + c];
        else { int kk = tid >> 3, n = (tid & 7) * 8; bf16x8 v = *(const bf16x8*)&b[(size_t)(k0 + kk) * ldb + bn + n];
          for (int j = 0; j < 8; ++j) Bs[n + j][kk] = v[j]; } }
      __syncthreads();
      for (int m = 0; m < 2; ++m) for (int n = 0; n < 2; ++n) {
        bf16x8 fa = *(const bf16x8*)&As[wr * 32 + m * 16 + (lane & 15)][(lane >> 4) * 8];
        bf16x8 fb = *(const bf16x8*)&Bs[wc * 32 + n * 16 + (lane & 15)][(lane >> 4) * 8];
        acc[m][n] = __builtin_amdgcn_mfma_f32_16x16x32_bf16(fa, fb, acc[m][n], 0, 0, 0);
      }
      __syncthreads();
    }
  }
  for (int m = 0; m < 2; ++m) for (int n = 0; n < 2; ++n) for (int j = 0; j < 4; ++j) {
    int r = bm + wr * 32 + m * 16 + (lane >> 4) * 4 + j, c = bn + wc * 32 + n * 16 + (lane & 15);
    float v = acc[m][n][j];
    if (MODE == 1) v = -v;
    if (MODE == 2) v = v + bf2f(res[(size_t)r * ldc + c]);
    if (MODE == 3) { v += bf2f(bias[c]); v = v > 0 ? v * v : 0; }
    if (MODE == 4) v = v + bf2f(bias[c]) + bf2f(res[(size_t)r * ldc + c]);
    C[(size_t)r * ldc + c] = f2bf(v);
  }
}


typedef unsigned u32x4 __attribute__((ext_vector_type(4)));
__global__ __launch_bounds__(512) void pref_k(const u32x4* a0, int n0, const u32x4* a1, int n1, const u32x4* a2, int n2, const u32x4* a3, int n3, const u32x4* a4, int n4,
   const u32x4* a5, int n5, const u32x4* a6, int n6, unsigned* sink) {
  const u32x4* ps[7] = {a0,a1,a2,a3,a4,a5,a6}; int ns[7] = {n0,n1,n2,n3,n4,n5,n6};
  u32x4 acc = {0,0,0,0};
  int t = blockIdx.x * 512 + threadIdx.x, T = gridDim.x * 512;
#pragma unroll
  for (int j = 0; j < 7; ++j) {
    const u32x4* p = ps[j]; int n = ns[j];
    for (int i = t; i < n; i += 2 * T) { u32x4 v0 = p[i]; u32x4 v1 = (i + T < n) ? p[i + T] : v0; acc ^= v0 ^ v1; }
  }
  if ((acc.x ^ acc.y ^ acc.z ^ acc.w) == 0x9abcdef1) sink[0] = 1;
}
extern "C" void kernel_launch(void* const* d_in, const int* in_sizes, int n_in,
                              void* d_out, int out_size, void* d_ws, size_t ws_size,
                              hipStream_t stream) {
  const u16* x = (const u16*)d_in[0];
  const u16* Cs = (const u16*)d_in[1]; const u16* Ss = (const u16*)d_in[2];
  const u16* Cd = (const u16*)d_in[3]; const u16* Sd = (const u16*)d_in[4];
  const u16* w1 = (const u16*)d_in[6]; const u16* b1 = (const u16*)d_in[7];
  const u16* w2 = (const u16*)d_in[8]; const u16* b2 = (const u16*)d_in[9];
  u16* out = (u16*)d_out;
  u16* ws = (u16*)d_ws;
  u16* xn = ws; u16* aT = xn + S * D; u16* bT = aT + S * D; u16* h = bT + S * D; u16* hn = h + S * D; u16* g = hn + S * D;
  pref_k<<<1024, 512, 0, stream>>>((const u32x4*)x, S*D/8, (const u32x4*)Cd, D*D/8, (const u32x4*)Sd, D*D/8, (const u32x4*)Cs, S*S/8, (const u32x4*)Ss, S*S/8, (const u32x4*)w1, D*F/8, (const u32x4*)w2, D*F/8, (unsigned*)d_ws + (50<<20));
  ln_kernel<<<S / 4, 256, 0, stream>>>(x, xn);
  gemm<true, 0><<<dim3(S / 64, D / 64), 256, 0, stream>>>(Cd, D, xn, D, nullptr, nullptr, D, aT, S, nullptr, nullptr);
  gemm<true, 1><<<dim3(S / 64, D / 64), 256, 0, stream>>>(Sd, D, xn, D, nullptr, nullptr, D, bT, S, nullptr, nullptr);
  gemm<true, 2><<<dim3(D / 64, S / 64), 256, 0, stream>>>(Cs, S, aT, S, Ss, bT, S, h, D, nullptr, x);
  ln_kernel<<<S / 4, 256, 0, stream>>>(h, hn);
  gemm<false, 3><<<dim3(F / 64, S / 64), 256, 0, stream>>>(hn, D, w1, F, nullptr, nullptr, D, g, F, b1, nullptr);
  gemm<false, 4><<<dim3(D / 64, S / 64), 256, 0, stream>>>(g, F, w2, D, nullptr, nullptr, F, out, D, b2, h);
}
```

```cpp
#include <hip/hip_runtime.h>

extern "C" __global__ __launch_bounds__(256, 1) void fnet_mlp_fused(const void* x, const void* cs, const void* ss, const void* cd, const void* sd,
    const void* w1, const void* b1, const void* w2, const void* b2, void* out, void* ws) {
  asm volatile(
"s_load_dwordx16 s[8:23], %0, 0\n"
"s_load_dwordx4 s[24:27], %0, 64\n"
"s_load_dwordx2 s[28:29], %0, 80\n"
"s_mov_b32 s30, %1\n"
"v_and_b32 v1, 63, %2\n"
"v_lshrrev_b32 v2, 6, %2\n"
"s_nop 0\n"
"v_readfirstlane_b32 s31, v2\n"
"s_lshr_b32 s75, s30, 3\n"
"v_lshrrev_b32 v100, 3, %2\n"
"v_lshl_add_u32 v10, s75, 5, v100\n"
"v_lshlrev_b32 v10, 7, v10\n"
"s_getpc_b64 s[88:89]\n"
".Lpc_%=:\n"
"v_min_u32 v10, .Lcode_end_%=-.Lpc_%=-4, v10\n"
"global_load_dword a225, v10, s[88:89]\n"
"v_lshlrev_b32 v8, 4, v1\n"
"v_mov_b32 v30, 0\n"
"s_and_b32 s34, s30, 3\n"
"s_bfe_u32 s35, s30, 0x30003\n"
"s_lshr_b32 s75, s30, 6\n"
"s_and_b32 s76, s30, 4\n"
"s_add_u32 s60, s75, s76\n"
"s_lshr_b32 s61, s34, 1\n"
"s_and_b32 s62, s34, 1\n"
"s_lshl2_add_u32 s77, s30, s31\n"
"s_lshl3_add_u32 s68, s62, s60\n"
"s_mov_b32 s39, 0x20000\n"
"s_mov_b32 s43, 0x20000\n"
"s_mov_b32 s47, 0x20000\n"
"s_mov_b32 s51, 0x20000\n"
"s_mov_b32 s55, 0x20000\n"
"s_mov_b32 s59, 0x20000\n"
"s_mov_b32 s7, 0x20000\n"
"v_lshrrev_b32 v100, 3, v1\n"
"v_lshrrev_b32 v101, 4, v1\n"
"v_and_b32 v102, 7, v1\n"
"v_and_b32 v103, 1, v101\n"
"v_or_b32 v103, 0, v103\n"
"v_xor_b32 v103, v103, v102\n"
"v_lshlrev_b32 v103, 4, v103\n"
"v_add_u32 v104, 0, v100\n"
"v_lshl_add_u32 v103, v104, 11, v103\n"
"v_subrev_u32 v4, 0, v103\n"
"v_add_u32 v103, 0, v101\n"
"v_and_b32 v103, 7, v103\n"
"v_xor_b32 v103, v103, v102\n"
"v_lshlrev_b32 v103, 4, v103\n"
"v_add_u32 v104, 0, v100\n"
"v_lshl_add_u32 v103, v104, 11, v103\n"
"v_subrev_u32 v16, 0, v103\n"
"v_and_b32 v103, 1, v101\n"
"v_or_b32 v103, 2, v103\n"
"v_xor_b32 v103, v103, v102\n"
"v_lshlrev_b32 v103, 4, v103\n"
"v_add_u32 v104, 8, v100\n"
"v_lshl_add_u32 v103, v104, 11, v103\n"
"v_subrev_u32 v5, 1024, v103\n"
"v_add_u32 v103, 4, v101\n"
"v_and_b32 v103, 7, v103\n"
"v_xor_b32 v103, v103, v102\n"
"v_lshlrev_b32 v103, 4, v103\n"
"v_add_u32 v104, 8, v100\n"
"v_lshl_add_u32 v103, v104, 11, v103\n"
"v_subrev_u32 v17, 1024, v103\n"
"v_and_b32 v103, 1, v101\n"
"v_or_b32 v103, 4, v103\n"
"v_xor_b32 v103, v103, v102\n"
"v_lshlrev_b32 v103, 4, v103\n"
"v_add_u32 v104, 16, v100\n"
"v_lshl_add_u32 v103, v104, 11, v103\n"
"v_subrev_u32 v6, 2048, v103\n"
"v_add_u32 v103, 8, v101\n"
"v_and_b32 v103, 7, v103\n"
"v_xor_b32 v103, v103, v102\n"
"v_lshlrev_b32 v103, 4, v103\n"
"v_add_u32 v104, 16, v100\n"
"v_lshl_add_u32 v103, v104, 11, v103\n"
"v_subrev_u32 v18, 2048, v103\n"
"v_and_b32 v103, 1, v101\n"
"v_or_b32 v103, 6, v103\n"
"v_xor_b32 v103, v103, v102\n"
"v_lshlrev_b32 v103, 4, v103\n"
"v_add_u32 v104, 24, v100\n"
"v_lshl_add_u32 v103, v104, 11, v103\n"
"v_subrev_u32 v7, 3072, v103\n"
"v_add_u32 v103, 12, v101\n"
"v_and_b32 v103, 7, v103\n"
"v_xor_b32 v103, v103, v102\n"
"v_lshlrev_b32 v103, 4, v103\n"
"v_add_u32 v104, 24, v100\n"
"v_lshl_add_u32 v103, v104, 11, v103\n"
"v_subrev_u32 v19, 3072, v103\n"
"s_lshl_b32 s74, s31, 13\n"
"s_mov_b32 s79, 0\n"
"s_lshl1_add_u32 s63, s60, s62\n"
"s_lshl1_add_u32 s64, s35, s61\n"
"s_lshl3_add_u32 s65, s61, s35\n"
"s_lshr_b32 s66, s65, 2\n"
"s_and_b32 s67, s35, 3\n"
"s_mov_b32 s72, 0\n"
"v_and_b32 v100, 15, v1\n"
"v_lshrrev_b32 v102, 1, v100\n"
"v_xor_b32 v102, v102, v101\n"
"v_lshlrev_b32 v102, 4, v102\n"
"v_lshl_add_u32 v12, v100, 7, v102\n"
"v_xor_b32 v13, 64, v12\n"
"v_add_u32 v14, 0x10000, v12\n"
"v_add_u32 v15, 0x10000, v13\n"
"v_lshrrev_b32 v103, 2, v100\n"
"v_lshl_add_u32 v208, v103, 9, v12\n"
"v_xor_b32 v209, 64, v208\n"
"v_add_u32 v210, 0x10000, v208\n"
"v_add_u32 v211, 0x10000, v209\n"
"s_lshl_b32 s75, s31, 12\n"
"s_lshl_b32 s76, s61, 11\n"
"s_add_u32 s75, s75, s76\n"
"s_add_u32 s75, s75, 16384\n"
"v_add_u32 v200, s75, v12\n"
"v_add_u32 v201, s75, v13\n"
"v_add_u32 v202, s75, v14\n"
"v_add_u32 v203, s75, v15\n"
"s_xor_b32 s75, s75, 2048\n"
"v_add_u32 v204, s75, v12\n"
"v_add_u32 v205, s75, v13\n"
"v_add_u32 v206, s75, v14\n"
"v_add_u32 v207, s75, v15\n"
"s_waitcnt lgkmcnt(0)\n"
"s_lshl_b32 s75, s77, 12\n"
"s_add_u32 s48, s8, s75\n"
"s_addc_u32 s49, s9, 0\n"
"s_sub_u32 s50, 4194304, s75\n"
"s_cmp_lt_u32 s77, 512\n"
"s_cselect_b32 s88, s14, s16\n"
"s_cselect_b32 s89, s15, s17\n"
"s_and_b32 s78, s77, 511\n"
"s_lshl_b32 s75, s78, 12\n"
"s_add_u32 s52, s88, s75\n"
"s_addc_u32 s53, s89, 0\n"
"s_sub_u32 s54, 2097152, s75\n"
"buffer_load_dwordx4 v[212:215], v8, s[48:51], 0 offen offset:0\n"
"buffer_load_dwordx4 v[228:231], v8, s[52:55], 0 offen offset:0\n"
"buffer_load_dwordx4 v[220:223], v8, s[48:51], 0 offen offset:2048\n"
"buffer_load_dwordx4 v[236:239], v8, s[52:55], 0 offen offset:2048\n"
"buffer_load_dwordx4 v[216:219], v8, s[48:51], 0 offen offset:1024\n"
"buffer_load_dwordx4 v[232:235], v8, s[52:55], 0 offen offset:1024\n"
"buffer_load_dwordx4 v[224:227], v8, s[48:51], 0 offen offset:3072\n"
"buffer_load_dwordx4 v[240:243], v8, s[52:55], 0 offen offset:3072\n"
"s_mov_b32 s36, s28\n"
"s_mov_b32 s37, s29\n"
"s_mov_b32 s38, 68157440\n"
"buffer_load_dwordx2 v[28:29], v30, s[36:39], 0 offen offset:0 sc1\n"
"s_lshl_b32 s75, s68, 18\n"
"s_lshl_b32 s76, s31, 16\n"
"s_add_u32 s75, s75, s76\n"
"s_add_u32 s40, s8, s75\n"
"s_addc_u32 s41, s9, 0\n"
"s_sub_u32 s42, 4194304, s75\n"
"s_cmp_eq_u32 s61, 0\n"
"s_cselect_b32 s88, s14, s16\n"
"s_cselect_b32 s89, s15, s17\n"
"s_lshl_b32 s75, s35, 18\n"
"s_lshl_b32 s76, s31, 16\n"
"s_add_u32 s75, s75, s76\n"
"s_add_u32 s44, s88, s75\n"
"s_addc_u32 s45, s89, 0\n"
"s_sub_u32 s46, 2097152, s75\n"
"s_lshl_b32 s14, s31, 12\n"
"s_lshl_b32 s93, s77, 4\n"
"s_add_u32 s93, s93, 65536\n"
"s_lshl_b32 s94, s77, 3\n"
"s_add_u32 s94, s94, 81920\n"
"s_lshl_b32 s85, s77, 3\n"
"s_add_u32 s85, s85, 4096\n"
"s_cmp_eq_u32 s61, 0\n"
"s_cselect_b32 s88, s10, s12\n"
"s_cselect_b32 s89, s11, s13\n"
"s_lshl_b32 s75, s60, 20\n"
"s_lshl_b32 s76, s35, 17\n"
"s_add_u32 s75, s75, s76\n"
"s_lshl_b32 s76, s31, 15\n"
"s_add_u32 s75, s75, s76\n"
"s_lshl_b32 s76, s62, 11\n"
"s_add_u32 s75, s75, s76\n"
"s_add_u32 s56, s88, s75\n"
"s_addc_u32 s57, s89, 0\n"
"s_sub_u32 s58, 8388608, s75\n"
"v_lshrrev_b32 v25, 3, v1\n"
"v_lshlrev_b32 v25, 12, v25\n"
"v_bfe_u32 v24, v1, 2, 1\n"
"v_lshl_add_u32 v24, v24, 7, v25\n"
"v_lshrrev_b32 v100, 3, v1\n"
"v_lshlrev_b32 v100, 11, v100\n"
"v_add_u32 v84, v100, v16\n"
"v_add_u32 v85, v100, v17\n"
"v_add_u32 v85, 16384, v85\n"
"v_add_u32 v86, v100, v18\n"
"v_add_u32 v86, 32768, v86\n"
"v_add_u32 v87, v100, v19\n"
"v_add_u32 v87, 49152, v87\n"
"v_add_u32 v88, 131072, v84\n"
"v_add_u32 v89, 131072, v85\n"
"v_add_u32 v90, 131072, v86\n"
"v_add_u32 v91, 131072, v87\n"
"s_lshl_b32 s78, s31, 6\n"
"s_lshl_b32 s76, s62, 7\n"
"s_add_u32 s78, s78, s76\n"
"s_and_b32 s78, s78, 255\n"
"s_cmp_eq_u32 s61, 0\n"
"s_cselect_b32 s88, s10, s12\n"
"s_cselect_b32 s89, s11, s13\n"
"s_lshl_b32 s75, s60, 20\n"
"s_lshl_b32 s76, s78, 12\n"
"s_add_u32 s75, s75, s76\n"
"s_lshl_b32 s76, s62, 11\n"
"s_add_u32 s75, s75, s76\n"
"s_add_u32 s4, s88, s75\n"
"s_addc_u32 s5, s89, 0\n"
"s_sub_u32 s6, 8388608, s75\n"
"v_lshrrev_b32 v100, 4, v1\n"
"v_and_b32 v101, 15, v1\n"
"v_add_u32 v102, 0, v100\n"
"v_xor_b32 v103, v102, v101\n"
"v_lshlrev_b32 v103, 4, v103\n"
"v_lshl_add_u32 v103, v102, 11, v103\n"
"v_subrev_u32 v72, 0, v103\n"
"v_add_u32 v76, 32768, v72\n"
"v_add_u32 v102, 4, v100\n"
"v_xor_b32 v103, v102, v101\n"
"v_lshlrev_b32 v103, 4, v103\n"
"v_lshl_add_u32 v103, v102, 11, v103\n"
"v_subrev_u32 v73, 1024, v103\n"
"v_add_u32 v77, 32768, v73\n"
"v_add_u32 v102, 8, v100\n"
"v_xor_b32 v103, v102, v101\n"
"v_lshlrev_b32 v103, 4, v103\n"
"v_lshl_add_u32 v103, v102, 11, v103\n"
"v_subrev_u32 v74, 2048, v103\n"
"v_add_u32 v78, 32768, v74\n"
"v_add_u32 v102, 12, v100\n"
"v_xor_b32 v103, v102, v101\n"
"v_lshlrev_b32 v103, 4, v103\n"
"v_lshl_add_u32 v103, v102, 11, v103\n"
"v_subrev_u32 v75, 3072, v103\n"
"v_add_u32 v79, 32768, v75\n"
"s_lshl_b32 s75, s31, 2\n"
"v_add_u32 v100, s75, v100\n"
"v_xor_b32 v100, v100, v101\n"
"v_lshlrev_b32 v100, 4, v100\n"
"v_lshl_add_u32 v80, v101, 8, v100\n"
"v_lshrrev_b32 v100, 1, v1\n"
"v_and_b32 v101, 1, v1\n"
"v_lshlrev_b32 v101, 4, v101\n"
"v_subrev_u32 v102, 0, v100\n"
"v_cmp_gt_u32 vcc, 28, v102\n"
"s_nop 1\n"
"v_cndmask_b32 v102, 0, v102, vcc\n"
"v_cmp_le_u32 vcc, 14, v102\n"
"s_nop 1\n"
"v_cndmask_b32 v103, 0, 14, vcc\n"
"v_sub_u32 v102, v102, v103\n"
"v_xor_b32 v102, 0, v102\n"
"v_lshl_add_u32 v102, v102, 5, v101\n"
"v_lshl_add_u32 v102, v103, 9, v102\n"
"v_add_u32 v64, 0, v102\n"
"v_add_u32 v68, 57344, v64\n"
"v_subrev_u32 v102, 0, v100\n"
"v_cmp_gt_u32 vcc, 28, v102\n"
"s_nop 1\n"
"v_cndmask_b32 v102, 0, v102, vcc\n"
"v_cmp_le_u32 vcc, 14, v102\n"
"s_add_u32 m0, s14, 0\n"
"s_nop 0\n"
"buffer_load_dwordx4 v4, s[40:43], s79 offen offset:0 lds\n"
"v_cndmask_b32 v103, 0, 14, vcc\n"
"v_sub_u32 v102, v102, v103\n"
"v_xor_b32 v102, 1, v102\n"
"v_lshl_add_u32 v102, v102, 5, v101\n"
"v_lshl_add_u32 v102, v103, 9, v102\n"
"v_add_u32 v65, 13312, v102\n"
"v_add_u32 v69, 57344, v65\n"
"v_subrev_u32 v102, 4, v100\n"
"v_cmp_gt_u32 vcc, 28, v102\n"
"s_nop 1\n"
"v_cndmask_b32 v102, 0, v102, vcc\n"
"v_cmp_le_u32 vcc, 14, v102\n"
"s_nop 1\n"
"v_cndmask_b32 v103, 0, 14, vcc\n"
"v_sub_u32 v102, v102, v103\n"
"v_xor_b32 v102, 0, v102\n"
"buffer_load_dwordx4 v5, s[40:43], s79 offen offset:1024 lds\n"
"v_lshl_add_u32 v102, v102, 5, v101\n"
"v_lshl_add_u32 v102, v103, 9, v102\n"
"v_add_u32 v66, 26624, v102\n"
"v_add_u32 v70, 57344, v66\n"
"v_subrev_u32 v102, 4, v100\n"
"v_cmp_gt_u32 vcc, 28, v102\n"
"s_nop 1\n"
"v_cndmask_b32 v102, 0, v102, vcc\n"
"v_cmp_le_u32 vcc, 14, v102\n"
"s_nop 1\n"
"v_cndmask_b32 v103, 0, 14, vcc\n"
"v_sub_u32 v102, v102, v103\n"
"v_xor_b32 v102, 1, v102\n"
"v_lshl_add_u32 v102, v102, 5, v101\n"
"v_lshl_add_u32 v102, v103, 9, v102\n"
"v_add_u32 v67, 39936, v102\n"
"buffer_load_dwordx4 v6, s[40:43], s79 offen offset:2048 lds\n"
"v_add_u32 v71, 57344, v67\n"
"v_lshrrev_b32 v100, 2, v1\n"
"v_lshrrev_b32 v101, 1, v100\n"
"v_lshlrev_b32 v102, 10, v101\n"
"v_and_b32 v103, 1, v100\n"
"v_mul_u32_u24 v103, 448, v103\n"
"v_add_u32 v102, v102, v103\n"
"v_bfe_u32 v103, v101, 1, 1\n"
"v_lshl_add_u32 v102, v103, 7, v102\n"
"v_and_b32 v103, 3, v1\n"
"v_lshl_add_u32 v102, v103, 3, v102\n"
"v_and_b32 v103, 1, v101\n"
"v_lshlrev_b32 v103, 5, v103\n"
"v_add_u32 v92, v102, v103\n"
"buffer_load_dwordx4 v7, s[40:43], s79 offen offset:3072 lds\n"
"v_sub_u32 v93, v102, v103\n"
"v_add_u32 v93, 32, v93\n"
"s_lshl_b32 s78, s31, 2\n"
"s_min_u32 s78, s78, 10\n"
"s_lshl_b32 s75, s78, 5\n"
"s_add_u32 s75, s75, 131072\n"
"v_add_u32 v82, s75, v92\n"
"v_add_u32 v83, s75, v93\n"
"s_lshl_b32 s75, s78, 6\n"
"v_lshrrev_b32 v81, 4, v1\n"
"v_lshl_add_u32 v81, v81, 4, s75\n"
"s_xor_b32 s75, s34, 1\n"
"s_and_b32 s99, s75, 1\n"
"s_lshl1_add_u32 s99, s60, s99\n"
"s_add_u32 m0, s14, 16384\n"
"s_nop 0\n"
"buffer_load_dwordx4 v16, s[44:47], s79 offen offset:0 lds\n"
"s_lshl3_add_u32 s99, s99, s35\n"
"s_lshr_b32 s75, s75, 1\n"
"s_lshl1_add_u32 s99, s99, s75\n"
"s_lshl_b32 s90, s99, 16\n"
"s_lshl_b32 s76, s31, 12\n"
"s_add_u32 s90, s90, s76\n"
"s_add_u32 s90, s90, 9453568\n"
"s_lshl_b32 s87, s99, 11\n"
"s_lshl_b32 s76, s31, 9\n"
"s_add_u32 s87, s87, s76\n"
"s_add_u32 s87, s87, 41943048\n"
"s_xor_b32 s75, s34, 2\n"
"s_and_b32 s99, s75, 1\n"
"s_lshl1_add_u32 s99, s60, s99\n"
"buffer_load_dwordx4 v17, s[44:47], s79 offen offset:1024 lds\n"
"s_lshl3_add_u32 s99, s99, s35\n"
"s_lshr_b32 s75, s75, 1\n"
"s_lshl1_add_u32 s99, s99, s75\n"
"s_lshl_b32 s91, s99, 16\n"
"s_lshl_b32 s76, s31, 12\n"
"s_add_u32 s91, s91, s76\n"
"s_add_u32 s91, s91, 9469952\n"
"s_lshl_b32 s83, s99, 11\n"
"s_lshl_b32 s76, s31, 9\n"
"s_add_u32 s83, s83, s76\n"
"s_add_u32 s83, s83, 41943056\n"
"s_xor_b32 s75, s34, 3\n"
"s_and_b32 s99, s75, 1\n"
"s_lshl1_add_u32 s99, s60, s99\n"
"buffer_load_dwordx4 v18, s[44:47], s79 offen offset:2048 lds\n"
"s_lshl3_add_u32 s99, s99, s35\n"
"s_lshr_b32 s75, s75, 1\n"
"s_lshl1_add_u32 s99, s99, s75\n"
"s_lshl_b32 s92, s99, 16\n"
"s_lshl_b32 s76, s31, 12\n"
"s_add_u32 s92, s92, s76\n"
"s_add_u32 s92, s92, 9486336\n"
"s_lshl_b32 s84, s99, 11\n"
"s_lshl_b32 s76, s31, 9\n"
"s_add_u32 s84, s84, s76\n"
"s_add_u32 s84, s84, 41943064\n"
"s_lshl_b32 s69, s63, 15\n"
"s_lshl_b32 s76, s64, 11\n"
"s_add_u32 s69, s69, s76\n"
"buffer_load_dwordx4 v19, s[44:47], s79 offen offset:3072 lds\n"
"s_add_u32 s79, s79, 128\n"
"s_lshl_b32 s76, s31, 9\n"
"s_add_u32 s69, s69, s76\n"
"s_add_u32 s69, s69, 41943040\n"
"s_lshl_b32 s97, s63, 20\n"
"s_lshl_b32 s76, s64, 16\n"
"s_add_u32 s97, s97, s76\n"
"s_lshl_b32 s76, s31, 12\n"
"s_add_u32 s97, s97, s76\n"
"s_add_u32 s97, s97, 9437184\n"
"s_lshl_b32 s78, s68, 9\n"
"s_lshl_b32 s76, s31, 7\n"
"s_add_u32 s78, s78, s76\n"
"s_add_u32 s78, s78, 4096\n"
"s_lshl_b32 s75, s61, 12\n"
"s_add_u32 m0, s14, 32768\n"
"s_nop 0\n"
"buffer_load_dwordx4 v4, s[40:43], s79 offen offset:0 lds\n"
"s_lshl_b32 s76, s35, 9\n"
"s_add_u32 s75, s75, s76\n"
"s_lshl_b32 s76, s31, 7\n"
"s_add_u32 s75, s75, s76\n"
"s_add_u32 s75, s75, 4096\n"
"v_mov_b32 v31, s78\n"
"v_mov_b32 v2, s75\n"
"v_cmp_gt_u32 vcc, 32, v1\n"
"s_nop 1\n"
"v_cndmask_b32 v31, v2, v31, vcc\n"
"v_bfe_u32 v2, v1, 1, 4\n"
"v_lshl_add_u32 v31, v2, 3, v31\n"
"s_lshl_b32 s75, s68, 10\n"
"s_lshl_b32 s76, s31, 8\n"
"s_add_u32 s75, s75, s76\n"
"buffer_load_dwordx4 v5, s[40:43], s79 offen offset:1024 lds\n"
"s_add_u32 s75, s75, 65536\n"
"v_mov_b32 v250, 0x3f803f80\n"
"v_lshrrev_b32 v251, 5, v1\n"
"v_lshlrev_b32 v252, 3, v251\n"
"v_lshlrev_b32 v251, 2, v251\n"
"s_waitcnt vmcnt(18)\n"
"v_dot2_f32_bf16 v244, v212, v250, 0\n"
"v_dot2_f32_bf16 v246, v212, v212, 0\n"
"s_waitcnt vmcnt(17)\n"
"v_dot2_f32_bf16 v248, v228, v250, 0\n"
"v_dot2_f32_bf16 v244, v213, v250, v244\n"
"v_dot2_f32_bf16 v246, v213, v213, v246\n"
"v_dot2_f32_bf16 v248, v229, v250, v248\n"
"v_dot2_f32_bf16 v244, v214, v250, v244\n"
"v_dot2_f32_bf16 v246, v214, v214, v246\n"
"v_dot2_f32_bf16 v248, v230, v250, v248\n"
"buffer_load_dwordx4 v6, s[40:43], s79 offen offset:2048 lds\n"
"v_dot2_f32_bf16 v244, v215, v250, v244\n"
"v_dot2_f32_bf16 v246, v215, v215, v246\n"
"v_dot2_f32_bf16 v248, v231, v250, v248\n"
"s_waitcnt vmcnt(15)\n"
"v_dot2_f32_bf16 v244, v216, v250, v244\n"
"v_dot2_f32_bf16 v246, v216, v216, v246\n"
"s_waitcnt vmcnt(14)\n"
"v_dot2_f32_bf16 v248, v232, v250, v248\n"
"v_dot2_f32_bf16 v244, v217, v250, v244\n"
"v_dot2_f32_bf16 v246, v217, v217, v246\n"
"v_dot2_f32_bf16 v248, v233, v250, v248\n"
"v_dot2_f32_bf16 v244, v218, v250, v244\n"
"v_dot2_f32_bf16 v246, v218, v218, v246\n"
"v_dot2_f32_bf16 v248, v234, v250, v248\n"
"v_dot2_f32_bf16 v244, v219, v250, v244\n"
"v_dot2_f32_bf16 v246, v219, v219, v246\n"
"buffer_load_dwordx4 v7, s[40:43], s79 offen offset:3072 lds\n"
"v_dot2_f32_bf16 v248, v235, v250, v248\n"
"v_dot2_f32_bf16 v245, v220, v250, 0\n"
"v_dot2_f32_bf16 v247, v220, v220, 0\n"
"v_dot2_f32_bf16 v249, v236, v250, 0\n"
"v_dot2_f32_bf16 v245, v221, v250, v245\n"
"v_dot2_f32_bf16 v247, v221, v221, v247\n"
"v_dot2_f32_bf16 v249, v237, v250, v249\n"
"v_dot2_f32_bf16 v245, v222, v250, v245\n"
"v_dot2_f32_bf16 v247, v222, v222, v247\n"
"v_dot2_f32_bf16 v249, v238, v250, v249\n"
"v_dot2_f32_bf16 v245, v223, v250, v245\n"
"v_dot2_f32_bf16 v247, v223, v223, v247\n"
"v_dot2_f32_bf16 v249, v239, v250, v249\n"
"s_waitcnt vmcnt(14)\n"
"v_dot2_f32_bf16 v245, v224, v250, v245\n"
"s_add_u32 m0, s14, 49152\n"
"s_nop 0\n"
"buffer_load_dwordx4 v16, s[44:47], s79 offen offset:0 lds\n"
"v_dot2_f32_bf16 v247, v224, v224, v247\n"
"s_waitcnt vmcnt(14)\n"
"v_dot2_f32_bf16 v249, v240, v250, v249\n"
"v_dot2_f32_bf16 v245, v225, v250, v245\n"
"v_dot2_f32_bf16 v247, v225, v225, v247\n"
"v_dot2_f32_bf16 v249, v241, v250, v249\n"
"v_dot2_f32_bf16 v245, v226, v250, v245\n"
"v_dot2_f32_bf16 v247, v226, v226, v247\n"
"v_dot2_f32_bf16 v249, v242, v250, v249\n"
"v_dot2_f32_bf16 v245, v227, v250, v245\n"
"v_dot2_f32_bf16 v247, v227, v227, v247\n"
"v_dot2_f32_bf16 v249, v243, v250, v249\n"
"s_nop 2\n"
"v_permlane32_swap_b32 v244, v245\n"
"v_permlane32_swap_b32 v246, v247\n"
"buffer_load_dwordx4 v17, s[44:47], s79 offen offset:1024 lds\n"
"v_permlane32_swap_b32 v248, v249\n"
"v_add_f32 v244, v244, v245\n"
"v_mov_b32 v245, v244\n"
"v_add_f32 v246, v246, v247\n"
"v_mov_b32 v247, v246\n"
"v_add_f32 v248, v248, v249\n"
"v_mov_b32 v249, v248\n"
"v_permlane16_swap_b32 v244, v245\n"
"v_permlane16_swap_b32 v246, v247\n"
"v_permlane16_swap_b32 v248, v249\n"
"v_add_f32 v244, v244, v245\n"
"v_add_f32 v246, v246, v247\n"
"v_add_f32 v248, v248, v249\n"
"v_add_f32_dpp v244, v244, v244 row_ror:8 row_mask:0xf bank_mask:0xf\n"
"buffer_load_dwordx4 v18, s[44:47], s79 offen offset:2048 lds\n"
"v_add_f32_dpp v246, v246, v246 row_ror:8 row_mask:0xf bank_mask:0xf\n"
"v_add_f32_dpp v248, v248, v248 row_ror:8 row_mask:0xf bank_mask:0xf\n"
"v_add_f32_dpp v244, v244, v244 row_ror:4 row_mask:0xf bank_mask:0xf\n"
"v_add_f32_dpp v246, v246, v246 row_ror:4 row_mask:0xf bank_mask:0xf\n"
"v_add_f32_dpp v248, v248, v248 row_ror:4 row_mask:0xf bank_mask:0xf\n"
"v_add_f32_dpp v244, v244, v244 row_ror:2 row_mask:0xf bank_mask:0xf\n"
"v_add_f32_dpp v246, v246, v246 row_ror:2 row_mask:0xf bank_mask:0xf\n"
"v_add_f32_dpp v248, v248, v248 row_ror:2 row_mask:0xf bank_mask:0xf\n"
"v_add_f32_dpp v244, v244, v244 row_ror:1 row_mask:0xf bank_mask:0xf\n"
"v_add_f32_dpp v246, v246, v246 row_ror:1 row_mask:0xf bank_mask:0xf\n"
"v_add_f32_dpp v248, v248, v248 row_ror:1 row_mask:0xf bank_mask:0xf\n"
"v_mul_f32 v244, 0x3a800000, v244\n"
"v_mul_f32 v246, 0x3a800000, v246\n"
"v_fma_f32 v246, -v244, v244, v246\n"
"buffer_load_dwordx4 v19, s[44:47], s79 offen offset:3072 lds\n"
"s_add_u32 s79, s79, 128\n"
"v_max_f32 v246, 0, v246\n"
"v_add_f32 v246, 0x3727c5ac, v246\n"
"v_rsq_f32 v254, v246\n"
"s_nop 0\n"
"v_mul_f32 v255, -v244, v254\n"
"buffer_store_dwordx2 v[254:255], v252, s[36:39], s93 offen offset:0 sc1\n"
"buffer_store_dword v248, v251, s[36:39], s94 offen offset:0 sc1\n"
"s_waitcnt vmcnt(18)\n"
"v_readfirstlane_b32 s70, v28\n"
"v_readfirstlane_b32 s71, v29\n"
"s_nop 0\n"
"s_add_u32 s70, s70, 1\n"
"s_addc_u32 s71, s71, 0\n"
"v_mov_b32 v28, s70\n"
"v_mov_b32 v29, s71\n"
"v_and_b32 v2, 31, v1\n"
"v_lshl_add_u32 v192, v2, 3, s75\n"
"s_waitcnt vmcnt(10)\n"
"s_barrier\n"
"ds_read_b128 a[0:3], v200 offset:0\n"
"ds_read_b128 a[4:7], v204 offset:0\n"
"ds_read_b128 a[64:67], v208 offset:0\n"
"ds_read_b128 a[68:71], v208 offset:512\n"
"ds_read_b128 a[72:75], v208 offset:4096\n"
"ds_read_b128 a[76:79], v208 offset:4608\n"
"ds_read_b128 a[80:83], v208 offset:8192\n"
"ds_read_b128 a[84:87], v208 offset:8704\n"
"ds_read_b128 a[88:91], v208 offset:12288\n"
"ds_read_b128 a[92:95], v208 offset:12800\n"
"s_waitcnt lgkmcnt(4)\n"
"v_mfma_f32_16x16x32_bf16 v[128:131], a[64:67], a[0:3], 0\n"
"ds_read_b128 a[96:99], v209 offset:0\n"
"s_add_u32 m0, s14, 65536\n"
"v_mfma_f32_16x16x32_bf16 v[132:135], a[64:67], a[4:7], 0\n"
"buffer_load_dwordx4 v4, s[40:43], s79 offen offset:0 lds\n"
"s_mul_i32 s75, s61, 4160\n"
"s_lshl_b32 s76, s35, 9\n"
"s_add_u32 s75, s75, s76\n"
"s_lshl_b32 s76, s31, 7\n"
"s_add_u32 s75, s75, s76\n"
"s_add_u32 s75, s75, 81920\n"
"v_and_b32 v193, 15, v1\n"
"v_lshl_add_u32 v193, v193, 2, s75\n"
"v_xor_b32 v194, 64, v193\n"
"s_lshl_b32 s75, s31, 7\n"
"s_add_u32 s75, s75, 131072\n"
"v_lshl_add_u32 v195, v2, 2, s75\n"
"v_lshrrev_b32 v9, 4, v1\n"
"v_mfma_f32_16x16x32_bf16 v[140:143], a[68:71], a[4:7], 0\n"
"ds_read_b128 a[100:103], v209 offset:512\n"
"buffer_load_dwordx4 v5, s[40:43], s79 offen offset:1024 lds\n"
"v_lshlrev_b32 v9, 5, v9\n"
"v_add_u32 v9, 131072, v9\n"
"s_mul_i32 s80, s61, 4195328\n"
"s_lshl_b32 s76, s35, 19\n"
"s_add_u32 s80, s80, s76\n"
"s_lshl_b32 s76, s31, 17\n"
"s_add_u32 s80, s80, s76\n"
"s_lshl_b32 s76, s68, 13\n"
"s_add_u32 s80, s80, s76\n"
"s_add_u32 s80, s80, 1048576\n"
"s_xor_b32 s96, s80, 1024\n"
"s_lshl_b32 s81, s61, 14\n"
"s_lshl_b32 s76, s35, 11\n"
"v_mfma_f32_16x16x32_bf16 v[136:139], a[68:71], a[0:3], 0\n"
"ds_read_b128 a[8:11], v201 offset:0\n"
"v_mfma_f32_16x16x32_bf16 v[144:147], a[72:75], a[0:3], 0\n"
"ds_read_b128 a[104:107], v209 offset:4096\n"
"buffer_load_dwordx4 v6, s[40:43], s79 offen offset:2048 lds\n"
"s_add_u32 s81, s81, s76\n"
"s_lshl_b32 s76, s62, 10\n"
"s_add_u32 s81, s81, s76\n"
"s_lshl_b32 s76, s31, 8\n"
"s_add_u32 s81, s81, s76\n"
"s_add_u32 s81, s81, 655360\n"
"v_and_b32 v100, 15, v1\n"
"s_lshl_b32 s78, s60, 1\n"
"v_subrev_u32 v101, 2, v100\n"
"v_cmp_gt_u32 vcc, s78, v101\n"
"s_nop 1\n"
"v_cndmask_b32 v101, v100, v101, vcc\n"
"v_add_u32 v103, s78, v100\n"
"v_cmp_gt_u32 vcc, 2, v100\n"
"v_mfma_f32_16x16x32_bf16 v[148:151], a[72:75], a[4:7], 0\n"
"v_mfma_f32_16x16x32_bf16 v[156:159], a[76:79], a[4:7], 0\n"
"ds_read_b128 a[108:111], v209 offset:4608\n"
"buffer_load_dwordx4 v7, s[40:43], s79 offen offset:3072 lds\n"
"v_cndmask_b32 v101, v101, v103, vcc\n"
"v_lshlrev_b32 v11, 7, v101\n"
"v_lshlrev_b32 v101, 12, v101\n"
"v_cmp_gt_u32 vcc, 16, v1\n"
"s_nop 1\n"
"v_cndmask_b32 v11, v101, v11, vcc\n"
"s_nop 0\n"
"v_readlane_b32 s82, v11, 18\n"
"v_readlane_b32 s95, v11, 0\n"
"v_and_b32 v100, 15, v1\n"
"v_lshrrev_b32 v101, 4, v1\n"
"v_lshlrev_b32 v102, 2, v101\n"
"v_sub_u32 v102, v100, v102\n"
"v_lshlrev_b32 v103, 3, v101\n"
"v_sub_u32 v103, v100, v103\n"
"v_mfma_f32_16x16x32_bf16 v[152:155], a[76:79], a[0:3], 0\n"
"ds_read_b128 a[12:15], v205 offset:0\n"
"s_waitcnt lgkmcnt(6)\n"
"v_mfma_f32_16x16x32_bf16 v[160:163], a[80:83], a[0:3], 0\n"
"ds_read_b128 a[112:115], v209 offset:8192\n"
"s_add_u32 m0, s14, 81920\n"
"s_nop 0\n"
"buffer_load_dwordx4 v16, s[44:47], s79 offen offset:0 lds\n"
"s_lshl_b32 s75, s61, 4\n"
"v_add_u32 v103, s75, v103\n"
"v_mov_b32 v109, 0x3f80\n"
"v_and_b32 v108, 1, v100\n"
"v_lshlrev_b32 v108, 4, v108\n"
"v_lshlrev_b32 v109, v108, v109\n"
"v_lshrrev_b32 v102, 1, v102\n"
"v_lshrrev_b32 v103, 1, v103\n"
"v_accvgpr_write_b32 a208, 0\n"
"v_accvgpr_write_b32 a209, 0\n"
"v_accvgpr_write_b32 a212, 0\n"
"v_accvgpr_write_b32 a213, 0\n"
"v_cmp_eq_u32 vcc, 0, v102\n"
"v_mfma_f32_16x16x32_bf16 v[164:167], a[80:83], a[4:7], 0\n"
"v_mfma_f32_16x16x32_bf16 v[172:175], a[84:87], a[4:7], 0\n"
"ds_read_b128 a[116:119], v209 offset:8704\n"
"buffer_load_dwordx4 v17, s[44:47], s79 offen offset:1024 lds\n"
"v_cndmask_b32 v108, 0, v109, vcc\n"
"v_accvgpr_write_b32 a210, v108\n"
"v_cmp_eq_u32 vcc, 1, v102\n"
"s_nop 1\n"
"v_cndmask_b32 v108, 0, v109, vcc\n"
"v_accvgpr_write_b32 a211, v108\n"
"v_cmp_eq_u32 vcc, 0, v103\n"
"s_nop 1\n"
"v_cndmask_b32 v108, 0, v109, vcc\n"
"v_accvgpr_write_b32 a216, v108\n"
"v_cmp_eq_u32 vcc, 1, v103\n"
"s_nop 1\n"
"v_cndmask_b32 v108, 0, v109, vcc\n"
"v_accvgpr_write_b32 a217, v108\n"
"v_cmp_eq_u32 vcc, 2, v103\n"
"s_nop 1\n"
"v_cndmask_b32 v108, 0, v109, vcc\n"
"v_mfma_f32_16x16x32_bf16 v[168:171], a[84:87], a[0:3], 0\n"
"v_mfma_f32_16x16x32_bf16 v[176:179], a[88:91], a[0:3], 0\n"
"ds_read_b128 a[120:123], v209 offset:12288\n"
"buffer_load_dwordx4 v18, s[44:47], s79 offen offset:2048 lds\n"
"v_accvgpr_write_b32 a218, v108\n"
"v_cmp_eq_u32 vcc, 3, v103\n"
"s_nop 1\n"
"v_cndmask_b32 v108, 0, v109, vcc\n"
"v_accvgpr_write_b32 a219, v108\n"
"v_mov_b32 v108, 0x3f803f80\n"
"v_accvgpr_write_b32 a220, v108\n"
"v_accvgpr_write_b32 a221, v108\n"
"v_accvgpr_write_b32 a222, v108\n"
"v_accvgpr_write_b32 a223, v108\n"
"v_mfma_f32_16x16x32_bf16 v[180:183], a[88:91], a[4:7], 0\n"
"v_mfma_f32_16x16x32_bf16 v[188:191], a[92:95], a[4:7], 0\n"
"ds_read_b128 a[124:127], v209 offset:12800\n"
"buffer_load_dwordx4 v19, s[44:47], s79 offen offset:3072 lds\n"
"s_add_u32 s79, s79, 128\n"
"v_mfma_f32_16x16x32_bf16 v[184:187], a[92:95], a[0:3], 0\n"
"s_add_u32 m0, s14, 98304\n"
"s_waitcnt lgkmcnt(5)\n"
"v_mfma_f32_16x16x32_bf16 v[128:131], a[96:99], a[8:11], v[128:131]\n"
"buffer_load_dwordx4 v4, s[40:43], s79 offen offset:0 lds\n"
"s_waitcnt lgkmcnt(4)\n"
"v_mfma_f32_16x16x32_bf16 v[132:135], a[96:99], a[12:15], v[132:135]\n"
"v_mfma_f32_16x16x32_bf16 v[140:143], a[100:103], a[12:15], v[140:143]\n"
"s_waitcnt vmcnt(11)\n"
"s_barrier\n"
"buffer_load_dwordx4 v5, s[40:43], s79 offen offset:1024 lds\n"
"v_mfma_f32_16x16x32_bf16 v[136:139], a[100:103], a[8:11], v[136:139]\n"
"ds_read_b128 a[64:67], v208 offset:32768\n"
"ds_read_b128 a[16:19], v200 offset:32768\n"
"v_mfma_f32_16x16x32_bf16 v[144:147], a[104:107], a[8:11], v[144:147]\n"
"ds_read_b128 a[68:71], v208 offset:33280\n"
"buffer_load_dwordx4 v6, s[40:43], s79 offen offset:2048 lds\n"
"v_mfma_f32_16x16x32_bf16 v[148:151], a[104:107], a[12:15], v[148:151]\n"
"ds_read_b128 a[72:75], v208 offset:36864\n"
"v_mfma_f32_16x16x32_bf16 v[156:159], a[108:111], a[12:15], v[156:159]\n"
"ds_read_b128 a[76:79], v208 offset:37376\n"
"buffer_load_dwordx4 v7, s[40:43], s79 offen offset:3072 lds\n"
"v_mfma_f32_16x16x32_bf16 v[152:155], a[108:111], a[8:11], v[152:155]\n"
"ds_read_b128 a[20:23], v204 offset:32768\n"
"s_waitcnt lgkmcnt(6)\n"
"v_mfma_f32_16x16x32_bf16 v[160:163], a[112:115], a[8:11], v[160:163]\n"
"ds_read_b128 a[80:83], v208 offset:40960\n"
"s_add_u32 m0, s14, 114688\n"
"s_nop 0\n"
"buffer_load_dwordx4 v16, s[44:47], s79 offen offset:0 lds\n"
"v_mfma_f32_16x16x32_bf16 v[164:167], a[112:115], a[12:15], v[164:167]\n"
"v_mfma_f32_16x16x32_bf16 v[172:175], a[116:119], a[12:15], v[172:175]\n"
"ds_read_b128 a[84:87], v208 offset:41472\n"
"buffer_load_dwordx4 v17, s[44:47], s79 offen offset:1024 lds\n"
"v_mfma_f32_16x16x32_bf16 v[168:171], a[116:119], a[8:11], v[168:171]\n"
"v_mfma_f32_16x16x32_bf16 v[176:179], a[120:123], a[8:11], v[176:179]\n"
"ds_read_b128 a[88:91], v208 offset:45056\n"
"buffer_load_dwordx4 v18, s[44:47], s79 offen offset:2048 lds\n"
"v_mfma_f32_16x16x32_bf16 v[180:183], a[120:123], a[12:15], v[180:183]\n"
"v_mfma_f32_16x16x32_bf16 v[188:191], a[124:127], a[12:15], v[188:191]\n"
"ds_read_b128 a[92:95], v208 offset:45568\n"
"buffer_load_dwordx4 v19, s[44:47], s79 offen offset:3072 lds\n"
"s_add_u32 s79, s79, 128\n"
"v_mfma_f32_16x16x32_bf16 v[184:187], a[124:127], a[8:11], v[184:187]\n"
"s_waitcnt vmcnt(8)\n"
"s_barrier\n"
"s_waitcnt lgkmcnt(5)\n"
"v_mfma_f32_16x16x32_bf16 v[128:131], a[64:67], a[16:19], v[128:131]\n"
"ds_read_b128 a[96:99], v209 offset:32768\n"
"s_add_u32 m0, s14, 0\n"
"s_waitcnt lgkmcnt(5)\n"
"v_mfma_f32_16x16x32_bf16 v[132:135], a[64:67], a[20:23], v[132:135]\n"
"buffer_load_dwordx4 v4, s[40:43], s79 offen offset:0 lds\n"
"v_mfma_f32_16x16x32_bf16 v[140:143], a[68:71], a[20:23], v[140:143]\n"
"ds_read_b128 a[100:103], v209 offset:33280\n"
"v_mfma_f32_16x16x32_bf16 v[136:139], a[68:71], a[16:19], v[136:139]\n"
"ds_read_b128 a[24:27], v201 offset:32768\n"
"v_mfma_f32_16x16x32_bf16 v[144:147], a[72:75], a[16:19], v[144:147]\n"
"ds_read_b128 a[104:107], v209 offset:36864\n"
"buffer_load_dwordx4 v5, s[40:43], s79 offen offset:1024 lds\n"
"v_mfma_f32_16x16x32_bf16 v[148:151], a[72:75], a[20:23], v[148:151]\n"
"v_mfma_f32_16x16x32_bf16 v[156:159], a[76:79], a[20:23], v[156:159]\n"
"ds_read_b128 a[108:111], v209 offset:37376\n"
"v_mfma_f32_16x16x32_bf16 v[152:155], a[76:79], a[16:19], v[152:155]\n"
"ds_read_b128 a[28:31], v205 offset:32768\n"
"s_waitcnt lgkmcnt(6)\n"
"v_mfma_f32_16x16x32_bf16 v[160:163], a[80:83], a[16:19], v[160:163]\n"
"ds_read_b128 a[112:115], v209 offset:40960\n"
"buffer_load_dword a226, v24, s[56:59], s95 offen offset:0\n"
"v_mfma_f32_16x16x32_bf16 v[164:167], a[80:83], a[20:23], v[164:167]\n"
"v_readlane_b32 s95, v11, 2\n"
"buffer_load_dwordx4 v6, s[40:43], s79 offen offset:2048 lds\n"
"v_mfma_f32_16x16x32_bf16 v[172:175], a[84:87], a[20:23], v[172:175]\n"
"ds_read_b128 a[116:119], v209 offset:41472\n"
"v_mfma_f32_16x16x32_bf16 v[168:171], a[84:87], a[16:19], v[168:171]\n"
"v_mfma_f32_16x16x32_bf16 v[176:179], a[88:91], a[16:19], v[176:179]\n"
"ds_read_b128 a[120:123], v209 offset:45056\n"
"buffer_load_dwordx4 v7, s[40:43], s79 offen offset:3072 lds\n"
"v_mfma_f32_16x16x32_bf16 v[180:183], a[88:91], a[20:23], v[180:183]\n"
"v_mfma_f32_16x16x32_bf16 v[188:191], a[92:95], a[20:23], v[188:191]\n"
"ds_read_b128 a[124:127], v209 offset:45568\n"
"v_mfma_f32_16x16x32_bf16 v[184:187], a[92:95], a[16:19], v[184:187]\n"
"s_add_u32 m0, s14, 16384\n"
"s_waitcnt lgkmcnt(5)\n"
"v_mfma_f32_16x16x32_bf16 v[128:131], a[96:99], a[24:27], v[128:131]\n"
"ds_read_b128 a[64:67], v210 offset:0\n"
"buffer_load_dwordx4 v16, s[44:47], s79 offen offset:0 lds\n"
"s_waitcnt lgkmcnt(5)\n"
"v_mfma_f32_16x16x32_bf16 v[132:135], a[96:99], a[28:31], v[132:135]\n"
"v_mfma_f32_16x16x32_bf16 v[140:143], a[100:103], a[28:31], v[140:143]\n"
"ds_read_b128 a[68:71], v210 offset:512\n"
"v_mfma_f32_16x16x32_bf16 v[136:139], a[100:103], a[24:27], v[136:139]\n"
"ds_read_b128 a[0:3], v202 offset:0\n"
"v_mfma_f32_16x16x32_bf16 v[144:147], a[104:107], a[24:27], v[144:147]\n"
"ds_read_b128 a[72:75], v210 offset:4096\n"
"buffer_load_dwordx4 v17, s[44:47], s79 offen offset:1024 lds\n"
"v_mfma_f32_16x16x32_bf16 v[148:151], a[104:107], a[28:31], v[148:151]\n"
"v_mfma_f32_16x16x32_bf16 v[156:159], a[108:111], a[28:31], v[156:159]\n"
"ds_read_b128 a[76:79], v210 offset:4608\n"
"v_mfma_f32_16x16x32_bf16 v[152:155], a[108:111], a[24:27], v[152:155]\n"
"ds_read_b128 a[4:7], v206 offset:0\n"
"s_waitcnt lgkmcnt(6)\n"
"v_mfma_f32_16x16x32_bf16 v[160:163], a[112:115], a[24:27], v[160:163]\n"
"ds_read_b128 a[80:83], v210 offset:8192\n"
"buffer_load_dword a227, v24, s[56:59], s95 offen offset:0\n"
"v_mfma_f32_16x16x32_bf16 v[164:167], a[112:115], a[28:31], v[164:167]\n"
"v_readlane_b32 s95, v11, 4\n"
"buffer_load_dwordx4 v18, s[44:47], s79 offen offset:2048 lds\n"
"v_mfma_f32_16x16x32_bf16 v[172:175], a[116:119], a[28:31], v[172:175]\n"
"ds_read_b128 a[84:87], v210 offset:8704\n"
"v_mfma_f32_16x16x32_bf16 v[168:171], a[116:119], a[24:27], v[168:171]\n"
"v_mfma_f32_16x16x32_bf16 v[176:179], a[120:123], a[24:27], v[176:179]\n"
"ds_read_b128 a[88:91], v210 offset:12288\n"
"buffer_load_dwordx4 v19, s[44:47], s79 offen offset:3072 lds\n"
"v_mfma_f32_16x16x32_bf16 v[180:183], a[120:123], a[28:31], v[180:183]\n"
"s_add_u32 s79, s79, 128\n"
"v_mfma_f32_16x16x32_bf16 v[188:191], a[124:127], a[28:31], v[188:191]\n"
"ds_read_b128 a[92:95], v210 offset:12800\n"
"v_mfma_f32_16x16x32_bf16 v[184:187], a[124:127], a[24:27], v[184:187]\n"
"s_waitcnt vmcnt(10)\n"
"s_barrier\n"
"s_waitcnt lgkmcnt(5)\n"
"v_mfma_f32_16x16x32_bf16 v[128:131], a[64:67], a[0:3], v[128:131]\n"
"ds_read_b128 a[96:99], v211 offset:0\n"
"s_add_u32 m0, s14, 32768\n"
"s_waitcnt lgkmcnt(5)\n"
"v_mfma_f32_16x16x32_bf16 v[132:135], a[64:67], a[4:7], v[132:135]\n"
"buffer_load_dwordx4 v4, s[40:43], s79 offen offset:0 lds\n"
"v_mfma_f32_16x16x32_bf16 v[140:143], a[68:71], a[4:7], v[140:143]\n"
"ds_read_b128 a[100:103], v211 offset:512\n"
"v_mfma_f32_16x16x32_bf16 v[136:139], a[68:71], a[0:3], v[136:139]\n"
"ds_read_b128 a[8:11], v203 offset:0\n"
"v_mfma_f32_16x16x32_bf16 v[144:147], a[72:75], a[0:3], v[144:147]\n"
"ds_read_b128 a[104:107], v211 offset:4096\n"
"buffer_load_dwordx4 v5, s[40:43], s79 offen offset:1024 lds\n"
"v_mfma_f32_16x16x32_bf16 v[148:151], a[72:75], a[4:7], v[148:151]\n"
"v_mfma_f32_16x16x32_bf16 v[156:159], a[76:79], a[4:7], v[156:159]\n"
"ds_read_b128 a[108:111], v211 offset:4608\n"
"v_mfma_f32_16x16x32_bf16 v[152:155], a[76:79], a[0:3], v[152:155]\n"
"ds_read_b128 a[12:15], v207 offset:0\n"
"s_waitcnt lgkmcnt(6)\n"
"v_mfma_f32_16x16x32_bf16 v[160:163], a[80:83], a[0:3], v[160:163]\n"
"ds_read_b128 a[112:115], v211 offset:8192\n"
"buffer_load_dwordx4 v6, s[40:43], s79 offen offset:2048 lds\n"
"v_mfma_f32_16x16x32_bf16 v[164:167], a[80:83], a[4:7], v[164:167]\n"
"v_mfma_f32_16x16x32_bf16 v[172:175], a[84:87], a[4:7], v[172:175]\n"
"ds_read_b128 a[116:119], v211 offset:8704\n"
"v_mfma_f32_16x16x32_bf16 v[168:171], a[84:87], a[0:3], v[168:171]\n"
"v_mfma_f32_16x16x32_bf16 v[176:179], a[88:91], a[0:3], v[176:179]\n"
"ds_read_b128 a[120:123], v211 offset:12288\n"
"buffer_load_dwordx4 v7, s[40:43], s79 offen offset:3072 lds\n"
"v_mfma_f32_16x16x32_bf16 v[180:183], a[88:91], a[4:7], v[180:183]\n"
"v_mfma_f32_16x16x32_bf16 v[188:191], a[92:95], a[4:7], v[188:191]\n"
"ds_read_b128 a[124:127], v211 offset:12800\n"
"v_mfma_f32_16x16x32_bf16 v[184:187], a[92:95], a[0:3], v[184:187]\n"
"s_add_u32 m0, s14, 49152\n"
"buffer_store_dwordx2 v[28:29], v30, s[36:39], s85 offen offset:0 sc1\n"
"s_waitcnt lgkmcnt(5)\n"
"v_mfma_f32_16x16x32_bf16 v[128:131], a[96:99], a[8:11], v[128:131]\n"
"ds_read_b128 a[64:67], v210 offset:32768\n"
"buffer_load_dwordx4 v16, s[44:47], s79 offen offset:0 lds\n"
"s_waitcnt lgkmcnt(5)\n"
"v_mfma_f32_16x16x32_bf16 v[132:135], a[96:99], a[12:15], v[132:135]\n"
"v_mfma_f32_16x16x32_bf16 v[140:143], a[100:103], a[12:15], v[140:143]\n"
"ds_read_b128 a[68:71], v210 offset:33280\n"
"v_mfma_f32_16x16x32_bf16 v[136:139], a[100:103], a[8:11], v[136:139]\n"
"ds_read_b128 a[16:19], v202 offset:32768\n"
"v_mfma_f32_16x16x32_bf16 v[144:147], a[104:107], a[8:11], v[144:147]\n"
"ds_read_b128 a[72:75], v210 offset:36864\n"
"buffer_load_dwordx4 v17, s[44:47], s79 offen offset:1024 lds\n"
"v_mfma_f32_16x16x32_bf16 v[148:151], a[104:107], a[12:15], v[148:151]\n"
"v_mfma_f32_16x16x32_bf16 v[156:159], a[108:111], a[12:15], v[156:159]\n"
"ds_read_b128 a[76:79], v210 offset:37376\n"
"v_mfma_f32_16x16x32_bf16 v[152:155], a[108:111], a[8:11], v[152:155]\n"
"ds_read_b128 a[20:23], v206 offset:32768\n"
"s_waitcnt lgkmcnt(6)\n"
"v_mfma_f32_16x16x32_bf16 v[160:163], a[112:115], a[8:11], v[160:163]\n"
"ds_read_b128 a[80:83], v210 offset:40960\n"
"buffer_load_dwordx4 v18, s[44:47], s79 offen offset:2048 lds\n"
"v_mfma_f32_16x16x32_bf16 v[164:167], a[112:115], a[12:15], v[164:167]\n"
"v_mfma_f32_16x16x32_bf16 v[172:175], a[116:119], a[12:15], v[172:175]\n"
"ds_read_b128 a[84:87], v210 offset:41472\n"
"v_mfma_f32_16x16x32_bf16 v[168:171], a[116:119], a[8:11], v[168:171]\n"
"v_mfma_f32_16x16x32_bf16 v[176:179], a[120:123], a[8:11], v[176:179]\n"
"ds_read_b128 a[88:91], v210 offset:45056\n"
"buffer_load_dwordx4 v19, s[44:47], s79 offen offset:3072 lds\n"
"v_mfma_f32_16x16x32_bf16 v[180:183], a[120:123], a[12:15], v[180:183]\n"
"s_add_u32 s79, s79, 128\n"
"v_mfma_f32_16x16x32_bf16 v[188:191], a[124:127], a[12:15], v[188:191]\n"
"ds_read_b128 a[92:95], v210 offset:45568\n"
"v_mfma_f32_16x16x32_bf16 v[184:187], a[124:127], a[8:11], v[184:187]\n"
"s_waitcnt vmcnt(9)\n"
"s_barrier\n"
"s_waitcnt lgkmcnt(5)\n"
"v_mfma_f32_16x16x32_bf16 v[128:131], a[64:67], a[16:19], v[128:131]\n"
"ds_read_b128 a[96:99], v211 offset:32768\n"
"s_add_u32 m0, s14, 65536\n"
"s_waitcnt lgkmcnt(5)\n"
"v_mfma_f32_16x16x32_bf16 v[132:135], a[64:67], a[20:23], v[132:135]\n"
"buffer_load_dwordx4 v4, s[40:43], s79 offen offset:0 lds\n"
"v_mfma_f32_16x16x32_bf16 v[140:143], a[68:71], a[20:23], v[140:143]\n"
"ds_read_b128 a[100:103], v211 offset:33280\n"
"v_mfma_f32_16x16x32_bf16 v[136:139], a[68:71], a[16:19], v[136:139]\n"
"ds_read_b128 a[24:27], v203 offset:32768\n"
"v_mfma_f32_16x16x32_bf16 v[144:147], a[72:75], a[16:19], v[144:147]\n"
"ds_read_b128 a[104:107], v211 offset:36864\n"
"buffer_load_dwordx4 v5, s[40:43], s79 offen offset:1024 lds\n"
"v_mfma_f32_16x16x32_bf16 v[148:151], a[72:75], a[20:23], v[148:151]\n"
"v_mfma_f32_16x16x32_bf16 v[156:159], a[76:79], a[20:23], v[156:159]\n"
"ds_read_b128 a[108:111], v211 offset:37376\n"
"v_mfma_f32_16x16x32_bf16 v[152:155], a[76:79], a[16:19], v[152:155]\n"
"ds_read_b128 a[28:31], v207 offset:32768\n"
"s_waitcnt lgkmcnt(6)\n"
"v_mfma_f32_16x16x32_bf16 v[160:163], a[80:83], a[16:19], v[160:163]\n"
"ds_read_b128 a[112:115], v211 offset:40960\n"
"buffer_load_dwordx4 v6, s[40:43], s79 offen offset:2048 lds\n"
"v_mfma_f32_16x16x32_bf16 v[164:167], a[80:83], a[20:23], v[164:167]\n"
"v_mfma_f32_16x16x32_bf16 v[172:175], a[84:87], a[20:23], v[172:175]\n"
"ds_read_b128 a[116:119], v211 offset:41472\n"
"v_mfma_f32_16x16x32_bf16 v[168:171], a[84:87], a[16:19], v[168:171]\n"
"v_mfma_f32_16x16x32_bf16 v[176:179], a[88:91], a[16:19], v[176:179]\n"
"ds_read_b128 a[120:123], v211 offset:45056\n"
"buffer_load_dwordx4 v7, s[40:43], s79 offen offset:3072 lds\n"
"v_mfma_f32_16x16x32_bf16 v[180:183], a[88:91], a[20:23], v[180:183]\n"
"v_mfma_f32_16x16x32_bf16 v[188:191], a[92:95], a[20:23], v[188:191]\n"
"ds_read_b128 a[124:127], v211 offset:45568\n"
"v_mfma_f32_16x16x32_bf16 v[184:187], a[92:95], a[16:19], v[184:187]\n"
"s_add_u32 m0, s14, 81920\n"
"s_waitcnt lgkmcnt(5)\n"
"v_mfma_f32_16x16x32_bf16 v[128:131], a[96:99], a[24:27], v[128:131]\n"
"ds_read_b128 a[64:67], v208 offset:0\n"
"buffer_load_dwordx4 v16, s[44:47], s79 offen offset:0 lds\n"
"s_waitcnt lgkmcnt(5)\n"
"v_mfma_f32_16x16x32_bf16 v[132:135], a[96:99], a[28:31], v[132:135]\n"
"v_mfma_f32_16x16x32_bf16 v[140:143], a[100:103], a[28:31], v[140:143]\n"
"ds_read_b128 a[68:71], v208 offset:512\n"
"v_mfma_f32_16x16x32_bf16 v[136:139], a[100:103], a[24:27], v[136:139]\n"
"ds_read_b128 a[0:3], v200 offset:0\n"
"v_mfma_f32_16x16x32_bf16 v[144:147], a[104:107], a[24:27], v[144:147]\n"
"ds_read_b128 a[72:75], v208 offset:4096\n"
"buffer_load_dwordx4 v17, s[44:47], s79 offen offset:1024 lds\n"
"v_mfma_f32_16x16x32_bf16 v[148:151], a[104:107], a[28:31], v[148:151]\n"
"v_mfma_f32_16x16x32_bf16 v[156:159], a[108:111], a[28:31], v[156:159]\n"
"ds_read_b128 a[76:79], v208 offset:4608\n"
"v_mfma_f32_16x16x32_bf16 v[152:155], a[108:111], a[24:27], v[152:155]\n"
"ds_read_b128 a[4:7], v204 offset:0\n"
"s_waitcnt lgkmcnt(6)\n"
"v_mfma_f32_16x16x32_bf16 v[160:163], a[112:115], a[24:27], v[160:163]\n"
"ds_read_b128 a[80:83], v208 offset:8192\n"
"buffer_load_dwordx4 v18, s[44:47], s79 offen offset:2048 lds\n"
"v_mfma_f32_16x16x32_bf16 v[164:167], a[112:115], a[28:31], v[164:167]\n"
"v_mfma_f32_16x16x32_bf16 v[172:175], a[116:119], a[28:31], v[172:175]\n"
"ds_read_b128 a[84:87], v208 offset:8704\n"
"v_mfma_f32_16x16x32_bf16 v[168:171], a[116:119], a[24:27], v[168:171]\n"
"v_mfma_f32_16x16x32_bf16 v[176:179], a[120:123], a[24:27], v[176:179]\n"
"ds_read_b128 a[88:91], v208 offset:12288\n"
"buffer_load_dwordx4 v19, s[44:47], s79 offen offset:3072 lds\n"
"v_mfma_f32_16x16x32_bf16 v[180:183], a[120:123], a[28:31], v[180:183]\n"
"s_add_u32 s79, s79, 128\n"
"v_mfma_f32_16x16x32_bf16 v[188:191], a[124:127], a[28:31], v[188:191]\n"
"ds_read_b128 a[92:95], v208 offset:12800\n"
"v_mfma_f32_16x16x32_bf16 v[184:187], a[124:127], a[24:27], v[184:187]\n"
"s_waitcnt vmcnt(8)\n"
"s_barrier\n"
"s_waitcnt lgkmcnt(5)\n"
"v_mfma_f32_16x16x32_bf16 v[128:131], a[64:67], a[0:3], v[128:131]\n"
"ds_read_b128 a[96:99], v209 offset:0\n"
"s_add_u32 m0, s14, 98304\n"
"s_waitcnt lgkmcnt(5)\n"
"v_mfma_f32_16x16x32_bf16 v[132:135], a[64:67], a[4:7], v[132:135]\n"
"buffer_load_dwordx4 v4, s[40:43], s79 offen offset:0 lds\n"
"v_mfma_f32_16x16x32_bf16 v[140:143], a[68:71], a[4:7], v[140:143]\n"
"ds_read_b128 a[100:103], v209 offset:512\n"
"v_mfma_f32_16x16x32_bf16 v[136:139], a[68:71], a[0:3], v[136:139]\n"
"ds_read_b128 a[8:11], v201 offset:0\n"
"v_mfma_f32_16x16x32_bf16 v[144:147], a[72:75], a[0:3], v[144:147]\n"
"ds_read_b128 a[104:107], v209 offset:4096\n"
"buffer_load_dwordx4 v5, s[40:43], s79 offen offset:1024 lds\n"
"v_mfma_f32_16x16x32_bf16 v[148:151], a[72:75], a[4:7], v[148:151]\n"
"v_mfma_f32_16x16x32_bf16 v[156:159], a[76:79], a[4:7], v[156:159]\n"
"ds_read_b128 a[108:111], v209 offset:4608\n"
"v_mfma_f32_16x16x32_bf16 v[152:155], a[76:79], a[0:3], v[152:155]\n"
"ds_read_b128 a[12:15], v205 offset:0\n"
"s_waitcnt lgkmcnt(6)\n"
"v_mfma_f32_16x16x32_bf16 v[160:163], a[80:83], a[0:3], v[160:163]\n"
"ds_read_b128 a[112:115], v209 offset:8192\n"
"buffer_load_dwordx4 v6, s[40:43], s79 offen offset:2048 lds\n"
"v_mfma_f32_16x16x32_bf16 v[164:167], a[80:83], a[4:7], v[164:167]\n"
"v_mfma_f32_16x16x32_bf16 v[172:175], a[84:87], a[4:7], v[172:175]\n"
"ds_read_b128 a[116:119], v209 offset:8704\n"
"v_mfma_f32_16x16x32_bf16 v[168:171], a[84:87], a[0:3], v[168:171]\n"
"v_mfma_f32_16x16x32_bf16 v[176:179], a[88:91], a[0:3], v[176:179]\n"
"ds_read_b128 a[120:123], v209 offset:12288\n"
"buffer_load_dwordx4 v7, s[40:43], s79 offen offset:3072 lds\n"
"v_mfma_f32_16x16x32_bf16 v[180:183], a[88:91], a[4:7], v[180:183]\n"
"v_mfma_f32_16x16x32_bf16 v[188:191], a[92:95], a[4:7], v[188:191]\n"
"ds_read_b128 a[124:127], v209 offset:12800\n"
"v_mfma_f32_16x16x32_bf16 v[184:187], a[92:95], a[0:3], v[184:187]\n"
"s_add_u32 m0, s14, 114688\n"
"s_waitcnt lgkmcnt(5)\n"
"v_mfma_f32_16x16x32_bf16 v[128:131], a[96:99], a[8:11], v[128:131]\n"
"ds_read_b128 a[64:67], v208 offset:32768\n"
"buffer_load_dwordx4 v16, s[44:47], s79 offen offset:0 lds\n"
"s_waitcnt lgkmcnt(5)\n"
"v_mfma_f32_16x16x32_bf16 v[132:135], a[96:99], a[12:15], v[132:135]\n"
"v_mfma_f32_16x16x32_bf16 v[140:143], a[100:103], a[12:15], v[140:143]\n"
"ds_read_b128 a[68:71], v208 offset:33280\n"
"v_mfma_f32_16x16x32_bf16 v[136:139], a[100:103], a[8:11], v[136:139]\n"
"ds_read_b128 a[16:19], v200 offset:32768\n"
"v_mfma_f32_16x16x32_bf16 v[144:147], a[104:107], a[8:11], v[144:147]\n"
"ds_read_b128 a[72:75], v208 offset:36864\n"
"buffer_load_dwordx4 v17, s[44:47], s79 offen offset:1024 lds\n"
"v_mfma_f32_16x16x32_bf16 v[148:151], a[104:107], a[12:15], v[148:151]\n"
"v_mfma_f32_16x16x32_bf16 v[156:159], a[108:111], a[12:15], v[156:159]\n"
"ds_read_b128 a[76:79], v208 offset:37376\n"
"v_mfma_f32_16x16x32_bf16 v[152:155], a[108:111], a[8:11], v[152:155]\n"
"ds_read_b128 a[20:23], v204 offset:32768\n"
"s_waitcnt lgkmcnt(6)\n"
"v_mfma_f32_16x16x32_bf16 v[160:163], a[112:115], a[8:11], v[160:163]\n"
"ds_read_b128 a[80:83], v208 offset:40960\n"
"buffer_load_dwordx4 v18, s[44:47], s79 offen offset:2048 lds\n"
"v_mfma_f32_16x16x32_bf16 v[164:167], a[112:115], a[12:15], v[164:167]\n"
"v_mfma_f32_16x16x32_bf16 v[172:175], a[116:119], a[12:15], v[172:175]\n"
"ds_read_b128 a[84:87], v208 offset:41472\n"
"v_mfma_f32_16x16x32_bf16 v[168:171], a[116:119], a[8:11], v[168:171]\n"
"v_mfma_f32_16x16x32_bf16 v[176:179], a[120:123], a[8:11], v[176:179]\n"
"ds_read_b128 a[88:91], v208 offset:45056\n"
"buffer_load_dwordx4 v19, s[44:47], s79 offen offset:3072 lds\n"
"v_mfma_f32_16x16x32_bf16 v[180:183], a[120:123], a[12:15], v[180:183]\n"
"s_add_u32 s79, s79, 128\n"
"v_mfma_f32_16x16x32_bf16 v[188:191], a[124:127], a[12:15], v[188:191]\n"
"ds_read_b128 a[92:95], v208 offset:45568\n"
"v_mfma_f32_16x16x32_bf16 v[184:187], a[124:127], a[8:11], v[184:187]\n"
"s_waitcnt vmcnt(8)\n"
"s_barrier\n"
"s_waitcnt lgkmcnt(5)\n"
"v_mfma_f32_16x16x32_bf16 v[128:131], a[64:67], a[16:19], v[128:131]\n"
"ds_read_b128 a[96:99], v209 offset:32768\n"
"s_add_u32 m0, s14, 0\n"
"s_waitcnt lgkmcnt(5)\n"
"v_mfma_f32_16x16x32_bf16 v[132:135], a[64:67], a[20:23], v[132:135]\n"
"buffer_load_dwordx4 v4, s[40:43], s79 offen offset:0 lds\n"
"v_mfma_f32_16x16x32_bf16 v[140:143], a[68:71], a[20:23], v[140:143]\n"
"ds_read_b128 a[100:103], v209 offset:33280\n"
"v_mfma_f32_16x16x32_bf16 v[136:139], a[68:71], a[16:19], v[136:139]\n"
"ds_read_b128 a[24:27], v201 offset:32768\n"
"v_mfma_f32_16x16x32_bf16 v[144:147], a[72:75], a[16:19], v[144:147]\n"
"ds_read_b128 a[104:107], v209 offset:36864\n"
"buffer_load_dwordx4 v5, s[40:43], s79 offen offset:1024 lds\n"
"v_mfma_f32_16x16x32_bf16 v[148:151], a[72:75], a[20:23], v[148:151]\n"
"v_mfma_f32_16x16x32_bf16 v[156:159], a[76:79], a[20:23], v[156:159]\n"
"ds_read_b128 a[108:111], v209 offset:37376\n"
"v_mfma_f32_16x16x32_bf16 v[152:155], a[76:79], a[16:19], v[152:155]\n"
"ds_read_b128 a[28:31], v205 offset:32768\n"
"s_waitcnt lgkmcnt(6)\n"
"v_mfma_f32_16x16x32_bf16 v[160:163], a[80:83], a[16:19], v[160:163]\n"
"ds_read_b128 a[112:115], v209 offset:40960\n"
"buffer_load_dwordx4 v6, s[40:43], s79 offen offset:2048 lds\n"
"v_mfma_f32_16x16x32_bf16 v[164:167], a[80:83], a[20:23], v[164:167]\n"
"v_mfma_f32_16x16x32_bf16 v[172:175], a[84:87], a[20:23], v[172:175]\n"
"ds_read_b128 a[116:119], v209 offset:41472\n"
"v_mfma_f32_16x16x32_bf16 v[168:171], a[84:87], a[16:19], v[168:171]\n"
"v_mfma_f32_16x16x32_bf16 v[176:179], a[88:91], a[16:19], v[176:179]\n"
"ds_read_b128 a[120:123], v209 offset:45056\n"
"buffer_load_dwordx4 v7, s[40:43], s79 offen offset:3072 lds\n"
"v_mfma_f32_16x16x32_bf16 v[180:183], a[88:91], a[20:23], v[180:183]\n"
"v_mfma_f32_16x16x32_bf16 v[188:191], a[92:95], a[20:23], v[188:191]\n"
"ds_read_b128 a[124:127], v209 offset:45568\n"
"v_mfma_f32_16x16x32_bf16 v[184:187], a[92:95], a[16:19], v[184:187]\n"
"s_add_u32 m0, s14, 16384\n"
"s_waitcnt lgkmcnt(5)\n"
"v_mfma_f32_16x16x32_bf16 v[128:131], a[96:99], a[24:27], v[128:131]\n"
"ds_read_b128 a[64:67], v210 offset:0\n"
"buffer_load_dwordx4 v16, s[44:47], s79 offen offset:0 lds\n"
"s_waitcnt lgkmcnt(5)\n"
"v_mfma_f32_16x16x32_bf16 v[132:135], a[96:99], a[28:31], v[132:135]\n"
"v_mfma_f32_16x16x32_bf16 v[140:143], a[100:103], a[28:31], v[140:143]\n"
"ds_read_b128 a[68:71], v210 offset:512\n"
"v_mfma_f32_16x16x32_bf16 v[136:139], a[100:103], a[24:27], v[136:139]\n"
"ds_read_b128 a[0:3], v202 offset:0\n"
"v_mfma_f32_16x16x32_bf16 v[144:147], a[104:107], a[24:27], v[144:147]\n"
"ds_read_b128 a[72:75], v210 offset:4096\n"
"buffer_load_dwordx4 v17, s[44:47], s79 offen offset:1024 lds\n"
"v_mfma_f32_16x16x32_bf16 v[148:151], a[104:107], a[28:31], v[148:151]\n"
"v_mfma_f32_16x16x32_bf16 v[156:159], a[108:111], a[28:31], v[156:159]\n"
"ds_read_b128 a[76:79], v210 offset:4608\n"
"v_mfma_f32_16x16x32_bf16 v[152:155], a[108:111], a[24:27], v[152:155]\n"
"ds_read_b128 a[4:7], v206 offset:0\n"
"s_waitcnt lgkmcnt(6)\n"
"v_mfma_f32_16x16x32_bf16 v[160:163], a[112:115], a[24:27], v[160:163]\n"
"ds_read_b128 a[80:83], v210 offset:8192\n"
"buffer_load_dwordx4 v18, s[44:47], s79 offen offset:2048 lds\n"
"v_mfma_f32_16x16x32_bf16 v[164:167], a[112:115], a[28:31], v[164:167]\n"
"v_mfma_f32_16x16x32_bf16 v[172:175], a[116:119], a[28:31], v[172:175]\n"
"ds_read_b128 a[84:87], v210 offset:8704\n"
"v_mfma_f32_16x16x32_bf16 v[168:171], a[116:119], a[24:27], v[168:171]\n"
"v_mfma_f32_16x16x32_bf16 v[176:179], a[120:123], a[24:27], v[176:179]\n"
"ds_read_b128 a[88:91], v210 offset:12288\n"
"buffer_load_dwordx4 v19, s[44:47], s79 offen offset:3072 lds\n"
"v_mfma_f32_16x16x32_bf16 v[180:183], a[120:123], a[28:31], v[180:183]\n"
"s_add_u32 s79, s79, 128\n"
"v_mfma_f32_16x16x32_bf16 v[188:191], a[124:127], a[28:31], v[188:191]\n"
"ds_read_b128 a[92:95], v210 offset:12800\n"
"v_mfma_f32_16x16x32_bf16 v[184:187], a[124:127], a[24:27], v[184:187]\n"
"s_waitcnt vmcnt(8)\n"
"s_barrier\n"
"s_waitcnt lgkmcnt(5)\n"
"v_mfma_f32_16x16x32_bf16 v[128:131], a[64:67], a[0:3], v[128:131]\n"
"ds_read_b128 a[96:99], v211 offset:0\n"
"s_add_u32 m0, s14, 32768\n"
"s_waitcnt lgkmcnt(5)\n"
"v_mfma_f32_16x16x32_bf16 v[132:135], a[64:67], a[4:7], v[132:135]\n"
"buffer_load_dwordx4 v4, s[40:43], s79 offen offset:0 lds\n"
"v_mfma_f32_16x16x32_bf16 v[140:143], a[68:71], a[4:7], v[140:143]\n"
"ds_read_b128 a[100:103], v211 offset:512\n"
"v_mfma_f32_16x16x32_bf16 v[136:139], a[68:71], a[0:3], v[136:139]\n"
"ds_read_b128 a[8:11], v203 offset:0\n"
"v_mfma_f32_16x16x32_bf16 v[144:147], a[72:75], a[0:3], v[144:147]\n"
"ds_read_b128 a[104:107], v211 offset:4096\n"
"buffer_load_dwordx4 v5, s[40:43], s79 offen offset:1024 lds\n"
"v_mfma_f32_16x16x32_bf16 v[148:151], a[72:75], a[4:7], v[148:151]\n"
"buffer_load_dword a228, v24, s[56:59], s95 offen offset:0\n"
"v_readlane_b32 s95, v11, 6\n"
"v_mfma_f32_16x16x32_bf16 v[156:159], a[76:79], a[4:7], v[156:159]\n"
"ds_read_b128 a[108:111], v211 offset:4608\n"
"v_mfma_f32_16x16x32_bf16 v[152:155], a[76:79], a[0:3], v[152:155]\n"
"ds_read_b128 a[12:15], v207 offset:0\n"
"s_waitcnt lgkmcnt(6)\n"
"v_mfma_f32_16x16x32_bf16 v[160:163], a[80:83], a[0:3], v[160:163]\n"
"ds_read_b128 a[112:115], v211 offset:8192\n"
"buffer_load_dwordx4 v6, s[40:43], s79 offen offset:2048 lds\n"
"v_mfma_f32_16x16x32_bf16 v[164:167], a[80:83], a[4:7], v[164:167]\n"
"v_mfma_f32_16x16x32_bf16 v[172:175], a[84:87], a[4:7], v[172:175]\n"
"ds_read_b128 a[116:119], v211 offset:8704\n"
"v_mfma_f32_16x16x32_bf16 v[168:171], a[84:87], a[0:3], v[168:171]\n"
"v_mfma_f32_16x16x32_bf16 v[176:179], a[88:91], a[0:3], v[176:179]\n"
"ds_read_b128 a[120:123], v211 offset:12288\n"
"buffer_load_dwordx4 v7, s[40:43], s79 offen offset:3072 lds\n"
"v_mfma_f32_16x16x32_bf16 v[180:183], a[88:91], a[4:7], v[180:183]\n"
"v_mfma_f32_16x16x32_bf16 v[188:191], a[92:95], a[4:7], v[188:191]\n"
"ds_read_b128 a[124:127], v211 offset:12800\n"
"v_mfma_f32_16x16x32_bf16 v[184:187], a[92:95], a[0:3], v[184:187]\n"
"s_add_u32 m0, s14, 49152\n"
"s_waitcnt lgkmcnt(5)\n"
"v_mfma_f32_16x16x32_bf16 v[128:131], a[96:99], a[8:11], v[128:131]\n"
"ds_read_b128 a[64:67], v210 offset:32768\n"
"buffer_load_dword a229, v24, s[56:59], s95 offen offset:0\n"
"s_waitcnt lgkmcnt(5)\n"
"v_mfma_f32_16x16x32_bf16 v[132:135], a[96:99], a[12:15], v[132:135]\n"
"v_readlane_b32 s95, v11, 8\n"
"buffer_load_dwordx4 v16, s[44:47], s79 offen offset:0 lds\n"
"v_mfma_f32_16x16x32_bf16 v[140:143], a[100:103], a[12:15], v[140:143]\n"
"ds_read_b128 a[68:71], v210 offset:33280\n"
"v_mfma_f32_16x16x32_bf16 v[136:139], a[100:103], a[8:11], v[136:139]\n"
"ds_read_b128 a[16:19], v202 offset:32768\n"
"v_mfma_f32_16x16x32_bf16 v[144:147], a[104:107], a[8:11], v[144:147]\n"
"ds_read_b128 a[72:75], v210 offset:36864\n"
"buffer_load_dwordx4 v17, s[44:47], s79 offen offset:1024 lds\n"
"v_mfma_f32_16x16x32_bf16 v[148:151], a[104:107], a[12:15], v[148:151]\n"
"v_mfma_f32_16x16x32_bf16 v[156:159], a[108:111], a[12:15], v[156:159]\n"
"ds_read_b128 a[76:79], v210 offset:37376\n"
"v_mfma_f32_16x16x32_bf16 v[152:155], a[108:111], a[8:11], v[152:155]\n"
"ds_read_b128 a[20:23], v206 offset:32768\n"
"s_waitcnt lgkmcnt(6)\n"
"v_mfma_f32_16x16x32_bf16 v[160:163], a[112:115], a[8:11], v[160:163]\n"
"ds_read_b128 a[80:83], v210 offset:40960\n"
"buffer_load_dwordx4 v18, s[44:47], s79 offen offset:2048 lds\n"
"v_mfma_f32_16x16x32_bf16 v[164:167], a[112:115], a[12:15], v[164:167]\n"
"buffer_load_dwordx2 v[26:27], v31, s[36:39], 0 offen offset:0 sc1\n"
"v_mfma_f32_16x16x32_bf16 v[172:175], a[116:119], a[12:15], v[172:175]\n"
"ds_read_b128 a[84:87], v210 offset:41472\n"
"v_mfma_f32_16x16x32_bf16 v[168:171], a[116:119], a[8:11], v[168:171]\n"
"v_mfma_f32_16x16x32_bf16 v[176:179], a[120:123], a[8:11], v[176:179]\n"
"ds_read_b128 a[88:91], v210 offset:45056\n"
"buffer_load_dwordx4 v19, s[44:47], s79 offen offset:3072 lds\n"
"v_mfma_f32_16x16x32_bf16 v[180:183], a[120:123], a[12:15], v[180:183]\n"
"s_add_u32 s79, s79, 128\n"
"v_mfma_f32_16x16x32_bf16 v[188:191], a[124:127], a[12:15], v[188:191]\n"
"ds_read_b128 a[92:95], v210 offset:45568\n"
"v_mfma_f32_16x16x32_bf16 v[184:187], a[124:127], a[8:11], v[184:187]\n"
"s_waitcnt vmcnt(11)\n"
"s_barrier\n"
"s_waitcnt lgkmcnt(5)\n"
"v_mfma_f32_16x16x32_bf16 v[128:131], a[64:67], a[16:19], v[128:131]\n"
"ds_read_b128 a[96:99], v211 offset:32768\n"
"s_add_u32 m0, s14, 65536\n"
"s_waitcnt lgkmcnt(5)\n"
"v_mfma_f32_16x16x32_bf16 v[132:135], a[64:67], a[20:23], v[132:135]\n"
"buffer_load_dwordx4 v4, s[40:43], s79 offen offset:0 lds\n"
"v_mfma_f32_16x16x32_bf16 v[140:143], a[68:71], a[20:23], v[140:143]\n"
"ds_read_b128 a[100:103], v211 offset:33280\n"
"v_mfma_f32_16x16x32_bf16 v[136:139], a[68:71], a[16:19], v[136:139]\n"
"ds_read_b128 a[24:27], v203 offset:32768\n"
"v_mfma_f32_16x16x32_bf16 v[144:147], a[72:75], a[16:19], v[144:147]\n"
"ds_read_b128 a[104:107], v211 offset:36864\n"
"buffer_load_dwordx4 v5, s[40:43], s79 offen offset:1024 lds\n"
"v_mfma_f32_16x16x32_bf16 v[148:151], a[72:75], a[20:23], v[148:151]\n"
"v_mfma_f32_16x16x32_bf16 v[156:159], a[76:79], a[20:23], v[156:159]\n"
"ds_read_b128 a[108:111], v211 offset:37376\n"
"v_mfma_f32_16x16x32_bf16 v[152:155], a[76:79], a[16:19], v[152:155]\n"
"ds_read_b128 a[28:31], v207 offset:32768\n"
"s_waitcnt lgkmcnt(6)\n"
"v_mfma_f32_16x16x32_bf16 v[160:163], a[80:83], a[16:19], v[160:163]\n"
"ds_read_b128 a[112:115], v211 offset:40960\n"
"buffer_load_dwordx4 v6, s[40:43], s79 offen offset:2048 lds\n"
"v_mfma_f32_16x16x32_bf16 v[164:167], a[80:83], a[20:23], v[164:167]\n"
"v_mfma_f32_16x16x32_bf16 v[172:175], a[84:87], a[20:23], v[172:175]\n"
"ds_read_b128 a[116:119], v211 offset:41472\n"
"v_mfma_f32_16x16x32_bf16 v[168:171], a[84:87], a[16:19], v[168:171]\n"
"v_mfma_f32_16x16x32_bf16 v[176:179], a[88:91], a[16:19], v[176:179]\n"
"ds_read_b128 a[120:123], v211 offset:45056\n"
"buffer_load_dwordx4 v7, s[40:43], s79 offen offset:3072 lds\n"
"v_mfma_f32_16x16x32_bf16 v[180:183], a[88:91], a[20:23], v[180:183]\n"
"v_mfma_f32_16x16x32_bf16 v[188:191], a[92:95], a[20:23], v[188:191]\n"
"ds_read_b128 a[124:127], v211 offset:45568\n"
"v_mfma_f32_16x16x32_bf16 v[184:187], a[92:95], a[16:19], v[184:187]\n"
"s_add_u32 m0, s14, 81920\n"
"s_waitcnt lgkmcnt(5)\n"
"v_mfma_f32_16x16x32_bf16 v[128:131], a[96:99], a[24:27], v[128:131]\n"
"ds_read_b128 a[64:67], v208 offset:0\n"
"buffer_load_dwordx4 v16, s[44:47], s79 offen offset:0 lds\n"
"s_waitcnt lgkmcnt(5)\n"
"v_mfma_f32_16x16x32_bf16 v[132:135], a[96:99], a[28:31], v[132:135]\n"
"v_mfma_f32_16x16x32_bf16 v[140:143], a[100:103], a[28:31], v[140:143]\n"
"ds_read_b128 a[68:71], v208 offset:512\n"
"v_mfma_f32_16x16x32_bf16 v[136:139], a[100:103], a[24:27], v[136:139]\n"
"ds_read_b128 a[0:3], v200 offset:0\n"
"v_mfma_f32_16x16x32_bf16 v[144:147], a[104:107], a[24:27], v[144:147]\n"
"ds_read_b128 a[72:75], v208 offset:4096\n"
"buffer_load_dwordx4 v17, s[44:47], s79 offen offset:1024 lds\n"
"v_mfma_f32_16x16x32_bf16 v[148:151], a[104:107], a[28:31], v[148:151]\n"
"v_mfma_f32_16x16x32_bf16 v[156:159], a[108:111], a[28:31], v[156:159]\n"
"ds_read_b128 a[76:79], v208 offset:4608\n"
"v_mfma_f32_16x16x32_bf16 v[152:155], a[108:111], a[24:27], v[152:155]\n"
"ds_read_b128 a[4:7], v204 offset:0\n"
"s_waitcnt lgkmcnt(6)\n"
"v_mfma_f32_16x16x32_bf16 v[160:163], a[112:115], a[24:27], v[160:163]\n"
"ds_read_b128 a[80:83], v208 offset:8192\n"
"buffer_load_dwordx4 v18, s[44:47], s79 offen offset:2048 lds\n"
"v_mfma_f32_16x16x32_bf16 v[164:167], a[112:115], a[28:31], v[164:167]\n"
"v_mfma_f32_16x16x32_bf16 v[172:175], a[116:119], a[28:31], v[172:175]\n"
"ds_read_b128 a[84:87], v208 offset:8704\n"
"v_mfma_f32_16x16x32_bf16 v[168:171], a[116:119], a[24:27], v[168:171]\n"
"v_mfma_f32_16x16x32_bf16 v[176:179], a[120:123], a[24:27], v[176:179]\n"
"ds_read_b128 a[88:91], v208 offset:12288\n"
"buffer_load_dwordx4 v19, s[44:47], s79 offen offset:3072 lds\n"
"v_mfma_f32_16x16x32_bf16 v[180:183], a[120:123], a[28:31], v[180:183]\n"
"s_add_u32 s79, s79, 128\n"
"v_mfma_f32_16x16x32_bf16 v[188:191], a[124:127], a[28:31], v[188:191]\n"
"ds_read_b128 a[92:95], v208 offset:12800\n"
"v_mfma_f32_16x16x32_bf16 v[184:187], a[124:127], a[24:27], v[184:187]\n"
"s_waitcnt vmcnt(8)\n"
"s_barrier\n"
"s_waitcnt lgkmcnt(5)\n"
"v_mfma_f32_16x16x32_bf16 v[128:131], a[64:67], a[0:3], v[128:131]\n"
"ds_read_b128 a[96:99], v209 offset:0\n"
"s_add_u32 m0, s14, 98304\n"
"s_waitcnt lgkmcnt(5)\n"
"v_mfma_f32_16x16x32_bf16 v[132:135], a[64:67], a[4:7], v[132:135]\n"
"buffer_load_dwordx4 v4, s[40:43], s79 offen offset:0 lds\n"
"v_mfma_f32_16x16x32_bf16 v[140:143], a[68:71], a[4:7], v[140:143]\n"
"ds_read_b128 a[100:103], v209 offset:512\n"
"v_mfma_f32_16x16x32_bf16 v[136:139], a[68:71], a[0:3], v[136:139]\n"
"ds_read_b128 a[8:11], v201 offset:0\n"
"v_mfma_f32_16x16x32_bf16 v[144:147], a[72:75], a[0:3], v[144:147]\n"
"ds_read_b128 a[104:107], v209 offset:4096\n"
"buffer_load_dwordx4 v5, s[40:43], s79 offen offset:1024 lds\n"
"v_mfma_f32_16x16x32_bf16 v[148:151], a[72:75], a[4:7], v[148:151]\n"
"v_mfma_f32_16x16x32_bf16 v[156:159], a[76:79], a[4:7], v[156:159]\n"
"ds_read_b128 a[108:111], v209 offset:4608\n"
"v_mfma_f32_16x16x32_bf16 v[152:155], a[76:79], a[0:3], v[152:155]\n"
"ds_read_b128 a[12:15], v205 offset:0\n"
"v_cmp_ne_u64 vcc, s[70:71], v[26:27]\n"
"s_cbranch_vccnz .Lpslow0_%=\n"
".Lpok0_%=:\n"
"s_waitcnt lgkmcnt(6)\n"
"v_mfma_f32_16x16x32_bf16 v[160:163], a[80:83], a[0:3], v[160:163]\n"
"ds_read_b128 a[112:115], v209 offset:8192\n"
"buffer_load_dwordx4 v6, s[40:43], s79 offen offset:2048 lds\n"
"v_mfma_f32_16x16x32_bf16 v[164:167], a[80:83], a[4:7], v[164:167]\n"
"v_mfma_f32_16x16x32_bf16 v[172:175], a[84:87], a[4:7], v[172:175]\n"
"ds_read_b128 a[116:119], v209 offset:8704\n"
"v_mfma_f32_16x16x32_bf16 v[168:171], a[84:87], a[0:3], v[168:171]\n"
"v_mfma_f32_16x16x32_bf16 v[176:179], a[88:91], a[0:3], v[176:179]\n"
"ds_read_b128 a[120:123], v209 offset:12288\n"
"buffer_load_dwordx4 v7, s[40:43], s79 offen offset:3072 lds\n"
"v_mfma_f32_16x16x32_bf16 v[180:183], a[88:91], a[4:7], v[180:183]\n"
"v_mfma_f32_16x16x32_bf16 v[188:191], a[92:95], a[4:7], v[188:191]\n"
"ds_read_b128 a[124:127], v209 offset:12800\n"
"v_mfma_f32_16x16x32_bf16 v[184:187], a[92:95], a[0:3], v[184:187]\n"
"s_add_u32 m0, s14, 114688\n"
"s_waitcnt lgkmcnt(5)\n"
"v_mfma_f32_16x16x32_bf16 v[128:131], a[96:99], a[8:11], v[128:131]\n"
"ds_read_b128 a[64:67], v208 offset:32768\n"
"buffer_load_dwordx4 v16, s[44:47], s79 offen offset:0 lds\n"
"s_waitcnt lgkmcnt(5)\n"
"v_mfma_f32_16x16x32_bf16 v[132:135], a[96:99], a[12:15], v[132:135]\n"
"v_mfma_f32_16x16x32_bf16 v[140:143], a[100:103], a[12:15], v[140:143]\n"
"ds_read_b128 a[68:71], v208 offset:33280\n"
"v_mfma_f32_16x16x32_bf16 v[136:139], a[100:103], a[8:11], v[136:139]\n"
"ds_read_b128 a[16:19], v200 offset:32768\n"
"v_mfma_f32_16x16x32_bf16 v[144:147], a[104:107], a[8:11], v[144:147]\n"
"ds_read_b128 a[72:75], v208 offset:36864\n"
"buffer_load_dwordx4 v17, s[44:47], s79 offen offset:1024 lds\n"
"v_mfma_f32_16x16x32_bf16 v[148:151], a[104:107], a[12:15], v[148:151]\n"
"v_mfma_f32_16x16x32_bf16 v[156:159], a[108:111], a[12:15], v[156:159]\n"
"ds_read_b128 a[76:79], v208 offset:37376\n"
"v_mfma_f32_16x16x32_bf16 v[152:155], a[108:111], a[8:11], v[152:155]\n"
"ds_read_b128 a[20:23], v204 offset:32768\n"
"s_waitcnt lgkmcnt(6)\n"
"v_mfma_f32_16x16x32_bf16 v[160:163], a[112:115], a[8:11], v[160:163]\n"
"ds_read_b128 a[80:83], v208 offset:40960\n"
"buffer_load_dwordx2 v[96:97], v192, s[36:39], 0 offen offset:0 sc1\n"
"v_mfma_f32_16x16x32_bf16 v[164:167], a[112:115], a[12:15], v[164:167]\n"
"buffer_load_dword v20, v193, s[36:39], 0 offen offset:0 sc1\n"
"buffer_load_dword v22, v194, s[36:39], 0 offen offset:0 sc1\n"
"v_mfma_f32_16x16x32_bf16 v[172:175], a[116:119], a[12:15], v[172:175]\n"
"ds_read_b128 a[84:87], v208 offset:41472\n"
"buffer_load_dwordx4 v18, s[44:47], s79 offen offset:2048 lds\n"
"v_mfma_f32_16x16x32_bf16 v[168:171], a[116:119], a[8:11], v[168:171]\n"
"v_mfma_f32_16x16x32_bf16 v[176:179], a[120:123], a[8:11], v[176:179]\n"
"ds_read_b128 a[88:91], v208 offset:45056\n"
"buffer_load_dwordx4 v19, s[44:47], s79 offen offset:3072 lds\n"
"v_mfma_f32_16x16x32_bf16 v[180:183], a[120:123], a[12:15], v[180:183]\n"
"s_add_u32 s79, s79, 128\n"
"v_mfma_f32_16x16x32_bf16 v[188:191], a[124:127], a[12:15], v[188:191]\n"
"ds_read_b128 a[92:95], v208 offset:45568\n"
"v_mfma_f32_16x16x32_bf16 v[184:187], a[124:127], a[8:11], v[184:187]\n"
"s_waitcnt vmcnt(11)\n"
"s_barrier\n"
"s_waitcnt lgkmcnt(5)\n"
"v_mfma_f32_16x16x32_bf16 v[128:131], a[64:67], a[16:19], v[128:131]\n"
"ds_read_b128 a[96:99], v209 offset:32768\n"
"s_add_u32 m0, s14, 0\n"
"s_waitcnt lgkmcnt(5)\n"
"v_mfma_f32_16x16x32_bf16 v[132:135], a[64:67], a[20:23], v[132:135]\n"
"buffer_load_dwordx4 v4, s[40:43], s79 offen offset:0 lds\n"
"v_mfma_f32_16x16x32_bf16 v[140:143], a[68:71], a[20:23], v[140:143]\n"
"ds_read_b128 a[100:103], v209 offset:33280\n"
"v_mfma_f32_16x16x32_bf16 v[136:139], a[68:71], a[16:19], v[136:139]\n"
"ds_read_b128 a[24:27], v201 offset:32768\n"
"v_mfma_f32_16x16x32_bf16 v[144:147], a[72:75], a[16:19], v[144:147]\n"
"ds_read_b128 a[104:107], v209 offset:36864\n"
"buffer_load_dwordx4 v5, s[40:43], s79 offen offset:1024 lds\n"
"v_mfma_f32_16x16x32_bf16 v[148:151], a[72:75], a[20:23], v[148:151]\n"
"v_mfma_f32_16x16x32_bf16 v[156:159], a[76:79], a[20:23], v[156:159]\n"
"ds_read_b128 a[108:111], v209 offset:37376\n"
"v_mfma_f32_16x16x32_bf16 v[152:155], a[76:79], a[16:19], v[152:155]\n"
"ds_read_b128 a[28:31], v205 offset:32768\n"
"s_waitcnt lgkmcnt(6)\n"
"v_mfma_f32_16x16x32_bf16 v[160:163], a[80:83], a[16:19], v[160:163]\n"
"ds_read_b128 a[112:115], v209 offset:40960\n"
"buffer_load_dwordx4 v6, s[40:43], s79 offen offset:2048 lds\n"
"v_mfma_f32_16x16x32_bf16 v[164:167], a[80:83], a[20:23], v[164:167]\n"
"v_mfma_f32_16x16x32_bf16 v[172:175], a[84:87], a[20:23], v[172:175]\n"
"ds_read_b128 a[116:119], v209 offset:41472\n"
"v_mfma_f32_16x16x32_bf16 v[168:171], a[84:87], a[16:19], v[168:171]\n"
"v_mfma_f32_16x16x32_bf16 v[176:179], a[88:91], a[16:19], v[176:179]\n"
"ds_read_b128 a[120:123], v209 offset:45056\n"
"buffer_load_dwordx4 v7, s[40:43], s79 offen offset:3072 lds\n"
"v_mfma_f32_16x16x32_bf16 v[180:183], a[88:91], a[20:23], v[180:183]\n"
"v_mfma_f32_16x16x32_bf16 v[188:191], a[92:95], a[20:23], v[188:191]\n"
"ds_read_b128 a[124:127], v209 offset:45568\n"
"v_mfma_f32_16x16x32_bf16 v[184:187], a[92:95], a[16:19], v[184:187]\n"
"s_add_u32 m0, s14, 16384\n"
"s_waitcnt lgkmcnt(5)\n"
"v_mfma_f32_16x16x32_bf16 v[128:131], a[96:99], a[24:27], v[128:131]\n"
"ds_read_b128 a[64:67], v210 offset:0\n"
"buffer_load_dwordx4 v16, s[44:47], s79 offen offset:0 lds\n"
"s_waitcnt lgkmcnt(5)\n"
"v_mfma_f32_16x16x32_bf16 v[132:135], a[96:99], a[28:31], v[132:135]\n"
"v_mfma_f32_16x16x32_bf16 v[140:143], a[100:103], a[28:31], v[140:143]\n"
"ds_read_b128 a[68:71], v210 offset:512\n"
"v_mfma_f32_16x16x32_bf16 v[136:139], a[100:103], a[24:27], v[136:139]\n"
"ds_read_b128 a[0:3], v202 offset:0\n"
"v_mfma_f32_16x16x32_bf16 v[144:147], a[104:107], a[24:27], v[144:147]\n"
"ds_read_b128 a[72:75], v210 offset:4096\n"
"buffer_load_dwordx4 v17, s[44:47], s79 offen offset:1024 lds\n"
"v_mfma_f32_16x16x32_bf16 v[148:151], a[104:107], a[28:31], v[148:151]\n"
"v_mfma_f32_16x16x32_bf16 v[156:159], a[108:111], a[28:31], v[156:159]\n"
"ds_read_b128 a[76:79], v210 offset:4608\n"
"v_mfma_f32_16x16x32_bf16 v[152:155], a[108:111], a[24:27], v[152:155]\n"
"ds_read_b128 a[4:7], v206 offset:0\n"
"s_waitcnt lgkmcnt(6)\n"
"v_mfma_f32_16x16x32_bf16 v[160:163], a[112:115], a[24:27], v[160:163]\n"
"ds_read_b128 a[80:83], v210 offset:8192\n"
"buffer_load_dwordx4 v18, s[44:47], s79 offen offset:2048 lds\n"
"v_mfma_f32_16x16x32_bf16 v[164:167], a[112:115], a[28:31], v[164:167]\n"
"v_mfma_f32_16x16x32_bf16 v[172:175], a[116:119], a[28:31], v[172:175]\n"
"ds_read_b128 a[84:87], v210 offset:8704\n"
"v_mfma_f32_16x16x32_bf16 v[168:171], a[116:119], a[24:27], v[168:171]\n"
"v_mfma_f32_16x16x32_bf16 v[176:179], a[120:123], a[24:27], v[176:179]\n"
"ds_read_b128 a[88:91], v210 offset:12288\n"
"buffer_load_dwordx4 v19, s[44:47], s79 offen offset:3072 lds\n"
"v_mfma_f32_16x16x32_bf16 v[180:183], a[120:123], a[28:31], v[180:183]\n"
"s_add_u32 s79, s79, 128\n"
"v_mfma_f32_16x16x32_bf16 v[188:191], a[124:127], a[28:31], v[188:191]\n"
"ds_read_b128 a[92:95], v210 offset:12800\n"
"v_mfma_f32_16x16x32_bf16 v[184:187], a[124:127], a[24:27], v[184:187]\n"
"s_waitcnt vmcnt(8)\n"
"s_barrier\n"
"s_waitcnt lgkmcnt(5)\n"
"v_mfma_f32_16x16x32_bf16 v[128:131], a[64:67], a[0:3], v[128:131]\n"
"ds_read_b128 a[96:99], v211 offset:0\n"
"s_add_u32 m0, s14, 32768\n"
"s_waitcnt lgkmcnt(5)\n"
"v_mfma_f32_16x16x32_bf16 v[132:135], a[64:67], a[4:7], v[132:135]\n"
"buffer_load_dwordx4 v4, s[40:43], s79 offen offset:0 lds\n"
"v_mfma_f32_16x16x32_bf16 v[140:143], a[68:71], a[4:7], v[140:143]\n"
"ds_read_b128 a[100:103], v211 offset:512\n"
"v_mfma_f32_16x16x32_bf16 v[136:139], a[68:71], a[0:3], v[136:139]\n"
"ds_read_b128 a[8:11], v203 offset:0\n"
"v_mfma_f32_16x16x32_bf16 v[144:147], a[72:75], a[0:3], v[144:147]\n"
"ds_read_b128 a[104:107], v211 offset:4096\n"
"buffer_load_dwordx4 v5, s[40:43], s79 offen offset:1024 lds\n"
"v_mfma_f32_16x16x32_bf16 v[148:151], a[72:75], a[4:7], v[148:151]\n"
"v_mfma_f32_16x16x32_bf16 v[156:159], a[76:79], a[4:7], v[156:159]\n"
"ds_read_b128 a[108:111], v211 offset:4608\n"
"v_mfma_f32_16x16x32_bf16 v[152:155], a[76:79], a[0:3], v[152:155]\n"
"ds_read_b128 a[12:15], v207 offset:0\n"
"s_waitcnt lgkmcnt(6)\n"
"v_mfma_f32_16x16x32_bf16 v[160:163], a[80:83], a[0:3], v[160:163]\n"
"ds_read_b128 a[112:115], v211 offset:8192\n"
"buffer_load_dwordx4 v6, s[40:43], s79 offen offset:2048 lds\n"
"v_mfma_f32_16x16x32_bf16 v[164:167], a[80:83], a[4:7], v[164:167]\n"
"v_mfma_f32_16x16x32_bf16 v[172:175], a[84:87], a[4:7], v[172:175]\n"
"ds_read_b128 a[116:119], v211 offset:8704\n"
"v_mfma_f32_16x16x32_bf16 v[168:171], a[84:87], a[0:3], v[168:171]\n"
"v_mfma_f32_16x16x32_bf16 v[176:179], a[88:91], a[0:3], v[176:179]\n"
"ds_read_b128 a[120:123], v211 offset:12288\n"
"buffer_load_dwordx4 v7, s[40:43], s79 offen offset:3072 lds\n"
"v_mfma_f32_16x16x32_bf16 v[180:183], a[88:91], a[4:7], v[180:183]\n"
"v_mfma_f32_16x16x32_bf16 v[188:191], a[92:95], a[4:7], v[188:191]\n"
"ds_read_b128 a[124:127], v211 offset:12800\n"
"v_mfma_f32_16x16x32_bf16 v[184:187], a[92:95], a[0:3], v[184:187]\n"
"s_add_u32 m0, s14, 49152\n"
"s_waitcnt lgkmcnt(5)\n"
"v_mfma_f32_16x16x32_bf16 v[128:131], a[96:99], a[8:11], v[128:131]\n"
"ds_read_b128 a[64:67], v210 offset:32768\n"
"s_lshl_b32 s75, s61, 31\n"
"s_waitcnt lgkmcnt(5)\n"
"v_mfma_f32_16x16x32_bf16 v[132:135], a[96:99], a[12:15], v[132:135]\n"
"v_xor_b32 v96, s75, v96\n"
"v_xor_b32 v97, s75, v97\n"
"v_mfma_f32_16x16x32_bf16 v[140:143], a[100:103], a[12:15], v[140:143]\n"
"ds_read_b128 a[68:71], v210 offset:33280\n"
"ds_write_b32 v195, v96\n"
"v_mfma_f32_16x16x32_bf16 v[136:139], a[100:103], a[8:11], v[136:139]\n"
"ds_write_b32 v195, v97 offset:512\n"
"v_mov_b32 v21, v20\n"
"v_mfma_f32_16x16x32_bf16 v[144:147], a[104:107], a[8:11], v[144:147]\n"
"ds_read_b128 a[72:75], v210 offset:36864\n"
"v_mov_b32 v23, v22\n"
"v_mfma_f32_16x16x32_bf16 v[148:151], a[104:107], a[12:15], v[148:151]\n"
"buffer_load_dwordx4 v16, s[44:47], s79 offen offset:0 lds\n"
"ds_read_b128 a[16:19], v202 offset:32768\n"
"v_mfma_f32_16x16x32_bf16 v[156:159], a[108:111], a[12:15], v[156:159]\n"
"ds_read_b128 a[76:79], v210 offset:37376\n"
"buffer_load_dwordx4 v17, s[44:47], s79 offen offset:1024 lds\n"
"v_mfma_f32_16x16x32_bf16 v[152:155], a[108:111], a[8:11], v[152:155]\n"
"ds_read_b128 a[20:23], v206 offset:32768\n"
"s_waitcnt lgkmcnt(8)\n"
"v_mfma_f32_16x16x32_bf16 v[160:163], a[112:115], a[8:11], v[160:163]\n"
"ds_read_b128 a[80:83], v210 offset:40960\n"
"buffer_load_dwordx4 v18, s[44:47], s79 offen offset:2048 lds\n"
"v_mfma_f32_16x16x32_bf16 v[164:167], a[112:115], a[12:15], v[164:167]\n"
"v_mfma_f32_16x16x32_bf16 v[172:175], a[116:119], a[12:15], v[172:175]\n"
"ds_read_b128 a[84:87], v210 offset:41472\n"
"v_mfma_f32_16x16x32_bf16 v[168:171], a[116:119], a[8:11], v[168:171]\n"
"v_mfma_f32_16x16x32_bf16 v[176:179], a[120:123], a[8:11], v[176:179]\n"
"ds_read_b128 a[88:91], v210 offset:45056\n"
"buffer_load_dwordx4 v19, s[44:47], s79 offen offset:3072 lds\n"
"v_mfma_f32_16x16x32_bf16 v[180:183], a[120:123], a[12:15], v[180:183]\n"
"s_add_u32 s79, s79, 128\n"
"v_mfma_f32_16x16x32_bf16 v[188:191], a[124:127], a[12:15], v[188:191]\n"
"ds_read_b128 a[92:95], v210 offset:45568\n"
"v_mfma_f32_16x16x32_bf16 v[184:187], a[124:127], a[8:11], v[184:187]\n"
"s_waitcnt vmcnt(8)\n"
"s_barrier\n"
"s_waitcnt lgkmcnt(5)\n"
"v_mfma_f32_16x16x32_bf16 v[128:131], a[64:67], a[16:19], v[128:131]\n"
"ds_read_b128 a[96:99], v211 offset:32768\n"
"s_add_u32 m0, s14, 65536\n"
"s_waitcnt lgkmcnt(5)\n"
"v_mfma_f32_16x16x32_bf16 v[132:135], a[64:67], a[20:23], v[132:135]\n"
"buffer_load_dwordx4 v4, s[40:43], s79 offen offset:0 lds\n"
"v_mfma_f32_16x16x32_bf16 v[140:143], a[68:71], a[20:23], v[140:143]\n"
"ds_read_b128 a[100:103], v211 offset:33280\n"
"v_mfma_f32_16x16x32_bf16 v[136:139], a[68:71], a[16:19], v[136:139]\n"
"ds_read_b128 a[24:27], v203 offset:32768\n"
"v_mfma_f32_16x16x32_bf16 v[144:147], a[72:75], a[16:19], v[144:147]\n"
"ds_read_b128 a[104:107], v211 offset:36864\n"
"buffer_load_dwordx4 v5, s[40:43], s79 offen offset:1024 lds\n"
"v_mfma_f32_16x16x32_bf16 v[148:151], a[72:75], a[20:23], v[148:151]\n"
"v_mfma_f32_16x16x32_bf16 v[156:159], a[76:79], a[20:23], v[156:159]\n"
"ds_read_b128 a[108:111], v211 offset:37376\n"
"v_mfma_f32_16x16x32_bf16 v[152:155], a[76:79], a[16:19], v[152:155]\n"
"ds_read_b128 a[28:31], v207 offset:32768\n"
"s_waitcnt lgkmcnt(6)\n"
"v_mfma_f32_16x16x32_bf16 v[160:163], a[80:83], a[16:19], v[160:163]\n"
"ds_read_b128 a[112:115], v211 offset:40960\n"
"buffer_load_dword a230, v24, s[56:59], s95 offen offset:0\n"
"v_mfma_f32_16x16x32_bf16 v[164:167], a[80:83], a[20:23], v[164:167]\n"
"v_readlane_b32 s95, v11, 10\n"
"buffer_load_dwordx4 v6, s[40:43], s79 offen offset:2048 lds\n"
"v_mfma_f32_16x16x32_bf16 v[172:175], a[84:87], a[20:23], v[172:175]\n"
"ds_read_b128 a[116:119], v211 offset:41472\n"
"v_mfma_f32_16x16x32_bf16 v[168:171], a[84:87], a[16:19], v[168:171]\n"
"v_mfma_f32_16x16x32_bf16 v[176:179], a[88:91], a[16:19], v[176:179]\n"
"ds_read_b128 a[120:123], v211 offset:45056\n"
"buffer_load_dwordx4 v7, s[40:43], s79 offen offset:3072 lds\n"
"v_mfma_f32_16x16x32_bf16 v[180:183], a[88:91], a[20:23], v[180:183]\n"
"v_mfma_f32_16x16x32_bf16 v[188:191], a[92:95], a[20:23], v[188:191]\n"
"ds_read_b128 a[124:127], v211 offset:45568\n"
"v_mfma_f32_16x16x32_bf16 v[184:187], a[92:95], a[16:19], v[184:187]\n"
"s_add_u32 m0, s14, 81920\n"
"s_waitcnt lgkmcnt(5)\n"
"v_mfma_f32_16x16x32_bf16 v[128:131], a[96:99], a[24:27], v[128:131]\n"
"ds_read_b128 a[64:67], v208 offset:0\n"
"buffer_load_dwordx4 v16, s[44:47], s79 offen offset:0 lds\n"
"s_waitcnt lgkmcnt(5)\n"
"v_mfma_f32_16x16x32_bf16 v[132:135], a[96:99], a[28:31], v[132:135]\n"
"v_mfma_f32_16x16x32_bf16 v[140:143], a[100:103], a[28:31], v[140:143]\n"
"ds_read_b128 a[68:71], v208 offset:512\n"
"v_mfma_f32_16x16x32_bf16 v[136:139], a[100:103], a[24:27], v[136:139]\n"
"ds_read_b128 a[0:3], v200 offset:0\n"
"v_mfma_f32_16x16x32_bf16 v[144:147], a[104:107], a[24:27], v[144:147]\n"
"ds_read_b128 a[72:75], v208 offset:4096\n"
"buffer_load_dwordx4 v17, s[44:47], s79 offen offset:1024 lds\n"
"v_mfma_f32_16x16x32_bf16 v[148:151], a[104:107], a[28:31], v[148:151]\n"
"v_mfma_f32_16x16x32_bf16 v[156:159], a[108:111], a[28:31], v[156:159]\n"
"ds_read_b128 a[76:79], v208 offset:4608\n"
"v_mfma_f32_16x16x32_bf16 v[152:155], a[108:111], a[24:27], v[152:155]\n"
"ds_read_b128 a[4:7], v204 offset:0\n"
"s_waitcnt lgkmcnt(6)\n"
"v_mfma_f32_16x16x32_bf16 v[160:163], a[112:115], a[24:27], v[160:163]\n"
"ds_read_b128 a[80:83], v208 offset:8192\n"
"buffer_load_dword a231, v24, s[56:59], s95 offen offset:0\n"
"v_mfma_f32_16x16x32_bf16 v[164:167], a[112:115], a[28:31], v[164:167]\n"
"v_readlane_b32 s95, v11, 12\n"
"buffer_load_dwordx4 v18, s[44:47], s79 offen offset:2048 lds\n"
"v_mfma_f32_16x16x32_bf16 v[172:175], a[116:119], a[28:31], v[172:175]\n"
"ds_read_b128 a[84:87], v208 offset:8704\n"
"v_mfma_f32_16x16x32_bf16 v[168:171], a[116:119], a[24:27], v[168:171]\n"
"v_mfma_f32_16x16x32_bf16 v[176:179], a[120:123], a[24:27], v[176:179]\n"
"ds_read_b128 a[88:91], v208 offset:12288\n"
"buffer_load_dwordx4 v19, s[44:47], s79 offen offset:3072 lds\n"
"v_mfma_f32_16x16x32_bf16 v[180:183], a[120:123], a[28:31], v[180:183]\n"
"s_add_u32 s79, s79, 128\n"
"v_mfma_f32_16x16x32_bf16 v[188:191], a[124:127], a[28:31], v[188:191]\n"
"ds_read_b128 a[92:95], v208 offset:12800\n"
"v_mfma_f32_16x16x32_bf16 v[184:187], a[124:127], a[24:27], v[184:187]\n"
"s_waitcnt vmcnt(10)\n"
"s_barrier\n"
"s_waitcnt lgkmcnt(5)\n"
"v_mfma_f32_16x16x32_bf16 v[128:131], a[64:67], a[0:3], v[128:131]\n"
"ds_read_b128 a[96:99], v209 offset:0\n"
"s_add_u32 m0, s14, 98304\n"
"s_waitcnt lgkmcnt(5)\n"
"v_mfma_f32_16x16x32_bf16 v[132:135], a[64:67], a[4:7], v[132:135]\n"
"buffer_load_dwordx4 v4, s[40:43], s79 offen offset:0 lds\n"
"v_mfma_f32_16x16x32_bf16 v[140:143], a[68:71], a[4:7], v[140:143]\n"
"ds_read_b128 a[100:103], v209 offset:512\n"
"v_mfma_f32_16x16x32_bf16 v[136:139], a[68:71], a[0:3], v[136:139]\n"
"ds_read_b128 a[8:11], v201 offset:0\n"
"v_mfma_f32_16x16x32_bf16 v[144:147], a[72:75], a[0:3], v[144:147]\n"
"ds_read_b128 a[104:107], v209 offset:4096\n"
"buffer_load_dwordx4 v5, s[40:43], s79 offen offset:1024 lds\n"
"v_mfma_f32_16x16x32_bf16 v[148:151], a[72:75], a[4:7], v[148:151]\n"
"v_mfma_f32_16x16x32_bf16 v[156:159], a[76:79], a[4:7], v[156:159]\n"
"ds_read_b128 a[108:111], v209 offset:4608\n"
"v_mfma_f32_16x16x32_bf16 v[152:155], a[76:79], a[0:3], v[152:155]\n"
"ds_read_b128 a[12:15], v205 offset:0\n"
"s_waitcnt lgkmcnt(6)\n"
"v_mfma_f32_16x16x32_bf16 v[160:163], a[80:83], a[0:3], v[160:163]\n"
"ds_read_b128 a[112:115], v209 offset:8192\n"
"buffer_load_dwordx4 v6, s[40:43], s79 offen offset:2048 lds\n"
"v_mfma_f32_16x16x32_bf16 v[164:167], a[80:83], a[4:7], v[164:167]\n"
"v_mfma_f32_16x16x32_bf16 v[172:175], a[84:87], a[4:7], v[172:175]\n"
"ds_read_b128 a[116:119], v209 offset:8704\n"
"v_mfma_f32_16x16x32_bf16 v[168:171], a[84:87], a[0:3], v[168:171]\n"
"v_mfma_f32_16x16x32_bf16 v[176:179], a[88:91], a[0:3], v[176:179]\n"
"ds_read_b128 a[120:123], v209 offset:12288\n"
"buffer_load_dwordx4 v7, s[40:43], s79 offen offset:3072 lds\n"
"v_mfma_f32_16x16x32_bf16 v[180:183], a[88:91], a[4:7], v[180:183]\n"
"v_mfma_f32_16x16x32_bf16 v[188:191], a[92:95], a[4:7], v[188:191]\n"
"ds_read_b128 a[124:127], v209 offset:12800\n"
"v_mfma_f32_16x16x32_bf16 v[184:187], a[92:95], a[0:3], v[184:187]\n"
"s_add_u32 m0, s14, 114688\n"
"s_lshl_b32 s75, s61, 22\n"
"s_waitcnt lgkmcnt(5)\n"
"v_mfma_f32_16x16x32_bf16 v[128:131], a[96:99], a[8:11], v[128:131]\n"
"ds_read_b128 a[64:67], v208 offset:32768\n"
"s_lshl_b32 s76, s35, 19\n"
"s_add_u32 s75, s75, s76\n"
"s_waitcnt lgkmcnt(5)\n"
"v_mfma_f32_16x16x32_bf16 v[132:135], a[96:99], a[12:15], v[132:135]\n"
"s_lshl_b32 s76, s31, 17\n"
"s_add_u32 s75, s75, s76\n"
"s_lshl_b32 s76, s62, 16\n"
"v_mfma_f32_16x16x32_bf16 v[140:143], a[100:103], a[12:15], v[140:143]\n"
"ds_read_b128 a[68:71], v208 offset:33280\n"
"s_add_u32 s75, s75, s76\n"
"s_add_u32 s75, s75, 1048576\n"
"v_mfma_f32_16x16x32_bf16 v[136:139], a[100:103], a[8:11], v[136:139]\n"
"s_add_u32 s52, s28, s75\n"
"s_addc_u32 s53, s29, 0\n"
"v_mfma_f32_16x16x32_bf16 v[144:147], a[104:107], a[8:11], v[144:147]\n"
"ds_read_b128 a[72:75], v208 offset:36864\n"
"s_sub_u32 s54, 68157440, s75\n"
"v_mfma_f32_16x16x32_bf16 v[148:151], a[104:107], a[12:15], v[148:151]\n"
"s_lshl3_add_u32 s85, s60, s81\n"
"s_cmp_eq_u32 s60, 0\n"
"v_mfma_f32_16x16x32_bf16 v[156:159], a[108:111], a[12:15], v[156:159]\n"
"ds_read_b128 a[76:79], v208 offset:37376\n"
"s_cselect_b32 s75, 8, 0\n"
"v_mfma_f32_16x16x32_bf16 v[152:155], a[108:111], a[8:11], v[152:155]\n"
"s_add_u32 s86, s75, s81\n"
"s_waitcnt lgkmcnt(4)\n"
"v_mfma_f32_16x16x32_bf16 v[160:163], a[112:115], a[8:11], v[160:163]\n"
"ds_read_b128 a[80:83], v208 offset:40960\n"
"v_lshrrev_b32 v31, 3, v1\n"
"v_mfma_f32_16x16x32_bf16 v[164:167], a[112:115], a[12:15], v[164:167]\n"
"v_lshlrev_b32 v31, 3, v31\n"
"v_add_u32 v31, 128, v31\n"
"v_mfma_f32_16x16x32_bf16 v[172:175], a[116:119], a[12:15], v[172:175]\n"
"ds_read_b128 a[84:87], v208 offset:41472\n"
"buffer_load_dwordx4 v16, s[44:47], s79 offen offset:0 lds\n"
"v_mfma_f32_16x16x32_bf16 v[168:171], a[116:119], a[8:11], v[168:171]\n"
"ds_read_b128 a[16:19], v200 offset:32768\n"
"buffer_load_dwordx4 v17, s[44:47], s79 offen offset:1024 lds\n"
"v_mfma_f32_16x16x32_bf16 v[176:179], a[120:123], a[8:11], v[176:179]\n"
"ds_read_b128 a[88:91], v208 offset:45056\n"
"ds_read_b128 a[20:23], v204 offset:32768\n"
"v_mfma_f32_16x16x32_bf16 v[180:183], a[120:123], a[12:15], v[180:183]\n"
"buffer_load_dwordx4 v18, s[44:47], s79 offen offset:2048 lds\n"
"buffer_load_dwordx4 v19, s[44:47], s79 offen offset:3072 lds\n"
"v_mfma_f32_16x16x32_bf16 v[188:191], a[124:127], a[12:15], v[188:191]\n"
"ds_read_b128 a[92:95], v208 offset:45568\n"
"s_add_u32 s79, s79, 128\n"
"v_mfma_f32_16x16x32_bf16 v[184:187], a[124:127], a[8:11], v[184:187]\n"
"s_waitcnt vmcnt(8)\n"
"s_barrier\n"
"s_waitcnt lgkmcnt(6)\n"
"s_waitcnt lgkmcnt(3)\n"
"v_mfma_f32_16x16x32_bf16 v[128:131], a[64:67], a[16:19], v[128:131]\n"
"ds_read_b128 a[96:99], v209 offset:32768\n"
"v_readlane_b32 s79, v11, 0\n"
"s_waitcnt lgkmcnt(2)\n"
"v_mfma_f32_16x16x32_bf16 v[132:135], a[64:67], a[20:23], v[132:135]\n"
"s_add_u32 m0, s74, 0\n"
"s_nop 1\n"
"buffer_load_dwordx4 v84, s[4:7], s79 offen offset:0 lds\n"
"v_mfma_f32_16x16x32_bf16 v[140:143], a[68:71], a[20:23], v[140:143]\n"
"ds_read_b128 a[100:103], v209 offset:33280\n"
"v_mfma_f32_16x16x32_bf16 v[136:139], a[68:71], a[16:19], v[136:139]\n"
"ds_read_b128 a[24:27], v201 offset:32768\n"
"v_mfma_f32_16x16x32_bf16 v[144:147], a[72:75], a[16:19], v[144:147]\n"
"ds_read_b128 a[104:107], v209 offset:36864\n"
"buffer_load_dwordx4 v85, s[4:7], s79 offen offset:1024 lds\n"
"v_mfma_f32_16x16x32_bf16 v[148:151], a[72:75], a[20:23], v[148:151]\n"
"v_mfma_f32_16x16x32_bf16 v[156:159], a[76:79], a[20:23], v[156:159]\n"
"ds_read_b128 a[108:111], v209 offset:37376\n"
"v_mfma_f32_16x16x32_bf16 v[152:155], a[76:79], a[16:19], v[152:155]\n"
"ds_read_b128 a[28:31], v205 offset:32768\n"
"s_waitcnt lgkmcnt(6)\n"
"v_mfma_f32_16x16x32_bf16 v[160:163], a[80:83], a[16:19], v[160:163]\n"
"ds_read_b128 a[112:115], v209 offset:40960\n"
"buffer_load_dwordx4 v86, s[4:7], s79 offen offset:2048 lds\n"
"v_mfma_f32_16x16x32_bf16 v[164:167], a[80:83], a[20:23], v[164:167]\n"
"v_mfma_f32_16x16x32_bf16 v[172:175], a[84:87], a[20:23], v[172:175]\n"
"ds_read_b128 a[116:119], v209 offset:41472\n"
"v_mfma_f32_16x16x32_bf16 v[168:171], a[84:87], a[16:19], v[168:171]\n"
"v_mfma_f32_16x16x32_bf16 v[176:179], a[88:91], a[16:19], v[176:179]\n"
"ds_read_b128 a[120:123], v209 offset:45056\n"
"buffer_load_dwordx4 v87, s[4:7], s79 offen offset:3072 lds\n"
"v_mfma_f32_16x16x32_bf16 v[180:183], a[88:91], a[20:23], v[180:183]\n"
"v_mfma_f32_16x16x32_bf16 v[188:191], a[92:95], a[20:23], v[188:191]\n"
"ds_read_b128 a[124:127], v209 offset:45568\n"
"v_mfma_f32_16x16x32_bf16 v[184:187], a[92:95], a[16:19], v[184:187]\n"
"s_add_u32 m0, s74, 4096\n"
"s_waitcnt lgkmcnt(5)\n"
"v_mfma_f32_16x16x32_bf16 v[128:131], a[96:99], a[24:27], v[128:131]\n"
"ds_read_b128 a[64:67], v210 offset:0\n"
"s_mul_i32 s75, s63, 458752\n"
"s_waitcnt lgkmcnt(5)\n"
"v_mfma_f32_16x16x32_bf16 v[132:135], a[96:99], a[28:31], v[132:135]\n"
"s_mul_i32 s76, s31, 114688\n"
"s_add_u32 s75, s75, s76\n"
"v_mfma_f32_16x16x32_bf16 v[140:143], a[100:103], a[28:31], v[140:143]\n"
"ds_read_b128 a[68:71], v210 offset:512\n"
"s_mul_i32 s76, s65, 448\n"
"v_mfma_f32_16x16x32_bf16 v[136:139], a[100:103], a[24:27], v[136:139]\n"
"s_add_u32 s75, s75, s76\n"
"s_add_u32 s48, s18, s75\n"
"v_mfma_f32_16x16x32_bf16 v[144:147], a[104:107], a[24:27], v[144:147]\n"
"ds_read_b128 a[72:75], v211 offset:0\n"
"s_addc_u32 s49, s19, 0\n"
"v_mfma_f32_16x16x32_bf16 v[148:151], a[104:107], a[28:31], v[148:151]\n"
"s_sub_u32 s50, 7340032, s75\n"
"buffer_load_dwordx4 v88, s[4:7], s79 offen offset:0 lds\n"
"v_mfma_f32_16x16x32_bf16 v[156:159], a[108:111], a[28:31], v[156:159]\n"
"ds_read_b128 a[76:79], v211 offset:512\n"
"ds_read_b128 a[0:3], v202 offset:0\n"
"v_mfma_f32_16x16x32_bf16 v[152:155], a[108:111], a[24:27], v[152:155]\n"
"buffer_load_dwordx4 v89, s[4:7], s79 offen offset:1024 lds\n"
"s_waitcnt lgkmcnt(5)\n"
"v_mfma_f32_16x16x32_bf16 v[160:163], a[112:115], a[24:27], v[160:163]\n"
"ds_read_b128 a[4:7], v206 offset:0\n"
"buffer_load_dwordx4 v90, s[4:7], s79 offen offset:2048 lds\n"
"v_mfma_f32_16x16x32_bf16 v[164:167], a[112:115], a[28:31], v[164:167]\n"
"v_mfma_f32_16x16x32_bf16 v[172:175], a[116:119], a[28:31], v[172:175]\n"
"v_mfma_f32_16x16x32_bf16 v[168:171], a[116:119], a[24:27], v[168:171]\n"
"v_mfma_f32_16x16x32_bf16 v[176:179], a[120:123], a[24:27], v[176:179]\n"
"buffer_load_dwordx4 v91, s[4:7], s79 offen offset:3072 lds\n"
"v_readlane_b32 s79, v11, 1\n"
"v_mfma_f32_16x16x32_bf16 v[180:183], a[120:123], a[28:31], v[180:183]\n"
"v_mfma_f32_16x16x32_bf16 v[188:191], a[124:127], a[28:31], v[188:191]\n"
"v_mfma_f32_16x16x32_bf16 v[184:187], a[124:127], a[24:27], v[184:187]\n"
"s_waitcnt vmcnt(8)\n"
"s_barrier\n"
"s_waitcnt lgkmcnt(2)\n"
"s_waitcnt lgkmcnt(1)\n"
"v_mfma_f32_16x16x32_bf16 v[128:131], a[64:67], a[0:3], v[128:131]\n"
"ds_read_b128 a[80:83], v210 offset:32768\n"
"ds_read_b128 a[84:87], v210 offset:33280\n"
"s_waitcnt lgkmcnt(2)\n"
"v_mfma_f32_16x16x32_bf16 v[132:135], a[64:67], a[4:7], v[132:135]\n"
"ds_read_b128 a[88:91], v211 offset:32768\n"
"ds_read_b128 a[8:11], v203 offset:0\n"
"v_mfma_f32_16x16x32_bf16 v[140:143], a[68:71], a[4:7], v[140:143]\n"
"ds_read_b128 a[92:95], v211 offset:33280\n"
"ds_read_b128 a[12:15], v207 offset:0\n"
"v_mfma_f32_16x16x32_bf16 v[136:139], a[68:71], a[0:3], v[136:139]\n"
"ds_read_b128 a[96:99], v210 offset:4096\n"
"ds_read_b128 a[16:19], v202 offset:32768\n"
"s_waitcnt lgkmcnt(4)\n"
"v_mfma_f32_16x16x32_bf16 v[128:131], a[72:75], a[8:11], v[128:131]\n"
"ds_read_b128 a[100:103], v210 offset:4608\n"
"ds_read_b128 a[20:23], v206 offset:32768\n"
"s_waitcnt lgkmcnt(4)\n"
"v_mfma_f32_16x16x32_bf16 v[132:135], a[72:75], a[12:15], v[132:135]\n"
"ds_read_b128 a[104:107], v211 offset:4096\n"
"ds_read_b128 a[24:27], v203 offset:32768\n"
"v_mfma_f32_16x16x32_bf16 v[140:143], a[76:79], a[12:15], v[140:143]\n"
"ds_read_b128 a[108:111], v211 offset:4608\n"
"ds_read_b128 a[28:31], v207 offset:32768\n"
"v_mfma_f32_16x16x32_bf16 v[136:139], a[76:79], a[8:11], v[136:139]\n"
"s_add_u32 m0, s74, 32768\n"
"s_waitcnt lgkmcnt(6)\n"
"v_mfma_f32_16x16x32_bf16 v[128:131], a[80:83], a[16:19], v[128:131]\n"
"ds_read_b128 a[112:115], v210 offset:36864\n"
"buffer_load_dwordx4 v84, s[4:7], s79 offen offset:0 lds\n"
"s_waitcnt lgkmcnt(5)\n"
"v_mfma_f32_16x16x32_bf16 v[132:135], a[80:83], a[20:23], v[132:135]\n"
"buffer_load_dwordx4 v85, s[4:7], s79 offen offset:1024 lds\n"
"buffer_load_dwordx4 v86, s[4:7], s79 offen offset:2048 lds\n"
"v_mfma_f32_16x16x32_bf16 v[140:143], a[84:87], a[20:23], v[140:143]\n"
"ds_read_b128 a[116:119], v210 offset:37376\n"
"buffer_load_dwordx4 v87, s[4:7], s79 offen offset:3072 lds\n"
"v_mfma_f32_16x16x32_bf16 v[136:139], a[84:87], a[16:19], v[136:139]\n"
"s_add_u32 m0, s74, 36864\n"
"s_nop 0\n"
"buffer_load_dwordx4 v88, s[4:7], s79 offen offset:0 lds\n"
"s_waitcnt lgkmcnt(4)\n"
"v_mfma_f32_16x16x32_bf16 v[128:131], a[88:91], a[24:27], v[128:131]\n"
"ds_read_b128 a[120:123], v211 offset:36864\n"
"buffer_load_dwordx4 v89, s[4:7], s79 offen offset:1024 lds\n"
"s_waitcnt lgkmcnt(3)\n"
"v_mfma_f32_16x16x32_bf16 v[132:135], a[88:91], a[28:31], v[132:135]\n"
"buffer_load_dwordx4 v90, s[4:7], s79 offen offset:2048 lds\n"
"v_mfma_f32_16x16x32_bf16 v[140:143], a[92:95], a[28:31], v[140:143]\n"
"ds_read_b128 a[124:127], v211 offset:37376\n"
"buffer_load_dwordx4 v91, s[4:7], s79 offen offset:3072 lds\n"
"v_mfma_f32_16x16x32_bf16 v[136:139], a[92:95], a[24:27], v[136:139]\n"
"v_readlane_b32 s79, v11, 2\n"
"v_mfma_f32_16x16x32_bf16 v[144:147], a[96:99], a[0:3], v[144:147]\n"
"ds_read_b128 a[64:67], v210 offset:8192\n"
"ds_read_b128 v[96:99], v9 offset:0\n"
"v_mfma_f32_16x16x32_bf16 v[148:151], a[96:99], a[4:7], v[148:151]\n"
"ds_read_b128 v[100:103], v9 offset:512\n"
"ds_read_b128 v[104:107], v9 offset:16\n"
"v_mfma_f32_16x16x32_bf16 v[156:159], a[100:103], a[4:7], v[156:159]\n"
"ds_read_b128 a[68:71], v210 offset:8704\n"
"ds_read_b128 v[108:111], v9 offset:528\n"
"v_mfma_f32_16x16x32_bf16 v[152:155], a[100:103], a[0:3], v[152:155]\n"
"v_mfma_f32_16x16x32_bf16 v[144:147], a[104:107], a[8:11], v[144:147]\n"
"ds_read_b128 a[72:75], v211 offset:8192\n"
"v_mfma_f32_16x16x32_bf16 v[148:151], a[104:107], a[12:15], v[148:151]\n"
"v_mfma_f32_16x16x32_bf16 v[156:159], a[108:111], a[12:15], v[156:159]\n"
"ds_read_b128 a[76:79], v211 offset:8704\n"
"v_mfma_f32_16x16x32_bf16 v[152:155], a[108:111], a[8:11], v[152:155]\n"
"s_waitcnt lgkmcnt(8)\n"
"v_mfma_f32_16x16x32_bf16 v[144:147], a[112:115], a[16:19], v[144:147]\n"
"ds_read_b128 a[80:83], v210 offset:40960\n"
"v_mfma_f32_16x16x32_bf16 v[148:151], a[112:115], a[20:23], v[148:151]\n"
"ds_read_b128 v[112:115], v9 offset:128\n"
"ds_read_b128 v[116:119], v9 offset:640\n"
"v_mfma_f32_16x16x32_bf16 v[156:159], a[116:119], a[20:23], v[156:159]\n"
"ds_read_b128 a[84:87], v210 offset:41472\n"
"ds_read_b128 v[120:123], v9 offset:144\n"
"v_mfma_f32_16x16x32_bf16 v[152:155], a[116:119], a[16:19], v[152:155]\n"
"ds_read_b128 v[124:127], v9 offset:656\n"
"v_mfma_f32_16x16x32_bf16 v[144:147], a[120:123], a[24:27], v[144:147]\n"
"ds_read_b128 a[88:91], v211 offset:40960\n"
"v_mfma_f32_16x16x32_bf16 v[148:151], a[120:123], a[28:31], v[148:151]\n"
"v_mfma_f32_16x16x32_bf16 v[156:159], a[124:127], a[28:31], v[156:159]\n"
"s_waitcnt lgkmcnt(14)\n"
"ds_read_b128 a[92:95], v211 offset:41472\n"
"v_mfma_f32_16x16x32_bf16 v[152:155], a[124:127], a[24:27], v[152:155]\n"
"s_waitcnt lgkmcnt(13)\n"
"v_pk_mul_f32 v[192:193], v[100:101], v[20:21]\n"
"v_pk_fma_f32 v[128:129], v[128:129], v[96:97], v[192:193]\n"
"v_pk_mul_f32 v[194:195], v[102:103], v[20:21]\n"
"v_pk_fma_f32 v[130:131], v[130:131], v[98:99], v[194:195]\n"
"s_waitcnt lgkmcnt(10)\n"
"v_pk_mul_f32 v[192:193], v[108:109], v[20:21]\n"
"v_pk_fma_f32 v[136:137], v[136:137], v[104:105], v[192:193]\n"
"v_pk_mul_f32 v[194:195], v[110:111], v[20:21]\n"
"v_pk_fma_f32 v[138:139], v[138:139], v[106:107], v[194:195]\n"
"v_cvt_pk_bf16_f32 v32, v128, v129\n"
"v_cvt_pk_bf16_f32 v33, v130, v131\n"
"v_cvt_pk_bf16_f32 v34, v136, v137\n"
"v_cvt_pk_bf16_f32 v35, v138, v139\n"
"buffer_store_dwordx4 v[32:35], v8, s[36:39], s80 offen offset:0 sc1\n"
"v_pk_mul_f32 v[192:193], v[100:101], v[22:23]\n"
"v_pk_fma_f32 v[132:133], v[132:133], v[96:97], v[192:193]\n"
"v_pk_mul_f32 v[194:195], v[102:103], v[22:23]\n"
"v_pk_fma_f32 v[134:135], v[134:135], v[98:99], v[194:195]\n"
"v_pk_mul_f32 v[192:193], v[108:109], v[22:23]\n"
"v_pk_fma_f32 v[140:141], v[140:141], v[104:105], v[192:193]\n"
"v_pk_mul_f32 v[194:195], v[110:111], v[22:23]\n"
"v_pk_fma_f32 v[142:143], v[142:143], v[106:107], v[194:195]\n"
"v_cvt_pk_bf16_f32 v36, v132, v133\n"
"v_cvt_pk_bf16_f32 v37, v134, v135\n"
"v_cvt_pk_bf16_f32 v38, v140, v141\n"
"v_cvt_pk_bf16_f32 v39, v142, v143\n"
"buffer_store_dwordx4 v[36:39], v8, s[36:39], s96 offen offset:0 sc1\n"
"s_waitcnt lgkmcnt(8)\n"
"v_mfma_f32_16x16x32_bf16 v[160:163], a[64:67], a[0:3], v[160:163]\n"
"ds_read_b128 a[96:99], v210 offset:12288\n"
"v_mfma_f32_16x16x32_bf16 v[164:167], a[64:67], a[4:7], v[164:167]\n"
"v_mfma_f32_16x16x32_bf16 v[172:175], a[68:71], a[4:7], v[172:175]\n"
"ds_read_b128 a[100:103], v210 offset:12800\n"
"v_mfma_f32_16x16x32_bf16 v[168:171], a[68:71], a[0:3], v[168:171]\n"
"v_mfma_f32_16x16x32_bf16 v[160:163], a[72:75], a[8:11], v[160:163]\n"
"ds_read_b128 a[104:107], v211 offset:12288\n"
"v_mfma_f32_16x16x32_bf16 v[164:167], a[72:75], a[12:15], v[164:167]\n"
"v_mfma_f32_16x16x32_bf16 v[172:175], a[76:79], a[12:15], v[172:175]\n"
"ds_read_b128 a[108:111], v211 offset:12800\n"
"v_mfma_f32_16x16x32_bf16 v[168:171], a[76:79], a[8:11], v[168:171]\n"
"s_waitcnt lgkmcnt(4)\n"
"v_mfma_f32_16x16x32_bf16 v[160:163], a[80:83], a[16:19], v[160:163]\n"
"ds_read_b128 a[112:115], v210 offset:45056\n"
"v_mfma_f32_16x16x32_bf16 v[164:167], a[80:83], a[20:23], v[164:167]\n"
"ds_read_b128 v[96:99], v9 offset:256\n"
"ds_read_b128 v[100:103], v9 offset:768\n"
"v_mfma_f32_16x16x32_bf16 v[172:175], a[84:87], a[20:23], v[172:175]\n"
"ds_read_b128 a[116:119], v210 offset:45568\n"
"ds_read_b128 v[104:107], v9 offset:272\n"
"v_mfma_f32_16x16x32_bf16 v[168:171], a[84:87], a[16:19], v[168:171]\n"
"ds_read_b128 v[108:111], v9 offset:784\n"
"v_mfma_f32_16x16x32_bf16 v[160:163], a[88:91], a[24:27], v[160:163]\n"
"ds_read_b128 a[120:123], v211 offset:45056\n"
"v_mfma_f32_16x16x32_bf16 v[164:167], a[88:91], a[28:31], v[164:167]\n"
"v_mfma_f32_16x16x32_bf16 v[172:175], a[92:95], a[28:31], v[172:175]\n"
"ds_read_b128 a[124:127], v211 offset:45568\n"
"v_mfma_f32_16x16x32_bf16 v[168:171], a[92:95], a[24:27], v[168:171]\n"
"v_pk_mul_f32 v[192:193], v[116:117], v[20:21]\n"
"v_pk_fma_f32 v[144:145], v[144:145], v[112:113], v[192:193]\n"
"v_pk_mul_f32 v[194:195], v[118:119], v[20:21]\n"
"v_pk_fma_f32 v[146:147], v[146:147], v[114:115], v[194:195]\n"
"v_pk_mul_f32 v[192:193], v[124:125], v[20:21]\n"
"v_pk_fma_f32 v[152:153], v[152:153], v[120:121], v[192:193]\n"
"v_pk_mul_f32 v[194:195], v[126:127], v[20:21]\n"
"v_pk_fma_f32 v[154:155], v[154:155], v[122:123], v[194:195]\n"
"v_cvt_pk_bf16_f32 v40, v144, v145\n"
"v_cvt_pk_bf16_f32 v41, v146, v147\n"
"v_cvt_pk_bf16_f32 v42, v152, v153\n"
"v_cvt_pk_bf16_f32 v43, v154, v155\n"
"buffer_store_dwordx4 v[40:43], v8, s[36:39], s80 offen offset:2048 sc1\n"
"v_pk_mul_f32 v[192:193], v[116:117], v[22:23]\n"
"v_pk_fma_f32 v[148:149], v[148:149], v[112:113], v[192:193]\n"
"v_pk_mul_f32 v[194:195], v[118:119], v[22:23]\n"
"v_pk_fma_f32 v[150:151], v[150:151], v[114:115], v[194:195]\n"
"v_pk_mul_f32 v[192:193], v[124:125], v[22:23]\n"
"v_pk_fma_f32 v[156:157], v[156:157], v[120:121], v[192:193]\n"
"v_pk_mul_f32 v[194:195], v[126:127], v[22:23]\n"
"v_pk_fma_f32 v[158:159], v[158:159], v[122:123], v[194:195]\n"
"v_cvt_pk_bf16_f32 v44, v148, v149\n"
"v_cvt_pk_bf16_f32 v45, v150, v151\n"
"v_cvt_pk_bf16_f32 v46, v156, v157\n"
"v_cvt_pk_bf16_f32 v47, v158, v159\n"
"buffer_store_dwordx4 v[44:47], v8, s[36:39], s96 offen offset:2048 sc1\n"
"s_add_u32 s80, s80, 4096\n"
"s_add_u32 s96, s96, 4096\n"
"s_waitcnt lgkmcnt(8)\n"
"v_mfma_f32_16x16x32_bf16 v[176:179], a[96:99], a[0:3], v[176:179]\n"
"v_mfma_f32_16x16x32_bf16 v[180:183], a[96:99], a[4:7], v[180:183]\n"
"v_mfma_f32_16x16x32_bf16 v[188:191], a[100:103], a[4:7], v[188:191]\n"
"v_mfma_f32_16x16x32_bf16 v[184:187], a[100:103], a[0:3], v[184:187]\n"
"v_mfma_f32_16x16x32_bf16 v[176:179], a[104:107], a[8:11], v[176:179]\n"
"v_mfma_f32_16x16x32_bf16 v[180:183], a[104:107], a[12:15], v[180:183]\n"
"v_mfma_f32_16x16x32_bf16 v[188:191], a[108:111], a[12:15], v[188:191]\n"
"v_mfma_f32_16x16x32_bf16 v[184:187], a[108:111], a[8:11], v[184:187]\n"
"s_waitcnt lgkmcnt(0)\n"
"v_mfma_f32_16x16x32_bf16 v[176:179], a[112:115], a[16:19], v[176:179]\n"
"v_mfma_f32_16x16x32_bf16 v[180:183], a[112:115], a[20:23], v[180:183]\n"
"ds_read_b128 v[112:115], v9 offset:384\n"
"ds_read_b128 v[116:119], v9 offset:896\n"
"v_mfma_f32_16x16x32_bf16 v[188:191], a[116:119], a[20:23], v[188:191]\n"
"ds_read_b128 v[120:123], v9 offset:400\n"
"ds_read_b128 v[124:127], v9 offset:912\n"
"v_mfma_f32_16x16x32_bf16 v[184:187], a[116:119], a[16:19], v[184:187]\n"
"v_mfma_f32_16x16x32_bf16 v[176:179], a[120:123], a[24:27], v[176:179]\n"
"v_mfma_f32_16x16x32_bf16 v[180:183], a[120:123], a[28:31], v[180:183]\n"
"v_mfma_f32_16x16x32_bf16 v[188:191], a[124:127], a[28:31], v[188:191]\n"
"v_mfma_f32_16x16x32_bf16 v[184:187], a[124:127], a[24:27], v[184:187]\n"
"v_pk_mul_f32 v[192:193], v[100:101], v[20:21]\n"
"v_pk_fma_f32 v[160:161], v[160:161], v[96:97], v[192:193]\n"
"v_pk_mul_f32 v[194:195], v[102:103], v[20:21]\n"
"v_pk_fma_f32 v[162:163], v[162:163], v[98:99], v[194:195]\n"
"v_pk_mul_f32 v[192:193], v[108:109], v[20:21]\n"
"v_pk_fma_f32 v[168:169], v[168:169], v[104:105], v[192:193]\n"
"v_pk_mul_f32 v[194:195], v[110:111], v[20:21]\n"
"v_pk_fma_f32 v[170:171], v[170:171], v[106:107], v[194:195]\n"
"v_cvt_pk_bf16_f32 v48, v160, v161\n"
"v_cvt_pk_bf16_f32 v49, v162, v163\n"
"v_cvt_pk_bf16_f32 v50, v168, v169\n"
"v_cvt_pk_bf16_f32 v51, v170, v171\n"
"buffer_store_dwordx4 v[48:51], v8, s[36:39], s80 offen offset:0 sc1\n"
"v_pk_mul_f32 v[192:193], v[100:101], v[22:23]\n"
"v_pk_fma_f32 v[164:165], v[164:165], v[96:97], v[192:193]\n"
"v_pk_mul_f32 v[194:195], v[102:103], v[22:23]\n"
"v_pk_fma_f32 v[166:167], v[166:167], v[98:99], v[194:195]\n"
"v_pk_mul_f32 v[192:193], v[108:109], v[22:23]\n"
"v_pk_fma_f32 v[172:173], v[172:173], v[104:105], v[192:193]\n"
"v_pk_mul_f32 v[194:195], v[110:111], v[22:23]\n"
"v_pk_fma_f32 v[174:175], v[174:175], v[106:107], v[194:195]\n"
"v_cvt_pk_bf16_f32 v52, v164, v165\n"
"v_cvt_pk_bf16_f32 v53, v166, v167\n"
"v_cvt_pk_bf16_f32 v54, v172, v173\n"
"v_cvt_pk_bf16_f32 v55, v174, v175\n"
"buffer_store_dwordx4 v[52:55], v8, s[36:39], s96 offen offset:0 sc1\n"
"s_waitcnt lgkmcnt(2)\n"
"v_pk_mul_f32 v[192:193], v[116:117], v[20:21]\n"
"v_pk_fma_f32 v[176:177], v[176:177], v[112:113], v[192:193]\n"
"v_pk_mul_f32 v[194:195], v[118:119], v[20:21]\n"
"v_pk_fma_f32 v[178:179], v[178:179], v[114:115], v[194:195]\n"
"s_waitcnt lgkmcnt(0)\n"
"v_pk_mul_f32 v[192:193], v[124:125], v[20:21]\n"
"v_pk_fma_f32 v[184:185], v[184:185], v[120:121], v[192:193]\n"
"v_pk_mul_f32 v[194:195], v[126:127], v[20:21]\n"
"v_pk_fma_f32 v[186:187], v[186:187], v[122:123], v[194:195]\n"
"v_cvt_pk_bf16_f32 v56, v176, v177\n"
"v_cvt_pk_bf16_f32 v57, v178, v179\n"
"v_cvt_pk_bf16_f32 v58, v184, v185\n"
"v_cvt_pk_bf16_f32 v59, v186, v187\n"
"buffer_store_dwordx4 v[56:59], v8, s[36:39], s80 offen offset:2048 sc1\n"
"v_pk_mul_f32 v[192:193], v[116:117], v[22:23]\n"
"v_pk_fma_f32 v[180:181], v[180:181], v[112:113], v[192:193]\n"
"v_pk_mul_f32 v[194:195], v[118:119], v[22:23]\n"
"v_pk_fma_f32 v[182:183], v[182:183], v[114:115], v[194:195]\n"
"v_pk_mul_f32 v[192:193], v[124:125], v[22:23]\n"
"v_pk_fma_f32 v[188:189], v[188:189], v[120:121], v[192:193]\n"
"v_pk_mul_f32 v[194:195], v[126:127], v[22:23]\n"
"v_pk_fma_f32 v[190:191], v[190:191], v[122:123], v[194:195]\n"
"v_cvt_pk_bf16_f32 v60, v180, v181\n"
"v_cvt_pk_bf16_f32 v61, v182, v183\n"
"v_cvt_pk_bf16_f32 v62, v188, v189\n"
"v_cvt_pk_bf16_f32 v63, v190, v191\n"
"buffer_store_dwordx4 v[60:63], v8, s[36:39], s96 offen offset:2048 sc1\n"
"s_lshl_b32 s75, s61, 10\n"
"v_add_u32 v9, s75, v8\n"
"v_xor_b32 v10, 1024, v9\n"
"s_waitcnt vmcnt(8)\n"
"s_barrier\n"
"ds_read_b128 a[64:67], v12 offset:0\n"
"ds_read_b128 a[68:71], v12 offset:2048\n"
"ds_read_b128 a[72:75], v12 offset:4096\n"
"ds_read_b128 a[76:79], v12 offset:6144\n"
"ds_read_b128 a[80:83], v12 offset:8192\n"
"ds_read_b128 a[84:87], v12 offset:10240\n"
"ds_read_b128 a[88:91], v12 offset:12288\n"
"ds_read_b128 a[92:95], v12 offset:14336\n"
"s_waitcnt lgkmcnt(4)\n"
"v_mfma_f32_16x16x32_bf16 v[128:131], v[32:35], a[64:67], 0\n"
"ds_read_b128 a[96:99], v12 offset:16384\n"
"v_mfma_f32_16x16x32_bf16 v[132:135], v[36:39], a[64:67], 0\n"
"s_waitcnt vmcnt(4)\n"
"buffer_store_dwordx2 v[28:29], v30, s[36:39], s85 offen offset:0 sc1\n"
"v_mfma_f32_16x16x32_bf16 v[140:143], v[36:39], a[68:71], 0\n"
"ds_read_b128 a[100:103], v12 offset:18432\n"
"v_mfma_f32_16x16x32_bf16 v[136:139], v[32:35], a[68:71], 0\n"
"v_mfma_f32_16x16x32_bf16 v[144:147], v[32:35], a[72:75], 0\n"
"ds_read_b128 a[104:107], v12 offset:20480\n"
"v_mfma_f32_16x16x32_bf16 v[148:151], v[36:39], a[72:75], 0\n"
"v_mfma_f32_16x16x32_bf16 v[156:159], v[36:39], a[76:79], 0\n"
"ds_read_b128 a[108:111], v12 offset:22528\n"
"v_mfma_f32_16x16x32_bf16 v[152:155], v[32:35], a[76:79], 0\n"
"s_waitcnt lgkmcnt(4)\n"
"v_mfma_f32_16x16x32_bf16 v[160:163], v[32:35], a[80:83], 0\n"
"ds_read_b128 a[112:115], v12 offset:24576\n"
"v_mfma_f32_16x16x32_bf16 v[164:167], v[36:39], a[80:83], 0\n"
"v_mfma_f32_16x16x32_bf16 v[172:175], v[36:39], a[84:87], 0\n"
"ds_read_b128 a[116:119], v12 offset:26624\n"
"v_mfma_f32_16x16x32_bf16 v[168:171], v[32:35], a[84:87], 0\n"
"v_mfma_f32_16x16x32_bf16 v[176:179], v[32:35], a[88:91], 0\n"
"ds_read_b128 a[120:123], v12 offset:28672\n"
"v_mfma_f32_16x16x32_bf16 v[180:183], v[36:39], a[88:91], 0\n"
"v_mfma_f32_16x16x32_bf16 v[188:191], v[36:39], a[92:95], 0\n"
"ds_read_b128 a[124:127], v12 offset:30720\n"
"v_mfma_f32_16x16x32_bf16 v[184:187], v[32:35], a[92:95], 0\n"
"s_waitcnt lgkmcnt(4)\n"
"v_mfma_f32_16x16x32_bf16 v[192:195], v[32:35], a[96:99], 0\n"
"ds_read_b128 a[64:67], v13 offset:0\n"
"v_mfma_f32_16x16x32_bf16 v[196:199], v[36:39], a[96:99], 0\n"
"v_mfma_f32_16x16x32_bf16 v[204:207], v[36:39], a[100:103], 0\n"
"ds_read_b128 a[68:71], v13 offset:2048\n"
"v_mfma_f32_16x16x32_bf16 v[200:203], v[32:35], a[100:103], 0\n"
"v_mfma_f32_16x16x32_bf16 v[208:211], v[32:35], a[104:107], 0\n"
"ds_read_b128 a[72:75], v13 offset:4096\n"
"v_mfma_f32_16x16x32_bf16 v[212:215], v[36:39], a[104:107], 0\n"
"v_mfma_f32_16x16x32_bf16 v[220:223], v[36:39], a[108:111], 0\n"
"ds_read_b128 a[76:79], v13 offset:6144\n"
"s_add_u32 m0, s74, 65536\n"
"v_mfma_f32_16x16x32_bf16 v[216:219], v[32:35], a[108:111], 0\n"
"buffer_load_dwordx4 v84, s[4:7], s79 offen offset:0 lds\n"
"s_waitcnt lgkmcnt(4)\n"
"v_mfma_f32_16x16x32_bf16 v[224:227], v[32:35], a[112:115], 0\n"
"ds_read_b128 a[80:83], v13 offset:8192\n"
"v_mfma_f32_16x16x32_bf16 v[228:231], v[36:39], a[112:115], 0\n"
"v_mfma_f32_16x16x32_bf16 v[236:239], v[36:39], a[116:119], 0\n"
"ds_read_b128 a[84:87], v13 offset:10240\n"
"v_mfma_f32_16x16x32_bf16 v[232:235], v[32:35], a[116:119], 0\n"
"v_mfma_f32_16x16x32_bf16 v[240:243], v[32:35], a[120:123], 0\n"
"ds_read_b128 a[88:91], v13 offset:12288\n"
"buffer_load_dwordx4 v85, s[4:7], s79 offen offset:1024 lds\n"
"v_mfma_f32_16x16x32_bf16 v[244:247], v[36:39], a[120:123], 0\n"
"v_mfma_f32_16x16x32_bf16 v[252:255], v[36:39], a[124:127], 0\n"
"ds_read_b128 a[92:95], v13 offset:14336\n"
"v_mfma_f32_16x16x32_bf16 v[248:251], v[32:35], a[124:127], 0\n"
"s_waitcnt lgkmcnt(4)\n"
"v_mfma_f32_16x16x32_bf16 v[128:131], v[40:43], a[64:67], v[128:131]\n"
"ds_read_b128 a[96:99], v13 offset:16384\n"
"v_mfma_f32_16x16x32_bf16 v[132:135], v[44:47], a[64:67], v[132:135]\n"
"buffer_load_dwordx4 v86, s[4:7], s79 offen offset:2048 lds\n"
"v_mfma_f32_16x16x32_bf16 v[140:143], v[44:47], a[68:71], v[140:143]\n"
"ds_read_b128 a[100:103], v13 offset:18432\n"
"v_mfma_f32_16x16x32_bf16 v[136:139], v[40:43], a[68:71], v[136:139]\n"
"v_mfma_f32_16x16x32_bf16 v[144:147], v[40:43], a[72:75], v[144:147]\n"
"ds_read_b128 a[104:107], v13 offset:20480\n"
"v_mfma_f32_16x16x32_bf16 v[148:151], v[44:47], a[72:75], v[148:151]\n"
"v_mfma_f32_16x16x32_bf16 v[156:159], v[44:47], a[76:79], v[156:159]\n"
"ds_read_b128 a[108:111], v13 offset:22528\n"
"buffer_load_dwordx4 v87, s[4:7], s79 offen offset:3072 lds\n"
"v_mfma_f32_16x16x32_bf16 v[152:155], v[40:43], a[76:79], v[152:155]\n"
"buffer_load_dwordx2 v[26:27], v30, s[36:39], s86 offen offset:0 sc1\n"
"s_waitcnt lgkmcnt(4)\n"
"v_mfma_f32_16x16x32_bf16 v[160:163], v[40:43], a[80:83], v[160:163]\n"
"ds_read_b128 a[112:115], v13 offset:24576\n"
"v_mfma_f32_16x16x32_bf16 v[164:167], v[44:47], a[80:83], v[164:167]\n"
"v_mfma_f32_16x16x32_bf16 v[172:175], v[44:47], a[84:87], v[172:175]\n"
"ds_read_b128 a[116:119], v13 offset:26624\n"
"s_add_u32 m0, s74, 69632\n"
"v_mfma_f32_16x16x32_bf16 v[168:171], v[40:43], a[84:87], v[168:171]\n"
"buffer_load_dwordx4 v88, s[4:7], s79 offen offset:0 lds\n"
"v_mfma_f32_16x16x32_bf16 v[176:179], v[40:43], a[88:91], v[176:179]\n"
"ds_read_b128 a[120:123], v13 offset:28672\n"
"v_mfma_f32_16x16x32_bf16 v[180:183], v[44:47], a[88:91], v[180:183]\n"
"v_mfma_f32_16x16x32_bf16 v[188:191], v[44:47], a[92:95], v[188:191]\n"
"ds_read_b128 a[124:127], v13 offset:30720\n"
"v_mfma_f32_16x16x32_bf16 v[184:187], v[40:43], a[92:95], v[184:187]\n"
"s_waitcnt lgkmcnt(4)\n"
"v_mfma_f32_16x16x32_bf16 v[192:195], v[40:43], a[96:99], v[192:195]\n"
"ds_read_b128 a[64:67], v12 offset:32768\n"
"buffer_load_dwordx4 v89, s[4:7], s79 offen offset:1024 lds\n"
"v_mfma_f32_16x16x32_bf16 v[196:199], v[44:47], a[96:99], v[196:199]\n"
"v_mfma_f32_16x16x32_bf16 v[204:207], v[44:47], a[100:103], v[204:207]\n"
"ds_read_b128 a[68:71], v12 offset:34816\n"
"v_mfma_f32_16x16x32_bf16 v[200:203], v[40:43], a[100:103], v[200:203]\n"
"v_mfma_f32_16x16x32_bf16 v[208:211], v[40:43], a[104:107], v[208:211]\n"
"ds_read_b128 a[72:75], v12 offset:36864\n"
"v_mfma_f32_16x16x32_bf16 v[212:215], v[44:47], a[104:107], v[212:215]\n"
"buffer_load_dwordx4 v90, s[4:7], s79 offen offset:2048 lds\n"
"v_mfma_f32_16x16x32_bf16 v[220:223], v[44:47], a[108:111], v[220:223]\n"
"ds_read_b128 a[76:79], v12 offset:38912\n"
"v_mfma_f32_16x16x32_bf16 v[216:219], v[40:43], a[108:111], v[216:219]\n"
"s_waitcnt lgkmcnt(4)\n"
"v_mfma_f32_16x16x32_bf16 v[224:227], v[40:43], a[112:115], v[224:227]\n"
"ds_read_b128 a[80:83], v12 offset:40960\n"
"v_mfma_f32_16x16x32_bf16 v[228:231], v[44:47], a[112:115], v[228:231]\n"
"v_mfma_f32_16x16x32_bf16 v[236:239], v[44:47], a[116:119], v[236:239]\n"
"ds_read_b128 a[84:87], v12 offset:43008\n"
"buffer_load_dwordx4 v91, s[4:7], s79 offen offset:3072 lds\n"
"v_mfma_f32_16x16x32_bf16 v[232:235], v[40:43], a[116:119], v[232:235]\n"
"v_readlane_b32 s79, v11, 3\n"
"v_mfma_f32_16x16x32_bf16 v[240:243], v[40:43], a[120:123], v[240:243]\n"
"ds_read_b128 a[88:91], v12 offset:45056\n"
"v_mfma_f32_16x16x32_bf16 v[244:247], v[44:47], a[120:123], v[244:247]\n"
"v_mfma_f32_16x16x32_bf16 v[252:255], v[44:47], a[124:127], v[252:255]\n"
"ds_read_b128 a[92:95], v12 offset:47104\n"
"s_waitcnt vmcnt(10)\n"
"buffer_store_dwordx2 v[28:29], v30, s[36:39], s85 offen offset:128 sc1\n"
"v_mfma_f32_16x16x32_bf16 v[248:251], v[40:43], a[124:127], v[248:251]\n"
"s_barrier\n"
"s_waitcnt lgkmcnt(4)\n"
"v_mfma_f32_16x16x32_bf16 v[128:131], v[48:51], a[64:67], v[128:131]\n"
"ds_read_b128 a[96:99], v12 offset:49152\n"
"v_mfma_f32_16x16x32_bf16 v[132:135], v[52:55], a[64:67], v[132:135]\n"
"v_mfma_f32_16x16x32_bf16 v[140:143], v[52:55], a[68:71], v[140:143]\n"
"ds_read_b128 a[100:103], v12 offset:51200\n"
"v_mfma_f32_16x16x32_bf16 v[136:139], v[48:51], a[68:71], v[136:139]\n"
"v_mfma_f32_16x16x32_bf16 v[144:147], v[48:51], a[72:75], v[144:147]\n"
"ds_read_b128 a[104:107], v12 offset:53248\n"
"v_mfma_f32_16x16x32_bf16 v[148:151], v[52:55], a[72:75], v[148:151]\n"
"s_waitcnt vmcnt(5)\n"
"v_cmp_ne_u64 vcc, s[70:71], v[26:27]\n"
"s_cbranch_vccnz .Lpslow1_%=\n"
".Lpok1_%=:\n"
"v_mfma_f32_16x16x32_bf16 v[156:159], v[52:55], a[76:79], v[156:159]\n"
"ds_read_b128 a[108:111], v12 offset:55296\n"
"v_mfma_f32_16x16x32_bf16 v[152:155], v[48:51], a[76:79], v[152:155]\n"
"buffer_load_dwordx4 a[32:35], v9, s[52:55], s82 offen offset:0 sc1\n"
"s_waitcnt lgkmcnt(4)\n"
"v_mfma_f32_16x16x32_bf16 v[160:163], v[48:51], a[80:83], v[160:163]\n"
"ds_read_b128 a[112:115], v12 offset:57344\n"
"v_mfma_f32_16x16x32_bf16 v[164:167], v[52:55], a[80:83], v[164:167]\n"
"v_mfma_f32_16x16x32_bf16 v[172:175], v[52:55], a[84:87], v[172:175]\n"
"ds_read_b128 a[116:119], v12 offset:59392\n"
"v_mfma_f32_16x16x32_bf16 v[168:171], v[48:51], a[84:87], v[168:171]\n"
"buffer_load_dwordx4 a[36:39], v10, s[52:55], s82 offen offset:0 sc1\n"
"v_mfma_f32_16x16x32_bf16 v[176:179], v[48:51], a[88:91], v[176:179]\n"
"ds_read_b128 a[120:123], v12 offset:61440\n"
"v_mfma_f32_16x16x32_bf16 v[180:183], v[52:55], a[88:91], v[180:183]\n"
"v_mfma_f32_16x16x32_bf16 v[188:191], v[52:55], a[92:95], v[188:191]\n"
"ds_read_b128 a[124:127], v12 offset:63488\n"
"v_mfma_f32_16x16x32_bf16 v[184:187], v[48:51], a[92:95], v[184:187]\n"
"s_waitcnt lgkmcnt(4)\n"
"v_mfma_f32_16x16x32_bf16 v[192:195], v[48:51], a[96:99], v[192:195]\n"
"ds_read_b128 a[64:67], v13 offset:32768\n"
"buffer_load_dwordx4 a[40:43], v9, s[52:55], s82 offen offset:2048 sc1\n"
"v_mfma_f32_16x16x32_bf16 v[196:199], v[52:55], a[96:99], v[196:199]\n"
"v_mfma_f32_16x16x32_bf16 v[204:207], v[52:55], a[100:103], v[204:207]\n"
"ds_read_b128 a[68:71], v13 offset:34816\n"
"v_mfma_f32_16x16x32_bf16 v[200:203], v[48:51], a[100:103], v[200:203]\n"
"v_mfma_f32_16x16x32_bf16 v[208:211], v[48:51], a[104:107], v[208:211]\n"
"ds_read_b128 a[72:75], v13 offset:36864\n"
"v_mfma_f32_16x16x32_bf16 v[212:215], v[52:55], a[104:107], v[212:215]\n"
"buffer_load_dwordx4 a[44:47], v10, s[52:55], s82 offen offset:2048 sc1\n"
"v_readlane_b32 s82, v11, 19\n"
"v_mfma_f32_16x16x32_bf16 v[220:223], v[52:55], a[108:111], v[220:223]\n"
"ds_read_b128 a[76:79], v13 offset:38912\n"
"v_mfma_f32_16x16x32_bf16 v[216:219], v[48:51], a[108:111], v[216:219]\n"
"s_waitcnt lgkmcnt(4)\n"
"v_mfma_f32_16x16x32_bf16 v[224:227], v[48:51], a[112:115], v[224:227]\n"
"ds_read_b128 a[80:83], v13 offset:40960\n"
"v_mfma_f32_16x16x32_bf16 v[228:231], v[52:55], a[112:115], v[228:231]\n"
"s_add_u32 m0, s74, 98304\n"
"v_mfma_f32_16x16x32_bf16 v[236:239], v[52:55], a[116:119], v[236:239]\n"
"ds_read_b128 a[84:87], v13 offset:43008\n"
"buffer_load_dwordx4 v84, s[4:7], s79 offen offset:0 lds\n"
"v_mfma_f32_16x16x32_bf16 v[232:235], v[48:51], a[116:119], v[232:235]\n"
"v_mfma_f32_16x16x32_bf16 v[240:243], v[48:51], a[120:123], v[240:243]\n"
"ds_read_b128 a[88:91], v13 offset:45056\n"
"v_mfma_f32_16x16x32_bf16 v[244:247], v[52:55], a[120:123], v[244:247]\n"
"v_mfma_f32_16x16x32_bf16 v[252:255], v[52:55], a[124:127], v[252:255]\n"
"ds_read_b128 a[92:95], v13 offset:47104\n"
"buffer_load_dwordx4 v85, s[4:7], s79 offen offset:1024 lds\n"
"v_mfma_f32_16x16x32_bf16 v[248:251], v[48:51], a[124:127], v[248:251]\n"
"s_waitcnt lgkmcnt(4)\n"
"v_mfma_f32_16x16x32_bf16 v[128:131], v[56:59], a[64:67], v[128:131]\n"
"ds_read_b128 a[96:99], v13 offset:49152\n"
"v_mfma_f32_16x16x32_bf16 v[132:135], v[60:63], a[64:67], v[132:135]\n"
"v_mfma_f32_16x16x32_bf16 v[140:143], v[60:63], a[68:71], v[140:143]\n"
"ds_read_b128 a[100:103], v13 offset:51200\n"
"v_mfma_f32_16x16x32_bf16 v[136:139], v[56:59], a[68:71], v[136:139]\n"
"buffer_load_dwordx4 v86, s[4:7], s79 offen offset:2048 lds\n"
"v_mfma_f32_16x16x32_bf16 v[144:147], v[56:59], a[72:75], v[144:147]\n"
"ds_read_b128 a[104:107], v13 offset:53248\n"
"v_mfma_f32_16x16x32_bf16 v[148:151], v[60:63], a[72:75], v[148:151]\n"
"v_mfma_f32_16x16x32_bf16 v[156:159], v[60:63], a[76:79], v[156:159]\n"
"ds_read_b128 a[108:111], v13 offset:55296\n"
"v_mfma_f32_16x16x32_bf16 v[152:155], v[56:59], a[76:79], v[152:155]\n"
"s_waitcnt lgkmcnt(4)\n"
"v_mfma_f32_16x16x32_bf16 v[160:163], v[56:59], a[80:83], v[160:163]\n"
"ds_read_b128 a[112:115], v13 offset:57344\n"
"buffer_load_dwordx4 v87, s[4:7], s79 offen offset:3072 lds\n"
"v_mfma_f32_16x16x32_bf16 v[164:167], v[60:63], a[80:83], v[164:167]\n"
"v_mfma_f32_16x16x32_bf16 v[172:175], v[60:63], a[84:87], v[172:175]\n"
"ds_read_b128 a[116:119], v13 offset:59392\n"
"v_mfma_f32_16x16x32_bf16 v[168:171], v[56:59], a[84:87], v[168:171]\n"
"buffer_load_dwordx2 v[120:121], v31, s[36:39], s81 offen offset:0 sc1\n"
"v_mfma_f32_16x16x32_bf16 v[176:179], v[56:59], a[88:91], v[176:179]\n"
"ds_read_b128 a[120:123], v13 offset:61440\n"
"s_add_u32 m0, s74, 102400\n"
"v_mfma_f32_16x16x32_bf16 v[180:183], v[60:63], a[88:91], v[180:183]\n"
"buffer_load_dwordx4 v88, s[4:7], s79 offen offset:0 lds\n"
"v_mfma_f32_16x16x32_bf16 v[188:191], v[60:63], a[92:95], v[188:191]\n"
"ds_read_b128 a[124:127], v13 offset:63488\n"
"v_mfma_f32_16x16x32_bf16 v[184:187], v[56:59], a[92:95], v[184:187]\n"
"s_waitcnt lgkmcnt(4)\n"
"v_mfma_f32_16x16x32_bf16 v[192:195], v[56:59], a[96:99], v[192:195]\n"
"v_mfma_f32_16x16x32_bf16 v[196:199], v[60:63], a[96:99], v[196:199]\n"
"buffer_load_dwordx4 v89, s[4:7], s79 offen offset:1024 lds\n"
"s_waitcnt vmcnt(12)\n"
"s_barrier\n"
"v_mfma_f32_16x16x32_bf16 v[204:207], v[60:63], a[100:103], v[204:207]\n"
"ds_read_b128 a[64:67], v14 offset:0\n"
"v_mfma_f32_16x16x32_bf16 v[200:203], v[56:59], a[100:103], v[200:203]\n"
"ds_read_b128 a[68:71], v14 offset:2048\n"
"v_mfma_f32_16x16x32_bf16 v[208:211], v[56:59], a[104:107], v[208:211]\n"
"ds_read_b128 a[72:75], v14 offset:4096\n"
"v_mfma_f32_16x16x32_bf16 v[212:215], v[60:63], a[104:107], v[212:215]\n"
"v_mfma_f32_16x16x32_bf16 v[220:223], v[60:63], a[108:111], v[220:223]\n"
"ds_read_b128 a[76:79], v14 offset:6144\n"
"buffer_load_dwordx4 v90, s[4:7], s79 offen offset:2048 lds\n"
"v_mfma_f32_16x16x32_bf16 v[216:219], v[56:59], a[108:111], v[216:219]\n"
"s_waitcnt lgkmcnt(4)\n"
"v_mfma_f32_16x16x32_bf16 v[224:227], v[56:59], a[112:115], v[224:227]\n"
"ds_read_b128 a[80:83], v14 offset:8192\n"
"v_mfma_f32_16x16x32_bf16 v[228:231], v[60:63], a[112:115], v[228:231]\n"
"v_mfma_f32_16x16x32_bf16 v[236:239], v[60:63], a[116:119], v[236:239]\n"
"ds_read_b128 a[84:87], v14 offset:10240\n"
"v_mfma_f32_16x16x32_bf16 v[232:235], v[56:59], a[116:119], v[232:235]\n"
"buffer_load_dwordx4 v91, s[4:7], s79 offen offset:3072 lds\n"
"v_readlane_b32 s79, v11, 4\n"
"v_mfma_f32_16x16x32_bf16 v[240:243], v[56:59], a[120:123], v[240:243]\n"
"ds_read_b128 a[88:91], v14 offset:12288\n"
"v_mfma_f32_16x16x32_bf16 v[244:247], v[60:63], a[120:123], v[244:247]\n"
"v_mfma_f32_16x16x32_bf16 v[252:255], v[60:63], a[124:127], v[252:255]\n"
"ds_read_b128 a[92:95], v14 offset:14336\n"
"v_mfma_f32_16x16x32_bf16 v[248:251], v[56:59], a[124:127], v[248:251]\n"
"s_waitcnt vmcnt(0)\n"
"s_barrier\n"
"s_waitcnt lgkmcnt(4)\n"
"v_mfma_f32_16x16x32_bf16 v[128:131], a[32:35], a[64:67], v[128:131]\n"
"ds_read_b128 a[96:99], v14 offset:16384\n"
"s_add_u32 m0, s74, 0\n"
"v_mfma_f32_16x16x32_bf16 v[132:135], a[36:39], a[64:67], v[132:135]\n"
"buffer_load_dwordx4 v84, s[4:7], s79 offen offset:0 lds\n"
"v_mfma_f32_16x16x32_bf16 v[140:143], a[36:39], a[68:71], v[140:143]\n"
"ds_read_b128 a[100:103], v14 offset:18432\n"
"v_mfma_f32_16x16x32_bf16 v[136:139], a[32:35], a[68:71], v[136:139]\n"
"v_mfma_f32_16x16x32_bf16 v[144:147], a[32:35], a[72:75], v[144:147]\n"
"ds_read_b128 a[104:107], v14 offset:20480\n"
"v_mfma_f32_16x16x32_bf16 v[148:151], a[36:39], a[72:75], v[148:151]\n"
"buffer_load_dwordx4 v85, s[4:7], s79 offen offset:1024 lds\n"
"v_mfma_f32_16x16x32_bf16 v[156:159], a[36:39], a[76:79], v[156:159]\n"
"ds_read_b128 a[108:111], v14 offset:22528\n"
"v_mfma_f32_16x16x32_bf16 v[152:155], a[32:35], a[76:79], v[152:155]\n"
"s_waitcnt lgkmcnt(4)\n"
"v_mfma_f32_16x16x32_bf16 v[160:163], a[32:35], a[80:83], v[160:163]\n"
"ds_read_b128 a[112:115], v14 offset:24576\n"
"v_mfma_f32_16x16x32_bf16 v[164:167], a[36:39], a[80:83], v[164:167]\n"
"v_cmp_ne_u64 vcc, s[70:71], v[120:121]\n"
"s_cbranch_vccnz .Lpslow2_%=\n"
".Lpok2_%=:\n"
"v_mfma_f32_16x16x32_bf16 v[172:175], a[36:39], a[84:87], v[172:175]\n"
"ds_read_b128 a[116:119], v14 offset:26624\n"
"v_mfma_f32_16x16x32_bf16 v[168:171], a[32:35], a[84:87], v[168:171]\n"
"buffer_load_dwordx4 a[48:51], v9, s[52:55], s82 offen offset:0 sc1\n"
"v_mfma_f32_16x16x32_bf16 v[176:179], a[32:35], a[88:91], v[176:179]\n"
"ds_read_b128 a[120:123], v14 offset:28672\n"
"v_mfma_f32_16x16x32_bf16 v[180:183], a[36:39], a[88:91], v[180:183]\n"
"v_mfma_f32_16x16x32_bf16 v[188:191], a[36:39], a[92:95], v[188:191]\n"
"ds_read_b128 a[124:127], v14 offset:30720\n"
"buffer_load_dwordx4 a[52:55], v10, s[52:55], s82 offen offset:0 sc1\n"
"v_mfma_f32_16x16x32_bf16 v[184:187], a[32:35], a[92:95], v[184:187]\n"
"s_waitcnt lgkmcnt(4)\n"
"v_mfma_f32_16x16x32_bf16 v[192:195], a[32:35], a[96:99], v[192:195]\n"
"ds_read_b128 a[64:67], v15 offset:0\n"
"v_mfma_f32_16x16x32_bf16 v[196:199], a[36:39], a[96:99], v[196:199]\n"
"v_mfma_f32_16x16x32_bf16 v[204:207], a[36:39], a[100:103], v[204:207]\n"
"ds_read_b128 a[68:71], v15 offset:2048\n"
"buffer_load_dwordx4 a[56:59], v9, s[52:55], s82 offen offset:2048 sc1\n"
"v_mfma_f32_16x16x32_bf16 v[200:203], a[32:35], a[100:103], v[200:203]\n"
"v_mfma_f32_16x16x32_bf16 v[208:211], a[32:35], a[104:107], v[208:211]\n"
"ds_read_b128 a[72:75], v15 offset:4096\n"
"v_mfma_f32_16x16x32_bf16 v[212:215], a[36:39], a[104:107], v[212:215]\n"
"v_mfma_f32_16x16x32_bf16 v[220:223], a[36:39], a[108:111], v[220:223]\n"
"ds_read_b128 a[76:79], v15 offset:6144\n"
"buffer_load_dwordx4 a[60:63], v10, s[52:55], s82 offen offset:2048 sc1\n"
"v_mfma_f32_16x16x32_bf16 v[216:219], a[32:35], a[108:111], v[216:219]\n"
"v_readlane_b32 s82, v11, 20\n"
"s_waitcnt lgkmcnt(4)\n"
"v_mfma_f32_16x16x32_bf16 v[224:227], a[32:35], a[112:115], v[224:227]\n"
"ds_read_b128 a[80:83], v15 offset:8192\n"
"v_mfma_f32_16x16x32_bf16 v[228:231], a[36:39], a[112:115], v[228:231]\n"
"v_mfma_f32_16x16x32_bf16 v[236:239], a[36:39], a[116:119], v[236:239]\n"
"ds_read_b128 a[84:87], v15 offset:10240\n"
"buffer_load_dwordx4 v86, s[4:7], s79 offen offset:2048 lds\n"
"v_mfma_f32_16x16x32_bf16 v[232:235], a[32:35], a[116:119], v[232:235]\n"
"v_mfma_f32_16x16x32_bf16 v[240:243], a[32:35], a[120:123], v[240:243]\n"
"ds_read_b128 a[88:91], v15 offset:12288\n"
"v_mfma_f32_16x16x32_bf16 v[244:247], a[36:39], a[120:123], v[244:247]\n"
"buffer_load_dwordx4 v87, s[4:7], s79 offen offset:3072 lds\n"
"v_mfma_f32_16x16x32_bf16 v[252:255], a[36:39], a[124:127], v[252:255]\n"
"ds_read_b128 a[92:95], v15 offset:14336\n"
"v_mfma_f32_16x16x32_bf16 v[248:251], a[32:35], a[124:127], v[248:251]\n"
"s_waitcnt lgkmcnt(4)\n"
"v_mfma_f32_16x16x32_bf16 v[128:131], a[40:43], a[64:67], v[128:131]\n"
"ds_read_b128 a[96:99], v15 offset:16384\n"
"s_add_u32 m0, s74, 4096\n"
"v_mfma_f32_16x16x32_bf16 v[132:135], a[44:47], a[64:67], v[132:135]\n"
"buffer_load_dwordx4 v88, s[4:7], s79 offen offset:0 lds\n"
"v_mfma_f32_16x16x32_bf16 v[140:143], a[44:47], a[68:71], v[140:143]\n"
"ds_read_b128 a[100:103], v15 offset:18432\n"
"v_mfma_f32_16x16x32_bf16 v[136:139], a[40:43], a[68:71], v[136:139]\n"
"v_mfma_f32_16x16x32_bf16 v[144:147], a[40:43], a[72:75], v[144:147]\n"
"ds_read_b128 a[104:107], v15 offset:20480\n"
"v_mfma_f32_16x16x32_bf16 v[148:151], a[44:47], a[72:75], v[148:151]\n"
"buffer_load_dwordx4 v89, s[4:7], s79 offen offset:1024 lds\n"
"v_mfma_f32_16x16x32_bf16 v[156:159], a[44:47], a[76:79], v[156:159]\n"
"ds_read_b128 a[108:111], v15 offset:22528\n"
"v_mfma_f32_16x16x32_bf16 v[152:155], a[40:43], a[76:79], v[152:155]\n"
"s_waitcnt lgkmcnt(4)\n"
"v_mfma_f32_16x16x32_bf16 v[160:163], a[40:43], a[80:83], v[160:163]\n"
"ds_read_b128 a[112:115], v15 offset:24576\n"
"v_mfma_f32_16x16x32_bf16 v[164:167], a[44:47], a[80:83], v[164:167]\n"
"buffer_load_dwordx4 v90, s[4:7], s79 offen offset:2048 lds\n"
"v_mfma_f32_16x16x32_bf16 v[172:175], a[44:47], a[84:87], v[172:175]\n"
"ds_read_b128 a[116:119], v15 offset:26624\n"
"v_mfma_f32_16x16x32_bf16 v[168:171], a[40:43], a[84:87], v[168:171]\n"
"v_mfma_f32_16x16x32_bf16 v[176:179], a[40:43], a[88:91], v[176:179]\n"
"ds_read_b128 a[120:123], v15 offset:28672\n"
"v_mfma_f32_16x16x32_bf16 v[180:183], a[44:47], a[88:91], v[180:183]\n"
"buffer_load_dwordx4 v91, s[4:7], s79 offen offset:3072 lds\n"
"v_readlane_b32 s79, v11, 5\n"
"v_mfma_f32_16x16x32_bf16 v[188:191], a[44:47], a[92:95], v[188:191]\n"
"ds_read_b128 a[124:127], v15 offset:30720\n"
"v_mfma_f32_16x16x32_bf16 v[184:187], a[40:43], a[92:95], v[184:187]\n"
"s_waitcnt lgkmcnt(4)\n"
"v_mfma_f32_16x16x32_bf16 v[192:195], a[40:43], a[96:99], v[192:195]\n"
"ds_read_b128 a[64:67], v14 offset:32768\n"
"buffer_load_dwordx4 a[0:3], v9, s[52:55], s82 offen offset:0 sc1\n"
"v_mfma_f32_16x16x32_bf16 v[196:199], a[44:47], a[96:99], v[196:199]\n"
"v_mfma_f32_16x16x32_bf16 v[204:207], a[44:47], a[100:103], v[204:207]\n"
"ds_read_b128 a[68:71], v14 offset:34816\n"
"v_mfma_f32_16x16x32_bf16 v[200:203], a[40:43], a[100:103], v[200:203]\n"
"v_mfma_f32_16x16x32_bf16 v[208:211], a[40:43], a[104:107], v[208:211]\n"
"ds_read_b128 a[72:75], v14 offset:36864\n"
"buffer_load_dwordx4 a[4:7], v10, s[52:55], s82 offen offset:0 sc1\n"
"v_mfma_f32_16x16x32_bf16 v[212:215], a[44:47], a[104:107], v[212:215]\n"
"v_mfma_f32_16x16x32_bf16 v[220:223], a[44:47], a[108:111], v[220:223]\n"
"ds_read_b128 a[76:79], v14 offset:38912\n"
"v_mfma_f32_16x16x32_bf16 v[216:219], a[40:43], a[108:111], v[216:219]\n"
"s_waitcnt lgkmcnt(4)\n"
"v_mfma_f32_16x16x32_bf16 v[224:227], a[40:43], a[112:115], v[224:227]\n"
"ds_read_b128 a[80:83], v14 offset:40960\n"
"buffer_load_dwordx4 a[8:11], v9, s[52:55], s82 offen offset:2048 sc1\n"
"v_mfma_f32_16x16x32_bf16 v[228:231], a[44:47], a[112:115], v[228:231]\n"
"v_mfma_f32_16x16x32_bf16 v[236:239], a[44:47], a[116:119], v[236:239]\n"
"ds_read_b128 a[84:87], v14 offset:43008\n"
"v_mfma_f32_16x16x32_bf16 v[232:235], a[40:43], a[116:119], v[232:235]\n"
"v_mfma_f32_16x16x32_bf16 v[240:243], a[40:43], a[120:123], v[240:243]\n"
"ds_read_b128 a[88:91], v14 offset:45056\n"
"buffer_load_dwordx4 a[12:15], v10, s[52:55], s82 offen offset:2048 sc1\n"
"v_mfma_f32_16x16x32_bf16 v[244:247], a[44:47], a[120:123], v[244:247]\n"
"v_readlane_b32 s82, v11, 21\n"
"v_mfma_f32_16x16x32_bf16 v[252:255], a[44:47], a[124:127], v[252:255]\n"
"ds_read_b128 a[92:95], v14 offset:47104\n"
"v_mfma_f32_16x16x32_bf16 v[248:251], a[40:43], a[124:127], v[248:251]\n"
"s_waitcnt vmcnt(4)\n"
"s_barrier\n"
"s_waitcnt lgkmcnt(4)\n"
"v_mfma_f32_16x16x32_bf16 v[128:131], a[48:51], a[64:67], v[128:131]\n"
"ds_read_b128 a[96:99], v14 offset:49152\n"
"s_add_u32 m0, s74, 32768\n"
"v_mfma_f32_16x16x32_bf16 v[132:135], a[52:55], a[64:67], v[132:135]\n"
"buffer_load_dwordx4 v84, s[4:7], s79 offen offset:0 lds\n"
"v_mfma_f32_16x16x32_bf16 v[140:143], a[52:55], a[68:71], v[140:143]\n"
"ds_read_b128 a[100:103], v14 offset:51200\n"
"v_mfma_f32_16x16x32_bf16 v[136:139], a[48:51], a[68:71], v[136:139]\n"
"v_mfma_f32_16x16x32_bf16 v[144:147], a[48:51], a[72:75], v[144:147]\n"
"ds_read_b128 a[104:107], v14 offset:53248\n"
"buffer_load_dwordx4 v85, s[4:7], s79 offen offset:1024 lds\n"
"v_mfma_f32_16x16x32_bf16 v[148:151], a[52:55], a[72:75], v[148:151]\n"
"v_mfma_f32_16x16x32_bf16 v[156:159], a[52:55], a[76:79], v[156:159]\n"
"ds_read_b128 a[108:111], v14 offset:55296\n"
"v_mfma_f32_16x16x32_bf16 v[152:155], a[48:51], a[76:79], v[152:155]\n"
"buffer_load_dwordx4 v86, s[4:7], s79 offen offset:2048 lds\n"
"s_waitcnt lgkmcnt(4)\n"
"v_mfma_f32_16x16x32_bf16 v[160:163], a[48:51], a[80:83], v[160:163]\n"
"ds_read_b128 a[112:115], v14 offset:57344\n"
"v_mfma_f32_16x16x32_bf16 v[164:167], a[52:55], a[80:83], v[164:167]\n"
"v_mfma_f32_16x16x32_bf16 v[172:175], a[52:55], a[84:87], v[172:175]\n"
"ds_read_b128 a[116:119], v14 offset:59392\n"
"buffer_load_dwordx4 v87, s[4:7], s79 offen offset:3072 lds\n"
"v_mfma_f32_16x16x32_bf16 v[168:171], a[48:51], a[84:87], v[168:171]\n"
"v_mfma_f32_16x16x32_bf16 v[176:179], a[48:51], a[88:91], v[176:179]\n"
"ds_read_b128 a[120:123], v14 offset:61440\n"
"s_add_u32 m0, s74, 36864\n"
"v_mfma_f32_16x16x32_bf16 v[180:183], a[52:55], a[88:91], v[180:183]\n"
"buffer_load_dwordx4 v88, s[4:7], s79 offen offset:0 lds\n"
"v_mfma_f32_16x16x32_bf16 v[188:191], a[52:55], a[92:95], v[188:191]\n"
"ds_read_b128 a[124:127], v14 offset:63488\n"
"v_mfma_f32_16x16x32_bf16 v[184:187], a[48:51], a[92:95], v[184:187]\n"
"s_waitcnt lgkmcnt(4)\n"
"v_mfma_f32_16x16x32_bf16 v[192:195], a[48:51], a[96:99], v[192:195]\n"
"ds_read_b128 a[64:67], v15 offset:32768\n"
"buffer_load_dwordx4 v89, s[4:7], s79 offen offset:1024 lds\n"
"v_mfma_f32_16x16x32_bf16 v[196:199], a[52:55], a[96:99], v[196:199]\n"
"v_mfma_f32_16x16x32_bf16 v[204:207], a[52:55], a[100:103], v[204:207]\n"
"ds_read_b128 a[68:71], v15 offset:34816\n"
"v_mfma_f32_16x16x32_bf16 v[200:203], a[48:51], a[100:103], v[200:203]\n"
"buffer_load_dwordx4 v90, s[4:7], s79 offen offset:2048 lds\n"
"v_mfma_f32_16x16x32_bf16 v[208:211], a[48:51], a[104:107], v[208:211]\n"
"ds_read_b128 a[72:75], v15 offset:36864\n"
"v_mfma_f32_16x16x32_bf16 v[212:215], a[52:55], a[104:107], v[212:215]\n"
"v_mfma_f32_16x16x32_bf16 v[220:223], a[52:55], a[108:111], v[220:223]\n"
"ds_read_b128 a[76:79], v15 offset:38912\n"
"v_mfma_f32_16x16x32_bf16 v[216:219], a[48:51], a[108:111], v[216:219]\n"
"buffer_load_dwordx4 v91, s[4:7], s79 offen offset:3072 lds\n"
"s_waitcnt lgkmcnt(4)\n"
"v_mfma_f32_16x16x32_bf16 v[224:227], a[48:51], a[112:115], v[224:227]\n"
"ds_read_b128 a[80:83], v15 offset:40960\n"
"v_readlane_b32 s79, v11, 6\n"
"v_mfma_f32_16x16x32_bf16 v[228:231], a[52:55], a[112:115], v[228:231]\n"
"v_mfma_f32_16x16x32_bf16 v[236:239], a[52:55], a[116:119], v[236:239]\n"
"ds_read_b128 a[84:87], v15 offset:43008\n"
"buffer_load_dwordx4 a[16:19], v9, s[52:55], s82 offen offset:0 sc1\n"
"v_mfma_f32_16x16x32_bf16 v[232:235], a[48:51], a[116:119], v[232:235]\n"
"v_mfma_f32_16x16x32_bf16 v[240:243], a[48:51], a[120:123], v[240:243]\n"
"ds_read_b128 a[88:91], v15 offset:45056\n"
"v_mfma_f32_16x16x32_bf16 v[244:247], a[52:55], a[120:123], v[244:247]\n"
"buffer_load_dwordx4 a[20:23], v10, s[52:55], s82 offen offset:0 sc1\n"
"v_mfma_f32_16x16x32_bf16 v[252:255], a[52:55], a[124:127], v[252:255]\n"
"ds_read_b128 a[92:95], v15 offset:47104\n"
"v_mfma_f32_16x16x32_bf16 v[248:251], a[48:51], a[124:127], v[248:251]\n"
"s_waitcnt lgkmcnt(4)\n"
"v_mfma_f32_16x16x32_bf16 v[128:131], a[56:59], a[64:67], v[128:131]\n"
"ds_read_b128 a[96:99], v15 offset:49152\n"
"buffer_load_dwordx4 a[24:27], v9, s[52:55], s82 offen offset:2048 sc1\n"
"v_mfma_f32_16x16x32_bf16 v[132:135], a[60:63], a[64:67], v[132:135]\n"
"v_mfma_f32_16x16x32_bf16 v[140:143], a[60:63], a[68:71], v[140:143]\n"
"ds_read_b128 a[100:103], v15 offset:51200\n"
"v_mfma_f32_16x16x32_bf16 v[136:139], a[56:59], a[68:71], v[136:139]\n"
"buffer_load_dwordx4 a[28:31], v10, s[52:55], s82 offen offset:2048 sc1\n"
"v_readlane_b32 s82, v11, 22\n"
"v_mfma_f32_16x16x32_bf16 v[144:147], a[56:59], a[72:75], v[144:147]\n"
"ds_read_b128 a[104:107], v15 offset:53248\n"
"v_mfma_f32_16x16x32_bf16 v[148:151], a[60:63], a[72:75], v[148:151]\n"
"s_add_u32 m0, s74, 65536\n"
"v_mfma_f32_16x16x32_bf16 v[156:159], a[60:63], a[76:79], v[156:159]\n"
"ds_read_b128 a[108:111], v15 offset:55296\n"
"buffer_load_dwordx4 v84, s[4:7], s79 offen offset:0 lds\n"
"v_mfma_f32_16x16x32_bf16 v[152:155], a[56:59], a[76:79], v[152:155]\n"
"s_waitcnt lgkmcnt(4)\n"
"v_mfma_f32_16x16x32_bf16 v[160:163], a[56:59], a[80:83], v[160:163]\n"
"ds_read_b128 a[112:115], v15 offset:57344\n"
"v_mfma_f32_16x16x32_bf16 v[164:167], a[60:63], a[80:83], v[164:167]\n"
"buffer_load_dwordx4 v85, s[4:7], s79 offen offset:1024 lds\n"
"v_mfma_f32_16x16x32_bf16 v[172:175], a[60:63], a[84:87], v[172:175]\n"
"ds_read_b128 a[116:119], v15 offset:59392\n"
"v_mfma_f32_16x16x32_bf16 v[168:171], a[56:59], a[84:87], v[168:171]\n"
"v_mfma_f32_16x16x32_bf16 v[176:179], a[56:59], a[88:91], v[176:179]\n"
"ds_read_b128 a[120:123], v15 offset:61440\n"
"v_mfma_f32_16x16x32_bf16 v[180:183], a[60:63], a[88:91], v[180:183]\n"
"buffer_load_dwordx4 v86, s[4:7], s79 offen offset:2048 lds\n"
"v_mfma_f32_16x16x32_bf16 v[188:191], a[60:63], a[92:95], v[188:191]\n"
"ds_read_b128 a[124:127], v15 offset:63488\n"
"v_mfma_f32_16x16x32_bf16 v[184:187], a[56:59], a[92:95], v[184:187]\n"
"s_waitcnt lgkmcnt(4)\n"
"v_mfma_f32_16x16x32_bf16 v[192:195], a[56:59], a[96:99], v[192:195]\n"
"ds_read_b128 a[64:67], v12 offset:0\n"
"buffer_load_dwordx4 v87, s[4:7], s79 offen offset:3072 lds\n"
"v_mfma_f32_16x16x32_bf16 v[196:199], a[60:63], a[96:99], v[196:199]\n"
"v_mfma_f32_16x16x32_bf16 v[204:207], a[60:63], a[100:103], v[204:207]\n"
"ds_read_b128 a[68:71], v12 offset:2048\n"
"s_add_u32 m0, s74, 69632\n"
"v_mfma_f32_16x16x32_bf16 v[200:203], a[56:59], a[100:103], v[200:203]\n"
"buffer_load_dwordx4 v88, s[4:7], s79 offen offset:0 lds\n"
"v_mfma_f32_16x16x32_bf16 v[208:211], a[56:59], a[104:107], v[208:211]\n"
"ds_read_b128 a[72:75], v12 offset:4096\n"
"v_mfma_f32_16x16x32_bf16 v[212:215], a[60:63], a[104:107], v[212:215]\n"
"v_mfma_f32_16x16x32_bf16 v[220:223], a[60:63], a[108:111], v[220:223]\n"
"ds_read_b128 a[76:79], v12 offset:6144\n"
"buffer_load_dwordx4 v89, s[4:7], s79 offen offset:1024 lds\n"
"v_mfma_f32_16x16x32_bf16 v[216:219], a[56:59], a[108:111], v[216:219]\n"
"s_waitcnt lgkmcnt(4)\n"
"v_mfma_f32_16x16x32_bf16 v[224:227], a[56:59], a[112:115], v[224:227]\n"
"ds_read_b128 a[80:83], v12 offset:8192\n"
"v_mfma_f32_16x16x32_bf16 v[228:231], a[60:63], a[112:115], v[228:231]\n"
"buffer_load_dwordx4 v90, s[4:7], s79 offen offset:2048 lds\n"
"v_mfma_f32_16x16x32_bf16 v[236:239], a[60:63], a[116:119], v[236:239]\n"
"ds_read_b128 a[84:87], v12 offset:10240\n"
"v_mfma_f32_16x16x32_bf16 v[232:235], a[56:59], a[116:119], v[232:235]\n"
"v_mfma_f32_16x16x32_bf16 v[240:243], a[56:59], a[120:123], v[240:243]\n"
"ds_read_b128 a[88:91], v12 offset:12288\n"
"buffer_load_dwordx4 v91, s[4:7], s79 offen offset:3072 lds\n"
"v_mfma_f32_16x16x32_bf16 v[244:247], a[60:63], a[120:123], v[244:247]\n"
"v_readlane_b32 s79, v11, 7\n"
"v_mfma_f32_16x16x32_bf16 v[252:255], a[60:63], a[124:127], v[252:255]\n"
"ds_read_b128 a[92:95], v12 offset:14336\n"
"v_mfma_f32_16x16x32_bf16 v[248:251], a[56:59], a[124:127], v[248:251]\n"
"s_waitcnt vmcnt(12)\n"
"s_barrier\n"
"s_waitcnt lgkmcnt(4)\n"
"v_mfma_f32_16x16x32_bf16 v[128:131], a[0:3], a[64:67], v[128:131]\n"
"ds_read_b128 a[96:99], v12 offset:16384\n"
"v_mfma_f32_16x16x32_bf16 v[132:135], a[4:7], a[64:67], v[132:135]\n"
"buffer_load_dwordx4 a[32:35], v9, s[52:55], s82 offen offset:0 sc1\n"
"v_mfma_f32_16x16x32_bf16 v[140:143], a[4:7], a[68:71], v[140:143]\n"
"ds_read_b128 a[100:103], v12 offset:18432\n"
"v_mfma_f32_16x16x32_bf16 v[136:139], a[0:3], a[68:71], v[136:139]\n"
"v_mfma_f32_16x16x32_bf16 v[144:147], a[0:3], a[72:75], v[144:147]\n"
"ds_read_b128 a[104:107], v12 offset:20480\n"
"buffer_load_dwordx4 a[36:39], v10, s[52:55], s82 offen offset:0 sc1\n"
"v_mfma_f32_16x16x32_bf16 v[148:151], a[4:7], a[72:75], v[148:151]\n"
"v_mfma_f32_16x16x32_bf16 v[156:159], a[4:7], a[76:79], v[156:159]\n"
"ds_read_b128 a[108:111], v12 offset:22528\n"
"v_mfma_f32_16x16x32_bf16 v[152:155], a[0:3], a[76:79], v[152:155]\n"
"s_waitcnt lgkmcnt(4)\n"
"v_mfma_f32_16x16x32_bf16 v[160:163], a[0:3], a[80:83], v[160:163]\n"
"ds_read_b128 a[112:115], v12 offset:24576\n"
"buffer_load_dwordx4 a[40:43], v9, s[52:55], s82 offen offset:2048 sc1\n"
"v_mfma_f32_16x16x32_bf16 v[164:167], a[4:7], a[80:83], v[164:167]\n"
"v_mfma_f32_16x16x32_bf16 v[172:175], a[4:7], a[84:87], v[172:175]\n"
"ds_read_b128 a[116:119], v12 offset:26624\n"
"v_mfma_f32_16x16x32_bf16 v[168:171], a[0:3], a[84:87], v[168:171]\n"
"v_mfma_f32_16x16x32_bf16 v[176:179], a[0:3], a[88:91], v[176:179]\n"
"ds_read_b128 a[120:123], v12 offset:28672\n"
"buffer_load_dwordx4 a[44:47], v10, s[52:55], s82 offen offset:2048 sc1\n"
"v_mfma_f32_16x16x32_bf16 v[180:183], a[4:7], a[88:91], v[180:183]\n"
"v_readlane_b32 s82, v11, 23\n"
"v_mfma_f32_16x16x32_bf16 v[188:191], a[4:7], a[92:95], v[188:191]\n"
"ds_read_b128 a[124:127], v12 offset:30720\n"
"v_mfma_f32_16x16x32_bf16 v[184:187], a[0:3], a[92:95], v[184:187]\n"
"s_add_u32 m0, s74, 98304\n"
"s_waitcnt lgkmcnt(4)\n"
"v_mfma_f32_16x16x32_bf16 v[192:195], a[0:3], a[96:99], v[192:195]\n"
"ds_read_b128 a[64:67], v13 offset:0\n"
"buffer_load_dwordx4 v84, s[4:7], s79 offen offset:0 lds\n"
"v_mfma_f32_16x16x32_bf16 v[196:199], a[4:7], a[96:99], v[196:199]\n"
"v_mfma_f32_16x16x32_bf16 v[204:207], a[4:7], a[100:103], v[204:207]\n"
"ds_read_b128 a[68:71], v13 offset:2048\n"
"v_mfma_f32_16x16x32_bf16 v[200:203], a[0:3], a[100:103], v[200:203]\n"
"v_mfma_f32_16x16x32_bf16 v[208:211], a[0:3], a[104:107], v[208:211]\n"
"ds_read_b128 a[72:75], v13 offset:4096\n"
"buffer_load_dwordx4 v85, s[4:7], s79 offen offset:1024 lds\n"
"v_mfma_f32_16x16x32_bf16 v[212:215], a[4:7], a[104:107], v[212:215]\n"
"v_mfma_f32_16x16x32_bf16 v[220:223], a[4:7], a[108:111], v[220:223]\n"
"ds_read_b128 a[76:79], v13 offset:6144\n"
"v_mfma_f32_16x16x32_bf16 v[216:219], a[0:3], a[108:111], v[216:219]\n"
"s_waitcnt lgkmcnt(4)\n"
"v_mfma_f32_16x16x32_bf16 v[224:227], a[0:3], a[112:115], v[224:227]\n"
"ds_read_b128 a[80:83], v13 offset:8192\n"
"buffer_load_dwordx4 v86, s[4:7], s79 offen offset:2048 lds\n"
"v_mfma_f32_16x16x32_bf16 v[228:231], a[4:7], a[112:115], v[228:231]\n"
"v_mfma_f32_16x16x32_bf16 v[236:239], a[4:7], a[116:119], v[236:239]\n"
"ds_read_b128 a[84:87], v13 offset:10240\n"
"v_mfma_f32_16x16x32_bf16 v[232:235], a[0:3], a[116:119], v[232:235]\n"
"v_mfma_f32_16x16x32_bf16 v[240:243], a[0:3], a[120:123], v[240:243]\n"
"ds_read_b128 a[88:91], v13 offset:12288\n"
"buffer_load_dwordx4 v87, s[4:7], s79 offen offset:3072 lds\n"
"v_mfma_f32_16x16x32_bf16 v[244:247], a[4:7], a[120:123], v[244:247]\n"
"v_mfma_f32_16x16x32_bf16 v[252:255], a[4:7], a[124:127], v[252:255]\n"
"ds_read_b128 a[92:95], v13 offset:14336\n"
"v_mfma_f32_16x16x32_bf16 v[248:251], a[0:3], a[124:127], v[248:251]\n"
"s_add_u32 m0, s74, 102400\n"
"s_waitcnt lgkmcnt(4)\n"
"v_mfma_f32_16x16x32_bf16 v[128:131], a[8:11], a[64:67], v[128:131]\n"
"ds_read_b128 a[96:99], v13 offset:16384\n"
"buffer_load_dwordx4 v88, s[4:7], s79 offen offset:0 lds\n"
"v_mfma_f32_16x16x32_bf16 v[132:135], a[12:15], a[64:67], v[132:135]\n"
"v_mfma_f32_16x16x32_bf16 v[140:143], a[12:15], a[68:71], v[140:143]\n"
"ds_read_b128 a[100:103], v13 offset:18432\n"
"v_mfma_f32_16x16x32_bf16 v[136:139], a[8:11], a[68:71], v[136:139]\n"
"v_mfma_f32_16x16x32_bf16 v[144:147], a[8:11], a[72:75], v[144:147]\n"
"ds_read_b128 a[104:107], v13 offset:20480\n"
"buffer_load_dwordx4 v89, s[4:7], s79 offen offset:1024 lds\n"
"v_mfma_f32_16x16x32_bf16 v[148:151], a[12:15], a[72:75], v[148:151]\n"
"v_mfma_f32_16x16x32_bf16 v[156:159], a[12:15], a[76:79], v[156:159]\n"
"ds_read_b128 a[108:111], v13 offset:22528\n"
"v_mfma_f32_16x16x32_bf16 v[152:155], a[8:11], a[76:79], v[152:155]\n"
"s_waitcnt lgkmcnt(4)\n"
"v_mfma_f32_16x16x32_bf16 v[160:163], a[8:11], a[80:83], v[160:163]\n"
"ds_read_b128 a[112:115], v13 offset:24576\n"
"buffer_load_dwordx4 v90, s[4:7], s79 offen offset:2048 lds\n"
"v_mfma_f32_16x16x32_bf16 v[164:167], a[12:15], a[80:83], v[164:167]\n"
"v_mfma_f32_16x16x32_bf16 v[172:175], a[12:15], a[84:87], v[172:175]\n"
"ds_read_b128 a[116:119], v13 offset:26624\n"
"v_mfma_f32_16x16x32_bf16 v[168:171], a[8:11], a[84:87], v[168:171]\n"
"v_mfma_f32_16x16x32_bf16 v[176:179], a[8:11], a[88:91], v[176:179]\n"
"ds_read_b128 a[120:123], v13 offset:28672\n"
"buffer_load_dwordx4 v91, s[4:7], s79 offen offset:3072 lds\n"
"v_mfma_f32_16x16x32_bf16 v[180:183], a[12:15], a[88:91], v[180:183]\n"
"v_readlane_b32 s79, v11, 8\n"
"v_mfma_f32_16x16x32_bf16 v[188:191], a[12:15], a[92:95], v[188:191]\n"
"ds_read_b128 a[124:127], v13 offset:30720\n"
"v_mfma_f32_16x16x32_bf16 v[184:187], a[8:11], a[92:95], v[184:187]\n"
"s_waitcnt lgkmcnt(4)\n"
"v_mfma_f32_16x16x32_bf16 v[192:195], a[8:11], a[96:99], v[192:195]\n"
"ds_read_b128 a[64:67], v12 offset:32768\n"
"buffer_load_dwordx4 a[48:51], v9, s[52:55], s82 offen offset:0 sc1\n"
"v_mfma_f32_16x16x32_bf16 v[196:199], a[12:15], a[96:99], v[196:199]\n"
"v_mfma_f32_16x16x32_bf16 v[204:207], a[12:15], a[100:103], v[204:207]\n"
"ds_read_b128 a[68:71], v12 offset:34816\n"
"v_mfma_f32_16x16x32_bf16 v[200:203], a[8:11], a[100:103], v[200:203]\n"
"v_mfma_f32_16x16x32_bf16 v[208:211], a[8:11], a[104:107], v[208:211]\n"
"ds_read_b128 a[72:75], v12 offset:36864\n"
"buffer_load_dwordx4 a[52:55], v10, s[52:55], s82 offen offset:0 sc1\n"
"v_mfma_f32_16x16x32_bf16 v[212:215], a[12:15], a[104:107], v[212:215]\n"
"v_mfma_f32_16x16x32_bf16 v[220:223], a[12:15], a[108:111], v[220:223]\n"
"ds_read_b128 a[76:79], v12 offset:38912\n"
"v_mfma_f32_16x16x32_bf16 v[216:219], a[8:11], a[108:111], v[216:219]\n"
"s_waitcnt lgkmcnt(4)\n"
"v_mfma_f32_16x16x32_bf16 v[224:227], a[8:11], a[112:115], v[224:227]\n"
"ds_read_b128 a[80:83], v12 offset:40960\n"
"buffer_load_dwordx4 a[56:59], v9, s[52:55], s82 offen offset:2048 sc1\n"
"v_mfma_f32_16x16x32_bf16 v[228:231], a[12:15], a[112:115], v[228:231]\n"
"v_mfma_f32_16x16x32_bf16 v[236:239], a[12:15], a[116:119], v[236:239]\n"
"ds_read_b128 a[84:87], v12 offset:43008\n"
"v_mfma_f32_16x16x32_bf16 v[232:235], a[8:11], a[116:119], v[232:235]\n"
"v_mfma_f32_16x16x32_bf16 v[240:243], a[8:11], a[120:123], v[240:243]\n"
"ds_read_b128 a[88:91], v12 offset:45056\n"
"buffer_load_dwordx4 a[60:63], v10, s[52:55], s82 offen offset:2048 sc1\n"
"v_mfma_f32_16x16x32_bf16 v[244:247], a[12:15], a[120:123], v[244:247]\n"
"v_readlane_b32 s82, v11, 24\n"
"v_mfma_f32_16x16x32_bf16 v[252:255], a[12:15], a[124:127], v[252:255]\n"
"ds_read_b128 a[92:95], v12 offset:47104\n"
"v_mfma_f32_16x16x32_bf16 v[248:251], a[8:11], a[124:127], v[248:251]\n"
"s_waitcnt vmcnt(16)\n"
"s_barrier\n"
"s_waitcnt lgkmcnt(4)\n"
"v_mfma_f32_16x16x32_bf16 v[128:131], a[16:19], a[64:67], v[128:131]\n"
"ds_read_b128 a[96:99], v12 offset:49152\n"
"buffer_load_dword a232, v24, s[56:59], s95 offen offset:0\n"
"v_mfma_f32_16x16x32_bf16 v[132:135], a[20:23], a[64:67], v[132:135]\n"
"v_readlane_b32 s95, v11, 14\n"
"s_nop 4\n"
"buffer_load_dword a233, v24, s[56:59], s95 offen offset:0\n"
"v_mfma_f32_16x16x32_bf16 v[140:143], a[20:23], a[68:71], v[140:143]\n"
"ds_read_b128 a[100:103], v12 offset:51200\n"
"s_add_u32 m0, s74, 0\n"
"v_mfma_f32_16x16x32_bf16 v[136:139], a[16:19], a[68:71], v[136:139]\n"
"buffer_load_dwordx4 v84, s[4:7], s79 offen offset:0 lds\n"
"v_mfma_f32_16x16x32_bf16 v[144:147], a[16:19], a[72:75], v[144:147]\n"
"ds_read_b128 a[104:107], v12 offset:53248\n"
"v_mfma_f32_16x16x32_bf16 v[148:151], a[20:23], a[72:75], v[148:151]\n"
"v_mfma_f32_16x16x32_bf16 v[156:159], a[20:23], a[76:79], v[156:159]\n"
"ds_read_b128 a[108:111], v12 offset:55296\n"
"v_mfma_f32_16x16x32_bf16 v[152:155], a[16:19], a[76:79], v[152:155]\n"
"s_waitcnt lgkmcnt(4)\n"
"v_mfma_f32_16x16x32_bf16 v[160:163], a[16:19], a[80:83], v[160:163]\n"
"ds_read_b128 a[112:115], v12 offset:57344\n"
"buffer_load_dwordx4 v85, s[4:7], s79 offen offset:1024 lds\n"
"v_mfma_f32_16x16x32_bf16 v[164:167], a[20:23], a[80:83], v[164:167]\n"
"v_mfma_f32_16x16x32_bf16 v[172:175], a[20:23], a[84:87], v[172:175]\n"
"ds_read_b128 a[116:119], v12 offset:59392\n"
"v_mfma_f32_16x16x32_bf16 v[168:171], a[16:19], a[84:87], v[168:171]\n"
"v_mfma_f32_16x16x32_bf16 v[176:179], a[16:19], a[88:91], v[176:179]\n"
"ds_read_b128 a[120:123], v12 offset:61440\n"
"v_mfma_f32_16x16x32_bf16 v[180:183], a[20:23], a[88:91], v[180:183]\n"
"v_mfma_f32_16x16x32_bf16 v[188:191], a[20:23], a[92:95], v[188:191]\n"
"ds_read_b128 a[124:127], v12 offset:63488\n"
"v_mfma_f32_16x16x32_bf16 v[184:187], a[16:19], a[92:95], v[184:187]\n"
"s_waitcnt lgkmcnt(4)\n"
"v_mfma_f32_16x16x32_bf16 v[192:195], a[16:19], a[96:99], v[192:195]\n"
"ds_read_b128 a[64:67], v13 offset:32768\n"
"buffer_load_dwordx4 v86, s[4:7], s79 offen offset:2048 lds\n"
"v_mfma_f32_16x16x32_bf16 v[196:199], a[20:23], a[96:99], v[196:199]\n"
"v_mfma_f32_16x16x32_bf16 v[204:207], a[20:23], a[100:103], v[204:207]\n"
"ds_read_b128 a[68:71], v13 offset:34816\n"
"v_mfma_f32_16x16x32_bf16 v[200:203], a[16:19], a[100:103], v[200:203]\n"
"v_mfma_f32_16x16x32_bf16 v[208:211], a[16:19], a[104:107], v[208:211]\n"
"ds_read_b128 a[72:75], v13 offset:36864\n"
"v_mfma_f32_16x16x32_bf16 v[212:215], a[20:23], a[104:107], v[212:215]\n"
"v_mfma_f32_16x16x32_bf16 v[220:223], a[20:23], a[108:111], v[220:223]\n"
"ds_read_b128 a[76:79], v13 offset:38912\n"
"v_mfma_f32_16x16x32_bf16 v[216:219], a[16:19], a[108:111], v[216:219]\n"
"s_waitcnt lgkmcnt(4)\n"
"v_mfma_f32_16x16x32_bf16 v[224:227], a[16:19], a[112:115], v[224:227]\n"
"ds_read_b128 a[80:83], v13 offset:40960\n"
"buffer_load_dwordx4 v87, s[4:7], s79 offen offset:3072 lds\n"
"v_mfma_f32_16x16x32_bf16 v[228:231], a[20:23], a[112:115], v[228:231]\n"
"v_mfma_f32_16x16x32_bf16 v[236:239], a[20:23], a[116:119], v[236:239]\n"
"ds_read_b128 a[84:87], v13 offset:43008\n"
"v_mfma_f32_16x16x32_bf16 v[232:235], a[16:19], a[116:119], v[232:235]\n"
"v_mfma_f32_16x16x32_bf16 v[240:243], a[16:19], a[120:123], v[240:243]\n"
"ds_read_b128 a[88:91], v13 offset:45056\n"
"v_mfma_f32_16x16x32_bf16 v[244:247], a[20:23], a[120:123], v[244:247]\n"
"v_mfma_f32_16x16x32_bf16 v[252:255], a[20:23], a[124:127], v[252:255]\n"
"ds_read_b128 a[92:95], v13 offset:47104\n"
"v_mfma_f32_16x16x32_bf16 v[248:251], a[16:19], a[124:127], v[248:251]\n"
"s_add_u32 m0, s74, 4096\n"
"s_waitcnt lgkmcnt(4)\n"
"v_mfma_f32_16x16x32_bf16 v[128:131], a[24:27], a[64:67], v[128:131]\n"
"ds_read_b128 a[96:99], v13 offset:49152\n"
"buffer_load_dwordx4 v88, s[4:7], s79 offen offset:0 lds\n"
"v_mfma_f32_16x16x32_bf16 v[132:135], a[28:31], a[64:67], v[132:135]\n"
"v_mfma_f32_16x16x32_bf16 v[140:143], a[28:31], a[68:71], v[140:143]\n"
"ds_read_b128 a[100:103], v13 offset:51200\n"
"v_mfma_f32_16x16x32_bf16 v[136:139], a[24:27], a[68:71], v[136:139]\n"
"v_mfma_f32_16x16x32_bf16 v[144:147], a[24:27], a[72:75], v[144:147]\n"
"ds_read_b128 a[104:107], v13 offset:53248\n"
"v_mfma_f32_16x16x32_bf16 v[148:151], a[28:31], a[72:75], v[148:151]\n"
"v_mfma_f32_16x16x32_bf16 v[156:159], a[28:31], a[76:79], v[156:159]\n"
"ds_read_b128 a[108:111], v13 offset:55296\n"
"v_mfma_f32_16x16x32_bf16 v[152:155], a[24:27], a[76:79], v[152:155]\n"
"s_waitcnt lgkmcnt(4)\n"
"v_mfma_f32_16x16x32_bf16 v[160:163], a[24:27], a[80:83], v[160:163]\n"
"ds_read_b128 a[112:115], v13 offset:57344\n"
"buffer_load_dwordx4 v89, s[4:7], s79 offen offset:1024 lds\n"
"v_mfma_f32_16x16x32_bf16 v[164:167], a[28:31], a[80:83], v[164:167]\n"
"v_mfma_f32_16x16x32_bf16 v[172:175], a[28:31], a[84:87], v[172:175]\n"
"ds_read_b128 a[116:119], v13 offset:59392\n"
"v_mfma_f32_16x16x32_bf16 v[168:171], a[24:27], a[84:87], v[168:171]\n"
"v_mfma_f32_16x16x32_bf16 v[176:179], a[24:27], a[88:91], v[176:179]\n"
"ds_read_b128 a[120:123], v13 offset:61440\n"
"v_mfma_f32_16x16x32_bf16 v[180:183], a[28:31], a[88:91], v[180:183]\n"
"v_mfma_f32_16x16x32_bf16 v[188:191], a[28:31], a[92:95], v[188:191]\n"
"ds_read_b128 a[124:127], v13 offset:63488\n"
"v_mfma_f32_16x16x32_bf16 v[184:187], a[24:27], a[92:95], v[184:187]\n"
"s_waitcnt lgkmcnt(4)\n"
"v_mfma_f32_16x16x32_bf16 v[192:195], a[24:27], a[96:99], v[192:195]\n"
"ds_read_b128 a[64:67], v14 offset:0\n"
"buffer_load_dwordx4 v90, s[4:7], s79 offen offset:2048 lds\n"
"v_mfma_f32_16x16x32_bf16 v[196:199], a[28:31], a[96:99], v[196:199]\n"
"v_mfma_f32_16x16x32_bf16 v[204:207], a[28:31], a[100:103], v[204:207]\n"
"ds_read_b128 a[68:71], v14 offset:2048\n"
"v_mfma_f32_16x16x32_bf16 v[200:203], a[24:27], a[100:103], v[200:203]\n"
"v_mfma_f32_16x16x32_bf16 v[208:211], a[24:27], a[104:107], v[208:211]\n"
"ds_read_b128 a[72:75], v14 offset:4096\n"
"v_mfma_f32_16x16x32_bf16 v[212:215], a[28:31], a[104:107], v[212:215]\n"
"v_mfma_f32_16x16x32_bf16 v[220:223], a[28:31], a[108:111], v[220:223]\n"
"ds_read_b128 a[76:79], v14 offset:6144\n"
"v_mfma_f32_16x16x32_bf16 v[216:219], a[24:27], a[108:111], v[216:219]\n"
"s_waitcnt lgkmcnt(4)\n"
"v_mfma_f32_16x16x32_bf16 v[224:227], a[24:27], a[112:115], v[224:227]\n"
"ds_read_b128 a[80:83], v14 offset:8192\n"
"buffer_load_dwordx4 v91, s[4:7], s79 offen offset:3072 lds\n"
"v_mfma_f32_16x16x32_bf16 v[228:231], a[28:31], a[112:115], v[228:231]\n"
"v_readlane_b32 s79, v11, 9\n"
"v_mfma_f32_16x16x32_bf16 v[236:239], a[28:31], a[116:119], v[236:239]\n"
"ds_read_b128 a[84:87], v14 offset:10240\n"
"v_mfma_f32_16x16x32_bf16 v[232:235], a[24:27], a[116:119], v[232:235]\n"
"v_mfma_f32_16x16x32_bf16 v[240:243], a[24:27], a[120:123], v[240:243]\n"
"ds_read_b128 a[88:91], v14 offset:12288\n"
"v_mfma_f32_16x16x32_bf16 v[244:247], a[28:31], a[120:123], v[244:247]\n"
"v_mfma_f32_16x16x32_bf16 v[252:255], a[28:31], a[124:127], v[252:255]\n"
"ds_read_b128 a[92:95], v14 offset:14336\n"
"v_mfma_f32_16x16x32_bf16 v[248:251], a[24:27], a[124:127], v[248:251]\n"
"s_waitcnt vmcnt(14)\n"
"s_barrier\n"
"s_waitcnt lgkmcnt(4)\n"
"v_mfma_f32_16x16x32_bf16 v[128:131], a[32:35], a[64:67], v[128:131]\n"
"ds_read_b128 a[96:99], v14 offset:16384\n"
"v_mfma_f32_16x16x32_bf16 v[132:135], a[36:39], a[64:67], v[132:135]\n"
"v_mfma_f32_16x16x32_bf16 v[140:143], a[36:39], a[68:71], v[140:143]\n"
"ds_read_b128 a[100:103], v14 offset:18432\n"
"v_mfma_f32_16x16x32_bf16 v[136:139], a[32:35], a[68:71], v[136:139]\n"
"v_mfma_f32_16x16x32_bf16 v[144:147], a[32:35], a[72:75], v[144:147]\n"
"ds_read_b128 a[104:107], v14 offset:20480\n"
"v_mfma_f32_16x16x32_bf16 v[148:151], a[36:39], a[72:75], v[148:151]\n"
"v_mfma_f32_16x16x32_bf16 v[156:159], a[36:39], a[76:79], v[156:159]\n"
"ds_read_b128 a[108:111], v14 offset:22528\n"
"v_mfma_f32_16x16x32_bf16 v[152:155], a[32:35], a[76:79], v[152:155]\n"
"s_waitcnt lgkmcnt(4)\n"
"v_mfma_f32_16x16x32_bf16 v[160:163], a[32:35], a[80:83], v[160:163]\n"
"ds_read_b128 a[112:115], v14 offset:24576\n"
"v_mfma_f32_16x16x32_bf16 v[164:167], a[36:39], a[80:83], v[164:167]\n"
"v_mfma_f32_16x16x32_bf16 v[172:175], a[36:39], a[84:87], v[172:175]\n"
"ds_read_b128 a[116:119], v14 offset:26624\n"
"v_mfma_f32_16x16x32_bf16 v[168:171], a[32:35], a[84:87], v[168:171]\n"
"v_mfma_f32_16x16x32_bf16 v[176:179], a[32:35], a[88:91], v[176:179]\n"
"ds_read_b128 a[120:123], v14 offset:28672\n"
"v_mfma_f32_16x16x32_bf16 v[180:183], a[36:39], a[88:91], v[180:183]\n"
"v_mfma_f32_16x16x32_bf16 v[188:191], a[36:39], a[92:95], v[188:191]\n"
"ds_read_b128 a[124:127], v14 offset:30720\n"
"v_mfma_f32_16x16x32_bf16 v[184:187], a[32:35], a[92:95], v[184:187]\n"
"s_waitcnt lgkmcnt(4)\n"
"v_mfma_f32_16x16x32_bf16 v[192:195], a[32:35], a[96:99], v[192:195]\n"
"ds_read_b128 a[64:67], v15 offset:0\n"
"v_mfma_f32_16x16x32_bf16 v[196:199], a[36:39], a[96:99], v[196:199]\n"
"v_mfma_f32_16x16x32_bf16 v[204:207], a[36:39], a[100:103], v[204:207]\n"
"ds_read_b128 a[68:71], v15 offset:2048\n"
"v_mfma_f32_16x16x32_bf16 v[200:203], a[32:35], a[100:103], v[200:203]\n"
"v_mfma_f32_16x16x32_bf16 v[208:211], a[32:35], a[104:107], v[208:211]\n"
"ds_read_b128 a[72:75], v15 offset:4096\n"
"v_mfma_f32_16x16x32_bf16 v[212:215], a[36:39], a[104:107], v[212:215]\n"
"v_mfma_f32_16x16x32_bf16 v[220:223], a[36:39], a[108:111], v[220:223]\n"
"ds_read_b128 a[76:79], v15 offset:6144\n"
"v_mfma_f32_16x16x32_bf16 v[216:219], a[32:35], a[108:111], v[216:219]\n"
"s_waitcnt lgkmcnt(4)\n"
"v_mfma_f32_16x16x32_bf16 v[224:227], a[32:35], a[112:115], v[224:227]\n"
"ds_read_b128 a[80:83], v15 offset:8192\n"
"v_mfma_f32_16x16x32_bf16 v[228:231], a[36:39], a[112:115], v[228:231]\n"
"v_mfma_f32_16x16x32_bf16 v[236:239], a[36:39], a[116:119], v[236:239]\n"
"ds_read_b128 a[84:87], v15 offset:10240\n"
"v_mfma_f32_16x16x32_bf16 v[232:235], a[32:35], a[116:119], v[232:235]\n"
"v_mfma_f32_16x16x32_bf16 v[240:243], a[32:35], a[120:123], v[240:243]\n"
"ds_read_b128 a[88:91], v15 offset:12288\n"
"v_mfma_f32_16x16x32_bf16 v[244:247], a[36:39], a[120:123], v[244:247]\n"
"v_mfma_f32_16x16x32_bf16 v[252:255], a[36:39], a[124:127], v[252:255]\n"
"ds_read_b128 a[92:95], v15 offset:14336\n"
"v_mfma_f32_16x16x32_bf16 v[248:251], a[32:35], a[124:127], v[248:251]\n"
"s_waitcnt lgkmcnt(4)\n"
"v_mfma_f32_16x16x32_bf16 v[128:131], a[40:43], a[64:67], v[128:131]\n"
"ds_read_b128 a[96:99], v15 offset:16384\n"
"v_mfma_f32_16x16x32_bf16 v[132:135], a[44:47], a[64:67], v[132:135]\n"
"v_mfma_f32_16x16x32_bf16 v[140:143], a[44:47], a[68:71], v[140:143]\n"
"ds_read_b128 a[100:103], v15 offset:18432\n"
"v_mfma_f32_16x16x32_bf16 v[136:139], a[40:43], a[68:71], v[136:139]\n"
"v_mfma_f32_16x16x32_bf16 v[144:147], a[40:43], a[72:75], v[144:147]\n"
"ds_read_b128 a[104:107], v15 offset:20480\n"
"v_mfma_f32_16x16x32_bf16 v[148:151], a[44:47], a[72:75], v[148:151]\n"
"v_mfma_f32_16x16x32_bf16 v[156:159], a[44:47], a[76:79], v[156:159]\n"
"ds_read_b128 a[108:111], v15 offset:22528\n"
"v_mfma_f32_16x16x32_bf16 v[152:155], a[40:43], a[76:79], v[152:155]\n"
"s_waitcnt lgkmcnt(4)\n"
"v_mfma_f32_16x16x32_bf16 v[160:163], a[40:43], a[80:83], v[160:163]\n"
"ds_read_b128 a[112:115], v15 offset:24576\n"
"v_mfma_f32_16x16x32_bf16 v[164:167], a[44:47], a[80:83], v[164:167]\n"
"v_mfma_f32_16x16x32_bf16 v[172:175], a[44:47], a[84:87], v[172:175]\n"
"ds_read_b128 a[116:119], v15 offset:26624\n"
"v_mfma_f32_16x16x32_bf16 v[168:171], a[40:43], a[84:87], v[168:171]\n"
"v_mfma_f32_16x16x32_bf16 v[176:179], a[40:43], a[88:91], v[176:179]\n"
"ds_read_b128 a[120:123], v15 offset:28672\n"
"v_mfma_f32_16x16x32_bf16 v[180:183], a[44:47], a[88:91], v[180:183]\n"
"v_mfma_f32_16x16x32_bf16 v[188:191], a[44:47], a[92:95], v[188:191]\n"
"ds_read_b128 a[124:127], v15 offset:30720\n"
"v_mfma_f32_16x16x32_bf16 v[184:187], a[40:43], a[92:95], v[184:187]\n"
"s_waitcnt lgkmcnt(4)\n"
"v_mfma_f32_16x16x32_bf16 v[192:195], a[40:43], a[96:99], v[192:195]\n"
"ds_read_b128 a[64:67], v14 offset:32768\n"
"v_mfma_f32_16x16x32_bf16 v[196:199], a[44:47], a[96:99], v[196:199]\n"
"v_mfma_f32_16x16x32_bf16 v[204:207], a[44:47], a[100:103], v[204:207]\n"
"ds_read_b128 a[68:71], v14 offset:34816\n"
"v_mfma_f32_16x16x32_bf16 v[200:203], a[40:43], a[100:103], v[200:203]\n"
"buffer_load_dwordx4 a[0:3], v9, s[52:55], s82 offen offset:0 sc1\n"
"v_mfma_f32_16x16x32_bf16 v[208:211], a[40:43], a[104:107], v[208:211]\n"
"ds_read_b128 a[72:75], v14 offset:36864\n"
"buffer_load_dwordx4 a[4:7], v10, s[52:55], s82 offen offset:0 sc1\n"
"v_mfma_f32_16x16x32_bf16 v[212:215], a[44:47], a[104:107], v[212:215]\n"
"v_mfma_f32_16x16x32_bf16 v[220:223], a[44:47], a[108:111], v[220:223]\n"
"ds_read_b128 a[76:79], v14 offset:38912\n"
"buffer_load_dwordx4 a[8:11], v9, s[52:55], s82 offen offset:2048 sc1\n"
"v_mfma_f32_16x16x32_bf16 v[216:219], a[40:43], a[108:111], v[216:219]\n"
"buffer_load_dwordx4 a[12:15], v10, s[52:55], s82 offen offset:2048 sc1\n"
"s_waitcnt lgkmcnt(4)\n"
"v_mfma_f32_16x16x32_bf16 v[224:227], a[40:43], a[112:115], v[224:227]\n"
"ds_read_b128 a[80:83], v14 offset:40960\n"
"v_readlane_b32 s82, v11, 25\n"
"v_mfma_f32_16x16x32_bf16 v[228:231], a[44:47], a[112:115], v[228:231]\n"
"s_add_u32 m0, s74, 32768\n"
"s_nop 0\n"
"buffer_load_dwordx4 v84, s[4:7], s79 offen offset:0 lds\n"
"v_mfma_f32_16x16x32_bf16 v[236:239], a[44:47], a[116:119], v[236:239]\n"
"ds_read_b128 a[84:87], v14 offset:43008\n"
"v_mfma_f32_16x16x32_bf16 v[232:235], a[40:43], a[116:119], v[232:235]\n"
"buffer_load_dwordx4 v85, s[4:7], s79 offen offset:1024 lds\n"
"v_mfma_f32_16x16x32_bf16 v[240:243], a[40:43], a[120:123], v[240:243]\n"
"ds_read_b128 a[88:91], v14 offset:45056\n"
"buffer_load_dwordx4 v86, s[4:7], s79 offen offset:2048 lds\n"
"v_mfma_f32_16x16x32_bf16 v[244:247], a[44:47], a[120:123], v[244:247]\n"
"v_mfma_f32_16x16x32_bf16 v[252:255], a[44:47], a[124:127], v[252:255]\n"
"ds_read_b128 a[92:95], v14 offset:47104\n"
"buffer_load_dwordx4 v87, s[4:7], s79 offen offset:3072 lds\n"
"v_mfma_f32_16x16x32_bf16 v[248:251], a[40:43], a[124:127], v[248:251]\n"
"s_waitcnt vmcnt(8)\n"
"s_barrier\n"
"s_waitcnt lgkmcnt(4)\n"
"v_mfma_f32_16x16x32_bf16 v[128:131], a[48:51], a[64:67], v[128:131]\n"
"ds_read_b128 a[96:99], v14 offset:49152\n"
"s_add_u32 m0, s74, 36864\n"
"v_mfma_f32_16x16x32_bf16 v[132:135], a[52:55], a[64:67], v[132:135]\n"
"buffer_load_dwordx4 v88, s[4:7], s79 offen offset:0 lds\n"
"v_mfma_f32_16x16x32_bf16 v[140:143], a[52:55], a[68:71], v[140:143]\n"
"ds_read_b128 a[100:103], v14 offset:51200\n"
"v_mfma_f32_16x16x32_bf16 v[136:139], a[48:51], a[68:71], v[136:139]\n"
"v_mfma_f32_16x16x32_bf16 v[144:147], a[48:51], a[72:75], v[144:147]\n"
"ds_read_b128 a[104:107], v14 offset:53248\n"
"buffer_load_dwordx4 v89, s[4:7], s79 offen offset:1024 lds\n"
"v_mfma_f32_16x16x32_bf16 v[148:151], a[52:55], a[72:75], v[148:151]\n"
"v_mfma_f32_16x16x32_bf16 v[156:159], a[52:55], a[76:79], v[156:159]\n"
"ds_read_b128 a[108:111], v14 offset:55296\n"
"v_mfma_f32_16x16x32_bf16 v[152:155], a[48:51], a[76:79], v[152:155]\n"
"s_waitcnt lgkmcnt(4)\n"
"v_mfma_f32_16x16x32_bf16 v[160:163], a[48:51], a[80:83], v[160:163]\n"
"ds_read_b128 a[112:115], v14 offset:57344\n"
"buffer_load_dwordx4 v90, s[4:7], s79 offen offset:2048 lds\n"
"v_mfma_f32_16x16x32_bf16 v[164:167], a[52:55], a[80:83], v[164:167]\n"
"v_mfma_f32_16x16x32_bf16 v[172:175], a[52:55], a[84:87], v[172:175]\n"
"ds_read_b128 a[116:119], v14 offset:59392\n"
"v_mfma_f32_16x16x32_bf16 v[168:171], a[48:51], a[84:87], v[168:171]\n"
"v_mfma_f32_16x16x32_bf16 v[176:179], a[48:51], a[88:91], v[176:179]\n"
"ds_read_b128 a[120:123], v14 offset:61440\n"
"buffer_load_dwordx4 v91, s[4:7], s79 offen offset:3072 lds\n"
"v_mfma_f32_16x16x32_bf16 v[180:183], a[52:55], a[88:91], v[180:183]\n"
"v_readlane_b32 s79, v11, 10\n"
"v_mfma_f32_16x16x32_bf16 v[188:191], a[52:55], a[92:95], v[188:191]\n"
"ds_read_b128 a[124:127], v14 offset:63488\n"
"v_mfma_f32_16x16x32_bf16 v[184:187], a[48:51], a[92:95], v[184:187]\n"
"s_waitcnt lgkmcnt(4)\n"
"v_mfma_f32_16x16x32_bf16 v[192:195], a[48:51], a[96:99], v[192:195]\n"
"ds_read_b128 a[64:67], v15 offset:32768\n"
"buffer_load_dwordx4 a[16:19], v9, s[52:55], s82 offen offset:0 sc1\n"
"v_mfma_f32_16x16x32_bf16 v[196:199], a[52:55], a[96:99], v[196:199]\n"
"v_mfma_f32_16x16x32_bf16 v[204:207], a[52:55], a[100:103], v[204:207]\n"
"ds_read_b128 a[68:71], v15 offset:34816\n"
"v_mfma_f32_16x16x32_bf16 v[200:203], a[48:51], a[100:103], v[200:203]\n"
"v_mfma_f32_16x16x32_bf16 v[208:211], a[48:51], a[104:107], v[208:211]\n"
"ds_read_b128 a[72:75], v15 offset:36864\n"
"buffer_load_dwordx4 a[20:23], v10, s[52:55], s82 offen offset:0 sc1\n"
"v_mfma_f32_16x16x32_bf16 v[212:215], a[52:55], a[104:107], v[212:215]\n"
"v_mfma_f32_16x16x32_bf16 v[220:223], a[52:55], a[108:111], v[220:223]\n"
"ds_read_b128 a[76:79], v15 offset:38912\n"
"v_mfma_f32_16x16x32_bf16 v[216:219], a[48:51], a[108:111], v[216:219]\n"
"s_waitcnt lgkmcnt(4)\n"
"v_mfma_f32_16x16x32_bf16 v[224:227], a[48:51], a[112:115], v[224:227]\n"
"ds_read_b128 a[80:83], v15 offset:40960\n"
"buffer_load_dwordx4 a[24:27], v9, s[52:55], s82 offen offset:2048 sc1\n"
"v_mfma_f32_16x16x32_bf16 v[228:231], a[52:55], a[112:115], v[228:231]\n"
"v_mfma_f32_16x16x32_bf16 v[236:239], a[52:55], a[116:119], v[236:239]\n"
"ds_read_b128 a[84:87], v15 offset:43008\n"
"v_mfma_f32_16x16x32_bf16 v[232:235], a[48:51], a[116:119], v[232:235]\n"
"v_mfma_f32_16x16x32_bf16 v[240:243], a[48:51], a[120:123], v[240:243]\n"
"ds_read_b128 a[88:91], v15 offset:45056\n"
"buffer_load_dwordx4 a[28:31], v10, s[52:55], s82 offen offset:2048 sc1\n"
"v_mfma_f32_16x16x32_bf16 v[244:247], a[52:55], a[120:123], v[244:247]\n"
"v_readlane_b32 s82, v11, 26\n"
"v_mfma_f32_16x16x32_bf16 v[252:255], a[52:55], a[124:127], v[252:255]\n"
"ds_read_b128 a[92:95], v15 offset:47104\n"
"v_mfma_f32_16x16x32_bf16 v[248:251], a[48:51], a[124:127], v[248:251]\n"
"s_add_u32 m0, s74, 65536\n"
"s_waitcnt lgkmcnt(4)\n"
"v_mfma_f32_16x16x32_bf16 v[128:131], a[56:59], a[64:67], v[128:131]\n"
"ds_read_b128 a[96:99], v15 offset:49152\n"
"buffer_load_dwordx4 v84, s[4:7], s79 offen offset:0 lds\n"
"v_mfma_f32_16x16x32_bf16 v[132:135], a[60:63], a[64:67], v[132:135]\n"
"v_mfma_f32_16x16x32_bf16 v[140:143], a[60:63], a[68:71], v[140:143]\n"
"ds_read_b128 a[100:103], v15 offset:51200\n"
"v_mfma_f32_16x16x32_bf16 v[136:139], a[56:59], a[68:71], v[136:139]\n"
"v_mfma_f32_16x16x32_bf16 v[144:147], a[56:59], a[72:75], v[144:147]\n"
"ds_read_b128 a[104:107], v15 offset:53248\n"
"buffer_load_dwordx4 v85, s[4:7], s79 offen offset:1024 lds\n"
"v_mfma_f32_16x16x32_bf16 v[148:151], a[60:63], a[72:75], v[148:151]\n"
"v_mfma_f32_16x16x32_bf16 v[156:159], a[60:63], a[76:79], v[156:159]\n"
"ds_read_b128 a[108:111], v15 offset:55296\n"
"v_mfma_f32_16x16x32_bf16 v[152:155], a[56:59], a[76:79], v[152:155]\n"
"s_waitcnt lgkmcnt(4)\n"
"v_mfma_f32_16x16x32_bf16 v[160:163], a[56:59], a[80:83], v[160:163]\n"
"ds_read_b128 a[112:115], v15 offset:57344\n"
"buffer_load_dwordx4 v86, s[4:7], s79 offen offset:2048 lds\n"
"v_mfma_f32_16x16x32_bf16 v[164:167], a[60:63], a[80:83], v[164:167]\n"
"v_mfma_f32_16x16x32_bf16 v[172:175], a[60:63], a[84:87], v[172:175]\n"
"ds_read_b128 a[116:119], v15 offset:59392\n"
"v_mfma_f32_16x16x32_bf16 v[168:171], a[56:59], a[84:87], v[168:171]\n"
"v_mfma_f32_16x16x32_bf16 v[176:179], a[56:59], a[88:91], v[176:179]\n"
"ds_read_b128 a[120:123], v15 offset:61440\n"
"buffer_load_dwordx4 v87, s[4:7], s79 offen offset:3072 lds\n"
"v_mfma_f32_16x16x32_bf16 v[180:183], a[60:63], a[88:91], v[180:183]\n"
"v_mfma_f32_16x16x32_bf16 v[188:191], a[60:63], a[92:95], v[188:191]\n"
"ds_read_b128 a[124:127], v15 offset:63488\n"
"v_mfma_f32_16x16x32_bf16 v[184:187], a[56:59], a[92:95], v[184:187]\n"
"s_add_u32 m0, s74, 69632\n"
"s_waitcnt lgkmcnt(4)\n"
"v_mfma_f32_16x16x32_bf16 v[192:195], a[56:59], a[96:99], v[192:195]\n"
"ds_read_b128 a[64:67], v12 offset:0\n"
"buffer_load_dwordx4 v88, s[4:7], s79 offen offset:0 lds\n"
"v_mfma_f32_16x16x32_bf16 v[196:199], a[60:63], a[96:99], v[196:199]\n"
"v_mfma_f32_16x16x32_bf16 v[204:207], a[60:63], a[100:103], v[204:207]\n"
"ds_read_b128 a[68:71], v12 offset:2048\n"
"v_mfma_f32_16x16x32_bf16 v[200:203], a[56:59], a[100:103], v[200:203]\n"
"v_mfma_f32_16x16x32_bf16 v[208:211], a[56:59], a[104:107], v[208:211]\n"
"ds_read_b128 a[72:75], v12 offset:4096\n"
"buffer_load_dwordx4 v89, s[4:7], s79 offen offset:1024 lds\n"
"v_mfma_f32_16x16x32_bf16 v[212:215], a[60:63], a[104:107], v[212:215]\n"
"v_mfma_f32_16x16x32_bf16 v[220:223], a[60:63], a[108:111], v[220:223]\n"
"ds_read_b128 a[76:79], v12 offset:6144\n"
"v_mfma_f32_16x16x32_bf16 v[216:219], a[56:59], a[108:111], v[216:219]\n"
"s_waitcnt lgkmcnt(4)\n"
"v_mfma_f32_16x16x32_bf16 v[224:227], a[56:59], a[112:115], v[224:227]\n"
"ds_read_b128 a[80:83], v12 offset:8192\n"
"buffer_load_dwordx4 v90, s[4:7], s79 offen offset:2048 lds\n"
"v_mfma_f32_16x16x32_bf16 v[228:231], a[60:63], a[112:115], v[228:231]\n"
"v_mfma_f32_16x16x32_bf16 v[236:239], a[60:63], a[116:119], v[236:239]\n"
"ds_read_b128 a[84:87], v12 offset:10240\n"
"v_mfma_f32_16x16x32_bf16 v[232:235], a[56:59], a[116:119], v[232:235]\n"
"v_mfma_f32_16x16x32_bf16 v[240:243], a[56:59], a[120:123], v[240:243]\n"
"ds_read_b128 a[88:91], v12 offset:12288\n"
"buffer_load_dwordx4 v91, s[4:7], s79 offen offset:3072 lds\n"
"v_mfma_f32_16x16x32_bf16 v[244:247], a[60:63], a[120:123], v[244:247]\n"
"v_readlane_b32 s79, v11, 11\n"
"v_mfma_f32_16x16x32_bf16 v[252:255], a[60:63], a[124:127], v[252:255]\n"
"ds_read_b128 a[92:95], v12 offset:14336\n"
"v_mfma_f32_16x16x32_bf16 v[248:251], a[56:59], a[124:127], v[248:251]\n"
"s_waitcnt vmcnt(12)\n"
"s_barrier\n"
"s_waitcnt lgkmcnt(4)\n"
"v_mfma_f32_16x16x32_bf16 v[128:131], a[0:3], a[64:67], v[128:131]\n"
"ds_read_b128 a[96:99], v12 offset:16384\n"
"v_mfma_f32_16x16x32_bf16 v[132:135], a[4:7], a[64:67], v[132:135]\n"
"buffer_load_dwordx4 a[32:35], v9, s[52:55], s82 offen offset:0 sc1\n"
"v_mfma_f32_16x16x32_bf16 v[140:143], a[4:7], a[68:71], v[140:143]\n"
"ds_read_b128 a[100:103], v12 offset:18432\n"
"v_mfma_f32_16x16x32_bf16 v[136:139], a[0:3], a[68:71], v[136:139]\n"
"v_mfma_f32_16x16x32_bf16 v[144:147], a[0:3], a[72:75], v[144:147]\n"
"ds_read_b128 a[104:107], v12 offset:20480\n"
"buffer_load_dwordx4 a[36:39], v10, s[52:55], s82 offen offset:0 sc1\n"
"v_mfma_f32_16x16x32_bf16 v[148:151], a[4:7], a[72:75], v[148:151]\n"
"v_mfma_f32_16x16x32_bf16 v[156:159], a[4:7], a[76:79], v[156:159]\n"
"ds_read_b128 a[108:111], v12 offset:22528\n"
"v_mfma_f32_16x16x32_bf16 v[152:155], a[0:3], a[76:79], v[152:155]\n"
"s_waitcnt lgkmcnt(4)\n"
"v_mfma_f32_16x16x32_bf16 v[160:163], a[0:3], a[80:83], v[160:163]\n"
"ds_read_b128 a[112:115], v12 offset:24576\n"
"buffer_load_dwordx4 a[40:43], v9, s[52:55], s82 offen offset:2048 sc1\n"
"v_mfma_f32_16x16x32_bf16 v[164:167], a[4:7], a[80:83], v[164:167]\n"
"v_mfma_f32_16x16x32_bf16 v[172:175], a[4:7], a[84:87], v[172:175]\n"
"ds_read_b128 a[116:119], v12 offset:26624\n"
"v_mfma_f32_16x16x32_bf16 v[168:171], a[0:3], a[84:87], v[168:171]\n"
"v_mfma_f32_16x16x32_bf16 v[176:179], a[0:3], a[88:91], v[176:179]\n"
"ds_read_b128 a[120:123], v12 offset:28672\n"
"buffer_load_dwordx4 a[44:47], v10, s[52:55], s82 offen offset:2048 sc1\n"
"v_mfma_f32_16x16x32_bf16 v[180:183], a[4:7], a[88:91], v[180:183]\n"
"v_readlane_b32 s82, v11, 27\n"
"v_mfma_f32_16x16x32_bf16 v[188:191], a[4:7], a[92:95], v[188:191]\n"
"ds_read_b128 a[124:127], v12 offset:30720\n"
"v_mfma_f32_16x16x32_bf16 v[184:187], a[0:3], a[92:95], v[184:187]\n"
"s_add_u32 m0, s74, 98304\n"
"s_waitcnt lgkmcnt(4)\n"
"v_mfma_f32_16x16x32_bf16 v[192:195], a[0:3], a[96:99], v[192:195]\n"
"ds_read_b128 a[64:67], v13 offset:0\n"
"buffer_load_dwordx4 v84, s[4:7], s79 offen offset:0 lds\n"
"v_mfma_f32_16x16x32_bf16 v[196:199], a[4:7], a[96:99], v[196:199]\n"
"v_mfma_f32_16x16x32_bf16 v[204:207], a[4:7], a[100:103], v[204:207]\n"
"ds_read_b128 a[68:71], v13 offset:2048\n"
"v_mfma_f32_16x16x32_bf16 v[200:203], a[0:3], a[100:103], v[200:203]\n"
"v_mfma_f32_16x16x32_bf16 v[208:211], a[0:3], a[104:107], v[208:211]\n"
"ds_read_b128 a[72:75], v13 offset:4096\n"
"buffer_load_dwordx4 v85, s[4:7], s79 offen offset:1024 lds\n"
"v_mfma_f32_16x16x32_bf16 v[212:215], a[4:7], a[104:107], v[212:215]\n"
"v_mfma_f32_16x16x32_bf16 v[220:223], a[4:7], a[108:111], v[220:223]\n"
"ds_read_b128 a[76:79], v13 offset:6144\n"
"v_mfma_f32_16x16x32_bf16 v[216:219], a[0:3], a[108:111], v[216:219]\n"
"s_waitcnt lgkmcnt(4)\n"
"v_mfma_f32_16x16x32_bf16 v[224:227], a[0:3], a[112:115], v[224:227]\n"
"ds_read_b128 a[80:83], v13 offset:8192\n"
"buffer_load_dwordx4 v86, s[4:7], s79 offen offset:2048 lds\n"
"v_mfma_f32_16x16x32_bf16 v[228:231], a[4:7], a[112:115], v[228:231]\n"
"v_mfma_f32_16x16x32_bf16 v[236:239], a[4:7], a[116:119], v[236:239]\n"
"ds_read_b128 a[84:87], v13 offset:10240\n"
"v_mfma_f32_16x16x32_bf16 v[232:235], a[0:3], a[116:119], v[232:235]\n"
"v_mfma_f32_16x16x32_bf16 v[240:243], a[0:3], a[120:123], v[240:243]\n"
"ds_read_b128 a[88:91], v13 offset:12288\n"
"buffer_load_dwordx4 v87, s[4:7], s79 offen offset:3072 lds\n"
"v_mfma_f32_16x16x32_bf16 v[244:247], a[4:7], a[120:123], v[244:247]\n"
"v_mfma_f32_16x16x32_bf16 v[252:255], a[4:7], a[124:127], v[252:255]\n"
"ds_read_b128 a[92:95], v13 offset:14336\n"
"v_mfma_f32_16x16x32_bf16 v[248:251], a[0:3], a[124:127], v[248:251]\n"
"s_add_u32 m0, s74, 102400\n"
"s_waitcnt lgkmcnt(4)\n"
"v_mfma_f32_16x16x32_bf16 v[128:131], a[8:11], a[64:67], v[128:131]\n"
"ds_read_b128 a[96:99], v13 offset:16384\n"
"buffer_load_dwordx4 v88, s[4:7], s79 offen offset:0 lds\n"
"v_mfma_f32_16x16x32_bf16 v[132:135], a[12:15], a[64:67], v[132:135]\n"
"v_mfma_f32_16x16x32_bf16 v[140:143], a[12:15], a[68:71], v[140:143]\n"
"ds_read_b128 a[100:103], v13 offset:18432\n"
"v_mfma_f32_16x16x32_bf16 v[136:139], a[8:11], a[68:71], v[136:139]\n"
"v_mfma_f32_16x16x32_bf16 v[144:147], a[8:11], a[72:75], v[144:147]\n"
"ds_read_b128 a[104:107], v13 offset:20480\n"
"buffer_load_dwordx4 v89, s[4:7], s79 offen offset:1024 lds\n"
"v_mfma_f32_16x16x32_bf16 v[148:151], a[12:15], a[72:75], v[148:151]\n"
"v_mfma_f32_16x16x32_bf16 v[156:159], a[12:15], a[76:79], v[156:159]\n"
"ds_read_b128 a[108:111], v13 offset:22528\n"
"v_mfma_f32_16x16x32_bf16 v[152:155], a[8:11], a[76:79], v[152:155]\n"
"s_waitcnt lgkmcnt(4)\n"
"v_mfma_f32_16x16x32_bf16 v[160:163], a[8:11], a[80:83], v[160:163]\n"
"ds_read_b128 a[112:115], v13 offset:24576\n"
"buffer_load_dwordx4 v90, s[4:7], s79 offen offset:2048 lds\n"
"v_mfma_f32_16x16x32_bf16 v[164:167], a[12:15], a[80:83], v[164:167]\n"
"v_mfma_f32_16x16x32_bf16 v[172:175], a[12:15], a[84:87], v[172:175]\n"
"ds_read_b128 a[116:119], v13 offset:26624\n"
"v_mfma_f32_16x16x32_bf16 v[168:171], a[8:11], a[84:87], v[168:171]\n"
"v_mfma_f32_16x16x32_bf16 v[176:179], a[8:11], a[88:91], v[176:179]\n"
"ds_read_b128 a[120:123], v13 offset:28672\n"
"buffer_load_dwordx4 v91, s[4:7], s79 offen offset:3072 lds\n"
"v_mfma_f32_16x16x32_bf16 v[180:183], a[12:15], a[88:91], v[180:183]\n"
"v_readlane_b32 s79, v11, 12\n"
"v_mfma_f32_16x16x32_bf16 v[188:191], a[12:15], a[92:95], v[188:191]\n"
"ds_read_b128 a[124:127], v13 offset:30720\n"
"v_mfma_f32_16x16x32_bf16 v[184:187], a[8:11], a[92:95], v[184:187]\n"
"s_waitcnt lgkmcnt(4)\n"
"v_mfma_f32_16x16x32_bf16 v[192:195], a[8:11], a[96:99], v[192:195]\n"
"ds_read_b128 a[64:67], v12 offset:32768\n"
"buffer_load_dwordx4 a[48:51], v9, s[52:55], s82 offen offset:0 sc1\n"
"v_mfma_f32_16x16x32_bf16 v[196:199], a[12:15], a[96:99], v[196:199]\n"
"v_mfma_f32_16x16x32_bf16 v[204:207], a[12:15], a[100:103], v[204:207]\n"
"ds_read_b128 a[68:71], v12 offset:34816\n"
"v_mfma_f32_16x16x32_bf16 v[200:203], a[8:11], a[100:103], v[200:203]\n"
"v_mfma_f32_16x16x32_bf16 v[208:211], a[8:11], a[104:107], v[208:211]\n"
"ds_read_b128 a[72:75], v12 offset:36864\n"
"buffer_load_dwordx4 a[52:55], v10, s[52:55], s82 offen offset:0 sc1\n"
"v_mfma_f32_16x16x32_bf16 v[212:215], a[12:15], a[104:107], v[212:215]\n"
"v_mfma_f32_16x16x32_bf16 v[220:223], a[12:15], a[108:111], v[220:223]\n"
"ds_read_b128 a[76:79], v12 offset:38912\n"
"v_mfma_f32_16x16x32_bf16 v[216:219], a[8:11], a[108:111], v[216:219]\n"
"s_waitcnt lgkmcnt(4)\n"
"v_mfma_f32_16x16x32_bf16 v[224:227], a[8:11], a[112:115], v[224:227]\n"
"ds_read_b128 a[80:83], v12 offset:40960\n"
"buffer_load_dwordx4 a[56:59], v9, s[52:55], s82 offen offset:2048 sc1\n"
"v_mfma_f32_16x16x32_bf16 v[228:231], a[12:15], a[112:115], v[228:231]\n"
"v_mfma_f32_16x16x32_bf16 v[236:239], a[12:15], a[116:119], v[236:239]\n"
"ds_read_b128 a[84:87], v12 offset:43008\n"
"v_mfma_f32_16x16x32_bf16 v[232:235], a[8:11], a[116:119], v[232:235]\n"
"v_mfma_f32_16x16x32_bf16 v[240:243], a[8:11], a[120:123], v[240:243]\n"
"ds_read_b128 a[88:91], v12 offset:45056\n"
"buffer_load_dwordx4 a[60:63], v10, s[52:55], s82 offen offset:2048 sc1\n"
"v_mfma_f32_16x16x32_bf16 v[244:247], a[12:15], a[120:123], v[244:247]\n"
"v_readlane_b32 s82, v11, 28\n"
"v_mfma_f32_16x16x32_bf16 v[252:255], a[12:15], a[124:127], v[252:255]\n"
"ds_read_b128 a[92:95], v12 offset:47104\n"
"v_mfma_f32_16x16x32_bf16 v[248:251], a[8:11], a[124:127], v[248:251]\n"
"s_waitcnt vmcnt(16)\n"
"s_barrier\n"
"s_waitcnt lgkmcnt(4)\n"
"v_mfma_f32_16x16x32_bf16 v[128:131], a[16:19], a[64:67], v[128:131]\n"
"ds_read_b128 a[96:99], v12 offset:49152\n"
"s_add_u32 m0, s74, 0\n"
"v_mfma_f32_16x16x32_bf16 v[132:135], a[20:23], a[64:67], v[132:135]\n"
"buffer_load_dwordx4 v84, s[4:7], s79 offen offset:0 lds\n"
"v_mfma_f32_16x16x32_bf16 v[140:143], a[20:23], a[68:71], v[140:143]\n"
"ds_read_b128 a[100:103], v12 offset:51200\n"
"v_mfma_f32_16x16x32_bf16 v[136:139], a[16:19], a[68:71], v[136:139]\n"
"v_mfma_f32_16x16x32_bf16 v[144:147], a[16:19], a[72:75], v[144:147]\n"
"ds_read_b128 a[104:107], v12 offset:53248\n"
"buffer_load_dwordx4 v85, s[4:7], s79 offen offset:1024 lds\n"
"v_mfma_f32_16x16x32_bf16 v[148:151], a[20:23], a[72:75], v[148:151]\n"
"v_mfma_f32_16x16x32_bf16 v[156:159], a[20:23], a[76:79], v[156:159]\n"
"ds_read_b128 a[108:111], v12 offset:55296\n"
"v_mfma_f32_16x16x32_bf16 v[152:155], a[16:19], a[76:79], v[152:155]\n"
"s_waitcnt lgkmcnt(4)\n"
"v_mfma_f32_16x16x32_bf16 v[160:163], a[16:19], a[80:83], v[160:163]\n"
"ds_read_b128 a[112:115], v12 offset:57344\n"
"buffer_load_dwordx4 v86, s[4:7], s79 offen offset:2048 lds\n"
"v_mfma_f32_16x16x32_bf16 v[164:167], a[20:23], a[80:83], v[164:167]\n"
"v_mfma_f32_16x16x32_bf16 v[172:175], a[20:23], a[84:87], v[172:175]\n"
"ds_read_b128 a[116:119], v12 offset:59392\n"
"v_mfma_f32_16x16x32_bf16 v[168:171], a[16:19], a[84:87], v[168:171]\n"
"v_mfma_f32_16x16x32_bf16 v[176:179], a[16:19], a[88:91], v[176:179]\n"
"ds_read_b128 a[120:123], v12 offset:61440\n"
"buffer_load_dwordx4 v87, s[4:7], s79 offen offset:3072 lds\n"
"v_mfma_f32_16x16x32_bf16 v[180:183], a[20:23], a[88:91], v[180:183]\n"
"v_mfma_f32_16x16x32_bf16 v[188:191], a[20:23], a[92:95], v[188:191]\n"
"ds_read_b128 a[124:127], v12 offset:63488\n"
"v_mfma_f32_16x16x32_bf16 v[184:187], a[16:19], a[92:95], v[184:187]\n"
"s_add_u32 m0, s74, 4096\n"
"s_waitcnt lgkmcnt(4)\n"
"v_mfma_f32_16x16x32_bf16 v[192:195], a[16:19], a[96:99], v[192:195]\n"
"ds_read_b128 a[64:67], v13 offset:32768\n"
"buffer_load_dwordx4 v88, s[4:7], s79 offen offset:0 lds\n"
"v_mfma_f32_16x16x32_bf16 v[196:199], a[20:23], a[96:99], v[196:199]\n"
"v_mfma_f32_16x16x32_bf16 v[204:207], a[20:23], a[100:103], v[204:207]\n"
"ds_read_b128 a[68:71], v13 offset:34816\n"
"v_mfma_f32_16x16x32_bf16 v[200:203], a[16:19], a[100:103], v[200:203]\n"
"v_mfma_f32_16x16x32_bf16 v[208:211], a[16:19], a[104:107], v[208:211]\n"
"ds_read_b128 a[72:75], v13 offset:36864\n"
"buffer_load_dwordx4 v89, s[4:7], s79 offen offset:1024 lds\n"
"v_mfma_f32_16x16x32_bf16 v[212:215], a[20:23], a[104:107], v[212:215]\n"
"v_mfma_f32_16x16x32_bf16 v[220:223], a[20:23], a[108:111], v[220:223]\n"
"ds_read_b128 a[76:79], v13 offset:38912\n"
"v_mfma_f32_16x16x32_bf16 v[216:219], a[16:19], a[108:111], v[216:219]\n"
"s_waitcnt lgkmcnt(4)\n"
"v_mfma_f32_16x16x32_bf16 v[224:227], a[16:19], a[112:115], v[224:227]\n"
"ds_read_b128 a[80:83], v13 offset:40960\n"
"buffer_load_dwordx4 v90, s[4:7], s79 offen offset:2048 lds\n"
"v_mfma_f32_16x16x32_bf16 v[228:231], a[20:23], a[112:115], v[228:231]\n"
"v_mfma_f32_16x16x32_bf16 v[236:239], a[20:23], a[116:119], v[236:239]\n"
"ds_read_b128 a[84:87], v13 offset:43008\n"
"v_mfma_f32_16x16x32_bf16 v[232:235], a[16:19], a[116:119], v[232:235]\n"
"v_mfma_f32_16x16x32_bf16 v[240:243], a[16:19], a[120:123], v[240:243]\n"
"ds_read_b128 a[88:91], v13 offset:45056\n"
"buffer_load_dwordx4 v91, s[4:7], s79 offen offset:3072 lds\n"
"v_mfma_f32_16x16x32_bf16 v[244:247], a[20:23], a[120:123], v[244:247]\n"
"v_readlane_b32 s79, v11, 13\n"
"v_mfma_f32_16x16x32_bf16 v[252:255], a[20:23], a[124:127], v[252:255]\n"
"ds_read_b128 a[92:95], v13 offset:47104\n"
"v_mfma_f32_16x16x32_bf16 v[248:251], a[16:19], a[124:127], v[248:251]\n"
"s_waitcnt lgkmcnt(4)\n"
"v_mfma_f32_16x16x32_bf16 v[128:131], a[24:27], a[64:67], v[128:131]\n"
"ds_read_b128 a[96:99], v13 offset:49152\n"
"buffer_load_dwordx4 a[0:3], v9, s[52:55], s82 offen offset:0 sc1\n"
"v_mfma_f32_16x16x32_bf16 v[132:135], a[28:31], a[64:67], v[132:135]\n"
"v_mfma_f32_16x16x32_bf16 v[140:143], a[28:31], a[68:71], v[140:143]\n"
"ds_read_b128 a[100:103], v13 offset:51200\n"
"v_mfma_f32_16x16x32_bf16 v[136:139], a[24:27], a[68:71], v[136:139]\n"
"v_mfma_f32_16x16x32_bf16 v[144:147], a[24:27], a[72:75], v[144:147]\n"
"ds_read_b128 a[104:107], v13 offset:53248\n"
"buffer_load_dwordx4 a[4:7], v10, s[52:55], s82 offen offset:0 sc1\n"
"v_mfma_f32_16x16x32_bf16 v[148:151], a[28:31], a[72:75], v[148:151]\n"
"v_mfma_f32_16x16x32_bf16 v[156:159], a[28:31], a[76:79], v[156:159]\n"
"ds_read_b128 a[108:111], v13 offset:55296\n"
"v_mfma_f32_16x16x32_bf16 v[152:155], a[24:27], a[76:79], v[152:155]\n"
"s_waitcnt lgkmcnt(4)\n"
"v_mfma_f32_16x16x32_bf16 v[160:163], a[24:27], a[80:83], v[160:163]\n"
"ds_read_b128 a[112:115], v13 offset:57344\n"
"buffer_load_dwordx4 a[8:11], v9, s[52:55], s82 offen offset:2048 sc1\n"
"v_mfma_f32_16x16x32_bf16 v[164:167], a[28:31], a[80:83], v[164:167]\n"
"v_mfma_f32_16x16x32_bf16 v[172:175], a[28:31], a[84:87], v[172:175]\n"
"ds_read_b128 a[116:119], v13 offset:59392\n"
"v_mfma_f32_16x16x32_bf16 v[168:171], a[24:27], a[84:87], v[168:171]\n"
"v_mfma_f32_16x16x32_bf16 v[176:179], a[24:27], a[88:91], v[176:179]\n"
"ds_read_b128 a[120:123], v13 offset:61440\n"
"buffer_load_dwordx4 a[12:15], v10, s[52:55], s82 offen offset:2048 sc1\n"
"v_mfma_f32_16x16x32_bf16 v[180:183], a[28:31], a[88:91], v[180:183]\n"
"v_readlane_b32 s82, v11, 29\n"
"v_mfma_f32_16x16x32_bf16 v[188:191], a[28:31], a[92:95], v[188:191]\n"
"ds_read_b128 a[124:127], v13 offset:63488\n"
"v_mfma_f32_16x16x32_bf16 v[184:187], a[24:27], a[92:95], v[184:187]\n"
"s_add_u32 m0, s74, 131072\n"
"s_waitcnt lgkmcnt(4)\n"
"v_mfma_f32_16x16x32_bf16 v[192:195], a[24:27], a[96:99], v[192:195]\n"
"ds_read_b128 a[64:67], v14 offset:0\n"
"buffer_load_dwordx4 v64, s[48:51], 0 offen offset:0 lds\n"
"v_mfma_f32_16x16x32_bf16 v[196:199], a[28:31], a[96:99], v[196:199]\n"
"v_mfma_f32_16x16x32_bf16 v[204:207], a[28:31], a[100:103], v[204:207]\n"
"ds_read_b128 a[68:71], v14 offset:2048\n"
"v_mfma_f32_16x16x32_bf16 v[200:203], a[24:27], a[100:103], v[200:203]\n"
"s_add_u32 m0, s74, 131072\n"
"v_mfma_f32_16x16x32_bf16 v[208:211], a[24:27], a[104:107], v[208:211]\n"
"ds_read_b128 a[72:75], v14 offset:4096\n"
"buffer_load_dwordx4 v65, s[48:51], 0 offen offset:1024 lds\n"
"v_mfma_f32_16x16x32_bf16 v[212:215], a[28:31], a[104:107], v[212:215]\n"
"v_mfma_f32_16x16x32_bf16 v[220:223], a[28:31], a[108:111], v[220:223]\n"
"ds_read_b128 a[76:79], v14 offset:6144\n"
"v_mfma_f32_16x16x32_bf16 v[216:219], a[24:27], a[108:111], v[216:219]\n"
"s_add_u32 m0, s74, 131072\n"
"s_waitcnt lgkmcnt(4)\n"
"v_mfma_f32_16x16x32_bf16 v[224:227], a[24:27], a[112:115], v[224:227]\n"
"ds_read_b128 a[80:83], v14 offset:8192\n"
"buffer_load_dwordx4 v66, s[48:51], 0 offen offset:2048 lds\n"
"v_mfma_f32_16x16x32_bf16 v[228:231], a[28:31], a[112:115], v[228:231]\n"
"v_mfma_f32_16x16x32_bf16 v[236:239], a[28:31], a[116:119], v[236:239]\n"
"ds_read_b128 a[84:87], v14 offset:10240\n"
"v_mfma_f32_16x16x32_bf16 v[232:235], a[24:27], a[116:119], v[232:235]\n"
"s_add_u32 m0, s74, 131072\n"
"v_mfma_f32_16x16x32_bf16 v[240:243], a[24:27], a[120:123], v[240:243]\n"
"ds_read_b128 a[88:91], v14 offset:12288\n"
"buffer_load_dwordx4 v67, s[48:51], 0 offen offset:3072 lds\n"
"v_mfma_f32_16x16x32_bf16 v[244:247], a[28:31], a[120:123], v[244:247]\n"
"v_mfma_f32_16x16x32_bf16 v[252:255], a[28:31], a[124:127], v[252:255]\n"
"ds_read_b128 a[92:95], v14 offset:14336\n"
"v_mfma_f32_16x16x32_bf16 v[248:251], a[24:27], a[124:127], v[248:251]\n"
"s_waitcnt vmcnt(20)\n"
"s_barrier\n"
"s_waitcnt lgkmcnt(4)\n"
"v_mfma_f32_16x16x32_bf16 v[128:131], a[32:35], a[64:67], v[128:131]\n"
"ds_read_b128 a[96:99], v14 offset:16384\n"
"s_add_u32 m0, s74, 32768\n"
"v_mfma_f32_16x16x32_bf16 v[132:135], a[36:39], a[64:67], v[132:135]\n"
"buffer_load_dwordx4 v84, s[4:7], s79 offen offset:0 lds\n"
"v_mfma_f32_16x16x32_bf16 v[140:143], a[36:39], a[68:71], v[140:143]\n"
"ds_read_b128 a[100:103], v14 offset:18432\n"
"v_mfma_f32_16x16x32_bf16 v[136:139], a[32:35], a[68:71], v[136:139]\n"
"v_mfma_f32_16x16x32_bf16 v[144:147], a[32:35], a[72:75], v[144:147]\n"
"ds_read_b128 a[104:107], v14 offset:20480\n"
"buffer_load_dwordx4 v85, s[4:7], s79 offen offset:1024 lds\n"
"v_mfma_f32_16x16x32_bf16 v[148:151], a[36:39], a[72:75], v[148:151]\n"
"v_mfma_f32_16x16x32_bf16 v[156:159], a[36:39], a[76:79], v[156:159]\n"
"ds_read_b128 a[108:111], v14 offset:22528\n"
"v_mfma_f32_16x16x32_bf16 v[152:155], a[32:35], a[76:79], v[152:155]\n"
"s_waitcnt lgkmcnt(4)\n"
"v_mfma_f32_16x16x32_bf16 v[160:163], a[32:35], a[80:83], v[160:163]\n"
"ds_read_b128 a[112:115], v14 offset:24576\n"
"buffer_load_dwordx4 v86, s[4:7], s79 offen offset:2048 lds\n"
"v_mfma_f32_16x16x32_bf16 v[164:167], a[36:39], a[80:83], v[164:167]\n"
"v_mfma_f32_16x16x32_bf16 v[172:175], a[36:39], a[84:87], v[172:175]\n"
"ds_read_b128 a[116:119], v14 offset:26624\n"
"v_mfma_f32_16x16x32_bf16 v[168:171], a[32:35], a[84:87], v[168:171]\n"
"v_mfma_f32_16x16x32_bf16 v[176:179], a[32:35], a[88:91], v[176:179]\n"
"ds_read_b128 a[120:123], v14 offset:28672\n"
"buffer_load_dwordx4 v87, s[4:7], s79 offen offset:3072 lds\n"
"v_mfma_f32_16x16x32_bf16 v[180:183], a[36:39], a[88:91], v[180:183]\n"
"v_mfma_f32_16x16x32_bf16 v[188:191], a[36:39], a[92:95], v[188:191]\n"
"ds_read_b128 a[124:127], v14 offset:30720\n"
"v_mfma_f32_16x16x32_bf16 v[184:187], a[32:35], a[92:95], v[184:187]\n"
"s_add_u32 m0, s74, 36864\n"
"s_waitcnt lgkmcnt(4)\n"
"v_mfma_f32_16x16x32_bf16 v[192:195], a[32:35], a[96:99], v[192:195]\n"
"ds_read_b128 a[64:67], v15 offset:0\n"
"buffer_load_dwordx4 v88, s[4:7], s79 offen offset:0 lds\n"
"v_mfma_f32_16x16x32_bf16 v[196:199], a[36:39], a[96:99], v[196:199]\n"
"v_mfma_f32_16x16x32_bf16 v[204:207], a[36:39], a[100:103], v[204:207]\n"
"ds_read_b128 a[68:71], v15 offset:2048\n"
"v_mfma_f32_16x16x32_bf16 v[200:203], a[32:35], a[100:103], v[200:203]\n"
"v_mfma_f32_16x16x32_bf16 v[208:211], a[32:35], a[104:107], v[208:211]\n"
"ds_read_b128 a[72:75], v15 offset:4096\n"
"buffer_load_dwordx4 v89, s[4:7], s79 offen offset:1024 lds\n"
"v_mfma_f32_16x16x32_bf16 v[212:215], a[36:39], a[104:107], v[212:215]\n"
"v_mfma_f32_16x16x32_bf16 v[220:223], a[36:39], a[108:111], v[220:223]\n"
"ds_read_b128 a[76:79], v15 offset:6144\n"
"v_mfma_f32_16x16x32_bf16 v[216:219], a[32:35], a[108:111], v[216:219]\n"
"s_waitcnt lgkmcnt(4)\n"
"v_mfma_f32_16x16x32_bf16 v[224:227], a[32:35], a[112:115], v[224:227]\n"
"ds_read_b128 a[80:83], v15 offset:8192\n"
"buffer_load_dwordx4 v90, s[4:7], s79 offen offset:2048 lds\n"
"v_mfma_f32_16x16x32_bf16 v[228:231], a[36:39], a[112:115], v[228:231]\n"
"v_mfma_f32_16x16x32_bf16 v[236:239], a[36:39], a[116:119], v[236:239]\n"
"ds_read_b128 a[84:87], v15 offset:10240\n"
"v_mfma_f32_16x16x32_bf16 v[232:235], a[32:35], a[116:119], v[232:235]\n"
"v_mfma_f32_16x16x32_bf16 v[240:243], a[32:35], a[120:123], v[240:243]\n"
"ds_read_b128 a[88:91], v15 offset:12288\n"
"buffer_load_dwordx4 v91, s[4:7], s79 offen offset:3072 lds\n"
"v_mfma_f32_16x16x32_bf16 v[244:247], a[36:39], a[120:123], v[244:247]\n"
"v_readlane_b32 s79, v11, 14\n"
"v_mfma_f32_16x16x32_bf16 v[252:255], a[36:39], a[124:127], v[252:255]\n"
"ds_read_b128 a[92:95], v15 offset:14336\n"
"v_mfma_f32_16x16x32_bf16 v[248:251], a[32:35], a[124:127], v[248:251]\n"
"s_waitcnt lgkmcnt(4)\n"
"v_mfma_f32_16x16x32_bf16 v[128:131], a[40:43], a[64:67], v[128:131]\n"
"ds_read_b128 a[96:99], v15 offset:16384\n"
"buffer_load_dwordx4 a[16:19], v9, s[52:55], s82 offen offset:0 sc1\n"
"v_mfma_f32_16x16x32_bf16 v[132:135], a[44:47], a[64:67], v[132:135]\n"
"v_mfma_f32_16x16x32_bf16 v[140:143], a[44:47], a[68:71], v[140:143]\n"
"ds_read_b128 a[100:103], v15 offset:18432\n"
"v_mfma_f32_16x16x32_bf16 v[136:139], a[40:43], a[68:71], v[136:139]\n"
"v_mfma_f32_16x16x32_bf16 v[144:147], a[40:43], a[72:75], v[144:147]\n"
"ds_read_b128 a[104:107], v15 offset:20480\n"
"buffer_load_dwordx4 a[20:23], v10, s[52:55], s82 offen offset:0 sc1\n"
"v_mfma_f32_16x16x32_bf16 v[148:151], a[44:47], a[72:75], v[148:151]\n"
"v_mfma_f32_16x16x32_bf16 v[156:159], a[44:47], a[76:79], v[156:159]\n"
"ds_read_b128 a[108:111], v15 offset:22528\n"
"v_mfma_f32_16x16x32_bf16 v[152:155], a[40:43], a[76:79], v[152:155]\n"
"s_waitcnt lgkmcnt(4)\n"
"v_mfma_f32_16x16x32_bf16 v[160:163], a[40:43], a[80:83], v[160:163]\n"
"ds_read_b128 a[112:115], v15 offset:24576\n"
"buffer_load_dwordx4 a[24:27], v9, s[52:55], s82 offen offset:2048 sc1\n"
"v_mfma_f32_16x16x32_bf16 v[164:167], a[44:47], a[80:83], v[164:167]\n"
"v_mfma_f32_16x16x32_bf16 v[172:175], a[44:47], a[84:87], v[172:175]\n"
"ds_read_b128 a[116:119], v15 offset:26624\n"
"v_mfma_f32_16x16x32_bf16 v[168:171], a[40:43], a[84:87], v[168:171]\n"
"v_mfma_f32_16x16x32_bf16 v[176:179], a[40:43], a[88:91], v[176:179]\n"
"ds_read_b128 a[120:123], v15 offset:28672\n"
"buffer_load_dwordx4 a[28:31], v10, s[52:55], s82 offen offset:2048 sc1\n"
"v_mfma_f32_16x16x32_bf16 v[180:183], a[44:47], a[88:91], v[180:183]\n"
"v_readlane_b32 s82, v11, 30\n"
"v_mfma_f32_16x16x32_bf16 v[188:191], a[44:47], a[92:95], v[188:191]\n"
"ds_read_b128 a[124:127], v15 offset:30720\n"
"v_mfma_f32_16x16x32_bf16 v[184:187], a[40:43], a[92:95], v[184:187]\n"
"s_add_u32 m0, s74, 135168\n"
"s_waitcnt lgkmcnt(4)\n"
"v_mfma_f32_16x16x32_bf16 v[192:195], a[40:43], a[96:99], v[192:195]\n"
"ds_read_b128 a[64:67], v14 offset:32768\n"
"buffer_load_dwordx4 v68, s[48:51], 0 offen offset:0 lds\n"
"v_mfma_f32_16x16x32_bf16 v[196:199], a[44:47], a[96:99], v[196:199]\n"
"v_mfma_f32_16x16x32_bf16 v[204:207], a[44:47], a[100:103], v[204:207]\n"
"ds_read_b128 a[68:71], v14 offset:34816\n"
"v_mfma_f32_16x16x32_bf16 v[200:203], a[40:43], a[100:103], v[200:203]\n"
"s_add_u32 m0, s74, 135168\n"
"v_mfma_f32_16x16x32_bf16 v[208:211], a[40:43], a[104:107], v[208:211]\n"
"ds_read_b128 a[72:75], v14 offset:36864\n"
"buffer_load_dwordx4 v69, s[48:51], 0 offen offset:1024 lds\n"
"v_mfma_f32_16x16x32_bf16 v[212:215], a[44:47], a[104:107], v[212:215]\n"
"v_mfma_f32_16x16x32_bf16 v[220:223], a[44:47], a[108:111], v[220:223]\n"
"ds_read_b128 a[76:79], v14 offset:38912\n"
"v_mfma_f32_16x16x32_bf16 v[216:219], a[40:43], a[108:111], v[216:219]\n"
"s_add_u32 m0, s74, 135168\n"
"s_waitcnt lgkmcnt(4)\n"
"v_mfma_f32_16x16x32_bf16 v[224:227], a[40:43], a[112:115], v[224:227]\n"
"ds_read_b128 a[80:83], v14 offset:40960\n"
"buffer_load_dwordx4 v70, s[48:51], 0 offen offset:2048 lds\n"
"v_mfma_f32_16x16x32_bf16 v[228:231], a[44:47], a[112:115], v[228:231]\n"
"v_mfma_f32_16x16x32_bf16 v[236:239], a[44:47], a[116:119], v[236:239]\n"
"ds_read_b128 a[84:87], v14 offset:43008\n"
"v_mfma_f32_16x16x32_bf16 v[232:235], a[40:43], a[116:119], v[232:235]\n"
"s_add_u32 m0, s74, 135168\n"
"v_mfma_f32_16x16x32_bf16 v[240:243], a[40:43], a[120:123], v[240:243]\n"
"ds_read_b128 a[88:91], v14 offset:45056\n"
"buffer_load_dwordx4 v71, s[48:51], 0 offen offset:3072 lds\n"
"v_mfma_f32_16x16x32_bf16 v[244:247], a[44:47], a[120:123], v[244:247]\n"
"v_mfma_f32_16x16x32_bf16 v[252:255], a[44:47], a[124:127], v[252:255]\n"
"ds_read_b128 a[92:95], v14 offset:47104\n"
"v_mfma_f32_16x16x32_bf16 v[248:251], a[40:43], a[124:127], v[248:251]\n"
"s_waitcnt vmcnt(24)\n"
"s_barrier\n"
"s_waitcnt lgkmcnt(4)\n"
"v_mfma_f32_16x16x32_bf16 v[128:131], a[48:51], a[64:67], v[128:131]\n"
"ds_read_b128 a[96:99], v14 offset:49152\n"
"s_add_u32 m0, s74, 65536\n"
"v_mfma_f32_16x16x32_bf16 v[132:135], a[52:55], a[64:67], v[132:135]\n"
"buffer_load_dwordx4 v84, s[4:7], s79 offen offset:0 lds\n"
"v_mfma_f32_16x16x32_bf16 v[140:143], a[52:55], a[68:71], v[140:143]\n"
"ds_read_b128 a[100:103], v14 offset:51200\n"
"v_mfma_f32_16x16x32_bf16 v[136:139], a[48:51], a[68:71], v[136:139]\n"
"v_mfma_f32_16x16x32_bf16 v[144:147], a[48:51], a[72:75], v[144:147]\n"
"ds_read_b128 a[104:107], v14 offset:53248\n"
"v_mfma_f32_16x16x32_bf16 v[148:151], a[52:55], a[72:75], v[148:151]\n"
"v_mfma_f32_16x16x32_bf16 v[156:159], a[52:55], a[76:79], v[156:159]\n"
"ds_read_b128 a[108:111], v14 offset:55296\n"
"buffer_load_dwordx4 v85, s[4:7], s79 offen offset:1024 lds\n"
"v_mfma_f32_16x16x32_bf16 v[152:155], a[48:51], a[76:79], v[152:155]\n"
"s_waitcnt lgkmcnt(4)\n"
"v_mfma_f32_16x16x32_bf16 v[160:163], a[48:51], a[80:83], v[160:163]\n"
"ds_read_b128 a[112:115], v14 offset:57344\n"
"v_mfma_f32_16x16x32_bf16 v[164:167], a[52:55], a[80:83], v[164:167]\n"
"v_mfma_f32_16x16x32_bf16 v[172:175], a[52:55], a[84:87], v[172:175]\n"
"ds_read_b128 a[116:119], v14 offset:59392\n"
"v_mfma_f32_16x16x32_bf16 v[168:171], a[48:51], a[84:87], v[168:171]\n"
"buffer_load_dwordx4 v86, s[4:7], s79 offen offset:2048 lds\n"
"v_mfma_f32_16x16x32_bf16 v[176:179], a[48:51], a[88:91], v[176:179]\n"
"ds_read_b128 a[120:123], v14 offset:61440\n"
"v_mfma_f32_16x16x32_bf16 v[180:183], a[52:55], a[88:91], v[180:183]\n"
"v_mfma_f32_16x16x32_bf16 v[188:191], a[52:55], a[92:95], v[188:191]\n"
"ds_read_b128 a[124:127], v14 offset:63488\n"
"v_mfma_f32_16x16x32_bf16 v[184:187], a[48:51], a[92:95], v[184:187]\n"
"s_waitcnt lgkmcnt(4)\n"
"v_mfma_f32_16x16x32_bf16 v[192:195], a[48:51], a[96:99], v[192:195]\n"
"ds_read_b128 a[64:67], v15 offset:32768\n"
"buffer_load_dwordx4 v87, s[4:7], s79 offen offset:3072 lds\n"
"v_mfma_f32_16x16x32_bf16 v[196:199], a[52:55], a[96:99], v[196:199]\n"
"v_mfma_f32_16x16x32_bf16 v[204:207], a[52:55], a[100:103], v[204:207]\n"
"ds_read_b128 a[68:71], v15 offset:34816\n"
"v_mfma_f32_16x16x32_bf16 v[200:203], a[48:51], a[100:103], v[200:203]\n"
"v_mfma_f32_16x16x32_bf16 v[208:211], a[48:51], a[104:107], v[208:211]\n"
"ds_read_b128 a[72:75], v15 offset:36864\n"
"v_mfma_f32_16x16x32_bf16 v[212:215], a[52:55], a[104:107], v[212:215]\n"
"s_add_u32 m0, s74, 69632\n"
"v_mfma_f32_16x16x32_bf16 v[220:223], a[52:55], a[108:111], v[220:223]\n"
"ds_read_b128 a[76:79], v15 offset:38912\n"
"buffer_load_dwordx4 v88, s[4:7], s79 offen offset:0 lds\n"
"v_mfma_f32_16x16x32_bf16 v[216:219], a[48:51], a[108:111], v[216:219]\n"
"s_waitcnt lgkmcnt(4)\n"
"v_mfma_f32_16x16x32_bf16 v[224:227], a[48:51], a[112:115], v[224:227]\n"
"ds_read_b128 a[80:83], v15 offset:40960\n"
"v_mfma_f32_16x16x32_bf16 v[228:231], a[52:55], a[112:115], v[228:231]\n"
"v_mfma_f32_16x16x32_bf16 v[236:239], a[52:55], a[116:119], v[236:239]\n"
"ds_read_b128 a[84:87], v15 offset:43008\n"
"v_mfma_f32_16x16x32_bf16 v[232:235], a[48:51], a[116:119], v[232:235]\n"
"buffer_load_dwordx4 v89, s[4:7], s79 offen offset:1024 lds\n"
"v_mfma_f32_16x16x32_bf16 v[240:243], a[48:51], a[120:123], v[240:243]\n"
"ds_read_b128 a[88:91], v15 offset:45056\n"
"v_mfma_f32_16x16x32_bf16 v[244:247], a[52:55], a[120:123], v[244:247]\n"
"v_mfma_f32_16x16x32_bf16 v[252:255], a[52:55], a[124:127], v[252:255]\n"
"ds_read_b128 a[92:95], v15 offset:47104\n"
"v_mfma_f32_16x16x32_bf16 v[248:251], a[48:51], a[124:127], v[248:251]\n"
"s_waitcnt lgkmcnt(4)\n"
"v_mfma_f32_16x16x32_bf16 v[128:131], a[56:59], a[64:67], v[128:131]\n"
"ds_read_b128 a[96:99], v15 offset:49152\n"
"buffer_load_dwordx4 v90, s[4:7], s79 offen offset:2048 lds\n"
"v_mfma_f32_16x16x32_bf16 v[132:135], a[60:63], a[64:67], v[132:135]\n"
"v_mfma_f32_16x16x32_bf16 v[140:143], a[60:63], a[68:71], v[140:143]\n"
"ds_read_b128 a[100:103], v15 offset:51200\n"
"v_mfma_f32_16x16x32_bf16 v[136:139], a[56:59], a[68:71], v[136:139]\n"
"v_mfma_f32_16x16x32_bf16 v[144:147], a[56:59], a[72:75], v[144:147]\n"
"ds_read_b128 a[104:107], v15 offset:53248\n"
"v_mfma_f32_16x16x32_bf16 v[148:151], a[60:63], a[72:75], v[148:151]\n"
"buffer_load_dwordx4 v91, s[4:7], s79 offen offset:3072 lds\n"
"v_readlane_b32 s79, v11, 15\n"
"v_mfma_f32_16x16x32_bf16 v[156:159], a[60:63], a[76:79], v[156:159]\n"
"ds_read_b128 a[108:111], v15 offset:55296\n"
"v_mfma_f32_16x16x32_bf16 v[152:155], a[56:59], a[76:79], v[152:155]\n"
"s_waitcnt lgkmcnt(4)\n"
"v_mfma_f32_16x16x32_bf16 v[160:163], a[56:59], a[80:83], v[160:163]\n"
"ds_read_b128 a[112:115], v15 offset:57344\n"
"v_mfma_f32_16x16x32_bf16 v[164:167], a[60:63], a[80:83], v[164:167]\n"
"v_mfma_f32_16x16x32_bf16 v[172:175], a[60:63], a[84:87], v[172:175]\n"
"ds_read_b128 a[116:119], v15 offset:59392\n"
"v_mfma_f32_16x16x32_bf16 v[168:171], a[56:59], a[84:87], v[168:171]\n"
"buffer_load_dwordx4 a[32:35], v9, s[52:55], s82 offen offset:0 sc1\n"
"v_mfma_f32_16x16x32_bf16 v[176:179], a[56:59], a[88:91], v[176:179]\n"
"ds_read_b128 a[120:123], v15 offset:61440\n"
"v_mfma_f32_16x16x32_bf16 v[180:183], a[60:63], a[88:91], v[180:183]\n"
"v_mfma_f32_16x16x32_bf16 v[188:191], a[60:63], a[92:95], v[188:191]\n"
"ds_read_b128 a[124:127], v15 offset:63488\n"
"v_mfma_f32_16x16x32_bf16 v[184:187], a[56:59], a[92:95], v[184:187]\n"
"s_waitcnt lgkmcnt(4)\n"
"v_mfma_f32_16x16x32_bf16 v[192:195], a[56:59], a[96:99], v[192:195]\n"
"ds_read_b128 a[64:67], v12 offset:0\n"
"buffer_load_dwordx4 a[36:39], v10, s[52:55], s82 offen offset:0 sc1\n"
"v_mfma_f32_16x16x32_bf16 v[196:199], a[60:63], a[96:99], v[196:199]\n"
"v_mfma_f32_16x16x32_bf16 v[204:207], a[60:63], a[100:103], v[204:207]\n"
"ds_read_b128 a[68:71], v12 offset:2048\n"
"v_mfma_f32_16x16x32_bf16 v[200:203], a[56:59], a[100:103], v[200:203]\n"
"v_mfma_f32_16x16x32_bf16 v[208:211], a[56:59], a[104:107], v[208:211]\n"
"ds_read_b128 a[72:75], v12 offset:4096\n"
"v_mfma_f32_16x16x32_bf16 v[212:215], a[60:63], a[104:107], v[212:215]\n"
"buffer_load_dwordx4 a[40:43], v9, s[52:55], s82 offen offset:2048 sc1\n"
"v_mfma_f32_16x16x32_bf16 v[220:223], a[60:63], a[108:111], v[220:223]\n"
"ds_read_b128 a[76:79], v12 offset:6144\n"
"v_mfma_f32_16x16x32_bf16 v[216:219], a[56:59], a[108:111], v[216:219]\n"
"s_waitcnt lgkmcnt(4)\n"
"v_mfma_f32_16x16x32_bf16 v[224:227], a[56:59], a[112:115], v[224:227]\n"
"ds_read_b128 a[80:83], v12 offset:8192\n"
"v_mfma_f32_16x16x32_bf16 v[228:231], a[60:63], a[112:115], v[228:231]\n"
"v_mfma_f32_16x16x32_bf16 v[236:239], a[60:63], a[116:119], v[236:239]\n"
"ds_read_b128 a[84:87], v12 offset:10240\n"
"buffer_load_dwordx4 a[44:47], v10, s[52:55], s82 offen offset:2048 sc1\n"
"v_mfma_f32_16x16x32_bf16 v[232:235], a[56:59], a[116:119], v[232:235]\n"
"v_readlane_b32 s82, v11, 31\n"
"v_mfma_f32_16x16x32_bf16 v[240:243], a[56:59], a[120:123], v[240:243]\n"
"ds_read_b128 a[88:91], v12 offset:12288\n"
"v_mfma_f32_16x16x32_bf16 v[244:247], a[60:63], a[120:123], v[244:247]\n"
"v_mfma_f32_16x16x32_bf16 v[252:255], a[60:63], a[124:127], v[252:255]\n"
"ds_read_b128 a[92:95], v12 offset:14336\n"
"v_mfma_f32_16x16x32_bf16 v[248:251], a[56:59], a[124:127], v[248:251]\n"
"s_waitcnt vmcnt(20)\n"
"s_barrier\n"
"s_waitcnt lgkmcnt(4)\n"
"v_mfma_f32_16x16x32_bf16 v[128:131], a[0:3], a[64:67], v[128:131]\n"
"ds_read_b128 a[96:99], v12 offset:16384\n"
"s_add_u32 m0, s74, 98304\n"
"v_mfma_f32_16x16x32_bf16 v[132:135], a[4:7], a[64:67], v[132:135]\n"
"buffer_load_dwordx4 v84, s[4:7], s79 offen offset:0 lds\n"
"v_mfma_f32_16x16x32_bf16 v[140:143], a[4:7], a[68:71], v[140:143]\n"
"ds_read_b128 a[100:103], v12 offset:18432\n"
"v_mfma_f32_16x16x32_bf16 v[136:139], a[0:3], a[68:71], v[136:139]\n"
"v_mfma_f32_16x16x32_bf16 v[144:147], a[0:3], a[72:75], v[144:147]\n"
"ds_read_b128 a[104:107], v12 offset:20480\n"
"v_mfma_f32_16x16x32_bf16 v[148:151], a[4:7], a[72:75], v[148:151]\n"
"v_mfma_f32_16x16x32_bf16 v[156:159], a[4:7], a[76:79], v[156:159]\n"
"ds_read_b128 a[108:111], v12 offset:22528\n"
"buffer_load_dwordx4 v85, s[4:7], s79 offen offset:1024 lds\n"
"v_mfma_f32_16x16x32_bf16 v[152:155], a[0:3], a[76:79], v[152:155]\n"
"s_waitcnt lgkmcnt(4)\n"
"v_mfma_f32_16x16x32_bf16 v[160:163], a[0:3], a[80:83], v[160:163]\n"
"ds_read_b128 a[112:115], v12 offset:24576\n"
"v_mfma_f32_16x16x32_bf16 v[164:167], a[4:7], a[80:83], v[164:167]\n"
"v_mfma_f32_16x16x32_bf16 v[172:175], a[4:7], a[84:87], v[172:175]\n"
"ds_read_b128 a[116:119], v12 offset:26624\n"
"v_mfma_f32_16x16x32_bf16 v[168:171], a[0:3], a[84:87], v[168:171]\n"
"buffer_load_dwordx4 v86, s[4:7], s79 offen offset:2048 lds\n"
"v_mfma_f32_16x16x32_bf16 v[176:179], a[0:3], a[88:91], v[176:179]\n"
"ds_read_b128 a[120:123], v12 offset:28672\n"
"v_mfma_f32_16x16x32_bf16 v[180:183], a[4:7], a[88:91], v[180:183]\n"
"v_mfma_f32_16x16x32_bf16 v[188:191], a[4:7], a[92:95], v[188:191]\n"
"ds_read_b128 a[124:127], v12 offset:30720\n"
"v_mfma_f32_16x16x32_bf16 v[184:187], a[0:3], a[92:95], v[184:187]\n"
"s_waitcnt lgkmcnt(4)\n"
"v_mfma_f32_16x16x32_bf16 v[192:195], a[0:3], a[96:99], v[192:195]\n"
"ds_read_b128 a[64:67], v13 offset:0\n"
"buffer_load_dwordx4 v87, s[4:7], s79 offen offset:3072 lds\n"
"v_mfma_f32_16x16x32_bf16 v[196:199], a[4:7], a[96:99], v[196:199]\n"
"v_mfma_f32_16x16x32_bf16 v[204:207], a[4:7], a[100:103], v[204:207]\n"
"ds_read_b128 a[68:71], v13 offset:2048\n"
"v_mfma_f32_16x16x32_bf16 v[200:203], a[0:3], a[100:103], v[200:203]\n"
"v_mfma_f32_16x16x32_bf16 v[208:211], a[0:3], a[104:107], v[208:211]\n"
"ds_read_b128 a[72:75], v13 offset:4096\n"
"v_mfma_f32_16x16x32_bf16 v[212:215], a[4:7], a[104:107], v[212:215]\n"
"s_add_u32 m0, s74, 102400\n"
"v_mfma_f32_16x16x32_bf16 v[220:223], a[4:7], a[108:111], v[220:223]\n"
"ds_read_b128 a[76:79], v13 offset:6144\n"
"buffer_load_dwordx4 v88, s[4:7], s79 offen offset:0 lds\n"
"v_mfma_f32_16x16x32_bf16 v[216:219], a[0:3], a[108:111], v[216:219]\n"
"s_waitcnt lgkmcnt(4)\n"
"v_mfma_f32_16x16x32_bf16 v[224:227], a[0:3], a[112:115], v[224:227]\n"
"ds_read_b128 a[80:83], v13 offset:8192\n"
"v_mfma_f32_16x16x32_bf16 v[228:231], a[4:7], a[112:115], v[228:231]\n"
"v_mfma_f32_16x16x32_bf16 v[236:239], a[4:7], a[116:119], v[236:239]\n"
"ds_read_b128 a[84:87], v13 offset:10240\n"
"v_mfma_f32_16x16x32_bf16 v[232:235], a[0:3], a[116:119], v[232:235]\n"
"buffer_load_dwordx4 v89, s[4:7], s79 offen offset:1024 lds\n"
"v_mfma_f32_16x16x32_bf16 v[240:243], a[0:3], a[120:123], v[240:243]\n"
"ds_read_b128 a[88:91], v13 offset:12288\n"
"v_mfma_f32_16x16x32_bf16 v[244:247], a[4:7], a[120:123], v[244:247]\n"
"v_mfma_f32_16x16x32_bf16 v[252:255], a[4:7], a[124:127], v[252:255]\n"
"ds_read_b128 a[92:95], v13 offset:14336\n"
"v_mfma_f32_16x16x32_bf16 v[248:251], a[0:3], a[124:127], v[248:251]\n"
"s_lshl_b32 s75, s63, 18\n"
"s_waitcnt lgkmcnt(4)\n"
"v_mfma_f32_16x16x32_bf16 v[128:131], a[8:11], a[64:67], v[128:131]\n"
"ds_read_b128 a[96:99], v13 offset:16384\n"
"s_lshl_b32 s76, s35, 8\n"
"v_mfma_f32_16x16x32_bf16 v[132:135], a[12:15], a[64:67], v[132:135]\n"
"s_add_u32 s75, s75, s76\n"
"s_lshl_b32 s76, s31, 16\n"
"v_mfma_f32_16x16x32_bf16 v[140:143], a[12:15], a[68:71], v[140:143]\n"
"ds_read_b128 a[100:103], v13 offset:18432\n"
"s_add_u32 s75, s75, s76\n"
"v_mfma_f32_16x16x32_bf16 v[136:139], a[8:11], a[68:71], v[136:139]\n"
"s_add_u32 s44, s8, s75\n"
"s_addc_u32 s45, s9, 0\n"
"v_mfma_f32_16x16x32_bf16 v[144:147], a[8:11], a[72:75], v[144:147]\n"
"ds_read_b128 a[104:107], v13 offset:20480\n"
"s_sub_u32 s46, 4194304, s75\n"
"v_mfma_f32_16x16x32_bf16 v[148:151], a[12:15], a[72:75], v[148:151]\n"
"buffer_load_dwordx4 v90, s[4:7], s79 offen offset:2048 lds\n"
"buffer_load_dwordx4 v91, s[4:7], s79 offen offset:3072 lds\n"
"v_mfma_f32_16x16x32_bf16 v[156:159], a[12:15], a[76:79], v[156:159]\n"
"ds_read_b128 a[108:111], v13 offset:22528\n"
"v_mfma_f32_16x16x32_bf16 v[152:155], a[8:11], a[76:79], v[152:155]\n"
"s_waitcnt lgkmcnt(4)\n"
"v_mfma_f32_16x16x32_bf16 v[160:163], a[8:11], a[80:83], v[160:163]\n"
"ds_read_b128 a[112:115], v13 offset:24576\n"
"v_mfma_f32_16x16x32_bf16 v[164:167], a[12:15], a[80:83], v[164:167]\n"
"v_mfma_f32_16x16x32_bf16 v[172:175], a[12:15], a[84:87], v[172:175]\n"
"ds_read_b128 a[116:119], v13 offset:26624\n"
"v_mfma_f32_16x16x32_bf16 v[168:171], a[8:11], a[84:87], v[168:171]\n"
"buffer_load_dwordx4 a[48:51], v9, s[52:55], s82 offen offset:0 sc1\n"
"v_mfma_f32_16x16x32_bf16 v[176:179], a[8:11], a[88:91], v[176:179]\n"
"ds_read_b128 a[120:123], v13 offset:28672\n"
"v_mfma_f32_16x16x32_bf16 v[180:183], a[12:15], a[88:91], v[180:183]\n"
"v_mfma_f32_16x16x32_bf16 v[188:191], a[12:15], a[92:95], v[188:191]\n"
"ds_read_b128 a[124:127], v13 offset:30720\n"
"ds_read_b128 a[64:67], v12 offset:49152\n"
"v_mfma_f32_16x16x32_bf16 v[184:187], a[8:11], a[92:95], v[184:187]\n"
"ds_read_b128 a[68:71], v12 offset:51200\n"
"s_waitcnt lgkmcnt(6)\n"
"v_mfma_f32_16x16x32_bf16 v[192:195], a[8:11], a[96:99], v[192:195]\n"
"ds_read_b128 a[72:75], v12 offset:53248\n"
"buffer_load_dwordx4 a[52:55], v10, s[52:55], s82 offen offset:0 sc1\n"
"v_mfma_f32_16x16x32_bf16 v[196:199], a[12:15], a[96:99], v[196:199]\n"
"ds_read_b128 a[76:79], v12 offset:55296\n"
"v_mfma_f32_16x16x32_bf16 v[204:207], a[12:15], a[100:103], v[204:207]\n"
"ds_read_b128 a[80:83], v12 offset:57344\n"
"v_mfma_f32_16x16x32_bf16 v[200:203], a[8:11], a[100:103], v[200:203]\n"
"ds_read_b128 a[84:87], v12 offset:59392\n"
"v_mfma_f32_16x16x32_bf16 v[208:211], a[8:11], a[104:107], v[208:211]\n"
"ds_read_b128 a[88:91], v12 offset:61440\n"
"v_mfma_f32_16x16x32_bf16 v[212:215], a[12:15], a[104:107], v[212:215]\n"
"buffer_load_dwordx4 a[56:59], v9, s[52:55], s82 offen offset:2048 sc1\n"
"v_mfma_f32_16x16x32_bf16 v[220:223], a[12:15], a[108:111], v[220:223]\n"
"ds_read_b128 a[92:95], v12 offset:63488\n"
"v_mfma_f32_16x16x32_bf16 v[216:219], a[8:11], a[108:111], v[216:219]\n"
"s_waitcnt lgkmcnt(8)\n"
"v_mfma_f32_16x16x32_bf16 v[224:227], a[8:11], a[112:115], v[224:227]\n"
"ds_read_b128 a[96:99], v13 offset:49152\n"
"v_mfma_f32_16x16x32_bf16 v[228:231], a[12:15], a[112:115], v[228:231]\n"
"v_mfma_f32_16x16x32_bf16 v[236:239], a[12:15], a[116:119], v[236:239]\n"
"ds_read_b128 a[100:103], v13 offset:51200\n"
"buffer_load_dwordx4 a[60:63], v10, s[52:55], s82 offen offset:2048 sc1\n"
"v_mfma_f32_16x16x32_bf16 v[232:235], a[8:11], a[116:119], v[232:235]\n"
"v_mfma_f32_16x16x32_bf16 v[240:243], a[8:11], a[120:123], v[240:243]\n"
"ds_read_b128 a[104:107], v13 offset:53248\n"
"v_mfma_f32_16x16x32_bf16 v[244:247], a[12:15], a[120:123], v[244:247]\n"
"v_mfma_f32_16x16x32_bf16 v[252:255], a[12:15], a[124:127], v[252:255]\n"
"ds_read_b128 a[108:111], v13 offset:55296\n"
"v_mfma_f32_16x16x32_bf16 v[248:251], a[8:11], a[124:127], v[248:251]\n"
"s_waitcnt vmcnt(16)\n"
"s_barrier\n"
"s_waitcnt lgkmcnt(8)\n"
"v_mfma_f32_16x16x32_bf16 v[192:195], a[16:19], a[64:67], v[192:195]\n"
"ds_read_b128 a[112:115], v13 offset:57344\n"
"s_add_u32 m0, s74, 0\n"
"v_mfma_f32_16x16x32_bf16 v[200:203], a[16:19], a[68:71], v[200:203]\n"
"ds_read_b128 a[116:119], v13 offset:59392\n"
"buffer_load_dwordx4 v72, s[44:47], 0 offen offset:0 lds\n"
"v_mfma_f32_16x16x32_bf16 v[208:211], a[16:19], a[72:75], v[208:211]\n"
"ds_read_b128 a[120:123], v13 offset:61440\n"
"v_mfma_f32_16x16x32_bf16 v[216:219], a[16:19], a[76:79], v[216:219]\n"
"ds_read_b128 a[124:127], v13 offset:63488\n"
"s_waitcnt lgkmcnt(8)\n"
"v_mfma_f32_16x16x32_bf16 v[224:227], a[16:19], a[80:83], v[224:227]\n"
"ds_read_b128 a[64:67], v14 offset:16384\n"
"buffer_load_dwordx4 v73, s[44:47], 0 offen offset:1024 lds\n"
"v_mfma_f32_16x16x32_bf16 v[232:235], a[16:19], a[84:87], v[232:235]\n"
"ds_read_b128 a[68:71], v14 offset:18432\n"
"v_mfma_f32_16x16x32_bf16 v[240:243], a[16:19], a[88:91], v[240:243]\n"
"ds_read_b128 a[72:75], v14 offset:20480\n"
"v_mfma_f32_16x16x32_bf16 v[248:251], a[16:19], a[92:95], v[248:251]\n"
"ds_read_b128 a[76:79], v14 offset:22528\n"
"s_waitcnt lgkmcnt(8)\n"
"v_mfma_f32_16x16x32_bf16 v[192:195], a[24:27], a[96:99], v[192:195]\n"
"ds_read_b128 a[80:83], v14 offset:24576\n"
"buffer_load_dwordx4 v74, s[44:47], 0 offen offset:2048 lds\n"
"v_mfma_f32_16x16x32_bf16 v[200:203], a[24:27], a[100:103], v[200:203]\n"
"ds_read_b128 a[84:87], v14 offset:26624\n"
"v_mfma_f32_16x16x32_bf16 v[208:211], a[24:27], a[104:107], v[208:211]\n"
"ds_read_b128 a[88:91], v14 offset:28672\n"
"v_mfma_f32_16x16x32_bf16 v[216:219], a[24:27], a[108:111], v[216:219]\n"
"ds_read_b128 a[92:95], v14 offset:30720\n"
"s_waitcnt lgkmcnt(8)\n"
"v_mfma_f32_16x16x32_bf16 v[224:227], a[24:27], a[112:115], v[224:227]\n"
"ds_read_b128 a[96:99], v15 offset:16384\n"
"buffer_load_dwordx4 v75, s[44:47], 0 offen offset:3072 lds\n"
"v_mfma_f32_16x16x32_bf16 v[232:235], a[24:27], a[116:119], v[232:235]\n"
"ds_read_b128 a[100:103], v15 offset:18432\n"
"v_mfma_f32_16x16x32_bf16 v[240:243], a[24:27], a[120:123], v[240:243]\n"
"ds_read_b128 a[104:107], v15 offset:20480\n"
"s_add_u32 m0, s74, 4096\n"
"v_mfma_f32_16x16x32_bf16 v[248:251], a[24:27], a[124:127], v[248:251]\n"
"ds_read_b128 a[108:111], v15 offset:22528\n"
"s_waitcnt lgkmcnt(8)\n"
"s_waitcnt vmcnt(19)\n"
"v_mfma_f32_16x16x32_bf16 v[192:195], a[32:35], a[64:67], v[192:195]\n"
"ds_read_b128 a[112:115], v15 offset:24576\n"
"buffer_load_dwordx4 v76, s[44:47], 0 offen offset:0 lds\n"
"v_mfma_f32_16x16x32_bf16 v[200:203], a[32:35], a[68:71], v[200:203]\n"
"ds_read_b128 a[116:119], v15 offset:26624\n"
"v_mfma_f32_16x16x32_bf16 v[208:211], a[32:35], a[72:75], v[208:211]\n"
"ds_read_b128 a[120:123], v15 offset:28672\n"
"buffer_load_dwordx4 v77, s[44:47], 0 offen offset:1024 lds\n"
"v_mfma_f32_16x16x32_bf16 v[216:219], a[32:35], a[76:79], v[216:219]\n"
"ds_read_b128 a[124:127], v15 offset:30720\n"
"s_waitcnt lgkmcnt(8)\n"
"v_mfma_f32_16x16x32_bf16 v[224:227], a[32:35], a[80:83], v[224:227]\n"
"v_mfma_f32_16x16x32_bf16 v[232:235], a[32:35], a[84:87], v[232:235]\n"
"v_mfma_f32_16x16x32_bf16 v[240:243], a[32:35], a[88:91], v[240:243]\n"
"buffer_load_dwordx4 v78, s[44:47], 0 offen offset:2048 lds\n"
"v_mfma_f32_16x16x32_bf16 v[248:251], a[32:35], a[92:95], v[248:251]\n"
"s_waitcnt lgkmcnt(4)\n"
"s_waitcnt vmcnt(20)\n"
"v_mfma_f32_16x16x32_bf16 v[192:195], a[40:43], a[96:99], v[192:195]\n"
"v_mfma_f32_16x16x32_bf16 v[200:203], a[40:43], a[100:103], v[200:203]\n"
"buffer_load_dwordx4 v79, s[44:47], 0 offen offset:3072 lds\n"
"v_mfma_f32_16x16x32_bf16 v[208:211], a[40:43], a[104:107], v[208:211]\n"
"v_mfma_f32_16x16x32_bf16 v[216:219], a[40:43], a[108:111], v[216:219]\n"
"s_waitcnt lgkmcnt(0)\n"
"v_mfma_f32_16x16x32_bf16 v[224:227], a[40:43], a[112:115], v[224:227]\n"
"v_mfma_f32_16x16x32_bf16 v[232:235], a[40:43], a[116:119], v[232:235]\n"
"v_mfma_f32_16x16x32_bf16 v[240:243], a[40:43], a[120:123], v[240:243]\n"
"v_mfma_f32_16x16x32_bf16 v[248:251], a[40:43], a[124:127], v[248:251]\n"
"s_waitcnt vmcnt(12)\n"
"s_barrier\n"
"ds_read_b128 a[64:67], v14 offset:49152\n"
"s_waitcnt lgkmcnt(0)\n"
"s_waitcnt vmcnt(11)\n"
"v_mfma_f32_16x16x32_bf16 v[192:195], a[48:51], a[64:67], v[192:195]\n"
"ds_read_b128 a[68:71], v14 offset:51200\n"
"ds_read_b128 a[72:75], v15 offset:49152\n"
"s_waitcnt lgkmcnt(1)\n"
"v_mfma_f32_16x16x32_bf16 v[200:203], a[48:51], a[68:71], v[200:203]\n"
"ds_read_b128 a[76:79], v15 offset:51200\n"
"ds_read_b128 a[80:83], v14 offset:53248\n"
"s_waitcnt vmcnt(9) lgkmcnt(2)\n"
"v_mfma_f32_16x16x32_bf16 v[192:195], a[56:59], a[72:75], v[192:195]\n"
"ds_read_b128 a[84:87], v14 offset:55296\n"
"ds_read_b128 a[88:91], v15 offset:53248\n"
"s_waitcnt lgkmcnt(3)\n"
"v_mfma_f32_16x16x32_bf16 v[200:203], a[56:59], a[76:79], v[200:203]\n"
"ds_read_b128 a[92:95], v15 offset:55296\n"
"ds_read_b128 a[96:99], v14 offset:57344\n"
"s_waitcnt lgkmcnt(1)\n"
"v_mfma_f32_16x16x32_bf16 v[208:211], a[48:51], a[80:83], v[208:211]\n"
"ds_read_b128 a[100:103], v14 offset:59392\n"
"ds_read_b128 a[104:107], v15 offset:57344\n"
"v_mfma_f32_16x16x32_bf16 v[216:219], a[48:51], a[84:87], v[216:219]\n"
"ds_read_b128 a[108:111], v15 offset:59392\n"
"ds_read_b128 a[112:115], v14 offset:61440\n"
"v_mfma_f32_16x16x32_bf16 v[208:211], a[56:59], a[88:91], v[208:211]\n"
"ds_read_b128 a[116:119], v14 offset:63488\n"
"ds_read_b128 a[120:123], v15 offset:61440\n"
"v_mfma_f32_16x16x32_bf16 v[216:219], a[56:59], a[92:95], v[216:219]\n"
"ds_read_b128 a[124:127], v15 offset:63488\n"
"ds_read_b128 a[64:67], v12 offset:49152\n"
"s_waitcnt lgkmcnt(5)\n"
"v_mfma_f32_16x16x32_bf16 v[224:227], a[48:51], a[96:99], v[224:227]\n"
"ds_read_b128 a[68:71], v12 offset:51200\n"
"ds_read_b128 a[72:75], v12 offset:53248\n"
"v_mfma_f32_16x16x32_bf16 v[232:235], a[48:51], a[100:103], v[232:235]\n"
"ds_read_b128 a[76:79], v12 offset:55296\n"
"v_cvt_pk_bf16_f32 v32, v192, v193\n"
"v_mfma_f32_16x16x32_bf16 v[224:227], a[56:59], a[104:107], v[224:227]\n"
"ds_read_b128 a[80:83], v12 offset:57344\n"
"v_cvt_pk_bf16_f32 v33, v194, v195\n"
"v_mfma_f32_16x16x32_bf16 v[232:235], a[56:59], a[108:111], v[232:235]\n"
"ds_read_b128 a[84:87], v12 offset:59392\n"
"s_waitcnt lgkmcnt(6)\n"
"v_mfma_f32_16x16x32_bf16 v[240:243], a[48:51], a[112:115], v[240:243]\n"
"ds_read_b128 a[88:91], v12 offset:61440\n"
"v_cvt_pk_bf16_f32 v34, v200, v201\n"
"v_mfma_f32_16x16x32_bf16 v[248:251], a[48:51], a[116:119], v[248:251]\n"
"ds_read_b128 a[92:95], v12 offset:63488\n"
"v_cvt_pk_bf16_f32 v35, v202, v203\n"
"v_mfma_f32_16x16x32_bf16 v[240:243], a[56:59], a[120:123], v[240:243]\n"
"ds_read_b128 a[96:99], v13 offset:49152\n"
"buffer_store_dwordx4 v[32:35], v8, s[36:39], s90 offen offset:0 sc1\n"
"v_mfma_f32_16x16x32_bf16 v[248:251], a[56:59], a[124:127], v[248:251]\n"
"ds_read_b128 a[100:103], v13 offset:51200\n"
"s_waitcnt lgkmcnt(6)\n"
"v_mfma_f32_16x16x32_bf16 v[196:199], a[20:23], a[64:67], v[196:199]\n"
"ds_read_b128 a[104:107], v13 offset:53248\n"
"v_cvt_pk_bf16_f32 v36, v208, v209\n"
"v_mfma_f32_16x16x32_bf16 v[204:207], a[20:23], a[68:71], v[204:207]\n"
"ds_read_b128 a[108:111], v13 offset:55296\n"
"v_cvt_pk_bf16_f32 v37, v210, v211\n"
"v_cvt_pk_bf16_f32 v38, v216, v217\n"
"v_mfma_f32_16x16x32_bf16 v[212:215], a[20:23], a[72:75], v[212:215]\n"
"ds_read_b128 a[112:115], v13 offset:57344\n"
"v_cvt_pk_bf16_f32 v39, v218, v219\n"
"v_mfma_f32_16x16x32_bf16 v[220:223], a[20:23], a[76:79], v[220:223]\n"
"ds_read_b128 a[116:119], v13 offset:59392\n"
"s_waitcnt lgkmcnt(6)\n"
"v_mfma_f32_16x16x32_bf16 v[228:231], a[20:23], a[80:83], v[228:231]\n"
"ds_read_b128 a[120:123], v13 offset:61440\n"
"buffer_store_dwordx4 v[36:39], v8, s[36:39], s90 offen offset:1024 sc1\n"
"v_mfma_f32_16x16x32_bf16 v[236:239], a[20:23], a[84:87], v[236:239]\n"
"ds_read_b128 a[124:127], v13 offset:63488\n"
"v_cvt_pk_bf16_f32 v40, v224, v225\n"
"v_mfma_f32_16x16x32_bf16 v[244:247], a[20:23], a[88:91], v[244:247]\n"
"ds_read_b128 a[64:67], v14 offset:16384\n"
"v_cvt_pk_bf16_f32 v41, v226, v227\n"
"v_mfma_f32_16x16x32_bf16 v[252:255], a[20:23], a[92:95], v[252:255]\n"
"ds_read_b128 a[68:71], v14 offset:18432\n"
"s_waitcnt lgkmcnt(6)\n"
"v_mfma_f32_16x16x32_bf16 v[196:199], a[28:31], a[96:99], v[196:199]\n"
"ds_read_b128 a[72:75], v14 offset:20480\n"
"v_cvt_pk_bf16_f32 v42, v232, v233\n"
"v_mfma_f32_16x16x32_bf16 v[204:207], a[28:31], a[100:103], v[204:207]\n"
"ds_read_b128 a[76:79], v14 offset:22528\n"
"v_cvt_pk_bf16_f32 v43, v234, v235\n"
"v_mfma_f32_16x16x32_bf16 v[212:215], a[28:31], a[104:107], v[212:215]\n"
"ds_read_b128 a[80:83], v14 offset:24576\n"
"buffer_store_dwordx4 v[40:43], v8, s[36:39], s90 offen offset:2048 sc1\n"
"v_mfma_f32_16x16x32_bf16 v[220:223], a[28:31], a[108:111], v[220:223]\n"
"ds_read_b128 a[84:87], v14 offset:26624\n"
"s_waitcnt lgkmcnt(6)\n"
"v_mfma_f32_16x16x32_bf16 v[228:231], a[28:31], a[112:115], v[228:231]\n"
"ds_read_b128 a[88:91], v14 offset:28672\n"
"v_cvt_pk_bf16_f32 v44, v240, v241\n"
"v_mfma_f32_16x16x32_bf16 v[236:239], a[28:31], a[116:119], v[236:239]\n"
"ds_read_b128 a[92:95], v14 offset:30720\n"
"v_cvt_pk_bf16_f32 v45, v242, v243\n"
"v_mfma_f32_16x16x32_bf16 v[244:247], a[28:31], a[120:123], v[244:247]\n"
"ds_read_b128 a[96:99], v15 offset:16384\n"
"v_cvt_pk_bf16_f32 v46, v248, v249\n"
"v_mfma_f32_16x16x32_bf16 v[252:255], a[28:31], a[124:127], v[252:255]\n"
"ds_read_b128 a[100:103], v15 offset:18432\n"
"s_waitcnt lgkmcnt(6)\n"
"v_mfma_f32_16x16x32_bf16 v[196:199], a[36:39], a[64:67], v[196:199]\n"
"ds_read_b128 a[104:107], v15 offset:20480\n"
"v_cvt_pk_bf16_f32 v47, v250, v251\n"
"v_mfma_f32_16x16x32_bf16 v[204:207], a[36:39], a[68:71], v[204:207]\n"
"ds_read_b128 a[108:111], v15 offset:22528\n"
"buffer_store_dwordx4 v[44:47], v8, s[36:39], s90 offen offset:3072 sc1\n"
"v_mfma_f32_16x16x32_bf16 v[212:215], a[36:39], a[72:75], v[212:215]\n"
"ds_read_b128 a[112:115], v15 offset:24576\n"
"v_mfma_f32_16x16x32_bf16 v[220:223], a[36:39], a[76:79], v[220:223]\n"
"ds_read_b128 a[116:119], v15 offset:26624\n"
"s_waitcnt lgkmcnt(6)\n"
"v_mfma_f32_16x16x32_bf16 v[228:231], a[36:39], a[80:83], v[228:231]\n"
"ds_read_b128 a[120:123], v15 offset:28672\n"
"v_mfma_f32_16x16x32_bf16 v[236:239], a[36:39], a[84:87], v[236:239]\n"
"ds_read_b128 a[124:127], v15 offset:30720\n"
"v_mfma_f32_16x16x32_bf16 v[244:247], a[36:39], a[88:91], v[244:247]\n"
"ds_read_b128 a[64:67], v14 offset:49152\n"
"v_mfma_f32_16x16x32_bf16 v[252:255], a[36:39], a[92:95], v[252:255]\n"
"ds_read_b128 a[68:71], v14 offset:51200\n"
"s_waitcnt lgkmcnt(6)\n"
"v_mfma_f32_16x16x32_bf16 v[196:199], a[44:47], a[96:99], v[196:199]\n"
"ds_read_b128 a[72:75], v15 offset:49152\n"
"v_mfma_f32_16x16x32_bf16 v[204:207], a[44:47], a[100:103], v[204:207]\n"
"ds_read_b128 a[76:79], v15 offset:51200\n"
"v_mfma_f32_16x16x32_bf16 v[212:215], a[44:47], a[104:107], v[212:215]\n"
"ds_read_b128 a[80:83], v14 offset:53248\n"
"v_mfma_f32_16x16x32_bf16 v[220:223], a[44:47], a[108:111], v[220:223]\n"
"ds_read_b128 a[84:87], v14 offset:55296\n"
"s_waitcnt lgkmcnt(6)\n"
"v_mfma_f32_16x16x32_bf16 v[228:231], a[44:47], a[112:115], v[228:231]\n"
"ds_read_b128 a[88:91], v15 offset:53248\n"
"v_mfma_f32_16x16x32_bf16 v[236:239], a[44:47], a[116:119], v[236:239]\n"
"ds_read_b128 a[92:95], v15 offset:55296\n"
"v_mfma_f32_16x16x32_bf16 v[244:247], a[44:47], a[120:123], v[244:247]\n"
"ds_read_b128 a[96:99], v14 offset:57344\n"
"v_mfma_f32_16x16x32_bf16 v[252:255], a[44:47], a[124:127], v[252:255]\n"
"ds_read_b128 a[100:103], v14 offset:59392\n"
"s_waitcnt lgkmcnt(6)\n"
"v_mfma_f32_16x16x32_bf16 v[196:199], a[52:55], a[64:67], v[196:199]\n"
"ds_read_b128 a[104:107], v15 offset:57344\n"
"v_mfma_f32_16x16x32_bf16 v[204:207], a[52:55], a[68:71], v[204:207]\n"
"ds_read_b128 a[108:111], v15 offset:59392\n"
"s_waitcnt vmcnt(12)\n"
"v_mfma_f32_16x16x32_bf16 v[196:199], a[60:63], a[72:75], v[196:199]\n"
"ds_read_b128 a[112:115], v14 offset:61440\n"
"v_mfma_f32_16x16x32_bf16 v[204:207], a[60:63], a[76:79], v[204:207]\n"
"ds_read_b128 a[116:119], v14 offset:63488\n"
"s_waitcnt lgkmcnt(6)\n"
"v_mfma_f32_16x16x32_bf16 v[212:215], a[52:55], a[80:83], v[212:215]\n"
"ds_read_b128 a[120:123], v15 offset:61440\n"
"v_mfma_f32_16x16x32_bf16 v[220:223], a[52:55], a[84:87], v[220:223]\n"
"ds_read_b128 a[124:127], v15 offset:63488\n"
"v_mfma_f32_16x16x32_bf16 v[212:215], a[60:63], a[88:91], v[212:215]\n"
"ds_read_b128 a[64:67], v12 offset:32768\n"
"v_mfma_f32_16x16x32_bf16 v[220:223], a[60:63], a[92:95], v[220:223]\n"
"ds_read_b128 a[68:71], v12 offset:34816\n"
"s_waitcnt lgkmcnt(6)\n"
"v_mfma_f32_16x16x32_bf16 v[228:231], a[52:55], a[96:99], v[228:231]\n"
"ds_read_b128 a[72:75], v12 offset:36864\n"
"v_cvt_pk_bf16_f32 v48, v196, v197\n"
"v_mfma_f32_16x16x32_bf16 v[236:239], a[52:55], a[100:103], v[236:239]\n"
"ds_read_b128 a[76:79], v12 offset:38912\n"
"v_cvt_pk_bf16_f32 v49, v198, v199\n"
"v_mfma_f32_16x16x32_bf16 v[228:231], a[60:63], a[104:107], v[228:231]\n"
"ds_read_b128 a[80:83], v12 offset:40960\n"
"v_cvt_pk_bf16_f32 v50, v204, v205\n"
"v_mfma_f32_16x16x32_bf16 v[236:239], a[60:63], a[108:111], v[236:239]\n"
"ds_read_b128 a[84:87], v12 offset:43008\n"
"s_waitcnt lgkmcnt(6)\n"
"v_mfma_f32_16x16x32_bf16 v[244:247], a[52:55], a[112:115], v[244:247]\n"
"ds_read_b128 a[88:91], v12 offset:45056\n"
"v_cvt_pk_bf16_f32 v51, v206, v207\n"
"v_mfma_f32_16x16x32_bf16 v[252:255], a[52:55], a[116:119], v[252:255]\n"
"ds_read_b128 a[92:95], v12 offset:47104\n"
"buffer_store_dwordx4 v[48:51], v8, s[36:39], s92 offen offset:0 sc1\n"
"v_mfma_f32_16x16x32_bf16 v[244:247], a[60:63], a[120:123], v[244:247]\n"
"ds_read_b128 a[96:99], v13 offset:32768\n"
"v_cvt_pk_bf16_f32 v52, v212, v213\n"
"v_mfma_f32_16x16x32_bf16 v[252:255], a[60:63], a[124:127], v[252:255]\n"
"ds_read_b128 a[100:103], v13 offset:34816\n"
"s_waitcnt lgkmcnt(6)\n"
"v_mfma_f32_16x16x32_bf16 v[132:135], a[20:23], a[64:67], v[132:135]\n"
"ds_read_b128 a[104:107], v13 offset:36864\n"
"v_cvt_pk_bf16_f32 v53, v214, v215\n"
"v_mfma_f32_16x16x32_bf16 v[140:143], a[20:23], a[68:71], v[140:143]\n"
"ds_read_b128 a[108:111], v13 offset:38912\n"
"v_cvt_pk_bf16_f32 v54, v220, v221\n"
"v_cvt_pk_bf16_f32 v55, v222, v223\n"
"v_mfma_f32_16x16x32_bf16 v[148:151], a[20:23], a[72:75], v[148:151]\n"
"ds_read_b128 a[112:115], v13 offset:40960\n"
"buffer_store_dwordx4 v[52:55], v8, s[36:39], s92 offen offset:1024 sc1\n"
"v_mfma_f32_16x16x32_bf16 v[156:159], a[20:23], a[76:79], v[156:159]\n"
"ds_read_b128 a[116:119], v13 offset:43008\n"
"s_waitcnt lgkmcnt(6)\n"
"v_mfma_f32_16x16x32_bf16 v[164:167], a[20:23], a[80:83], v[164:167]\n"
"ds_read_b128 a[120:123], v13 offset:45056\n"
"v_cvt_pk_bf16_f32 v56, v228, v229\n"
"v_mfma_f32_16x16x32_bf16 v[172:175], a[20:23], a[84:87], v[172:175]\n"
"ds_read_b128 a[124:127], v13 offset:47104\n"
"v_cvt_pk_bf16_f32 v57, v230, v231\n"
"v_mfma_f32_16x16x32_bf16 v[180:183], a[20:23], a[88:91], v[180:183]\n"
"ds_read_b128 a[64:67], v14 offset:0\n"
"v_cvt_pk_bf16_f32 v58, v236, v237\n"
"v_mfma_f32_16x16x32_bf16 v[188:191], a[20:23], a[92:95], v[188:191]\n"
"ds_read_b128 a[68:71], v14 offset:2048\n"
"s_waitcnt lgkmcnt(6)\n"
"v_mfma_f32_16x16x32_bf16 v[132:135], a[28:31], a[96:99], v[132:135]\n"
"ds_read_b128 a[72:75], v14 offset:4096\n"
"v_cvt_pk_bf16_f32 v59, v238, v239\n"
"v_mfma_f32_16x16x32_bf16 v[140:143], a[28:31], a[100:103], v[140:143]\n"
"ds_read_b128 a[76:79], v14 offset:6144\n"
"buffer_store_dwordx4 v[56:59], v8, s[36:39], s92 offen offset:2048 sc1\n"
"v_mfma_f32_16x16x32_bf16 v[148:151], a[28:31], a[104:107], v[148:151]\n"
"ds_read_b128 a[80:83], v14 offset:8192\n"
"v_cvt_pk_bf16_f32 v60, v244, v245\n"
"v_mfma_f32_16x16x32_bf16 v[156:159], a[28:31], a[108:111], v[156:159]\n"
"ds_read_b128 a[84:87], v14 offset:10240\n"
"s_waitcnt lgkmcnt(6)\n"
"v_mfma_f32_16x16x32_bf16 v[164:167], a[28:31], a[112:115], v[164:167]\n"
"ds_read_b128 a[88:91], v14 offset:12288\n"
"v_cvt_pk_bf16_f32 v61, v246, v247\n"
"v_mfma_f32_16x16x32_bf16 v[172:175], a[28:31], a[116:119], v[172:175]\n"
"ds_read_b128 a[92:95], v14 offset:14336\n"
"v_cvt_pk_bf16_f32 v62, v252, v253\n"
"v_mfma_f32_16x16x32_bf16 v[180:183], a[28:31], a[120:123], v[180:183]\n"
"ds_read_b128 a[96:99], v15 offset:0\n"
"v_cvt_pk_bf16_f32 v63, v254, v255\n"
"v_mfma_f32_16x16x32_bf16 v[188:191], a[28:31], a[124:127], v[188:191]\n"
"ds_read_b128 a[100:103], v15 offset:2048\n"
"s_waitcnt lgkmcnt(6)\n"
"v_mfma_f32_16x16x32_bf16 v[132:135], a[36:39], a[64:67], v[132:135]\n"
"ds_read_b128 a[104:107], v15 offset:4096\n"
"buffer_store_dwordx4 v[60:63], v8, s[36:39], s92 offen offset:3072 sc1\n"
"v_mfma_f32_16x16x32_bf16 v[140:143], a[36:39], a[68:71], v[140:143]\n"
"ds_read_b128 a[108:111], v15 offset:6144\n"
"v_mfma_f32_16x16x32_bf16 v[148:151], a[36:39], a[72:75], v[148:151]\n"
"ds_read_b128 a[112:115], v15 offset:8192\n"
"v_mfma_f32_16x16x32_bf16 v[156:159], a[36:39], a[76:79], v[156:159]\n"
"ds_read_b128 a[116:119], v15 offset:10240\n"
"s_waitcnt lgkmcnt(6)\n"
"v_mfma_f32_16x16x32_bf16 v[164:167], a[36:39], a[80:83], v[164:167]\n"
"ds_read_b128 a[120:123], v15 offset:12288\n"
"v_mfma_f32_16x16x32_bf16 v[172:175], a[36:39], a[84:87], v[172:175]\n"
"ds_read_b128 a[124:127], v15 offset:14336\n"
"v_mfma_f32_16x16x32_bf16 v[180:183], a[36:39], a[88:91], v[180:183]\n"
"ds_read_b128 a[64:67], v14 offset:32768\n"
"v_mfma_f32_16x16x32_bf16 v[188:191], a[36:39], a[92:95], v[188:191]\n"
"ds_read_b128 a[68:71], v14 offset:34816\n"
"s_waitcnt lgkmcnt(6)\n"
"v_mfma_f32_16x16x32_bf16 v[132:135], a[44:47], a[96:99], v[132:135]\n"
"ds_read_b128 a[72:75], v15 offset:32768\n"
"v_mfma_f32_16x16x32_bf16 v[140:143], a[44:47], a[100:103], v[140:143]\n"
"ds_read_b128 a[76:79], v15 offset:34816\n"
"v_mfma_f32_16x16x32_bf16 v[148:151], a[44:47], a[104:107], v[148:151]\n"
"ds_read_b128 a[80:83], v14 offset:36864\n"
"v_mfma_f32_16x16x32_bf16 v[156:159], a[44:47], a[108:111], v[156:159]\n"
"ds_read_b128 a[84:87], v14 offset:38912\n"
"s_waitcnt lgkmcnt(6)\n"
"v_mfma_f32_16x16x32_bf16 v[164:167], a[44:47], a[112:115], v[164:167]\n"
"ds_read_b128 a[88:91], v15 offset:36864\n"
"v_mfma_f32_16x16x32_bf16 v[172:175], a[44:47], a[116:119], v[172:175]\n"
"ds_read_b128 a[92:95], v15 offset:38912\n"
"v_mfma_f32_16x16x32_bf16 v[180:183], a[44:47], a[120:123], v[180:183]\n"
"ds_read_b128 a[96:99], v14 offset:40960\n"
"v_mfma_f32_16x16x32_bf16 v[188:191], a[44:47], a[124:127], v[188:191]\n"
"ds_read_b128 a[100:103], v14 offset:43008\n"
"s_waitcnt lgkmcnt(6)\n"
"v_mfma_f32_16x16x32_bf16 v[132:135], a[52:55], a[64:67], v[132:135]\n"
"ds_read_b128 a[104:107], v15 offset:40960\n"
"v_mfma_f32_16x16x32_bf16 v[140:143], a[52:55], a[68:71], v[140:143]\n"
"ds_read_b128 a[108:111], v15 offset:43008\n"
"v_mfma_f32_16x16x32_bf16 v[132:135], a[60:63], a[72:75], v[132:135]\n"
"ds_read_b128 a[112:115], v14 offset:45056\n"
"v_mfma_f32_16x16x32_bf16 v[140:143], a[60:63], a[76:79], v[140:143]\n"
"ds_read_b128 a[116:119], v14 offset:47104\n"
"s_waitcnt lgkmcnt(6)\n"
"v_mfma_f32_16x16x32_bf16 v[148:151], a[52:55], a[80:83], v[148:151]\n"
"ds_read_b128 a[120:123], v15 offset:45056\n"
"v_mfma_f32_16x16x32_bf16 v[156:159], a[52:55], a[84:87], v[156:159]\n"
"ds_read_b128 a[124:127], v15 offset:47104\n"
"v_mfma_f32_16x16x32_bf16 v[148:151], a[60:63], a[88:91], v[148:151]\n"
"ds_read_b128 a[64:67], v12 offset:32768\n"
"v_mfma_f32_16x16x32_bf16 v[156:159], a[60:63], a[92:95], v[156:159]\n"
"ds_read_b128 a[68:71], v12 offset:34816\n"
"s_waitcnt lgkmcnt(6)\n"
"v_mfma_f32_16x16x32_bf16 v[164:167], a[52:55], a[96:99], v[164:167]\n"
"ds_read_b128 a[72:75], v12 offset:36864\n"
"v_cvt_pk_bf16_f32 v32, v132, v133\n"
"v_mfma_f32_16x16x32_bf16 v[172:175], a[52:55], a[100:103], v[172:175]\n"
"v_cvt_pk_bf16_f32 v33, v134, v135\n"
"v_cvt_pk_bf16_f32 v34, v140, v141\n"
"v_mfma_f32_16x16x32_bf16 v[164:167], a[60:63], a[104:107], v[164:167]\n"
"v_cvt_pk_bf16_f32 v35, v142, v143\n"
"buffer_store_dwordx4 v[32:35], v8, s[36:39], s91 offen offset:0 sc1\n"
"v_mfma_f32_16x16x32_bf16 v[172:175], a[60:63], a[108:111], v[172:175]\n"
"v_cvt_pk_bf16_f32 v36, v148, v149\n"
"v_cvt_pk_bf16_f32 v37, v150, v151\n"
"v_cvt_pk_bf16_f32 v38, v156, v157\n"
"v_cvt_pk_bf16_f32 v39, v158, v159\n"
"s_waitcnt vmcnt(5)\n"
"buffer_store_dwordx2 v[28:29], v30, s[36:39], s87 offen offset:0 sc1\n"
"s_waitcnt lgkmcnt(3)\n"
"v_mfma_f32_16x16x32_bf16 v[180:183], a[52:55], a[112:115], v[180:183]\n"
"buffer_store_dwordx4 v[36:39], v8, s[36:39], s91 offen offset:1024 sc1\n"
"v_mfma_f32_16x16x32_bf16 v[188:191], a[52:55], a[116:119], v[188:191]\n"
"v_mfma_f32_16x16x32_bf16 v[180:183], a[60:63], a[120:123], v[180:183]\n"
"v_mfma_f32_16x16x32_bf16 v[188:191], a[60:63], a[124:127], v[188:191]\n"
"v_cvt_pk_bf16_f32 v40, v164, v165\n"
"s_waitcnt lgkmcnt(0)\n"
"v_mfma_f32_16x16x32_bf16 v[128:131], a[16:19], a[64:67], v[128:131]\n"
"v_cvt_pk_bf16_f32 v41, v166, v167\n"
"v_cvt_pk_bf16_f32 v42, v172, v173\n"
"v_mfma_f32_16x16x32_bf16 v[136:139], a[16:19], a[68:71], v[136:139]\n"
"v_cvt_pk_bf16_f32 v43, v174, v175\n"
"buffer_store_dwordx4 v[40:43], v8, s[36:39], s91 offen offset:2048 sc1\n"
"v_mfma_f32_16x16x32_bf16 v[144:147], a[16:19], a[72:75], v[144:147]\n"
"v_cvt_pk_bf16_f32 v44, v180, v181\n"
"v_cvt_pk_bf16_f32 v45, v182, v183\n"
"v_cvt_pk_bf16_f32 v46, v188, v189\n"
"v_cvt_pk_bf16_f32 v47, v190, v191\n"
"buffer_store_dwordx4 v[44:47], v8, s[36:39], s91 offen offset:3072 sc1\n"
"s_barrier\n"
"ds_read_b64_tr_b16 a[76:77], v82 offset:0\n"
"ds_read_b64_tr_b16 a[78:79], v82 offset:8192\n"
"s_waitcnt lgkmcnt(0)\n"
"v_mfma_f32_16x16x32_bf16 v[104:107], a[76:79], a[220:223], 0\n"
"ds_read_b64_tr_b16 a[80:81], v82 offset:16384\n"
"ds_read_b64_tr_b16 a[82:83], v82 offset:24576\n"
"s_waitcnt lgkmcnt(0)\n"
"v_mfma_f32_16x16x32_bf16 v[104:107], a[80:83], a[220:223], v[104:107]\n"
"ds_read_b64_tr_b16 a[84:85], v83 offset:0\n"
"ds_read_b64_tr_b16 a[86:87], v83 offset:8192\n"
"s_waitcnt lgkmcnt(0)\n"
"v_mfma_f32_16x16x32_bf16 v[108:111], a[84:87], a[220:223], 0\n"
"ds_read_b64_tr_b16 a[88:89], v83 offset:16384\n"
"ds_read_b64_tr_b16 a[90:91], v83 offset:24576\n"
"s_waitcnt lgkmcnt(0)\n"
"v_mfma_f32_16x16x32_bf16 v[108:111], a[88:91], a[220:223], v[108:111]\n"
"ds_read_b64_tr_b16 a[92:93], v82 offset:64\n"
"ds_read_b64_tr_b16 a[94:95], v82 offset:8256\n"
"s_waitcnt lgkmcnt(0)\n"
"v_mfma_f32_16x16x32_bf16 v[112:115], a[92:95], a[220:223], 0\n"
"ds_read_b64_tr_b16 a[96:97], v82 offset:16448\n"
"ds_read_b64_tr_b16 a[98:99], v82 offset:24640\n"
"s_waitcnt lgkmcnt(0)\n"
"v_mfma_f32_16x16x32_bf16 v[112:115], a[96:99], a[220:223], v[112:115]\n"
"ds_read_b64_tr_b16 a[100:101], v83 offset:64\n"
"ds_read_b64_tr_b16 a[102:103], v83 offset:8256\n"
"s_waitcnt lgkmcnt(0)\n"
"v_mfma_f32_16x16x32_bf16 v[116:119], a[100:103], a[220:223], 0\n"
"ds_read_b64_tr_b16 a[104:105], v83 offset:16448\n"
"ds_read_b64_tr_b16 a[106:107], v83 offset:24640\n"
"s_waitcnt lgkmcnt(0)\n"
"v_mfma_f32_16x16x32_bf16 v[116:119], a[104:107], a[220:223], v[116:119]\n"
"ds_read_b128 a[108:111], v12 offset:38912\n"
"ds_read_b128 a[112:115], v12 offset:40960\n"
"s_waitcnt lgkmcnt(1)\n"
"v_mfma_f32_16x16x32_bf16 v[152:155], a[16:19], a[108:111], v[152:155]\n"
"ds_read_b128 a[116:119], v12 offset:43008\n"
"ds_read_b128 a[120:123], v12 offset:45056\n"
"s_waitcnt lgkmcnt(0)\n"
"v_mfma_f32_16x16x32_bf16 v[160:163], a[16:19], a[112:115], v[160:163]\n"
"ds_read_b128 a[124:127], v12 offset:47104\n"
"ds_read_b128 a[64:67], v13 offset:32768\n"
"v_mfma_f32_16x16x32_bf16 v[168:171], a[16:19], a[116:119], v[168:171]\n"
"ds_read_b128 a[68:71], v13 offset:34816\n"
"ds_read_b128 a[72:75], v13 offset:36864\n"
"v_mfma_f32_16x16x32_bf16 v[176:179], a[16:19], a[120:123], v[176:179]\n"
"ds_read_b128 a[76:79], v13 offset:38912\n"
"ds_read_b128 a[80:83], v13 offset:40960\n"
"s_waitcnt lgkmcnt(5)\n"
"v_mfma_f32_16x16x32_bf16 v[184:187], a[16:19], a[124:127], v[184:187]\n"
"ds_read_b128 a[84:87], v13 offset:43008\n"
"ds_read_b128 a[88:91], v13 offset:45056\n"
"s_waitcnt lgkmcnt(3)\n"
"v_mfma_f32_16x16x32_bf16 v[128:131], a[24:27], a[64:67], v[128:131]\n"
"ds_read_b128 a[92:95], v13 offset:47104\n"
"ds_read_b128 a[96:99], v14 offset:0\n"
"v_mfma_f32_16x16x32_bf16 v[136:139], a[24:27], a[68:71], v[136:139]\n"
"ds_read_b128 a[100:103], v14 offset:2048\n"
"ds_read_b128 a[104:107], v14 offset:4096\n"
"v_mfma_f32_16x16x32_bf16 v[144:147], a[24:27], a[72:75], v[144:147]\n"
"ds_read_b128 a[108:111], v14 offset:6144\n"
"ds_read_b128 a[112:115], v14 offset:8192\n"
"v_mfma_f32_16x16x32_bf16 v[152:155], a[24:27], a[76:79], v[152:155]\n"
"ds_read_b128 a[116:119], v14 offset:10240\n"
"s_waitcnt lgkmcnt(6)\n"
"v_mfma_f32_16x16x32_bf16 v[160:163], a[24:27], a[80:83], v[160:163]\n"
"ds_read_b128 a[120:123], v14 offset:12288\n"
"ds_read_b128 a[176:179], v80 offset:0\n"
"v_mfma_f32_16x16x32_bf16 v[168:171], a[24:27], a[84:87], v[168:171]\n"
"ds_read_b128 a[124:127], v14 offset:14336\n"
"ds_read_b128 a[180:183], v80 offset:4096\n"
"v_mfma_f32_16x16x32_bf16 v[176:179], a[24:27], a[88:91], v[176:179]\n"
"ds_read_b128 a[64:67], v15 offset:0\n"
"ds_read_b128 a[184:187], v80 offset:8192\n"
"v_mfma_f32_16x16x32_bf16 v[184:187], a[24:27], a[92:95], v[184:187]\n"
"ds_read_b128 a[68:71], v15 offset:2048\n"
"s_waitcnt lgkmcnt(9)\n"
"v_mfma_f32_16x16x32_bf16 v[128:131], a[32:35], a[96:99], v[128:131]\n"
"ds_read_b128 a[72:75], v15 offset:4096\n"
"ds_read_b128 a[188:191], v80 offset:12288\n"
"v_mfma_f32_16x16x32_bf16 v[136:139], a[32:35], a[100:103], v[136:139]\n"
"ds_read_b128 a[76:79], v15 offset:6144\n"
"ds_read_b128 a[192:195], v80 offset:16384\n"
"v_mfma_f32_16x16x32_bf16 v[144:147], a[32:35], a[104:107], v[144:147]\n"
"ds_read_b128 a[80:83], v15 offset:8192\n"
"ds_read_b128 a[196:199], v80 offset:20480\n"
"s_waitcnt vmcnt(5)\n"
"buffer_store_dwordx2 v[28:29], v30, s[36:39], s84 offen offset:0 sc1\n"
"v_mfma_f32_16x16x32_bf16 v[152:155], a[32:35], a[108:111], v[152:155]\n"
"s_waitcnt lgkmcnt(14)\n"
"ds_read_b128 a[84:87], v15 offset:10240\n"
"s_waitcnt lgkmcnt(11)\n"
"v_mfma_f32_16x16x32_bf16 v[160:163], a[32:35], a[112:115], v[160:163]\n"
"ds_read_b128 a[88:91], v15 offset:12288\n"
"ds_read_b128 a[200:203], v80 offset:24576\n"
"v_mfma_f32_16x16x32_bf16 v[168:171], a[32:35], a[116:119], v[168:171]\n"
"ds_read_b128 a[92:95], v15 offset:14336\n"
"ds_read_b128 a[204:207], v80 offset:28672\n"
"v_mfma_f32_16x16x32_bf16 v[176:179], a[32:35], a[120:123], v[176:179]\n"
"s_waitcnt lgkmcnt(14)\n"
"ds_read_b128 a[96:99], v14 offset:32768\n"
"v_mfma_f32_16x16x32_bf16 v[184:187], a[32:35], a[124:127], v[184:187]\n"
"s_waitcnt lgkmcnt(14)\n"
"ds_read_b128 a[100:103], v14 offset:34816\n"
"s_waitcnt lgkmcnt(10)\n"
"v_mfma_f32_16x16x32_bf16 v[128:131], a[40:43], a[64:67], v[128:131]\n"
"ds_read_b128 a[104:107], v15 offset:32768\n"
"s_mul_i32 s75, s31, 229376\n"
"s_mul_i32 s76, s65, 448\n"
"v_mfma_f32_16x16x32_bf16 v[136:139], a[40:43], a[68:71], v[136:139]\n"
"ds_read_b128 a[108:111], v15 offset:34816\n"
"s_add_u32 s75, s75, s76\n"
"s_add_u32 s40, s18, s75\n"
"v_mfma_f32_16x16x32_bf16 v[144:147], a[40:43], a[72:75], v[144:147]\n"
"ds_read_b128 a[112:115], v14 offset:36864\n"
"s_addc_u32 s41, s19, 0\n"
"s_sub_u32 s42, 7340032, s75\n"
"v_mfma_f32_16x16x32_bf16 v[152:155], a[40:43], a[76:79], v[152:155]\n"
"ds_read_b128 a[116:119], v14 offset:38912\n"
"v_and_b32 v100, 15, v1\n"
"s_waitcnt lgkmcnt(7)\n"
"v_mfma_f32_16x16x32_bf16 v[160:163], a[40:43], a[80:83], v[160:163]\n"
"ds_read_b128 a[120:123], v15 offset:36864\n"
"v_subrev_u32 v101, 1, v100\n"
"v_cmp_gt_u32 vcc, s64, v101\n"
"v_mfma_f32_16x16x32_bf16 v[168:171], a[40:43], a[84:87], v[168:171]\n"
"ds_read_b128 a[124:127], v15 offset:38912\n"
"v_cndmask_b32 v101, v100, v101, vcc\n"
"v_add_u32 v103, s64, v100\n"
"v_mfma_f32_16x16x32_bf16 v[176:179], a[40:43], a[88:91], v[176:179]\n"
"ds_read_b128 a[64:67], v14 offset:40960\n"
"v_cmp_gt_u32 vcc, 1, v100\n"
"v_mfma_f32_16x16x32_bf16 v[184:187], a[40:43], a[92:95], v[184:187]\n"
"ds_read_b128 a[68:71], v14 offset:43008\n"
"s_waitcnt lgkmcnt(6)\n"
"v_mfma_f32_16x16x32_bf16 v[128:131], a[48:51], a[96:99], v[128:131]\n"
"ds_read_b128 a[72:75], v15 offset:40960\n"
"v_cndmask_b32 v101, v101, v103, vcc\n"
"v_mfma_f32_16x16x32_bf16 v[136:139], a[48:51], a[100:103], v[136:139]\n"
"ds_read_b128 a[76:79], v15 offset:43008\n"
"v_lshrrev_b32 v102, 1, v101\n"
"v_mfma_f32_16x16x32_bf16 v[128:131], a[56:59], a[104:107], v[128:131]\n"
"ds_read_b128 a[80:83], v14 offset:45056\n"
"v_lshlrev_b32 v102, 3, v102\n"
"v_mfma_f32_16x16x32_bf16 v[136:139], a[56:59], a[108:111], v[136:139]\n"
"ds_read_b128 a[84:87], v14 offset:47104\n"
"s_waitcnt lgkmcnt(6)\n"
"v_mfma_f32_16x16x32_bf16 v[144:147], a[48:51], a[112:115], v[144:147]\n"
"ds_read_b128 a[88:91], v15 offset:45056\n"
"v_and_b32 v20, 1, v101\n"
"v_mfma_f32_16x16x32_bf16 v[152:155], a[48:51], a[116:119], v[152:155]\n"
"ds_read_b128 a[92:95], v15 offset:47104\n"
"v_add_u32 v102, v102, v20\n"
"v_mfma_f32_16x16x32_bf16 v[144:147], a[56:59], a[120:123], v[144:147]\n"
"v_mul_u32_u24 v20, 114688, v102\n"
"v_lshlrev_b32 v101, 16, v101\n"
"v_mfma_f32_16x16x32_bf16 v[152:155], a[56:59], a[124:127], v[152:155]\n"
"v_cmp_gt_u32 vcc, 16, v1\n"
"s_waitcnt lgkmcnt(4)\n"
"v_mfma_f32_16x16x32_bf16 v[160:163], a[48:51], a[64:67], v[160:163]\n"
"v_cndmask_b32 v20, v101, v20, vcc\n"
"s_nop 0\n"
"v_readlane_b32 s79, v20, 0\n"
"v_mfma_f32_16x16x32_bf16 v[168:171], a[48:51], a[68:71], v[168:171]\n"
"buffer_load_dwordx2 v[122:123], v30, s[36:39], s69 offen offset:8 sc1\n"
"v_mfma_f32_16x16x32_bf16 v[160:163], a[56:59], a[72:75], v[160:163]\n"
"v_mfma_f32_16x16x32_bf16 v[168:171], a[56:59], a[76:79], v[168:171]\n"
"s_waitcnt lgkmcnt(0)\n"
"v_mfma_f32_16x16x32_bf16 v[176:179], a[48:51], a[80:83], v[176:179]\n"
"v_mfma_f32_16x16x32_bf16 v[184:187], a[48:51], a[84:87], v[184:187]\n"
"buffer_load_dwordx2 v[126:127], v30, s[36:39], s69 offen offset:24 sc1\n"
"v_mfma_f32_16x16x32_bf16 v[176:179], a[56:59], a[88:91], v[176:179]\n"
"v_mfma_f32_16x16x32_bf16 v[184:187], a[56:59], a[92:95], v[184:187]\n"
"v_mfma_f32_16x16x32_bf16 v[128:131], a[216:219], a[176:179], v[128:131]\n"
"v_mfma_f32_16x16x32_bf16 v[136:139], a[216:219], a[180:183], v[136:139]\n"
"v_mfma_f32_16x16x32_bf16 v[144:147], a[216:219], a[184:187], v[144:147]\n"
"v_mfma_f32_16x16x32_bf16 v[152:155], a[216:219], a[188:191], v[152:155]\n"
"v_mfma_f32_16x16x32_bf16 v[160:163], a[216:219], a[192:195], v[160:163]\n"
"v_mfma_f32_16x16x32_bf16 v[168:171], a[216:219], a[196:199], v[168:171]\n"
"v_mfma_f32_16x16x32_bf16 v[176:179], a[216:219], a[200:203], v[176:179]\n"
"v_mfma_f32_16x16x32_bf16 v[184:187], a[216:219], a[204:207], v[184:187]\n"
"s_waitcnt vmcnt(3)\n"
"buffer_store_dwordx2 v[28:29], v30, s[36:39], s83 offen offset:0 sc1\n"
"s_waitcnt vmcnt(2)\n"
"v_cmp_ne_u64 vcc, s[70:71], v[122:123]\n"
"s_cbranch_vccnz .Lpslow3_%=\n"
".Lpok3_%=:\n"
"s_add_u32 s77, s97, 16384\n"
"buffer_load_dwordx4 a[128:131], v8, s[36:39], s77 offen offset:0 sc1\n"
"buffer_load_dwordx4 a[132:135], v8, s[36:39], s77 offen offset:1024 sc1\n"
"buffer_load_dwordx4 a[136:139], v8, s[36:39], s77 offen offset:2048 sc1\n"
"buffer_load_dwordx4 a[140:143], v8, s[36:39], s77 offen offset:3072 sc1\n"
"s_barrier\n"
"s_lshl_b32 s75, s31, 12\n"
"s_add_u32 s75, s75, 131072\n"
"v_add_u32 v101, s75, v8\n"
"s_lshr_b32 s77, s31, 1\n"
"s_and_b32 s78, s31, 1\n"
"s_lshl_b32 s75, s77, 10\n"
"s_lshl_b32 s76, s78, 3\n"
"s_add_u32 s75, s75, s76\n"
"s_add_u32 s75, s75, 131072\n"
"v_add_u32 v100, s75, v8\n"
"v_mov_b32 v10, v92\n"
"v_mov_b32 v11, v93\n"
"v_add_u32 v12, 0x10000, v10\n"
"v_add_u32 v13, 0x10000, v11\n"
"s_lshl_b32 s86, s63, 9\n"
"s_lshl_b32 s76, s31, 7\n"
"s_add_u32 s86, s86, s76\n"
"s_add_u32 s86, s86, 12288\n"
"s_lshl3_add_u32 s85, s64, s86\n"
"s_lshl_b32 s75, s63, 20\n"
"s_lshl_b32 s76, s31, 12\n"
"s_add_u32 s75, s75, s76\n"
"s_add_u32 s75, s75, 9453568\n"
"s_add_u32 s52, s28, s75\n"
"s_addc_u32 s53, s29, 0\n"
"s_sub_u32 s54, 68157440, s75\n"
"v_readlane_b32 s82, v20, 17\n"
"s_mul_i32 s75, s66, 1835008\n"
"s_mul_i32 s76, s63, 114688\n"
"s_add_u32 s75, s75, s76\n"
"s_mul_i32 s76, s31, 28672\n"
"s_add_u32 s75, s75, s76\n"
"s_lshl_b32 s76, s67, 9\n"
"s_add_u32 s75, s75, s76\n"
"s_add_u32 s56, s22, s75\n"
"s_addc_u32 s57, s23, 0\n"
"s_sub_u32 s58, 7340032, s75\n"
"s_mov_b32 s95, 0\n"
"v_lshrrev_b32 v24, 3, v1\n"
"v_min_u32 v24, 6, v24\n"
"v_lshlrev_b32 v24, 11, v24\n"
"v_bfe_u32 v121, v1, 1, 2\n"
"v_lshl_add_u32 v24, v121, 7, v24\n"
"v_lshrrev_b32 v102, 5, v1\n"
"v_bfe_u32 v103, v1, 1, 4\n"
"v_and_b32 v120, 1, v1\n"
"v_lshlrev_b32 v120, 4, v120\n"
"s_lshl_b32 s75, s66, 2\n"
"v_add_u32 v121, 0, v102\n"
"v_xor_b32 v122, v121, v103\n"
"v_add_u32 v122, s75, v122\n"
"v_and_b32 v122, 15, v122\n"
"v_lshl_add_u32 v122, v122, 5, v120\n"
"v_lshl_add_u32 v122, v121, 11, v122\n"
"v_subrev_u32 v4, 0, v122\n"
"v_add_u32 v16, 16384, v4\n"
"v_add_u32 v121, 2, v102\n"
"v_xor_b32 v122, v121, v103\n"
"v_add_u32 v122, s75, v122\n"
"v_and_b32 v122, 15, v122\n"
"v_lshl_add_u32 v122, v122, 5, v120\n"
"v_lshl_add_u32 v122, v121, 11, v122\n"
"v_subrev_u32 v5, 1024, v122\n"
"v_add_u32 v17, 16384, v5\n"
"v_add_u32 v121, 4, v102\n"
"v_xor_b32 v122, v121, v103\n"
"v_add_u32 v122, s75, v122\n"
"v_and_b32 v122, 15, v122\n"
"v_lshl_add_u32 v122, v122, 5, v120\n"
"v_lshl_add_u32 v122, v121, 11, v122\n"
"v_subrev_u32 v6, 2048, v122\n"
"v_add_u32 v18, 16384, v6\n"
"v_add_u32 v121, 6, v102\n"
"v_xor_b32 v122, v121, v103\n"
"v_add_u32 v122, s75, v122\n"
"v_and_b32 v122, 15, v122\n"
"v_lshl_add_u32 v122, v122, 5, v120\n"
"v_lshl_add_u32 v122, v121, 11, v122\n"
"v_subrev_u32 v7, 3072, v122\n"
"v_add_u32 v19, 16384, v7\n"
"s_mul_i32 s75, s66, 1835008\n"
"s_lshl_b32 s76, s67, 9\n"
"s_add_u32 s75, s75, s76\n"
"s_add_u32 s4, s22, s75\n"
"s_addc_u32 s5, s23, 0\n"
"s_sub_u32 s6, 7340032, s75\n"
"s_lshl_b32 s75, s67, 9\n"
"s_lshl_b32 s76, s66, 7\n"
"s_add_u32 s75, s75, s76\n"
"s_add_u32 s44, s24, s75\n"
"s_addc_u32 s45, s25, 0\n"
"s_sub_u32 s46, 2048, s75\n"
"s_mul_i32 s75, s65, 448\n"
"s_add_u32 s48, s20, s75\n"
"s_addc_u32 s49, s21, 0\n"
"s_sub_u32 s50, 7168, s75\n"
"s_mul_i32 s93, s63, 917504\n"
"s_mul_i32 s76, s31, 229376\n"
"s_add_u32 s93, s93, s76\n"
"s_mul_i32 s76, s65, 14336\n"
"s_add_u32 s93, s93, s76\n"
"s_add_u32 s93, s93, 26214400\n"
"v_lshrrev_b32 v9, 4, v1\n"
"v_lshlrev_b32 v22, 3, v9\n"
"v_lshlrev_b32 v9, 4, v9\n"
"v_add_u32 v9, 147456, v9\n"
"s_lshl_b32 s80, s63, 14\n"
"s_lshl_b32 s76, s31, 12\n"
"s_add_u32 s80, s80, s76\n"
"s_add_u32 s80, s80, 131072\n"
"s_lshl_b32 s96, s65, 9\n"
"s_add_u32 s96, s96, 28672\n"
"s_lshl_b32 s81, s65, 14\n"
"s_add_u32 s81, s81, 393216\n"
"v_lshlrev_b32 v25, 3, v1\n"
"v_min_u32 v14, 55, v1\n"
"s_mul_i32 s75, s31, 56\n"
"v_add_u32 v14, s75, v14\n"
"v_lshlrev_b32 v15, 2, v14\n"
"v_lshlrev_b32 v14, 1, v14\n"
"s_lshr_b32 s75, s31, 1\n"
"v_lshl_add_u32 v102, v1, 1, s75\n"
"v_subrev_u32 v121, 16, v1\n"
"v_cmp_gt_u32 vcc, 16, v1\n"
"s_nop 1\n"
"v_cndmask_b32 v102, v121, v102, vcc\n"
"s_mul_i32 s78, s67, 7\n"
"v_subrev_u32 v121, 7, v102\n"
"v_cmp_gt_u32 vcc, s78, v121\n"
"s_nop 1\n"
"v_cndmask_b32 v121, v102, v121, vcc\n"
"v_add_u32 v103, s78, v102\n"
"v_cmp_gt_u32 vcc, 7, v102\n"
"s_nop 1\n"
"v_cndmask_b32 v121, v121, v103, vcc\n"
"s_and_b32 s75, s31, 1\n"
"v_lshl_add_u32 v102, v121, 1, s75\n"
"v_lshlrev_b32 v102, 15, v102\n"
"v_lshlrev_b32 v121, 11, v121\n"
"v_cmp_gt_u32 vcc, 16, v1\n"
"s_nop 1\n"
"v_cndmask_b32 v21, v121, v102, vcc\n"
"s_sleep 2\n"
"s_waitcnt vmcnt(5)\n"
"v_cmp_ne_u64 vcc, s[70:71], v[126:127]\n"
"s_cbranch_vccnz .Lpslow4_%=\n"
".Lpok4_%=:\n"
"buffer_load_dwordx2 v[124:125], v30, s[36:39], s69 offen offset:16 sc1\n"
"s_add_u32 s77, s97, 49152\n"
"buffer_load_dwordx4 a[160:163], v8, s[36:39], s77 offen offset:0 sc1\n"
"buffer_load_dwordx4 a[164:167], v8, s[36:39], s77 offen offset:1024 sc1\n"
"buffer_load_dwordx4 a[168:171], v8, s[36:39], s77 offen offset:2048 sc1\n"
"buffer_load_dwordx4 a[172:175], v8, s[36:39], s77 offen offset:3072 sc1\n"
"s_add_u32 m0, s74, 0\n"
"s_nop 0\n"
"buffer_load_dwordx4 v64, s[40:43], s79 offen offset:0 lds\n"
"buffer_load_dwordx4 v65, s[40:43], s79 offen offset:1024 lds\n"
"buffer_load_dwordx4 v66, s[40:43], s79 offen offset:2048 lds\n"
"buffer_load_dwordx4 v67, s[40:43], s79 offen offset:3072 lds\n"
"s_add_u32 m0, s74, 4096\n"
"s_nop 0\n"
"buffer_load_dwordx4 v68, s[40:43], s79 offen offset:0 lds\n"
"buffer_load_dwordx4 v69, s[40:43], s79 offen offset:1024 lds\n"
"buffer_load_dwordx4 v70, s[40:43], s79 offen offset:2048 lds\n"
"buffer_load_dwordx4 v71, s[40:43], s79 offen offset:3072 lds\n"
"v_readlane_b32 s79, v20, 1\n"
"s_lshl_b32 s77, s65, 14\n"
"s_lshl_b32 s76, s63, 10\n"
"s_add_u32 s77, s77, s76\n"
"s_add_u32 s77, s77, 393216\n"
"buffer_store_dwordx4 v[104:107], v81, s[36:39], s77 offen offset:0 sc1\n"
"buffer_store_dwordx4 v[108:111], v81, s[36:39], s77 offen offset:64 sc1\n"
"buffer_store_dwordx4 v[112:115], v81, s[36:39], s77 offen offset:128 sc1\n"
"buffer_store_dwordx4 v[116:119], v81, s[36:39], s77 offen offset:192 sc1\n"
"s_lshl_b32 s94, s65, 9\n"
"s_lshl_b32 s76, s63, 5\n"
"s_add_u32 s94, s94, s76\n"
"s_lshl_b32 s76, s31, 3\n"
"s_add_u32 s94, s94, s76\n"
"s_add_u32 s94, s94, 28672\n"
"s_waitcnt vmcnt(20)\n"
"v_mfma_f32_16x16x32_bf16 v[128:131], a[210:213], a[128:131], v[128:131]\n"
"v_mfma_f32_16x16x32_bf16 v[136:139], a[208:211], a[128:131], v[136:139]\n"
"s_waitcnt vmcnt(19)\n"
"v_mfma_f32_16x16x32_bf16 v[144:147], a[210:213], a[132:135], v[144:147]\n"
"v_mfma_f32_16x16x32_bf16 v[152:155], a[208:211], a[132:135], v[152:155]\n"
"s_waitcnt vmcnt(18)\n"
"v_mfma_f32_16x16x32_bf16 v[160:163], a[210:213], a[136:139], v[160:163]\n"
"v_mfma_f32_16x16x32_bf16 v[168:171], a[208:211], a[136:139], v[168:171]\n"
"s_waitcnt vmcnt(17)\n"
"v_mfma_f32_16x16x32_bf16 v[176:179], a[210:213], a[140:143], v[176:179]\n"
"v_mfma_f32_16x16x32_bf16 v[184:187], a[208:211], a[140:143], v[184:187]\n"
"s_waitcnt vmcnt(16)\n"
"v_cmp_ne_u64 vcc, s[70:71], v[124:125]\n"
"s_cbranch_vccnz .Lpslow5_%=\n"
".Lpok5_%=:\n"
"s_add_u32 s77, s97, 32768\n"
"buffer_load_dwordx4 a[144:147], v8, s[36:39], s77 offen offset:0 sc1\n"
"buffer_load_dwordx4 a[148:151], v8, s[36:39], s77 offen offset:1024 sc1\n"
"buffer_load_dwordx4 a[152:155], v8, s[36:39], s77 offen offset:2048 sc1\n"
"buffer_load_dwordx4 a[156:159], v8, s[36:39], s77 offen offset:3072 sc1\n"
"s_waitcnt vmcnt(19)\n"
"v_mfma_f32_16x16x32_bf16 v[128:131], a[210:213], a[160:163], v[128:131]\n"
"v_mfma_f32_16x16x32_bf16 v[136:139], a[208:211], a[160:163], v[136:139]\n"
"s_waitcnt vmcnt(18)\n"
"v_mfma_f32_16x16x32_bf16 v[144:147], a[210:213], a[164:167], v[144:147]\n"
"v_mfma_f32_16x16x32_bf16 v[152:155], a[208:211], a[164:167], v[152:155]\n"
"s_waitcnt vmcnt(17)\n"
"v_mfma_f32_16x16x32_bf16 v[160:163], a[210:213], a[168:171], v[160:163]\n"
"v_mfma_f32_16x16x32_bf16 v[168:171], a[208:211], a[168:171], v[168:171]\n"
"s_waitcnt vmcnt(16)\n"
"v_mfma_f32_16x16x32_bf16 v[176:179], a[210:213], a[172:175], v[176:179]\n"
"v_mfma_f32_16x16x32_bf16 v[184:187], a[208:211], a[172:175], v[184:187]\n"
"s_waitcnt vmcnt(3)\n"
"v_mfma_f32_16x16x32_bf16 v[128:131], a[210:213], a[144:147], v[128:131]\n"
"v_mfma_f32_16x16x32_bf16 v[136:139], a[208:211], a[144:147], v[136:139]\n"
"s_waitcnt vmcnt(2)\n"
"v_mfma_f32_16x16x32_bf16 v[144:147], a[210:213], a[148:151], v[144:147]\n"
"v_mfma_f32_16x16x32_bf16 v[152:155], a[208:211], a[148:151], v[152:155]\n"
"s_nop 3\n"
"v_cvt_pk_bf16_f32 v32, v128, v129\n"
"v_cvt_pk_bf16_f32 v33, v130, v131\n"
"ds_write_b64 v100, v[32:33] offset:0\n"
"v_cvt_pk_bf16_f32 v34, v136, v137\n"
"v_cvt_pk_bf16_f32 v35, v138, v139\n"
"ds_write_b64 v100, v[34:35] offset:2048\n"
"s_waitcnt vmcnt(1)\n"
"v_mfma_f32_16x16x32_bf16 v[160:163], a[210:213], a[152:155], v[160:163]\n"
"v_mfma_f32_16x16x32_bf16 v[168:171], a[208:211], a[152:155], v[168:171]\n"
"v_cvt_pk_bf16_f32 v36, v144, v145\n"
"v_cvt_pk_bf16_f32 v37, v146, v147\n"
"ds_write_b64 v100, v[36:37] offset:4096\n"
"v_cvt_pk_bf16_f32 v38, v152, v153\n"
"v_cvt_pk_bf16_f32 v39, v154, v155\n"
"ds_write_b64 v100, v[38:39] offset:6144\n"
"s_waitcnt vmcnt(0)\n"
"v_mfma_f32_16x16x32_bf16 v[176:179], a[210:213], a[156:159], v[176:179]\n"
"v_mfma_f32_16x16x32_bf16 v[184:187], a[208:211], a[156:159], v[184:187]\n"
"v_cvt_pk_bf16_f32 v40, v160, v161\n"
"v_cvt_pk_bf16_f32 v41, v162, v163\n"
"ds_write_b64 v100, v[40:41] offset:8192\n"
"v_cvt_pk_bf16_f32 v42, v168, v169\n"
"v_cvt_pk_bf16_f32 v43, v170, v171\n"
"ds_write_b64 v100, v[42:43] offset:10240\n"
"s_nop 0\n"
"v_cvt_pk_bf16_f32 v44, v176, v177\n"
"v_cvt_pk_bf16_f32 v45, v178, v179\n"
"ds_write_b64 v100, v[44:45] offset:12288\n"
"v_cvt_pk_bf16_f32 v46, v184, v185\n"
"v_cvt_pk_bf16_f32 v47, v186, v187\n"
"ds_write_b64 v100, v[46:47] offset:14336\n"
"s_add_u32 s97, s97, 16384\n"
"s_waitcnt lgkmcnt(0)\n"
"s_barrier\n"
"ds_read_b128 a[0:3], v101 offset:0\n"
"ds_read_b128 a[4:7], v101 offset:2048\n"
"ds_read_b128 a[8:11], v101 offset:1024\n"
"ds_read_b128 a[12:15], v101 offset:3072\n"
"s_waitcnt lgkmcnt(3)\n"
"buffer_store_dwordx4 a[0:3], v8, s[36:39], s97 offen offset:0 sc1\n"
"s_waitcnt lgkmcnt(2)\n"
"buffer_store_dwordx4 a[4:7], v8, s[36:39], s97 offen offset:1024 sc1\n"
"s_waitcnt lgkmcnt(1)\n"
"buffer_store_dwordx4 a[8:11], v8, s[36:39], s97 offen offset:2048 sc1\n"
"s_waitcnt lgkmcnt(0)\n"
"buffer_store_dwordx4 a[12:15], v8, s[36:39], s97 offen offset:3072 sc1\n"
"ds_read_b128 v[48:51], v101 offset:0\n"
"ds_read_b128 v[56:59], v101 offset:2048\n"
"ds_read_b128 v[52:55], v101 offset:1024\n"
"ds_read_b128 v[60:63], v101 offset:3072\n"
"v_lshrrev_b32 v31, 2, v1\n"
"v_lshlrev_b32 v31, 3, v31\n"
"ds_read_b64_tr_b16 a[96:97], v10 offset:0\n"
"ds_read_b64_tr_b16 a[98:99], v10 offset:8192\n"
"ds_read_b64_tr_b16 a[100:101], v11 offset:0\n"
"ds_read_b64_tr_b16 a[102:103], v11 offset:8192\n"
"ds_read_b64_tr_b16 a[104:105], v10 offset:64\n"
"ds_read_b64_tr_b16 a[106:107], v10 offset:8256\n"
"ds_read_b64_tr_b16 a[108:109], v11 offset:64\n"
"ds_read_b64_tr_b16 a[110:111], v11 offset:8256\n"
"ds_read_b64_tr_b16 a[112:113], v10 offset:128\n"
"ds_read_b64_tr_b16 a[114:115], v10 offset:8320\n"
"ds_read_b64_tr_b16 a[116:117], v11 offset:128\n"
"s_waitcnt lgkmcnt(14)\n"
"ds_read_b64_tr_b16 a[118:119], v11 offset:8320\n"
"s_waitcnt lgkmcnt(14)\n"
"ds_read_b64_tr_b16 a[120:121], v10 offset:192\n"
"s_waitcnt lgkmcnt(14)\n"
"ds_read_b64_tr_b16 a[122:123], v10 offset:8384\n"
"s_waitcnt lgkmcnt(6)\n"
"v_mfma_f32_16x16x32_bf16 v[128:131], a[96:99], a[0:3], 0\n"
"ds_read_b64_tr_b16 a[124:125], v11 offset:192\n"
"s_add_u32 m0, s74, 32768\n"
"v_mfma_f32_16x16x32_bf16 v[132:135], a[96:99], a[4:7], 0\n"
"ds_read_b64_tr_b16 a[126:127], v11 offset:8384\n"
"buffer_load_dwordx4 v64, s[40:43], s79 offen offset:0 lds\n"
"v_mfma_f32_16x16x32_bf16 v[140:143], a[100:103], a[4:7], 0\n"
"ds_read_b64_tr_b16 a[64:65], v10 offset:256\n"
"v_mfma_f32_16x16x32_bf16 v[136:139], a[100:103], a[0:3], 0\n"
"ds_read_b64_tr_b16 a[66:67], v10 offset:8448\n"
"v_mfma_f32_16x16x32_bf16 v[144:147], a[104:107], a[0:3], 0\n"
"ds_read_b64_tr_b16 a[68:69], v11 offset:256\n"
"v_mfma_f32_16x16x32_bf16 v[148:151], a[104:107], a[4:7], 0\n"
"ds_read_b64_tr_b16 a[70:71], v11 offset:8448\n"
"buffer_load_dwordx4 v65, s[40:43], s79 offen offset:1024 lds\n"
"v_mfma_f32_16x16x32_bf16 v[156:159], a[108:111], a[4:7], 0\n"
"ds_read_b64_tr_b16 a[72:73], v10 offset:320\n"
"v_mfma_f32_16x16x32_bf16 v[152:155], a[108:111], a[0:3], 0\n"
"ds_read_b64_tr_b16 a[74:75], v10 offset:8512\n"
"s_waitcnt lgkmcnt(6)\n"
"v_mfma_f32_16x16x32_bf16 v[160:163], a[112:115], a[0:3], 0\n"
"ds_read_b64_tr_b16 a[76:77], v11 offset:320\n"
"v_mfma_f32_16x16x32_bf16 v[164:167], a[112:115], a[4:7], 0\n"
"ds_read_b64_tr_b16 a[78:79], v11 offset:8512\n"
"buffer_load_dwordx4 v66, s[40:43], s79 offen offset:2048 lds\n"
"v_mfma_f32_16x16x32_bf16 v[172:175], a[116:119], a[4:7], 0\n"
"ds_read_b64_tr_b16 a[80:81], v10 offset:384\n"
"v_mfma_f32_16x16x32_bf16 v[168:171], a[116:119], a[0:3], 0\n"
"ds_read_b64_tr_b16 a[82:83], v10 offset:8576\n"
"v_mfma_f32_16x16x32_bf16 v[176:179], a[120:123], a[0:3], 0\n"
"ds_read_b64_tr_b16 a[84:85], v11 offset:384\n"
"v_mfma_f32_16x16x32_bf16 v[180:183], a[120:123], a[4:7], 0\n"
"ds_read_b64_tr_b16 a[86:87], v11 offset:8576\n"
"v_mfma_f32_16x16x32_bf16 v[188:191], a[124:127], a[4:7], 0\n"
"ds_read_b64_tr_b16 a[88:89], v10 offset:16384\n"
"buffer_load_dwordx4 v67, s[40:43], s79 offen offset:3072 lds\n"
"v_mfma_f32_16x16x32_bf16 v[184:187], a[124:127], a[0:3], 0\n"
"ds_read_b64_tr_b16 a[90:91], v10 offset:24576\n"
"s_waitcnt lgkmcnt(6)\n"
"v_mfma_f32_16x16x32_bf16 v[192:195], a[64:67], a[0:3], 0\n"
"ds_read_b64_tr_b16 a[92:93], v11 offset:16384\n"
"v_mfma_f32_16x16x32_bf16 v[196:199], a[64:67], a[4:7], 0\n"
"ds_read_b64_tr_b16 a[94:95], v11 offset:24576\n"
"s_add_u32 m0, s74, 36864\n"
"v_mfma_f32_16x16x32_bf16 v[204:207], a[68:71], a[4:7], 0\n"
"ds_read_b64_tr_b16 a[96:97], v10 offset:16448\n"
"buffer_load_dwordx4 v68, s[40:43], s79 offen offset:0 lds\n"
"v_mfma_f32_16x16x32_bf16 v[200:203], a[68:71], a[0:3], 0\n"
"ds_read_b64_tr_b16 a[98:99], v10 offset:24640\n"
"v_mfma_f32_16x16x32_bf16 v[208:211], a[72:75], a[0:3], 0\n"
"ds_read_b64_tr_b16 a[100:101], v11 offset:16448\n"
"v_mfma_f32_16x16x32_bf16 v[212:215], a[72:75], a[4:7], 0\n"
"ds_read_b64_tr_b16 a[102:103], v11 offset:24640\n"
"v_mfma_f32_16x16x32_bf16 v[220:223], a[76:79], a[4:7], 0\n"
"ds_read_b64_tr_b16 a[104:105], v10 offset:16512\n"
"buffer_load_dwordx4 v69, s[40:43], s79 offen offset:1024 lds\n"
"v_mfma_f32_16x16x32_bf16 v[216:219], a[76:79], a[0:3], 0\n"
"ds_read_b64_tr_b16 a[106:107], v10 offset:24704\n"
"s_waitcnt lgkmcnt(6)\n"
"v_mfma_f32_16x16x32_bf16 v[224:227], a[80:83], a[0:3], 0\n"
"ds_read_b64_tr_b16 a[108:109], v11 offset:16512\n"
"v_mfma_f32_16x16x32_bf16 v[228:231], a[80:83], a[4:7], 0\n"
"ds_read_b64_tr_b16 a[110:111], v11 offset:24704\n"
"v_mfma_f32_16x16x32_bf16 v[236:239], a[84:87], a[4:7], 0\n"
"ds_read_b64_tr_b16 a[112:113], v10 offset:16576\n"
"v_mfma_f32_16x16x32_bf16 v[232:235], a[84:87], a[0:3], 0\n"
"ds_read_b64_tr_b16 a[114:115], v10 offset:24768\n"
"buffer_load_dwordx4 v70, s[40:43], s79 offen offset:2048 lds\n"
"v_mfma_f32_16x16x32_bf16 v[128:131], a[88:91], a[8:11], v[128:131]\n"
"ds_read_b64_tr_b16 a[116:117], v11 offset:16576\n"
"v_mfma_f32_16x16x32_bf16 v[132:135], a[88:91], a[12:15], v[132:135]\n"
"ds_read_b64_tr_b16 a[118:119], v11 offset:24768\n"
"v_mfma_f32_16x16x32_bf16 v[140:143], a[92:95], a[12:15], v[140:143]\n"
"ds_read_b64_tr_b16 a[120:121], v10 offset:16640\n"
"v_mfma_f32_16x16x32_bf16 v[136:139], a[92:95], a[8:11], v[136:139]\n"
"ds_read_b64_tr_b16 a[122:123], v10 offset:24832\n"
"s_waitcnt lgkmcnt(6)\n"
"v_mfma_f32_16x16x32_bf16 v[144:147], a[96:99], a[8:11], v[144:147]\n"
"ds_read_b64_tr_b16 a[124:125], v11 offset:16640\n"
"buffer_load_dwordx4 v71, s[40:43], s79 offen offset:3072 lds\n"
"v_mfma_f32_16x16x32_bf16 v[148:151], a[96:99], a[12:15], v[148:151]\n"
"ds_read_b64_tr_b16 a[126:127], v11 offset:24832\n"
"v_readlane_b32 s79, v20, 2\n"
"v_mfma_f32_16x16x32_bf16 v[156:159], a[100:103], a[12:15], v[156:159]\n"
"ds_read_b64_tr_b16 a[64:65], v10 offset:16704\n"
"v_mfma_f32_16x16x32_bf16 v[152:155], a[100:103], a[8:11], v[152:155]\n"
"ds_read_b64_tr_b16 a[66:67], v10 offset:24896\n"
"s_waitcnt vmcnt(8)\n"
"buffer_store_dwordx2 v[28:29], v30, s[36:39], s85 offen offset:0 sc1\n"
"buffer_store_dwordx2 v[28:29], v30, s[36:39], s94 offen offset:0 sc1\n"
"v_mfma_f32_16x16x32_bf16 v[160:163], a[104:107], a[8:11], v[160:163]\n"
"ds_read_b64_tr_b16 a[68:69], v11 offset:16704\n"
"v_mfma_f32_16x16x32_bf16 v[164:167], a[104:107], a[12:15], v[164:167]\n"
"ds_read_b64_tr_b16 a[70:71], v11 offset:24896\n"
"v_mfma_f32_16x16x32_bf16 v[172:175], a[108:111], a[12:15], v[172:175]\n"
"ds_read_b64_tr_b16 a[72:73], v10 offset:16768\n"
"v_mfma_f32_16x16x32_bf16 v[168:171], a[108:111], a[8:11], v[168:171]\n"
"ds_read_b64_tr_b16 a[74:75], v10 offset:24960\n"
"s_waitcnt lgkmcnt(6)\n"
"v_mfma_f32_16x16x32_bf16 v[176:179], a[112:115], a[8:11], v[176:179]\n"
"ds_read_b64_tr_b16 a[76:77], v11 offset:16768\n"
"v_mfma_f32_16x16x32_bf16 v[180:183], a[112:115], a[12:15], v[180:183]\n"
"ds_read_b64_tr_b16 a[78:79], v11 offset:24960\n"
"v_mfma_f32_16x16x32_bf16 v[188:191], a[116:119], a[12:15], v[188:191]\n"
"v_mfma_f32_16x16x32_bf16 v[184:187], a[116:119], a[8:11], v[184:187]\n"
"s_waitcnt vmcnt(2)\n"
"s_barrier\n"
"v_mfma_f32_16x16x32_bf16 v[192:195], a[120:123], a[8:11], v[192:195]\n"
"ds_read_b64_tr_b16 a[80:81], v10 offset:32768\n"
"s_add_u32 m0, s74, 65536\n"
"v_mfma_f32_16x16x32_bf16 v[196:199], a[120:123], a[12:15], v[196:199]\n"
"ds_read_b64_tr_b16 a[82:83], v10 offset:40960\n"
"buffer_load_dwordx4 v64, s[40:43], s79 offen offset:0 lds\n"
"v_mfma_f32_16x16x32_bf16 v[204:207], a[124:127], a[12:15], v[204:207]\n"
"ds_read_b64_tr_b16 a[84:85], v11 offset:32768\n"
"buffer_load_dwordx4 v65, s[40:43], s79 offen offset:1024 lds\n"
"v_mfma_f32_16x16x32_bf16 v[200:203], a[124:127], a[8:11], v[200:203]\n"
"ds_read_b64_tr_b16 a[86:87], v11 offset:40960\n"
"s_waitcnt lgkmcnt(4)\n"
"v_mfma_f32_16x16x32_bf16 v[208:211], a[64:67], a[8:11], v[208:211]\n"
"ds_read_b64_tr_b16 a[88:89], v10 offset:32832\n"
"buffer_load_dwordx4 v66, s[40:43], s79 offen offset:2048 lds\n"
"v_mfma_f32_16x16x32_bf16 v[212:215], a[64:67], a[12:15], v[212:215]\n"
"ds_read_b64_tr_b16 a[90:91], v10 offset:41024\n"
"buffer_load_dwordx4 v67, s[40:43], s79 offen offset:3072 lds\n"
"v_mfma_f32_16x16x32_bf16 v[220:223], a[68:71], a[12:15], v[220:223]\n"
"ds_read_b64_tr_b16 a[92:93], v11 offset:32832\n"
"s_add_u32 m0, s74, 69632\n"
"v_mfma_f32_16x16x32_bf16 v[216:219], a[68:71], a[8:11], v[216:219]\n"
"ds_read_b64_tr_b16 a[94:95], v11 offset:41024\n"
"buffer_load_dwordx4 v68, s[40:43], s79 offen offset:0 lds\n"
"v_mfma_f32_16x16x32_bf16 v[224:227], a[72:75], a[8:11], v[224:227]\n"
"ds_read_b64_tr_b16 a[96:97], v10 offset:32896\n"
"buffer_load_dwordx4 v69, s[40:43], s79 offen offset:1024 lds\n"
"v_mfma_f32_16x16x32_bf16 v[228:231], a[72:75], a[12:15], v[228:231]\n"
"ds_read_b64_tr_b16 a[98:99], v10 offset:41088\n"
"buffer_load_dwordx4 v70, s[40:43], s79 offen offset:2048 lds\n"
"v_mfma_f32_16x16x32_bf16 v[236:239], a[76:79], a[12:15], v[236:239]\n"
"ds_read_b64_tr_b16 a[100:101], v11 offset:32896\n"
"buffer_load_dwordx4 v71, s[40:43], s79 offen offset:3072 lds\n"
"v_mfma_f32_16x16x32_bf16 v[232:235], a[76:79], a[8:11], v[232:235]\n"
"ds_read_b64_tr_b16 a[102:103], v11 offset:41088\n"
"v_readlane_b32 s79, v20, 3\n"
"s_add_u32 m0, s74, 98304\n"
"s_nop 3\n"
"buffer_load_dwordx4 v64, s[40:43], s79 offen offset:0 lds\n"
"buffer_load_dwordx4 v65, s[40:43], s79 offen offset:1024 lds\n"
"buffer_load_dwordx4 v66, s[40:43], s79 offen offset:2048 lds\n"
"buffer_load_dwordx4 v67, s[40:43], s79 offen offset:3072 lds\n"
"s_add_u32 m0, s74, 102400\n"
"s_nop 0\n"
"buffer_load_dwordx4 v68, s[40:43], s79 offen offset:0 lds\n"
"buffer_load_dwordx4 v69, s[40:43], s79 offen offset:1024 lds\n"
"buffer_load_dwordx4 v70, s[40:43], s79 offen offset:2048 lds\n"
"buffer_load_dwordx4 v71, s[40:43], s79 offen offset:3072 lds\n"
"v_readlane_b32 s79, v20, 4\n"
"s_add_u32 s75, s66, 1\n"
"s_and_b32 s75, s75, 3\n"
"s_lshl2_add_u32 s75, s75, s67\n"
"s_and_b32 s99, s75, 7\n"
"s_lshl3_add_u32 s99, s63, s99\n"
"s_lshr_b32 s75, s75, 3\n"
"s_lshl1_add_u32 s99, s99, s75\n"
"s_lshl_b32 s90, s99, 16\n"
"s_lshl_b32 s76, s31, 12\n"
"s_add_u32 s90, s90, s76\n"
"s_add_u32 s90, s90, 9437184\n"
"s_lshl_b32 s87, s99, 11\n"
"s_lshl_b32 s76, s31, 9\n"
"s_add_u32 s87, s87, s76\n"
"s_add_u32 s87, s87, 42991624\n"
"s_add_u32 s75, s66, 2\n"
"s_and_b32 s75, s75, 3\n"
"s_lshl2_add_u32 s75, s75, s67\n"
"s_and_b32 s99, s75, 7\n"
"s_lshl3_add_u32 s99, s63, s99\n"
"s_lshr_b32 s75, s75, 3\n"
"s_lshl1_add_u32 s99, s99, s75\n"
"s_lshl_b32 s91, s99, 16\n"
"s_lshl_b32 s76, s31, 12\n"
"s_add_u32 s91, s91, s76\n"
"s_add_u32 s91, s91, 9469952\n"
"s_lshl_b32 s83, s99, 11\n"
"s_lshl_b32 s76, s31, 9\n"
"s_add_u32 s83, s83, s76\n"
"s_add_u32 s83, s83, 42991632\n"
"s_add_u32 s75, s66, 3\n"
"s_and_b32 s75, s75, 3\n"
"s_lshl2_add_u32 s75, s75, s67\n"
"s_and_b32 s99, s75, 7\n"
"s_lshl3_add_u32 s99, s63, s99\n"
"s_lshr_b32 s75, s75, 3\n"
"s_lshl1_add_u32 s99, s99, s75\n"
"s_lshl_b32 s92, s99, 16\n"
"s_lshl_b32 s76, s31, 12\n"
"s_add_u32 s92, s92, s76\n"
"s_add_u32 s92, s92, 9486336\n"
"s_lshl_b32 s84, s99, 11\n"
"s_lshl_b32 s76, s31, 9\n"
"s_add_u32 s84, s84, s76\n"
"s_add_u32 s84, s84, 42991640\n"
"s_lshl_b32 s69, s63, 15\n"
"s_lshl_b32 s76, s64, 11\n"
"s_add_u32 s69, s69, s76\n"
"s_lshl_b32 s76, s31, 9\n"
"s_add_u32 s69, s69, s76\n"
"s_add_u32 s69, s69, 42991616\n"
"s_lshl_b32 s97, s63, 20\n"
"s_lshl_b32 s76, s64, 16\n"
"s_add_u32 s97, s97, s76\n"
"s_lshl_b32 s76, s31, 12\n"
"s_add_u32 s97, s97, s76\n"
"s_add_u32 s97, s97, 9437184\n"
"v_mov_b32 v100, 0x3f803f80\n"
"v_dot2_f32_bf16 v80, v48, v100, 0\n"
"v_dot2_f32_bf16 v82, v48, v48, 0\n"
"v_dot2_f32_bf16 v84, v56, v100, 0\n"
"v_dot2_f32_bf16 v86, v56, v56, 0\n"
"v_dot2_f32_bf16 v80, v49, v100, v80\n"
"v_dot2_f32_bf16 v82, v49, v49, v82\n"
"v_dot2_f32_bf16 v84, v57, v100, v84\n"
"v_dot2_f32_bf16 v86, v57, v57, v86\n"
"v_dot2_f32_bf16 v80, v50, v100, v80\n"
"v_dot2_f32_bf16 v82, v50, v50, v82\n"
"v_dot2_f32_bf16 v84, v58, v100, v84\n"
"v_dot2_f32_bf16 v86, v58, v58, v86\n"
"v_dot2_f32_bf16 v80, v51, v100, v80\n"
"v_dot2_f32_bf16 v82, v51, v51, v82\n"
"v_dot2_f32_bf16 v84, v59, v100, v84\n"
"v_dot2_f32_bf16 v86, v59, v59, v86\n"
"v_dot2_f32_bf16 v80, v52, v100, v80\n"
"v_dot2_f32_bf16 v82, v52, v52, v82\n"
"v_dot2_f32_bf16 v84, v60, v100, v84\n"
"v_dot2_f32_bf16 v86, v60, v60, v86\n"
"v_dot2_f32_bf16 v80, v53, v100, v80\n"
"v_dot2_f32_bf16 v82, v53, v53, v82\n"
"v_dot2_f32_bf16 v84, v61, v100, v84\n"
"v_dot2_f32_bf16 v86, v61, v61, v86\n"
"v_dot2_f32_bf16 v80, v54, v100, v80\n"
"v_dot2_f32_bf16 v82, v54, v54, v82\n"
"v_dot2_f32_bf16 v84, v62, v100, v84\n"
"v_dot2_f32_bf16 v86, v62, v62, v86\n"
"v_dot2_f32_bf16 v80, v55, v100, v80\n"
"v_dot2_f32_bf16 v82, v55, v55, v82\n"
"v_dot2_f32_bf16 v84, v63, v100, v84\n"
"v_dot2_f32_bf16 v86, v63, v63, v86\n"
"s_nop 2\n"
"v_permlane16_swap_b32 v80, v84\n"
"v_permlane16_swap_b32 v82, v86\n"
"v_add_f32 v96, v80, v84\n"
"v_add_f32 v97, v82, v86\n"
"v_mov_b32 v98, v96\n"
"v_mov_b32 v99, v97\n"
"s_nop 0\n"
"v_permlane32_swap_b32 v96, v98\n"
"v_permlane32_swap_b32 v97, v99\n"
"v_add_f32 v96, v96, v98\n"
"v_add_f32 v97, v97, v99\n"
"v_and_b32 v23, 31, v1\n"
"v_lshlrev_b32 v23, 3, v23\n"
"s_lshl_b32 s77, s64, 8\n"
"s_add_u32 s77, s77, s80\n"
"s_sleep 4\n"
"buffer_load_dwordx2 v[26:27], v31, s[36:39], s86 offen offset:0 sc1\n"
"s_waitcnt vmcnt(0)\n"
"v_cmp_ne_u64 vcc, s[70:71], v[26:27]\n"
"s_cbranch_vccnz .Lpslow6_%=\n"
".Lpok6_%=:\n"
"buffer_load_dwordx4 a[16:19], v8, s[52:55], s82 offen offset:0 sc1\n"
"buffer_load_dwordx4 a[20:23], v8, s[52:55], s82 offen offset:1024 sc1\n"
"buffer_load_dwordx4 a[24:27], v8, s[52:55], s82 offen offset:2048 sc1\n"
"buffer_load_dwordx4 a[28:31], v8, s[52:55], s82 offen offset:3072 sc1\n"
"v_readlane_b32 s82, v20, 18\n"
"s_nop 4\n"
"buffer_load_dwordx4 a[32:35], v8, s[52:55], s82 offen offset:0 sc1\n"
"buffer_load_dwordx4 a[36:39], v8, s[52:55], s82 offen offset:1024 sc1\n"
"buffer_load_dwordx4 a[40:43], v8, s[52:55], s82 offen offset:2048 sc1\n"
"buffer_load_dwordx4 a[44:47], v8, s[52:55], s82 offen offset:3072 sc1\n"
"v_readlane_b32 s82, v20, 19\n"
"s_nop 4\n"
"buffer_load_dwordx4 a[48:51], v8, s[52:55], s82 offen offset:0 sc1\n"
"buffer_load_dwordx4 a[52:55], v8, s[52:55], s82 offen offset:1024 sc1\n"
"buffer_load_dwordx4 a[56:59], v8, s[52:55], s82 offen offset:2048 sc1\n"
"buffer_load_dwordx4 a[60:63], v8, s[52:55], s82 offen offset:3072 sc1\n"
"v_readlane_b32 s82, v20, 20\n"
"buffer_store_dwordx2 v[96:97], v23, s[36:39], s77 offen offset:0 sc1\n"
"s_barrier\n"
"s_waitcnt lgkmcnt(4)\n"
"s_waitcnt vmcnt(12)\n"
"v_mfma_f32_16x16x32_bf16 v[128:131], a[80:83], a[16:19], v[128:131]\n"
"ds_read_b64_tr_b16 a[104:105], v10 offset:32960\n"
"s_add_u32 s85, s85, 8192\n"
"s_waitcnt vmcnt(11)\n"
"v_mfma_f32_16x16x32_bf16 v[132:135], a[80:83], a[20:23], v[132:135]\n"
"ds_read_b64_tr_b16 a[106:107], v10 offset:41152\n"
"s_add_u32 s86, s86, 8192\n"
"v_mfma_f32_16x16x32_bf16 v[140:143], a[84:87], a[20:23], v[140:143]\n"
"ds_read_b64_tr_b16 a[108:109], v11 offset:32960\n"
"s_add_u32 m0, s74, 0\n"
"v_mfma_f32_16x16x32_bf16 v[136:139], a[84:87], a[16:19], v[136:139]\n"
"ds_read_b64_tr_b16 a[110:111], v11 offset:41152\n"
"buffer_load_dwordx4 v64, s[40:43], s79 offen offset:0 lds\n"
"v_mfma_f32_16x16x32_bf16 v[144:147], a[88:91], a[16:19], v[144:147]\n"
"ds_read_b64_tr_b16 a[112:113], v10 offset:33024\n"
"v_mfma_f32_16x16x32_bf16 v[148:151], a[88:91], a[20:23], v[148:151]\n"
"ds_read_b64_tr_b16 a[114:115], v10 offset:41216\n"
"buffer_load_dwordx4 v65, s[40:43], s79 offen offset:1024 lds\n"
"v_mfma_f32_16x16x32_bf16 v[156:159], a[92:95], a[20:23], v[156:159]\n"
"ds_read_b64_tr_b16 a[116:117], v11 offset:33024\n"
"v_mfma_f32_16x16x32_bf16 v[152:155], a[92:95], a[16:19], v[152:155]\n"
"ds_read_b64_tr_b16 a[118:119], v11 offset:41216\n"
"s_waitcnt lgkmcnt(4)\n"
"v_mfma_f32_16x16x32_bf16 v[160:163], a[96:99], a[16:19], v[160:163]\n"
"ds_read_b64_tr_b16 a[120:121], v10 offset:33088\n"
"v_mfma_f32_16x16x32_bf16 v[164:167], a[96:99], a[20:23], v[164:167]\n"
"ds_read_b64_tr_b16 a[122:123], v10 offset:41280\n"
"v_mfma_f32_16x16x32_bf16 v[172:175], a[100:103], a[20:23], v[172:175]\n"
"ds_read_b64_tr_b16 a[124:125], v11 offset:33088\n"
"buffer_load_dwordx4 v66, s[40:43], s79 offen offset:2048 lds\n"
"v_mfma_f32_16x16x32_bf16 v[168:171], a[100:103], a[16:19], v[168:171]\n"
"ds_read_b64_tr_b16 a[126:127], v11 offset:41280\n"
"v_mfma_f32_16x16x32_bf16 v[176:179], a[104:107], a[16:19], v[176:179]\n"
"ds_read_b64_tr_b16 a[64:65], v10 offset:33152\n"
"v_mfma_f32_16x16x32_bf16 v[180:183], a[104:107], a[20:23], v[180:183]\n"
"ds_read_b64_tr_b16 a[66:67], v10 offset:41344\n"
"v_mfma_f32_16x16x32_bf16 v[188:191], a[108:111], a[20:23], v[188:191]\n"
"ds_read_b64_tr_b16 a[68:69], v11 offset:33152\n"
"buffer_load_dwordx4 v67, s[40:43], s79 offen offset:3072 lds\n"
"v_mfma_f32_16x16x32_bf16 v[184:187], a[108:111], a[16:19], v[184:187]\n"
"ds_read_b64_tr_b16 a[70:71], v11 offset:41344\n"
"s_waitcnt lgkmcnt(4)\n"
"v_mfma_f32_16x16x32_bf16 v[192:195], a[112:115], a[16:19], v[192:195]\n"
"ds_read_b64_tr_b16 a[72:73], v10 offset:49152\n"
"v_mfma_f32_16x16x32_bf16 v[196:199], a[112:115], a[20:23], v[196:199]\n"
"ds_read_b64_tr_b16 a[74:75], v10 offset:57344\n"
"v_mfma_f32_16x16x32_bf16 v[204:207], a[116:119], a[20:23], v[204:207]\n"
"ds_read_b64_tr_b16 a[76:77], v11 offset:49152\n"
"s_add_u32 m0, s74, 4096\n"
"v_mfma_f32_16x16x32_bf16 v[200:203], a[116:119], a[16:19], v[200:203]\n"
"ds_read_b64_tr_b16 a[78:79], v11 offset:57344\n"
"buffer_load_dwordx4 v68, s[40:43], s79 offen offset:0 lds\n"
"v_mfma_f32_16x16x32_bf16 v[208:211], a[120:123], a[16:19], v[208:211]\n"
"ds_read_b64_tr_b16 a[80:81], v10 offset:49216\n"
"v_mfma_f32_16x16x32_bf16 v[212:215], a[120:123], a[20:23], v[212:215]\n"
"ds_read_b64_tr_b16 a[82:83], v10 offset:57408\n"
"v_mfma_f32_16x16x32_bf16 v[220:223], a[124:127], a[20:23], v[220:223]\n"
"ds_read_b64_tr_b16 a[84:85], v11 offset:49216\n"
"v_mfma_f32_16x16x32_bf16 v[216:219], a[124:127], a[16:19], v[216:219]\n"
"ds_read_b64_tr_b16 a[86:87], v11 offset:57408\n"
"s_waitcnt lgkmcnt(4)\n"
"v_mfma_f32_16x16x32_bf16 v[224:227], a[64:67], a[16:19], v[224:227]\n"
"ds_read_b64_tr_b16 a[88:89], v10 offset:49280\n"
"buffer_load_dwordx4 v69, s[40:43], s79 offen offset:1024 lds\n"
"v_mfma_f32_16x16x32_bf16 v[228:231], a[64:67], a[20:23], v[228:231]\n"
"ds_read_b64_tr_b16 a[90:91], v10 offset:57472\n"
"v_mfma_f32_16x16x32_bf16 v[236:239], a[68:71], a[20:23], v[236:239]\n"
"ds_read_b64_tr_b16 a[92:93], v11 offset:49280\n"
"v_mfma_f32_16x16x32_bf16 v[232:235], a[68:71], a[16:19], v[232:235]\n"
"ds_read_b64_tr_b16 a[94:95], v11 offset:57472\n"
"s_waitcnt vmcnt(16)\n"
"v_mfma_f32_16x16x32_bf16 v[128:131], a[72:75], a[24:27], v[128:131]\n"
"ds_read_b64_tr_b16 a[96:97], v10 offset:49344\n"
"buffer_load_dwordx4 v70, s[40:43], s79 offen offset:2048 lds\n"
"s_waitcnt vmcnt(16)\n"
"v_mfma_f32_16x16x32_bf16 v[132:135], a[72:75], a[28:31], v[132:135]\n"
"ds_read_b64_tr_b16 a[98:99], v10 offset:57536\n"
"v_mfma_f32_16x16x32_bf16 v[140:143], a[76:79], a[28:31], v[140:143]\n"
"ds_read_b64_tr_b16 a[100:101], v11 offset:49344\n"
"v_mfma_f32_16x16x32_bf16 v[136:139], a[76:79], a[24:27], v[136:139]\n"
"ds_read_b64_tr_b16 a[102:103], v11 offset:57536\n"
"s_waitcnt lgkmcnt(4)\n"
"v_mfma_f32_16x16x32_bf16 v[144:147], a[80:83], a[24:27], v[144:147]\n"
"ds_read_b64_tr_b16 a[104:105], v10 offset:49408\n"
"v_mfma_f32_16x16x32_bf16 v[148:151], a[80:83], a[28:31], v[148:151]\n"
"ds_read_b64_tr_b16 a[106:107], v10 offset:57600\n"
"buffer_load_dwordx4 v71, s[40:43], s79 offen offset:3072 lds\n"
"v_mfma_f32_16x16x32_bf16 v[156:159], a[84:87], a[28:31], v[156:159]\n"
"ds_read_b64_tr_b16 a[108:109], v11 offset:49408\n"
"v_readlane_b32 s79, v20, 5\n"
"v_mfma_f32_16x16x32_bf16 v[152:155], a[84:87], a[24:27], v[152:155]\n"
"ds_read_b64_tr_b16 a[110:111], v11 offset:57600\n"
"v_mfma_f32_16x16x32_bf16 v[160:163], a[88:91], a[24:27], v[160:163]\n"
"ds_read_b64_tr_b16 a[112:113], v10 offset:49472\n"
"v_mfma_f32_16x16x32_bf16 v[164:167], a[88:91], a[28:31], v[164:167]\n"
"ds_read_b64_tr_b16 a[114:115], v10 offset:57664\n"
"buffer_load_dwordx4 a[0:3], v8, s[52:55], s82 offen offset:0 sc1\n"
"v_mfma_f32_16x16x32_bf16 v[172:175], a[92:95], a[28:31], v[172:175]\n"
"ds_read_b64_tr_b16 a[116:117], v11 offset:49472\n"
"v_mfma_f32_16x16x32_bf16 v[168:171], a[92:95], a[24:27], v[168:171]\n"
"ds_read_b64_tr_b16 a[118:119], v11 offset:57664\n"
"s_waitcnt lgkmcnt(4)\n"
"v_mfma_f32_16x16x32_bf16 v[176:179], a[96:99], a[24:27], v[176:179]\n"
"ds_read_b64_tr_b16 a[120:121], v10 offset:49536\n"
"v_mfma_f32_16x16x32_bf16 v[180:183], a[96:99], a[28:31], v[180:183]\n"
"ds_read_b64_tr_b16 a[122:123], v10 offset:57728\n"
"v_mfma_f32_16x16x32_bf16 v[188:191], a[100:103], a[28:31], v[188:191]\n"
"ds_read_b64_tr_b16 a[124:125], v11 offset:49536\n"
"buffer_load_dwordx4 a[4:7], v8, s[52:55], s82 offen offset:1024 sc1\n"
"v_mfma_f32_16x16x32_bf16 v[184:187], a[100:103], a[24:27], v[184:187]\n"
"ds_read_b64_tr_b16 a[126:127], v11 offset:57728\n"
"v_mfma_f32_16x16x32_bf16 v[192:195], a[104:107], a[24:27], v[192:195]\n"
"ds_read_b64_tr_b16 a[64:65], v12 offset:0\n"
"v_mfma_f32_16x16x32_bf16 v[196:199], a[104:107], a[28:31], v[196:199]\n"
"ds_read_b64_tr_b16 a[66:67], v12 offset:8192\n"
"v_mfma_f32_16x16x32_bf16 v[204:207], a[108:111], a[28:31], v[204:207]\n"
"ds_read_b64_tr_b16 a[68:69], v13 offset:0\n"
"buffer_load_dwordx4 a[8:11], v8, s[52:55], s82 offen offset:2048 sc1\n"
"v_mfma_f32_16x16x32_bf16 v[200:203], a[108:111], a[24:27], v[200:203]\n"
"ds_read_b64_tr_b16 a[70:71], v13 offset:8192\n"
"s_waitcnt lgkmcnt(4)\n"
"v_mfma_f32_16x16x32_bf16 v[208:211], a[112:115], a[24:27], v[208:211]\n"
"ds_read_b64_tr_b16 a[72:73], v12 offset:64\n"
"v_mfma_f32_16x16x32_bf16 v[212:215], a[112:115], a[28:31], v[212:215]\n"
"ds_read_b64_tr_b16 a[74:75], v12 offset:8256\n"
"v_mfma_f32_16x16x32_bf16 v[220:223], a[116:119], a[28:31], v[220:223]\n"
"ds_read_b64_tr_b16 a[76:77], v13 offset:64\n"
"v_mfma_f32_16x16x32_bf16 v[216:219], a[116:119], a[24:27], v[216:219]\n"
"ds_read_b64_tr_b16 a[78:79], v13 offset:8256\n"
"buffer_load_dwordx4 a[12:15], v8, s[52:55], s82 offen offset:3072 sc1\n"
"v_mfma_f32_16x16x32_bf16 v[224:227], a[120:123], a[24:27], v[224:227]\n"
"ds_read_b64_tr_b16 a[80:81], v12 offset:128\n"
"v_readlane_b32 s82, v20, 21\n"
"v_mfma_f32_16x16x32_bf16 v[228:231], a[120:123], a[28:31], v[228:231]\n"
"ds_read_b64_tr_b16 a[82:83], v12 offset:8320\n"
"v_mfma_f32_16x16x32_bf16 v[236:239], a[124:127], a[28:31], v[236:239]\n"
"ds_read_b64_tr_b16 a[84:85], v13 offset:128\n"
"v_mfma_f32_16x16x32_bf16 v[232:235], a[124:127], a[24:27], v[232:235]\n"
"ds_read_b64_tr_b16 a[86:87], v13 offset:8320\n"
"s_barrier\n"
"s_waitcnt lgkmcnt(4)\n"
"s_waitcnt vmcnt(20)\n"
"v_mfma_f32_16x16x32_bf16 v[128:131], a[64:67], a[32:35], v[128:131]\n"
"ds_read_b64_tr_b16 a[88:89], v12 offset:192\n"
"s_add_u32 m0, s74, 32768\n"
"s_waitcnt vmcnt(19)\n"
"v_mfma_f32_16x16x32_bf16 v[132:135], a[64:67], a[36:39], v[132:135]\n"
"ds_read_b64_tr_b16 a[90:91], v12 offset:8384\n"
"buffer_load_dwordx4 v64, s[40:43], s79 offen offset:0 lds\n"
"v_mfma_f32_16x16x32_bf16 v[140:143], a[68:71], a[36:39], v[140:143]\n"
"ds_read_b64_tr_b16 a[92:93], v13 offset:192\n"
"v_mfma_f32_16x16x32_bf16 v[136:139], a[68:71], a[32:35], v[136:139]\n"
"ds_read_b64_tr_b16 a[94:95], v13 offset:8384\n"
"v_mfma_f32_16x16x32_bf16 v[144:147], a[72:75], a[32:35], v[144:147]\n"
"ds_read_b64_tr_b16 a[96:97], v12 offset:256\n"
"v_mfma_f32_16x16x32_bf16 v[148:151], a[72:75], a[36:39], v[148:151]\n"
"ds_read_b64_tr_b16 a[98:99], v12 offset:8448\n"
"buffer_load_dwordx4 v65, s[40:43], s79 offen offset:1024 lds\n"
"v_mfma_f32_16x16x32_bf16 v[156:159], a[76:79], a[36:39], v[156:159]\n"
"ds_read_b64_tr_b16 a[100:101], v13 offset:256\n"
"v_mfma_f32_16x16x32_bf16 v[152:155], a[76:79], a[32:35], v[152:155]\n"
"ds_read_b64_tr_b16 a[102:103], v13 offset:8448\n"
"s_waitcnt lgkmcnt(4)\n"
"v_mfma_f32_16x16x32_bf16 v[160:163], a[80:83], a[32:35], v[160:163]\n"
"ds_read_b64_tr_b16 a[104:105], v12 offset:320\n"
"v_mfma_f32_16x16x32_bf16 v[164:167], a[80:83], a[36:39], v[164:167]\n"
"ds_read_b64_tr_b16 a[106:107], v12 offset:8512\n"
"v_mfma_f32_16x16x32_bf16 v[172:175], a[84:87], a[36:39], v[172:175]\n"
"ds_read_b64_tr_b16 a[108:109], v13 offset:320\n"
"buffer_load_dwordx4 v66, s[40:43], s79 offen offset:2048 lds\n"
"v_mfma_f32_16x16x32_bf16 v[168:171], a[84:87], a[32:35], v[168:171]\n"
"ds_read_b64_tr_b16 a[110:111], v13 offset:8512\n"
"v_mfma_f32_16x16x32_bf16 v[176:179], a[88:91], a[32:35], v[176:179]\n"
"ds_read_b64_tr_b16 a[112:113], v12 offset:384\n"
"v_mfma_f32_16x16x32_bf16 v[180:183], a[88:91], a[36:39], v[180:183]\n"
"ds_read_b64_tr_b16 a[114:115], v12 offset:8576\n"
"buffer_load_dword a234, v24, s[56:59], s95 offen offset:0\n"
"v_mfma_f32_16x16x32_bf16 v[188:191], a[92:95], a[36:39], v[188:191]\n"
"ds_read_b64_tr_b16 a[116:117], v13 offset:384\n"
"s_add_u32 s95, s95, 14336\n"
"v_mfma_f32_16x16x32_bf16 v[184:187], a[92:95], a[32:35], v[184:187]\n"
"ds_read_b64_tr_b16 a[118:119], v13 offset:8576\n"
"s_waitcnt lgkmcnt(4)\n"
"v_mfma_f32_16x16x32_bf16 v[192:195], a[96:99], a[32:35], v[192:195]\n"
"ds_read_b64_tr_b16 a[120:121], v12 offset:16384\n"
"buffer_load_dwordx4 v67, s[40:43], s79 offen offset:3072 lds\n"
"v_mfma_f32_16x16x32_bf16 v[196:199], a[96:99], a[36:39], v[196:199]\n"
"ds_read_b64_tr_b16 a[122:123], v12 offset:24576\n"
"v_mfma_f32_16x16x32_bf16 v[204:207], a[100:103], a[36:39], v[204:207]\n"
"ds_read_b64_tr_b16 a[124:125], v13 offset:16384\n"
"s_add_u32 m0, s74, 36864\n"
"v_mfma_f32_16x16x32_bf16 v[200:203], a[100:103], a[32:35], v[200:203]\n"
"ds_read_b64_tr_b16 a[126:127], v13 offset:24576\n"
"buffer_load_dwordx4 v68, s[40:43], s79 offen offset:0 lds\n"
"v_mfma_f32_16x16x32_bf16 v[208:211], a[104:107], a[32:35], v[208:211]\n"
"ds_read_b64_tr_b16 a[64:65], v12 offset:16448\n"
"v_mfma_f32_16x16x32_bf16 v[212:215], a[104:107], a[36:39], v[212:215]\n"
"ds_read_b64_tr_b16 a[66:67], v12 offset:24640\n"
"v_mfma_f32_16x16x32_bf16 v[220:223], a[108:111], a[36:39], v[220:223]\n"
"ds_read_b64_tr_b16 a[68:69], v13 offset:16448\n"
"v_mfma_f32_16x16x32_bf16 v[216:219], a[108:111], a[32:35], v[216:219]\n"
"ds_read_b64_tr_b16 a[70:71], v13 offset:24640\n"
"s_waitcnt lgkmcnt(4)\n"
"v_mfma_f32_16x16x32_bf16 v[224:227], a[112:115], a[32:35], v[224:227]\n"
"ds_read_b64_tr_b16 a[72:73], v12 offset:16512\n"
"buffer_load_dwordx4 v69, s[40:43], s79 offen offset:1024 lds\n"
"v_mfma_f32_16x16x32_bf16 v[228:231], a[112:115], a[36:39], v[228:231]\n"
"ds_read_b64_tr_b16 a[74:75], v12 offset:24704\n"
"v_mfma_f32_16x16x32_bf16 v[236:239], a[116:119], a[36:39], v[236:239]\n"
"ds_read_b64_tr_b16 a[76:77], v13 offset:16512\n"
"v_mfma_f32_16x16x32_bf16 v[232:235], a[116:119], a[32:35], v[232:235]\n"
"ds_read_b64_tr_b16 a[78:79], v13 offset:24704\n"
"s_waitcnt vmcnt(25)\n"
"v_mfma_f32_16x16x32_bf16 v[128:131], a[120:123], a[40:43], v[128:131]\n"
"ds_read_b64_tr_b16 a[80:81], v12 offset:16576\n"
"buffer_load_dwordx4 v70, s[40:43], s79 offen offset:2048 lds\n"
"s_waitcnt vmcnt(25)\n"
"v_mfma_f32_16x16x32_bf16 v[132:135], a[120:123], a[44:47], v[132:135]\n"
"ds_read_b64_tr_b16 a[82:83], v12 offset:24768\n"
"v_mfma_f32_16x16x32_bf16 v[140:143], a[124:127], a[44:47], v[140:143]\n"
"ds_read_b64_tr_b16 a[84:85], v13 offset:16576\n"
"v_mfma_f32_16x16x32_bf16 v[136:139], a[124:127], a[40:43], v[136:139]\n"
"ds_read_b64_tr_b16 a[86:87], v13 offset:24768\n"
"s_waitcnt lgkmcnt(4)\n"
"v_mfma_f32_16x16x32_bf16 v[144:147], a[64:67], a[40:43], v[144:147]\n"
"ds_read_b64_tr_b16 a[88:89], v12 offset:16640\n"
"v_mfma_f32_16x16x32_bf16 v[148:151], a[64:67], a[44:47], v[148:151]\n"
"ds_read_b64_tr_b16 a[90:91], v12 offset:24832\n"
"buffer_load_dwordx4 v71, s[40:43], s79 offen offset:3072 lds\n"
"v_mfma_f32_16x16x32_bf16 v[156:159], a[68:71], a[44:47], v[156:159]\n"
"ds_read_b64_tr_b16 a[92:93], v13 offset:16640\n"
"v_readlane_b32 s79, v20, 6\n"
"v_mfma_f32_16x16x32_bf16 v[152:155], a[68:71], a[40:43], v[152:155]\n"
"ds_read_b64_tr_b16 a[94:95], v13 offset:24832\n"
"v_mfma_f32_16x16x32_bf16 v[160:163], a[72:75], a[40:43], v[160:163]\n"
"ds_read_b64_tr_b16 a[96:97], v12 offset:16704\n"
"v_mfma_f32_16x16x32_bf16 v[164:167], a[72:75], a[44:47], v[164:167]\n"
"ds_read_b64_tr_b16 a[98:99], v12 offset:24896\n"
"buffer_load_dwordx4 a[16:19], v8, s[52:55], s82 offen offset:0 sc1\n"
"v_mfma_f32_16x16x32_bf16 v[172:175], a[76:79], a[44:47], v[172:175]\n"
"ds_read_b64_tr_b16 a[100:101], v13 offset:16704\n"
"v_mfma_f32_16x16x32_bf16 v[168:171], a[76:79], a[40:43], v[168:171]\n"
"ds_read_b64_tr_b16 a[102:103], v13 offset:24896\n"
"s_waitcnt lgkmcnt(4)\n"
"v_mfma_f32_16x16x32_bf16 v[176:179], a[80:83], a[40:43], v[176:179]\n"
"ds_read_b64_tr_b16 a[104:105], v12 offset:16768\n"
"v_mfma_f32_16x16x32_bf16 v[180:183], a[80:83], a[44:47], v[180:183]\n"
"ds_read_b64_tr_b16 a[106:107], v12 offset:24960\n"
"s_waitcnt vmcnt(22)\n"
"buffer_store_dwordx2 v[28:29], v30, s[36:39], s85 offen offset:0 sc1\n"
"v_mfma_f32_16x16x32_bf16 v[188:191], a[84:87], a[44:47], v[188:191]\n"
"ds_read_b64_tr_b16 a[108:109], v13 offset:16768\n"
"buffer_load_dwordx4 a[20:23], v8, s[52:55], s82 offen offset:1024 sc1\n"
"v_mfma_f32_16x16x32_bf16 v[184:187], a[84:87], a[40:43], v[184:187]\n"
"ds_read_b64_tr_b16 a[110:111], v13 offset:24960\n"
"v_mfma_f32_16x16x32_bf16 v[192:195], a[88:91], a[40:43], v[192:195]\n"
"ds_read_b64_tr_b16 a[112:113], v12 offset:32768\n"
"v_mfma_f32_16x16x32_bf16 v[196:199], a[88:91], a[44:47], v[196:199]\n"
"ds_read_b64_tr_b16 a[114:115], v12 offset:40960\n"
"v_mfma_f32_16x16x32_bf16 v[204:207], a[92:95], a[44:47], v[204:207]\n"
"ds_read_b64_tr_b16 a[116:117], v13 offset:32768\n"
"buffer_load_dwordx4 a[24:27], v8, s[52:55], s82 offen offset:2048 sc1\n"
"v_mfma_f32_16x16x32_bf16 v[200:203], a[92:95], a[40:43], v[200:203]\n"
"ds_read_b64_tr_b16 a[118:119], v13 offset:40960\n"
"s_waitcnt lgkmcnt(4)\n"
"v_mfma_f32_16x16x32_bf16 v[208:211], a[96:99], a[40:43], v[208:211]\n"
"ds_read_b64_tr_b16 a[120:121], v12 offset:32832\n"
"v_mfma_f32_16x16x32_bf16 v[212:215], a[96:99], a[44:47], v[212:215]\n"
"ds_read_b64_tr_b16 a[122:123], v12 offset:41024\n"
"v_mfma_f32_16x16x32_bf16 v[220:223], a[100:103], a[44:47], v[220:223]\n"
"ds_read_b64_tr_b16 a[124:125], v13 offset:32832\n"
"v_mfma_f32_16x16x32_bf16 v[216:219], a[100:103], a[40:43], v[216:219]\n"
"ds_read_b64_tr_b16 a[126:127], v13 offset:41024\n"
"buffer_load_dwordx4 a[28:31], v8, s[52:55], s82 offen offset:3072 sc1\n"
"v_mfma_f32_16x16x32_bf16 v[224:227], a[104:107], a[40:43], v[224:227]\n"
"ds_read_b64_tr_b16 a[64:65], v12 offset:32896\n"
"v_readlane_b32 s82, v20, 22\n"
"v_mfma_f32_16x16x32_bf16 v[228:231], a[104:107], a[44:47], v[228:231]\n"
"ds_read_b64_tr_b16 a[66:67], v12 offset:41088\n"
"v_mfma_f32_16x16x32_bf16 v[236:239], a[108:111], a[44:47], v[236:239]\n"
"ds_read_b64_tr_b16 a[68:69], v13 offset:32896\n"
"v_mfma_f32_16x16x32_bf16 v[232:235], a[108:111], a[40:43], v[232:235]\n"
"ds_read_b64_tr_b16 a[70:71], v13 offset:41088\n"
"s_waitcnt vmcnt(18)\n"
"s_barrier\n"
"s_waitcnt lgkmcnt(4)\n"
"v_mfma_f32_16x16x32_bf16 v[128:131], a[112:115], a[48:51], v[128:131]\n"
"ds_read_b64_tr_b16 a[72:73], v12 offset:32960\n"
"s_add_u32 m0, s74, 65536\n"
"v_mfma_f32_16x16x32_bf16 v[132:135], a[112:115], a[52:55], v[132:135]\n"
"ds_read_b64_tr_b16 a[74:75], v12 offset:41152\n"
"buffer_load_dwordx4 v64, s[40:43], s79 offen offset:0 lds\n"
"v_mfma_f32_16x16x32_bf16 v[140:143], a[116:119], a[52:55], v[140:143]\n"
"ds_read_b64_tr_b16 a[76:77], v13 offset:32960\n"
"v_mfma_f32_16x16x32_bf16 v[136:139], a[116:119], a[48:51], v[136:139]\n"
"ds_read_b64_tr_b16 a[78:79], v13 offset:41152\n"
"v_mfma_f32_16x16x32_bf16 v[144:147], a[120:123], a[48:51], v[144:147]\n"
"ds_read_b64_tr_b16 a[80:81], v12 offset:33024\n"
"v_mfma_f32_16x16x32_bf16 v[148:151], a[120:123], a[52:55], v[148:151]\n"
"ds_read_b64_tr_b16 a[82:83], v12 offset:41216\n"
"buffer_load_dwordx4 v65, s[40:43], s79 offen offset:1024 lds\n"
"v_mfma_f32_16x16x32_bf16 v[156:159], a[124:127], a[52:55], v[156:159]\n"
"ds_read_b64_tr_b16 a[84:85], v13 offset:33024\n"
"v_mfma_f32_16x16x32_bf16 v[152:155], a[124:127], a[48:51], v[152:155]\n"
"ds_read_b64_tr_b16 a[86:87], v13 offset:41216\n"
"s_waitcnt lgkmcnt(4)\n"
"v_mfma_f32_16x16x32_bf16 v[160:163], a[64:67], a[48:51], v[160:163]\n"
"ds_read_b64_tr_b16 a[88:89], v12 offset:33088\n"
"v_mfma_f32_16x16x32_bf16 v[164:167], a[64:67], a[52:55], v[164:167]\n"
"ds_read_b64_tr_b16 a[90:91], v12 offset:41280\n"
"v_mfma_f32_16x16x32_bf16 v[172:175], a[68:71], a[52:55], v[172:175]\n"
"ds_read_b64_tr_b16 a[92:93], v13 offset:33088\n"
"buffer_load_dwordx4 v66, s[40:43], s79 offen offset:2048 lds\n"
"v_mfma_f32_16x16x32_bf16 v[168:171], a[68:71], a[48:51], v[168:171]\n"
"ds_read_b64_tr_b16 a[94:95], v13 offset:41280\n"
"v_mfma_f32_16x16x32_bf16 v[176:179], a[72:75], a[48:51], v[176:179]\n"
"ds_read_b64_tr_b16 a[96:97], v12 offset:33152\n"
"v_mfma_f32_16x16x32_bf16 v[180:183], a[72:75], a[52:55], v[180:183]\n"
"ds_read_b64_tr_b16 a[98:99], v12 offset:41344\n"
"v_mfma_f32_16x16x32_bf16 v[188:191], a[76:79], a[52:55], v[188:191]\n"
"ds_read_b64_tr_b16 a[100:101], v13 offset:33152\n"
"buffer_load_dwordx4 v67, s[40:43], s79 offen offset:3072 lds\n"
"v_mfma_f32_16x16x32_bf16 v[184:187], a[76:79], a[48:51], v[184:187]\n"
"ds_read_b64_tr_b16 a[102:103], v13 offset:41344\n"
"s_waitcnt lgkmcnt(4)\n"
"v_mfma_f32_16x16x32_bf16 v[192:195], a[80:83], a[48:51], v[192:195]\n"
"ds_read_b64_tr_b16 a[104:105], v12 offset:49152\n"
"v_mfma_f32_16x16x32_bf16 v[196:199], a[80:83], a[52:55], v[196:199]\n"
"ds_read_b64_tr_b16 a[106:107], v12 offset:57344\n"
"v_mfma_f32_16x16x32_bf16 v[204:207], a[84:87], a[52:55], v[204:207]\n"
"ds_read_b64_tr_b16 a[108:109], v13 offset:49152\n"
"s_add_u32 m0, s74, 69632\n"
"v_mfma_f32_16x16x32_bf16 v[200:203], a[84:87], a[48:51], v[200:203]\n"
"ds_read_b64_tr_b16 a[110:111], v13 offset:57344\n"
"buffer_load_dwordx4 v68, s[40:43], s79 offen offset:0 lds\n"
"v_mfma_f32_16x16x32_bf16 v[208:211], a[88:91], a[48:51], v[208:211]\n"
"ds_read_b64_tr_b16 a[112:113], v12 offset:49216\n"
"v_mfma_f32_16x16x32_bf16 v[212:215], a[88:91], a[52:55], v[212:215]\n"
"ds_read_b64_tr_b16 a[114:115], v12 offset:57408\n"
"v_mfma_f32_16x16x32_bf16 v[220:223], a[92:95], a[52:55], v[220:223]\n"
"ds_read_b64_tr_b16 a[116:117], v13 offset:49216\n"
"v_mfma_f32_16x16x32_bf16 v[216:219], a[92:95], a[48:51], v[216:219]\n"
"ds_read_b64_tr_b16 a[118:119], v13 offset:57408\n"
"s_waitcnt lgkmcnt(4)\n"
"v_mfma_f32_16x16x32_bf16 v[224:227], a[96:99], a[48:51], v[224:227]\n"
"ds_read_b64_tr_b16 a[120:121], v12 offset:49280\n"
"buffer_load_dwordx4 v69, s[40:43], s79 offen offset:1024 lds\n"
"v_mfma_f32_16x16x32_bf16 v[228:231], a[96:99], a[52:55], v[228:231]\n"
"ds_read_b64_tr_b16 a[122:123], v12 offset:57472\n"
"v_mfma_f32_16x16x32_bf16 v[236:239], a[100:103], a[52:55], v[236:239]\n"
"ds_read_b64_tr_b16 a[124:125], v13 offset:49280\n"
"v_mfma_f32_16x16x32_bf16 v[232:235], a[100:103], a[48:51], v[232:235]\n"
"ds_read_b64_tr_b16 a[126:127], v13 offset:57472\n"
"buffer_load_dwordx2 v[120:121], v25, s[36:39], s96 offen offset:0 sc1\n"
"v_mfma_f32_16x16x32_bf16 v[128:131], a[104:107], a[56:59], v[128:131]\n"
"ds_read_b64_tr_b16 a[64:65], v12 offset:49344\n"
"buffer_load_ushort v123, v14, s[48:51], 0 offen\n"
"v_mfma_f32_16x16x32_bf16 v[132:135], a[104:107], a[60:63], v[132:135]\n"
"ds_read_b64_tr_b16 a[66:67], v12 offset:57536\n"
"buffer_load_dwordx4 v70, s[40:43], s79 offen offset:2048 lds\n"
"v_mfma_f32_16x16x32_bf16 v[140:143], a[108:111], a[60:63], v[140:143]\n"
"ds_read_b64_tr_b16 a[68:69], v13 offset:49344\n"
"v_mfma_f32_16x16x32_bf16 v[136:139], a[108:111], a[56:59], v[136:139]\n"
"ds_read_b64_tr_b16 a[70:71], v13 offset:57536\n"
"s_waitcnt lgkmcnt(4)\n"
"v_mfma_f32_16x16x32_bf16 v[144:147], a[112:115], a[56:59], v[144:147]\n"
"ds_read_b64_tr_b16 a[72:73], v12 offset:49408\n"
"v_mfma_f32_16x16x32_bf16 v[148:151], a[112:115], a[60:63], v[148:151]\n"
"ds_read_b64_tr_b16 a[74:75], v12 offset:57600\n"
"buffer_load_dwordx4 v71, s[40:43], s79 offen offset:3072 lds\n"
"v_mfma_f32_16x16x32_bf16 v[156:159], a[116:119], a[60:63], v[156:159]\n"
"ds_read_b64_tr_b16 a[76:77], v13 offset:49408\n"
"v_readlane_b32 s79, v20, 7\n"
"v_mfma_f32_16x16x32_bf16 v[152:155], a[116:119], a[56:59], v[152:155]\n"
"ds_read_b64_tr_b16 a[78:79], v13 offset:57600\n"
"v_mfma_f32_16x16x32_bf16 v[160:163], a[120:123], a[56:59], v[160:163]\n"
"ds_read_b64_tr_b16 a[80:81], v12 offset:49472\n"
"v_mfma_f32_16x16x32_bf16 v[164:167], a[120:123], a[60:63], v[164:167]\n"
"ds_read_b64_tr_b16 a[82:83], v12 offset:57664\n"
"buffer_load_dwordx4 a[32:35], v8, s[52:55], s82 offen offset:0 sc1\n"
"v_mfma_f32_16x16x32_bf16 v[172:175], a[124:127], a[60:63], v[172:175]\n"
"ds_read_b64_tr_b16 a[84:85], v13 offset:49472\n"
"v_mfma_f32_16x16x32_bf16 v[168:171], a[124:127], a[56:59], v[168:171]\n"
"ds_read_b64_tr_b16 a[86:87], v13 offset:57664\n"
"s_waitcnt lgkmcnt(4)\n"
"v_mfma_f32_16x16x32_bf16 v[176:179], a[64:67], a[56:59], v[176:179]\n"
"ds_read_b64_tr_b16 a[88:89], v12 offset:49536\n"
"v_mfma_f32_16x16x32_bf16 v[180:183], a[64:67], a[60:63], v[180:183]\n"
"ds_read_b64_tr_b16 a[90:91], v12 offset:57728\n"
"v_mfma_f32_16x16x32_bf16 v[188:191], a[68:71], a[60:63], v[188:191]\n"
"ds_read_b64_tr_b16 a[92:93], v13 offset:49536\n"
"buffer_load_dwordx4 a[36:39], v8, s[52:55], s82 offen offset:1024 sc1\n"
"v_mfma_f32_16x16x32_bf16 v[184:187], a[68:71], a[56:59], v[184:187]\n"
"ds_read_b64_tr_b16 a[94:95], v13 offset:57728\n"
"v_mfma_f32_16x16x32_bf16 v[192:195], a[72:75], a[56:59], v[192:195]\n"
"ds_read_b64_tr_b16 a[96:97], v10 offset:0\n"
"v_mfma_f32_16x16x32_bf16 v[196:199], a[72:75], a[60:63], v[196:199]\n"
"ds_read_b64_tr_b16 a[98:99], v10 offset:8192\n"
"v_mfma_f32_16x16x32_bf16 v[204:207], a[76:79], a[60:63], v[204:207]\n"
"ds_read_b64_tr_b16 a[100:101], v11 offset:0\n"
"buffer_load_dwordx4 a[40:43], v8, s[52:55], s82 offen offset:2048 sc1\n"
"v_mfma_f32_16x16x32_bf16 v[200:203], a[76:79], a[56:59], v[200:203]\n"
"ds_read_b64_tr_b16 a[102:103], v11 offset:8192\n"
"s_waitcnt lgkmcnt(4)\n"
"v_mfma_f32_16x16x32_bf16 v[208:211], a[80:83], a[56:59], v[208:211]\n"
"ds_read_b64_tr_b16 a[104:105], v10 offset:64\n"
"v_mfma_f32_16x16x32_bf16 v[212:215], a[80:83], a[60:63], v[212:215]\n"
"ds_read_b64_tr_b16 a[106:107], v10 offset:8256\n"
"v_mfma_f32_16x16x32_bf16 v[220:223], a[84:87], a[60:63], v[220:223]\n"
"ds_read_b64_tr_b16 a[108:109], v11 offset:64\n"
"v_mfma_f32_16x16x32_bf16 v[216:219], a[84:87], a[56:59], v[216:219]\n"
"ds_read_b64_tr_b16 a[110:111], v11 offset:8256\n"
"buffer_load_dwordx4 a[44:47], v8, s[52:55], s82 offen offset:3072 sc1\n"
"v_mfma_f32_16x16x32_bf16 v[224:227], a[88:91], a[56:59], v[224:227]\n"
"ds_read_b64_tr_b16 a[112:113], v10 offset:128\n"
"v_readlane_b32 s82, v20, 23\n"
"v_mfma_f32_16x16x32_bf16 v[228:231], a[88:91], a[60:63], v[228:231]\n"
"ds_read_b64_tr_b16 a[114:115], v10 offset:8320\n"
"v_mfma_f32_16x16x32_bf16 v[236:239], a[92:95], a[60:63], v[236:239]\n"
"ds_read_b64_tr_b16 a[116:117], v11 offset:128\n"
"v_mfma_f32_16x16x32_bf16 v[232:235], a[92:95], a[56:59], v[232:235]\n"
"ds_read_b64_tr_b16 a[118:119], v11 offset:8320\n"
"s_waitcnt vmcnt(19)\n"
"s_barrier\n"
"s_waitcnt lgkmcnt(4)\n"
"v_mfma_f32_16x16x32_bf16 v[128:131], a[96:99], a[0:3], v[128:131]\n"
"ds_read_b64_tr_b16 a[120:121], v10 offset:192\n"
"s_add_u32 m0, s74, 98304\n"
"v_mfma_f32_16x16x32_bf16 v[132:135], a[96:99], a[4:7], v[132:135]\n"
"ds_read_b64_tr_b16 a[122:123], v10 offset:8384\n"
"buffer_load_dwordx4 v64, s[40:43], s79 offen offset:0 lds\n"
"v_mfma_f32_16x16x32_bf16 v[140:143], a[100:103], a[4:7], v[140:143]\n"
"ds_read_b64_tr_b16 a[124:125], v11 offset:192\n"
"v_mfma_f32_16x16x32_bf16 v[136:139], a[100:103], a[0:3], v[136:139]\n"
"ds_read_b64_tr_b16 a[126:127], v11 offset:8384\n"
"v_mfma_f32_16x16x32_bf16 v[144:147], a[104:107], a[0:3], v[144:147]\n"
"ds_read_b64_tr_b16 a[64:65], v10 offset:256\n"
"v_mfma_f32_16x16x32_bf16 v[148:151], a[104:107], a[4:7], v[148:151]\n"
"ds_read_b64_tr_b16 a[66:67], v10 offset:8448\n"
"buffer_load_dwordx4 v65, s[40:43], s79 offen offset:1024 lds\n"
"v_mfma_f32_16x16x32_bf16 v[156:159], a[108:111], a[4:7], v[156:159]\n"
"ds_read_b64_tr_b16 a[68:69], v11 offset:256\n"
"v_mfma_f32_16x16x32_bf16 v[152:155], a[108:111], a[0:3], v[152:155]\n"
"ds_read_b64_tr_b16 a[70:71], v11 offset:8448\n"
"s_waitcnt lgkmcnt(4)\n"
"v_mfma_f32_16x16x32_bf16 v[160:163], a[112:115], a[0:3], v[160:163]\n"
"ds_read_b64_tr_b16 a[72:73], v10 offset:320\n"
"v_mfma_f32_16x16x32_bf16 v[164:167], a[112:115], a[4:7], v[164:167]\n"
"ds_read_b64_tr_b16 a[74:75], v10 offset:8512\n"
"v_mfma_f32_16x16x32_bf16 v[172:175], a[116:119], a[4:7], v[172:175]\n"
"ds_read_b64_tr_b16 a[76:77], v11 offset:320\n"
"buffer_load_dwordx4 v66, s[40:43], s79 offen offset:2048 lds\n"
"v_mfma_f32_16x16x32_bf16 v[168:171], a[116:119], a[0:3], v[168:171]\n"
"ds_read_b64_tr_b16 a[78:79], v11 offset:8512\n"
"v_mfma_f32_16x16x32_bf16 v[176:179], a[120:123], a[0:3], v[176:179]\n"
"ds_read_b64_tr_b16 a[80:81], v10 offset:384\n"
"v_mfma_f32_16x16x32_bf16 v[180:183], a[120:123], a[4:7], v[180:183]\n"
"ds_read_b64_tr_b16 a[82:83], v10 offset:8576\n"
"v_mfma_f32_16x16x32_bf16 v[188:191], a[124:127], a[4:7], v[188:191]\n"
"ds_read_b64_tr_b16 a[84:85], v11 offset:384\n"
"buffer_load_dwordx4 v67, s[40:43], s79 offen offset:3072 lds\n"
"v_mfma_f32_16x16x32_bf16 v[184:187], a[124:127], a[0:3], v[184:187]\n"
"ds_read_b64_tr_b16 a[86:87], v11 offset:8576\n"
"s_waitcnt lgkmcnt(4)\n"
"v_mfma_f32_16x16x32_bf16 v[192:195], a[64:67], a[0:3], v[192:195]\n"
"ds_read_b64_tr_b16 a[88:89], v10 offset:16384\n"
"v_mfma_f32_16x16x32_bf16 v[196:199], a[64:67], a[4:7], v[196:199]\n"
"ds_read_b64_tr_b16 a[90:91], v10 offset:24576\n"
"v_mfma_f32_16x16x32_bf16 v[204:207], a[68:71], a[4:7], v[204:207]\n"
"ds_read_b64_tr_b16 a[92:93], v11 offset:16384\n"
"s_add_u32 m0, s74, 102400\n"
"v_mfma_f32_16x16x32_bf16 v[200:203], a[68:71], a[0:3], v[200:203]\n"
"ds_read_b64_tr_b16 a[94:95], v11 offset:24576\n"
"buffer_load_dwordx4 v68, s[40:43], s79 offen offset:0 lds\n"
"v_mfma_f32_16x16x32_bf16 v[208:211], a[72:75], a[0:3], v[208:211]\n"
"ds_read_b64_tr_b16 a[96:97], v10 offset:16448\n"
"v_mfma_f32_16x16x32_bf16 v[212:215], a[72:75], a[4:7], v[212:215]\n"
"ds_read_b64_tr_b16 a[98:99], v10 offset:24640\n"
"v_mfma_f32_16x16x32_bf16 v[220:223], a[76:79], a[4:7], v[220:223]\n"
"ds_read_b64_tr_b16 a[100:101], v11 offset:16448\n"
"v_mfma_f32_16x16x32_bf16 v[216:219], a[76:79], a[0:3], v[216:219]\n"
"ds_read_b64_tr_b16 a[102:103], v11 offset:24640\n"
"s_waitcnt lgkmcnt(4)\n"
"v_mfma_f32_16x16x32_bf16 v[224:227], a[80:83], a[0:3], v[224:227]\n"
"ds_read_b64_tr_b16 a[104:105], v10 offset:16512\n"
"buffer_load_dwordx4 v69, s[40:43], s79 offen offset:1024 lds\n"
"v_mfma_f32_16x16x32_bf16 v[228:231], a[80:83], a[4:7], v[228:231]\n"
"ds_read_b64_tr_b16 a[106:107], v10 offset:24704\n"
"v_mfma_f32_16x16x32_bf16 v[236:239], a[84:87], a[4:7], v[236:239]\n"
"ds_read_b64_tr_b16 a[108:109], v11 offset:16512\n"
"v_mfma_f32_16x16x32_bf16 v[232:235], a[84:87], a[0:3], v[232:235]\n"
"ds_read_b64_tr_b16 a[110:111], v11 offset:24704\n"
"v_mfma_f32_16x16x32_bf16 v[128:131], a[88:91], a[8:11], v[128:131]\n"
"ds_read_b64_tr_b16 a[112:113], v10 offset:16576\n"
"buffer_load_dwordx4 v70, s[40:43], s79 offen offset:2048 lds\n"
"v_mfma_f32_16x16x32_bf16 v[132:135], a[88:91], a[12:15], v[132:135]\n"
"ds_read_b64_tr_b16 a[114:115], v10 offset:24768\n"
"v_mfma_f32_16x16x32_bf16 v[140:143], a[92:95], a[12:15], v[140:143]\n"
"ds_read_b64_tr_b16 a[116:117], v11 offset:16576\n"
"v_mfma_f32_16x16x32_bf16 v[136:139], a[92:95], a[8:11], v[136:139]\n"
"ds_read_b64_tr_b16 a[118:119], v11 offset:24768\n"
"s_waitcnt lgkmcnt(4)\n"
"v_mfma_f32_16x16x32_bf16 v[144:147], a[96:99], a[8:11], v[144:147]\n"
"ds_read_b64_tr_b16 a[120:121], v10 offset:16640\n"
"v_mfma_f32_16x16x32_bf16 v[148:151], a[96:99], a[12:15], v[148:151]\n"
"ds_read_b64_tr_b16 a[122:123], v10 offset:24832\n"
"buffer_load_dwordx4 v71, s[40:43], s79 offen offset:3072 lds\n"
"v_mfma_f32_16x16x32_bf16 v[156:159], a[100:103], a[12:15], v[156:159]\n"
"ds_read_b64_tr_b16 a[124:125], v11 offset:16640\n"
"v_readlane_b32 s79, v20, 8\n"
"v_mfma_f32_16x16x32_bf16 v[152:155], a[100:103], a[8:11], v[152:155]\n"
"ds_read_b64_tr_b16 a[126:127], v11 offset:24832\n"
"v_mfma_f32_16x16x32_bf16 v[160:163], a[104:107], a[8:11], v[160:163]\n"
"ds_read_b64_tr_b16 a[64:65], v10 offset:16704\n"
"v_mfma_f32_16x16x32_bf16 v[164:167], a[104:107], a[12:15], v[164:167]\n"
"ds_read_b64_tr_b16 a[66:67], v10 offset:24896\n"
"buffer_load_dwordx4 a[48:51], v8, s[52:55], s82 offen offset:0 sc1\n"
"v_mfma_f32_16x16x32_bf16 v[172:175], a[108:111], a[12:15], v[172:175]\n"
"ds_read_b64_tr_b16 a[68:69], v11 offset:16704\n"
"v_mfma_f32_16x16x32_bf16 v[168:171], a[108:111], a[8:11], v[168:171]\n"
"ds_read_b64_tr_b16 a[70:71], v11 offset:24896\n"
"s_waitcnt lgkmcnt(4)\n"
"v_mfma_f32_16x16x32_bf16 v[176:179], a[112:115], a[8:11], v[176:179]\n"
"ds_read_b64_tr_b16 a[72:73], v10 offset:16768\n"
"v_mfma_f32_16x16x32_bf16 v[180:183], a[112:115], a[12:15], v[180:183]\n"
"ds_read_b64_tr_b16 a[74:75], v10 offset:24960\n"
"v_mfma_f32_16x16x32_bf16 v[188:191], a[116:119], a[12:15], v[188:191]\n"
"ds_read_b64_tr_b16 a[76:77], v11 offset:16768\n"
"buffer_load_dwordx4 a[52:55], v8, s[52:55], s82 offen offset:1024 sc1\n"
"v_mfma_f32_16x16x32_bf16 v[184:187], a[116:119], a[8:11], v[184:187]\n"
"ds_read_b64_tr_b16 a[78:79], v11 offset:24960\n"
"v_mfma_f32_16x16x32_bf16 v[192:195], a[120:123], a[8:11], v[192:195]\n"
"ds_read_b64_tr_b16 a[80:81], v10 offset:32768\n"
"v_mfma_f32_16x16x32_bf16 v[196:199], a[120:123], a[12:15], v[196:199]\n"
"ds_read_b64_tr_b16 a[82:83], v10 offset:40960\n"
"v_mfma_f32_16x16x32_bf16 v[204:207], a[124:127], a[12:15], v[204:207]\n"
"ds_read_b64_tr_b16 a[84:85], v11 offset:32768\n"
"buffer_load_dwordx4 a[56:59], v8, s[52:55], s82 offen offset:2048 sc1\n"
"v_mfma_f32_16x16x32_bf16 v[200:203], a[124:127], a[8:11], v[200:203]\n"
"ds_read_b64_tr_b16 a[86:87], v11 offset:40960\n"
"s_waitcnt lgkmcnt(4)\n"
"v_mfma_f32_16x16x32_bf16 v[208:211], a[64:67], a[8:11], v[208:211]\n"
"ds_read_b64_tr_b16 a[88:89], v10 offset:32832\n"
"v_mfma_f32_16x16x32_bf16 v[212:215], a[64:67], a[12:15], v[212:215]\n"
"ds_read_b64_tr_b16 a[90:91], v10 offset:41024\n"
"v_mfma_f32_16x16x32_bf16 v[220:223], a[68:71], a[12:15], v[220:223]\n"
"ds_read_b64_tr_b16 a[92:93], v11 offset:32832\n"
"v_mfma_f32_16x16x32_bf16 v[216:219], a[68:71], a[8:11], v[216:219]\n"
"ds_read_b64_tr_b16 a[94:95], v11 offset:41024\n"
"buffer_load_dwordx4 a[60:63], v8, s[52:55], s82 offen offset:3072 sc1\n"
"v_mfma_f32_16x16x32_bf16 v[224:227], a[72:75], a[8:11], v[224:227]\n"
"ds_read_b64_tr_b16 a[96:97], v10 offset:32896\n"
"v_readlane_b32 s82, v20, 24\n"
"v_mfma_f32_16x16x32_bf16 v[228:231], a[72:75], a[12:15], v[228:231]\n"
"ds_read_b64_tr_b16 a[98:99], v10 offset:41088\n"
"v_mfma_f32_16x16x32_bf16 v[236:239], a[76:79], a[12:15], v[236:239]\n"
"ds_read_b64_tr_b16 a[100:101], v11 offset:32896\n"
"v_mfma_f32_16x16x32_bf16 v[232:235], a[76:79], a[8:11], v[232:235]\n"
"ds_read_b64_tr_b16 a[102:103], v11 offset:41088\n"
"s_waitcnt vmcnt(16)\n"
"s_barrier\n"
"s_waitcnt lgkmcnt(4)\n"
"v_mfma_f32_16x16x32_bf16 v[128:131], a[80:83], a[16:19], v[128:131]\n"
"ds_read_b64_tr_b16 a[104:105], v10 offset:32960\n"
"s_add_u32 m0, s74, 0\n"
"v_mfma_f32_16x16x32_bf16 v[132:135], a[80:83], a[20:23], v[132:135]\n"
"ds_read_b64_tr_b16 a[106:107], v10 offset:41152\n"
"buffer_load_dwordx4 v64, s[40:43], s79 offen offset:0 lds\n"
"v_cmp_ne_u64 vcc, s[70:71], v[120:121]\n"
"s_cbranch_vccnz .Lpslow7_%=\n"
".Lpok7_%=:\n"
"v_mfma_f32_16x16x32_bf16 v[140:143], a[84:87], a[20:23], v[140:143]\n"
"ds_read_b64_tr_b16 a[108:109], v11 offset:32960\n"
"v_mfma_f32_16x16x32_bf16 v[136:139], a[84:87], a[16:19], v[136:139]\n"
"ds_read_b64_tr_b16 a[110:111], v11 offset:41152\n"
"v_mfma_f32_16x16x32_bf16 v[144:147], a[88:91], a[16:19], v[144:147]\n"
"ds_read_b64_tr_b16 a[112:113], v10 offset:33024\n"
"buffer_load_dword v104, v15, s[36:39], s81 offen offset:0 sc1\n"
"v_mfma_f32_16x16x32_bf16 v[148:151], a[88:91], a[20:23], v[148:151]\n"
"ds_read_b64_tr_b16 a[114:115], v10 offset:41216\n"
"buffer_load_dwordx4 v65, s[40:43], s79 offen offset:1024 lds\n"
"v_mfma_f32_16x16x32_bf16 v[156:159], a[92:95], a[20:23], v[156:159]\n"
"ds_read_b64_tr_b16 a[116:117], v11 offset:33024\n"
"v_mfma_f32_16x16x32_bf16 v[152:155], a[92:95], a[16:19], v[152:155]\n"
"ds_read_b64_tr_b16 a[118:119], v11 offset:41216\n"
"s_waitcnt lgkmcnt(4)\n"
"v_mfma_f32_16x16x32_bf16 v[160:163], a[96:99], a[16:19], v[160:163]\n"
"ds_read_b64_tr_b16 a[120:121], v10 offset:33088\n"
"buffer_load_dword v105, v15, s[36:39], s81 offen offset:1024 sc1\n"
"v_mfma_f32_16x16x32_bf16 v[164:167], a[96:99], a[20:23], v[164:167]\n"
"ds_read_b64_tr_b16 a[122:123], v10 offset:41280\n"
"v_mfma_f32_16x16x32_bf16 v[172:175], a[100:103], a[20:23], v[172:175]\n"
"ds_read_b64_tr_b16 a[124:125], v11 offset:33088\n"
"buffer_load_dwordx4 v66, s[40:43], s79 offen offset:2048 lds\n"
"v_mfma_f32_16x16x32_bf16 v[168:171], a[100:103], a[16:19], v[168:171]\n"
"ds_read_b64_tr_b16 a[126:127], v11 offset:41280\n"
"buffer_load_dword v106, v15, s[36:39], s81 offen offset:2048 sc1\n"
"v_mfma_f32_16x16x32_bf16 v[176:179], a[104:107], a[16:19], v[176:179]\n"
"ds_read_b64_tr_b16 a[64:65], v10 offset:33152\n"
"v_mfma_f32_16x16x32_bf16 v[180:183], a[104:107], a[20:23], v[180:183]\n"
"ds_read_b64_tr_b16 a[66:67], v10 offset:41344\n"
"v_mfma_f32_16x16x32_bf16 v[188:191], a[108:111], a[20:23], v[188:191]\n"
"ds_read_b64_tr_b16 a[68:69], v11 offset:33152\n"
"buffer_load_dwordx4 v67, s[40:43], s79 offen offset:3072 lds\n"
"v_mfma_f32_16x16x32_bf16 v[184:187], a[108:111], a[16:19], v[184:187]\n"
"ds_read_b64_tr_b16 a[70:71], v11 offset:41344\n"
"s_waitcnt lgkmcnt(4)\n"
"v_mfma_f32_16x16x32_bf16 v[192:195], a[112:115], a[16:19], v[192:195]\n"
"ds_read_b64_tr_b16 a[72:73], v10 offset:49152\n"
"buffer_load_dword v107, v15, s[36:39], s81 offen offset:3072 sc1\n"
"v_mfma_f32_16x16x32_bf16 v[196:199], a[112:115], a[20:23], v[196:199]\n"
"ds_read_b64_tr_b16 a[74:75], v10 offset:57344\n"
"s_add_u32 s81, s81, 4096\n"
"v_mfma_f32_16x16x32_bf16 v[204:207], a[116:119], a[20:23], v[204:207]\n"
"ds_read_b64_tr_b16 a[76:77], v11 offset:49152\n"
"buffer_load_dword v108, v15, s[36:39], s81 offen offset:0 sc1\n"
"v_mfma_f32_16x16x32_bf16 v[200:203], a[116:119], a[16:19], v[200:203]\n"
"ds_read_b64_tr_b16 a[78:79], v11 offset:57344\n"
"s_add_u32 m0, s74, 4096\n"
"v_mfma_f32_16x16x32_bf16 v[208:211], a[120:123], a[16:19], v[208:211]\n"
"ds_read_b64_tr_b16 a[80:81], v10 offset:49216\n"
"buffer_load_dwordx4 v68, s[40:43], s79 offen offset:0 lds\n"
"v_mfma_f32_16x16x32_bf16 v[212:215], a[120:123], a[20:23], v[212:215]\n"
"ds_read_b64_tr_b16 a[82:83], v10 offset:57408\n"
"buffer_load_dword v109, v15, s[36:39], s81 offen offset:1024 sc1\n"
"v_mfma_f32_16x16x32_bf16 v[220:223], a[124:127], a[20:23], v[220:223]\n"
"ds_read_b64_tr_b16 a[84:85], v11 offset:49216\n"
"v_mfma_f32_16x16x32_bf16 v[216:219], a[124:127], a[16:19], v[216:219]\n"
"ds_read_b64_tr_b16 a[86:87], v11 offset:57408\n"
"s_waitcnt lgkmcnt(4)\n"
"v_mfma_f32_16x16x32_bf16 v[224:227], a[64:67], a[16:19], v[224:227]\n"
"ds_read_b64_tr_b16 a[88:89], v10 offset:49280\n"
"buffer_load_dwordx4 v69, s[40:43], s79 offen offset:1024 lds\n"
"v_mfma_f32_16x16x32_bf16 v[228:231], a[64:67], a[20:23], v[228:231]\n"
"ds_read_b64_tr_b16 a[90:91], v10 offset:57472\n"
"buffer_load_dword v110, v15, s[36:39], s81 offen offset:2048 sc1\n"
"v_mfma_f32_16x16x32_bf16 v[236:239], a[68:71], a[20:23], v[236:239]\n"
"ds_read_b64_tr_b16 a[92:93], v11 offset:49280\n"
"v_mfma_f32_16x16x32_bf16 v[232:235], a[68:71], a[16:19], v[232:235]\n"
"ds_read_b64_tr_b16 a[94:95], v11 offset:57472\n"
"buffer_load_dword v111, v15, s[36:39], s81 offen offset:3072 sc1\n"
"v_mfma_f32_16x16x32_bf16 v[128:131], a[72:75], a[24:27], v[128:131]\n"
"ds_read_b64_tr_b16 a[96:97], v10 offset:49344\n"
"s_add_u32 s81, s81, 4096\n"
"v_mfma_f32_16x16x32_bf16 v[132:135], a[72:75], a[28:31], v[132:135]\n"
"ds_read_b64_tr_b16 a[98:99], v10 offset:57536\n"
"buffer_load_dwordx4 v70, s[40:43], s79 offen offset:2048 lds\n"
"v_mfma_f32_16x16x32_bf16 v[140:143], a[76:79], a[28:31], v[140:143]\n"
"ds_read_b64_tr_b16 a[100:101], v11 offset:49344\n"
"buffer_load_dword v112, v15, s[36:39], s81 offen offset:0 sc1\n"
"v_mfma_f32_16x16x32_bf16 v[136:139], a[76:79], a[24:27], v[136:139]\n"
"ds_read_b64_tr_b16 a[102:103], v11 offset:57536\n"
"s_waitcnt lgkmcnt(4)\n"
"v_mfma_f32_16x16x32_bf16 v[144:147], a[80:83], a[24:27], v[144:147]\n"
"ds_read_b64_tr_b16 a[104:105], v10 offset:49408\n"
"v_mfma_f32_16x16x32_bf16 v[148:151], a[80:83], a[28:31], v[148:151]\n"
"ds_read_b64_tr_b16 a[106:107], v10 offset:57600\n"
"buffer_load_dwordx4 v71, s[40:43], s79 offen offset:3072 lds\n"
"v_mfma_f32_16x16x32_bf16 v[156:159], a[84:87], a[28:31], v[156:159]\n"
"ds_read_b64_tr_b16 a[108:109], v11 offset:49408\n"
"v_readlane_b32 s79, v20, 9\n"
"v_mfma_f32_16x16x32_bf16 v[152:155], a[84:87], a[24:27], v[152:155]\n"
"ds_read_b64_tr_b16 a[110:111], v11 offset:57600\n"
"buffer_load_dword v113, v15, s[36:39], s81 offen offset:1024 sc1\n"
"v_mfma_f32_16x16x32_bf16 v[160:163], a[88:91], a[24:27], v[160:163]\n"
"ds_read_b64_tr_b16 a[112:113], v10 offset:49472\n"
"v_mfma_f32_16x16x32_bf16 v[164:167], a[88:91], a[28:31], v[164:167]\n"
"ds_read_b64_tr_b16 a[114:115], v10 offset:57664\n"
"buffer_load_dwordx4 a[0:3], v8, s[52:55], s82 offen offset:0 sc1\n"
"v_mfma_f32_16x16x32_bf16 v[172:175], a[92:95], a[28:31], v[172:175]\n"
"ds_read_b64_tr_b16 a[116:117], v11 offset:49472\n"
"buffer_load_dword v114, v15, s[36:39], s81 offen offset:2048 sc1\n"
"v_mfma_f32_16x16x32_bf16 v[168:171], a[92:95], a[24:27], v[168:171]\n"
"ds_read_b64_tr_b16 a[118:119], v11 offset:57664\n"
"s_waitcnt lgkmcnt(4)\n"
"v_mfma_f32_16x16x32_bf16 v[176:179], a[96:99], a[24:27], v[176:179]\n"
"ds_read_b64_tr_b16 a[120:121], v10 offset:49536\n"
"buffer_load_dword v115, v15, s[36:39], s81 offen offset:3072 sc1\n"
"v_mfma_f32_16x16x32_bf16 v[180:183], a[96:99], a[28:31], v[180:183]\n"
"ds_read_b64_tr_b16 a[122:123], v10 offset:57728\n"
"s_add_u32 s81, s81, 4096\n"
"v_mfma_f32_16x16x32_bf16 v[188:191], a[100:103], a[28:31], v[188:191]\n"
"ds_read_b64_tr_b16 a[124:125], v11 offset:49536\n"
"buffer_load_dwordx4 a[4:7], v8, s[52:55], s82 offen offset:1024 sc1\n"
"v_mfma_f32_16x16x32_bf16 v[184:187], a[100:103], a[24:27], v[184:187]\n"
"ds_read_b64_tr_b16 a[126:127], v11 offset:57728\n"
"buffer_load_dword v116, v15, s[36:39], s81 offen offset:0 sc1\n"
"v_mfma_f32_16x16x32_bf16 v[192:195], a[104:107], a[24:27], v[192:195]\n"
"ds_read_b64_tr_b16 a[64:65], v12 offset:0\n"
"v_mfma_f32_16x16x32_bf16 v[196:199], a[104:107], a[28:31], v[196:199]\n"
"ds_read_b64_tr_b16 a[66:67], v12 offset:8192\n"
"v_mfma_f32_16x16x32_bf16 v[204:207], a[108:111], a[28:31], v[204:207]\n"
"ds_read_b64_tr_b16 a[68:69], v13 offset:0\n"
"buffer_load_dwordx4 a[8:11], v8, s[52:55], s82 offen offset:2048 sc1\n"
"v_mfma_f32_16x16x32_bf16 v[200:203], a[108:111], a[24:27], v[200:203]\n"
"ds_read_b64_tr_b16 a[70:71], v13 offset:8192\n"
"s_waitcnt lgkmcnt(4)\n"
"v_mfma_f32_16x16x32_bf16 v[208:211], a[112:115], a[24:27], v[208:211]\n"
"ds_read_b64_tr_b16 a[72:73], v12 offset:64\n"
"buffer_load_dword v117, v15, s[36:39], s81 offen offset:1024 sc1\n"
"v_mfma_f32_16x16x32_bf16 v[212:215], a[112:115], a[28:31], v[212:215]\n"
"ds_read_b64_tr_b16 a[74:75], v12 offset:8256\n"
"v_mfma_f32_16x16x32_bf16 v[220:223], a[116:119], a[28:31], v[220:223]\n"
"ds_read_b64_tr_b16 a[76:77], v13 offset:64\n"
"buffer_load_dword v118, v15, s[36:39], s81 offen offset:2048 sc1\n"
"v_mfma_f32_16x16x32_bf16 v[216:219], a[116:119], a[24:27], v[216:219]\n"
"ds_read_b64_tr_b16 a[78:79], v13 offset:8256\n"
"buffer_load_dwordx4 a[12:15], v8, s[52:55], s82 offen offset:3072 sc1\n"
"v_mfma_f32_16x16x32_bf16 v[224:227], a[120:123], a[24:27], v[224:227]\n"
"ds_read_b64_tr_b16 a[80:81], v12 offset:128\n"
"v_readlane_b32 s82, v20, 25\n"
"v_mfma_f32_16x16x32_bf16 v[228:231], a[120:123], a[28:31], v[228:231]\n"
"ds_read_b64_tr_b16 a[82:83], v12 offset:8320\n"
"buffer_load_dword v119, v15, s[36:39], s81 offen offset:3072 sc1\n"
"v_mfma_f32_16x16x32_bf16 v[236:239], a[124:127], a[28:31], v[236:239]\n"
"ds_read_b64_tr_b16 a[84:85], v13 offset:128\n"
"v_mfma_f32_16x16x32_bf16 v[232:235], a[124:127], a[24:27], v[232:235]\n"
"ds_read_b64_tr_b16 a[86:87], v13 offset:8320\n"
"s_waitcnt vmcnt(32)\n"
"s_barrier\n"
"s_waitcnt lgkmcnt(4)\n"
"v_mfma_f32_16x16x32_bf16 v[128:131], a[64:67], a[32:35], v[128:131]\n"
"ds_read_b64_tr_b16 a[88:89], v12 offset:192\n"
"s_add_u32 m0, s74, 32768\n"
"v_mfma_f32_16x16x32_bf16 v[132:135], a[64:67], a[36:39], v[132:135]\n"
"ds_read_b64_tr_b16 a[90:91], v12 offset:8384\n"
"buffer_load_dwordx4 v64, s[40:43], s79 offen offset:0 lds\n"
"v_mfma_f32_16x16x32_bf16 v[140:143], a[68:71], a[36:39], v[140:143]\n"
"ds_read_b64_tr_b16 a[92:93], v13 offset:192\n"
"v_mfma_f32_16x16x32_bf16 v[136:139], a[68:71], a[32:35], v[136:139]\n"
"ds_read_b64_tr_b16 a[94:95], v13 offset:8384\n"
"v_mfma_f32_16x16x32_bf16 v[144:147], a[72:75], a[32:35], v[144:147]\n"
"ds_read_b64_tr_b16 a[96:97], v12 offset:256\n"
"v_mfma_f32_16x16x32_bf16 v[148:151], a[72:75], a[36:39], v[148:151]\n"
"ds_read_b64_tr_b16 a[98:99], v12 offset:8448\n"
"buffer_load_dwordx4 v65, s[40:43], s79 offen offset:1024 lds\n"
"v_mfma_f32_16x16x32_bf16 v[156:159], a[76:79], a[36:39], v[156:159]\n"
"ds_read_b64_tr_b16 a[100:101], v13 offset:256\n"
"v_mfma_f32_16x16x32_bf16 v[152:155], a[76:79], a[32:35], v[152:155]\n"
"ds_read_b64_tr_b16 a[102:103], v13 offset:8448\n"
"s_waitcnt lgkmcnt(4)\n"
"v_mfma_f32_16x16x32_bf16 v[160:163], a[80:83], a[32:35], v[160:163]\n"
"ds_read_b64_tr_b16 a[104:105], v12 offset:320\n"
"v_mfma_f32_16x16x32_bf16 v[164:167], a[80:83], a[36:39], v[164:167]\n"
"ds_read_b64_tr_b16 a[106:107], v12 offset:8512\n"
"v_mfma_f32_16x16x32_bf16 v[172:175], a[84:87], a[36:39], v[172:175]\n"
"ds_read_b64_tr_b16 a[108:109], v13 offset:320\n"
"buffer_load_dwordx4 v66, s[40:43], s79 offen offset:2048 lds\n"
"v_mfma_f32_16x16x32_bf16 v[168:171], a[84:87], a[32:35], v[168:171]\n"
"ds_read_b64_tr_b16 a[110:111], v13 offset:8512\n"
"v_mfma_f32_16x16x32_bf16 v[176:179], a[88:91], a[32:35], v[176:179]\n"
"ds_read_b64_tr_b16 a[112:113], v12 offset:384\n"
"v_mfma_f32_16x16x32_bf16 v[180:183], a[88:91], a[36:39], v[180:183]\n"
"ds_read_b64_tr_b16 a[114:115], v12 offset:8576\n"
"v_mfma_f32_16x16x32_bf16 v[188:191], a[92:95], a[36:39], v[188:191]\n"
"ds_read_b64_tr_b16 a[116:117], v13 offset:384\n"
"buffer_load_dwordx4 v67, s[40:43], s79 offen offset:3072 lds\n"
"v_mfma_f32_16x16x32_bf16 v[184:187], a[92:95], a[32:35], v[184:187]\n"
"ds_read_b64_tr_b16 a[118:119], v13 offset:8576\n"
"s_waitcnt lgkmcnt(4)\n"
"v_mfma_f32_16x16x32_bf16 v[192:195], a[96:99], a[32:35], v[192:195]\n"
"ds_read_b64_tr_b16 a[120:121], v12 offset:16384\n"
"v_mfma_f32_16x16x32_bf16 v[196:199], a[96:99], a[36:39], v[196:199]\n"
"ds_read_b64_tr_b16 a[122:123], v12 offset:24576\n"
"v_mfma_f32_16x16x32_bf16 v[204:207], a[100:103], a[36:39], v[204:207]\n"
"ds_read_b64_tr_b16 a[124:125], v13 offset:16384\n"
"s_add_u32 m0, s74, 36864\n"
"v_mfma_f32_16x16x32_bf16 v[200:203], a[100:103], a[32:35], v[200:203]\n"
"ds_read_b64_tr_b16 a[126:127], v13 offset:24576\n"
"buffer_load_dwordx4 v68, s[40:43], s79 offen offset:0 lds\n"
"v_mfma_f32_16x16x32_bf16 v[208:211], a[104:107], a[32:35], v[208:211]\n"
"ds_read_b64_tr_b16 a[64:65], v12 offset:16448\n"
"v_mfma_f32_16x16x32_bf16 v[212:215], a[104:107], a[36:39], v[212:215]\n"
"ds_read_b64_tr_b16 a[66:67], v12 offset:24640\n"
"v_mfma_f32_16x16x32_bf16 v[220:223], a[108:111], a[36:39], v[220:223]\n"
"ds_read_b64_tr_b16 a[68:69], v13 offset:16448\n"
"v_mfma_f32_16x16x32_bf16 v[216:219], a[108:111], a[32:35], v[216:219]\n"
"ds_read_b64_tr_b16 a[70:71], v13 offset:24640\n"
"s_waitcnt lgkmcnt(4)\n"
"v_mfma_f32_16x16x32_bf16 v[224:227], a[112:115], a[32:35], v[224:227]\n"
"ds_read_b64_tr_b16 a[72:73], v12 offset:16512\n"
"buffer_load_dwordx4 v69, s[40:43], s79 offen offset:1024 lds\n"
"v_mfma_f32_16x16x32_bf16 v[228:231], a[112:115], a[36:39], v[228:231]\n"
"ds_read_b64_tr_b16 a[74:75], v12 offset:24704\n"
"v_mfma_f32_16x16x32_bf16 v[236:239], a[116:119], a[36:39], v[236:239]\n"
"ds_read_b64_tr_b16 a[76:77], v13 offset:16512\n"
"v_mfma_f32_16x16x32_bf16 v[232:235], a[116:119], a[32:35], v[232:235]\n"
"ds_read_b64_tr_b16 a[78:79], v13 offset:24704\n"
"v_mfma_f32_16x16x32_bf16 v[128:131], a[120:123], a[40:43], v[128:131]\n"
"ds_read_b64_tr_b16 a[80:81], v12 offset:16576\n"
"buffer_load_dwordx4 v70, s[40:43], s79 offen offset:2048 lds\n"
"v_mfma_f32_16x16x32_bf16 v[132:135], a[120:123], a[44:47], v[132:135]\n"
"ds_read_b64_tr_b16 a[82:83], v12 offset:24768\n"
"v_mfma_f32_16x16x32_bf16 v[140:143], a[124:127], a[44:47], v[140:143]\n"
"ds_read_b64_tr_b16 a[84:85], v13 offset:16576\n"
"v_mfma_f32_16x16x32_bf16 v[136:139], a[124:127], a[40:43], v[136:139]\n"
"ds_read_b64_tr_b16 a[86:87], v13 offset:24768\n"
"s_waitcnt lgkmcnt(4)\n"
"v_mfma_f32_16x16x32_bf16 v[144:147], a[64:67], a[40:43], v[144:147]\n"
"ds_read_b64_tr_b16 a[88:89], v12 offset:16640\n"
"v_mfma_f32_16x16x32_bf16 v[148:151], a[64:67], a[44:47], v[148:151]\n"
"ds_read_b64_tr_b16 a[90:91], v12 offset:24832\n"
"buffer_load_dwordx4 v71, s[40:43], s79 offen offset:3072 lds\n"
"v_mfma_f32_16x16x32_bf16 v[156:159], a[68:71], a[44:47], v[156:159]\n"
"ds_read_b64_tr_b16 a[92:93], v13 offset:16640\n"
"v_readlane_b32 s79, v20, 10\n"
"v_mfma_f32_16x16x32_bf16 v[152:155], a[68:71], a[40:43], v[152:155]\n"
"ds_read_b64_tr_b16 a[94:95], v13 offset:24832\n"
"v_mfma_f32_16x16x32_bf16 v[160:163], a[72:75], a[40:43], v[160:163]\n"
"ds_read_b64_tr_b16 a[96:97], v12 offset:16704\n"
"v_mfma_f32_16x16x32_bf16 v[164:167], a[72:75], a[44:47], v[164:167]\n"
"ds_read_b64_tr_b16 a[98:99], v12 offset:24896\n"
"buffer_load_dwordx4 a[16:19], v8, s[52:55], s82 offen offset:0 sc1\n"
"v_mfma_f32_16x16x32_bf16 v[172:175], a[76:79], a[44:47], v[172:175]\n"
"ds_read_b64_tr_b16 a[100:101], v13 offset:16704\n"
"v_mfma_f32_16x16x32_bf16 v[168:171], a[76:79], a[40:43], v[168:171]\n"
"ds_read_b64_tr_b16 a[102:103], v13 offset:24896\n"
"s_waitcnt lgkmcnt(4)\n"
"v_mfma_f32_16x16x32_bf16 v[176:179], a[80:83], a[40:43], v[176:179]\n"
"ds_read_b64_tr_b16 a[104:105], v12 offset:16768\n"
"v_mfma_f32_16x16x32_bf16 v[180:183], a[80:83], a[44:47], v[180:183]\n"
"ds_read_b64_tr_b16 a[106:107], v12 offset:24960\n"
"v_mfma_f32_16x16x32_bf16 v[188:191], a[84:87], a[44:47], v[188:191]\n"
"ds_read_b64_tr_b16 a[108:109], v13 offset:16768\n"
"buffer_load_dwordx4 a[20:23], v8, s[52:55], s82 offen offset:1024 sc1\n"
"v_mfma_f32_16x16x32_bf16 v[184:187], a[84:87], a[40:43], v[184:187]\n"
"ds_read_b64_tr_b16 a[110:111], v13 offset:24960\n"
"v_mfma_f32_16x16x32_bf16 v[192:195], a[88:91], a[40:43], v[192:195]\n"
"ds_read_b64_tr_b16 a[112:113], v12 offset:32768\n"
"v_mfma_f32_16x16x32_bf16 v[196:199], a[88:91], a[44:47], v[196:199]\n"
"ds_read_b64_tr_b16 a[114:115], v12 offset:40960\n"
"v_mfma_f32_16x16x32_bf16 v[204:207], a[92:95], a[44:47], v[204:207]\n"
"ds_read_b64_tr_b16 a[116:117], v13 offset:32768\n"
"buffer_load_dwordx4 a[24:27], v8, s[52:55], s82 offen offset:2048 sc1\n"
"v_mfma_f32_16x16x32_bf16 v[200:203], a[92:95], a[40:43], v[200:203]\n"
"ds_read_b64_tr_b16 a[118:119], v13 offset:40960\n"
"s_waitcnt lgkmcnt(4)\n"
"v_mfma_f32_16x16x32_bf16 v[208:211], a[96:99], a[40:43], v[208:211]\n"
"ds_read_b64_tr_b16 a[120:121], v12 offset:32832\n"
"v_mfma_f32_16x16x32_bf16 v[212:215], a[96:99], a[44:47], v[212:215]\n"
"ds_read_b64_tr_b16 a[122:123], v12 offset:41024\n"
"v_mfma_f32_16x16x32_bf16 v[220:223], a[100:103], a[44:47], v[220:223]\n"
"ds_read_b64_tr_b16 a[124:125], v13 offset:32832\n"
"v_mfma_f32_16x16x32_bf16 v[216:219], a[100:103], a[40:43], v[216:219]\n"
"ds_read_b64_tr_b16 a[126:127], v13 offset:41024\n"
"buffer_load_dwordx4 a[28:31], v8, s[52:55], s82 offen offset:3072 sc1\n"
"v_mfma_f32_16x16x32_bf16 v[224:227], a[104:107], a[40:43], v[224:227]\n"
"ds_read_b64_tr_b16 a[64:65], v12 offset:32896\n"
"v_readlane_b32 s82, v20, 26\n"
"v_mfma_f32_16x16x32_bf16 v[228:231], a[104:107], a[44:47], v[228:231]\n"
"ds_read_b64_tr_b16 a[66:67], v12 offset:41088\n"
"v_mfma_f32_16x16x32_bf16 v[236:239], a[108:111], a[44:47], v[236:239]\n"
"ds_read_b64_tr_b16 a[68:69], v13 offset:32896\n"
"v_mfma_f32_16x16x32_bf16 v[232:235], a[108:111], a[40:43], v[232:235]\n"
"ds_read_b64_tr_b16 a[70:71], v13 offset:41088\n"
"s_waitcnt vmcnt(23)\n"
"s_barrier\n"
"s_waitcnt lgkmcnt(4)\n"
"v_mfma_f32_16x16x32_bf16 v[128:131], a[112:115], a[48:51], v[128:131]\n"
"ds_read_b64_tr_b16 a[72:73], v12 offset:32960\n"
"s_add_u32 m0, s74, 65536\n"
"v_mfma_f32_16x16x32_bf16 v[132:135], a[112:115], a[52:55], v[132:135]\n"
"ds_read_b64_tr_b16 a[74:75], v12 offset:41152\n"
"buffer_load_dwordx4 v64, s[40:43], s79 offen offset:0 lds\n"
"v_mfma_f32_16x16x32_bf16 v[140:143], a[116:119], a[52:55], v[140:143]\n"
"ds_read_b64_tr_b16 a[76:77], v13 offset:32960\n"
"v_mfma_f32_16x16x32_bf16 v[136:139], a[116:119], a[48:51], v[136:139]\n"
"ds_read_b64_tr_b16 a[78:79], v13 offset:41152\n"
"v_mfma_f32_16x16x32_bf16 v[144:147], a[120:123], a[48:51], v[144:147]\n"
"ds_read_b64_tr_b16 a[80:81], v12 offset:33024\n"
"v_mfma_f32_16x16x32_bf16 v[148:151], a[120:123], a[52:55], v[148:151]\n"
"ds_read_b64_tr_b16 a[82:83], v12 offset:41216\n"
"buffer_load_dwordx4 v65, s[40:43], s79 offen offset:1024 lds\n"
"v_mfma_f32_16x16x32_bf16 v[156:159], a[124:127], a[52:55], v[156:159]\n"
"ds_read_b64_tr_b16 a[84:85], v13 offset:33024\n"
"v_mfma_f32_16x16x32_bf16 v[152:155], a[124:127], a[48:51], v[152:155]\n"
"ds_read_b64_tr_b16 a[86:87], v13 offset:41216\n"
"s_waitcnt lgkmcnt(4)\n"
"v_mfma_f32_16x16x32_bf16 v[160:163], a[64:67], a[48:51], v[160:163]\n"
"ds_read_b64_tr_b16 a[88:89], v12 offset:33088\n"
"v_mfma_f32_16x16x32_bf16 v[164:167], a[64:67], a[52:55], v[164:167]\n"
"ds_read_b64_tr_b16 a[90:91], v12 offset:41280\n"
"v_mfma_f32_16x16x32_bf16 v[172:175], a[68:71], a[52:55], v[172:175]\n"
"ds_read_b64_tr_b16 a[92:93], v13 offset:33088\n"
"buffer_load_dwordx4 v66, s[40:43], s79 offen offset:2048 lds\n"
"v_mfma_f32_16x16x32_bf16 v[168:171], a[68:71], a[48:51], v[168:171]\n"
"ds_read_b64_tr_b16 a[94:95], v13 offset:41280\n"
"v_mfma_f32_16x16x32_bf16 v[176:179], a[72:75], a[48:51], v[176:179]\n"
"ds_read_b64_tr_b16 a[96:97], v12 offset:33152\n"
"v_mfma_f32_16x16x32_bf16 v[180:183], a[72:75], a[52:55], v[180:183]\n"
"ds_read_b64_tr_b16 a[98:99], v12 offset:41344\n"
"buffer_load_dword a235, v24, s[56:59], s95 offen offset:0\n"
"v_mfma_f32_16x16x32_bf16 v[188:191], a[76:79], a[52:55], v[188:191]\n"
"ds_read_b64_tr_b16 a[100:101], v13 offset:33152\n"
"s_add_u32 s95, s95, 14336\n"
"v_mfma_f32_16x16x32_bf16 v[184:187], a[76:79], a[48:51], v[184:187]\n"
"ds_read_b64_tr_b16 a[102:103], v13 offset:41344\n"
"s_waitcnt lgkmcnt(4)\n"
"v_mfma_f32_16x16x32_bf16 v[192:195], a[80:83], a[48:51], v[192:195]\n"
"ds_read_b64_tr_b16 a[104:105], v12 offset:49152\n"
"buffer_load_dwordx4 v67, s[40:43], s79 offen offset:3072 lds\n"
"v_mfma_f32_16x16x32_bf16 v[196:199], a[80:83], a[52:55], v[196:199]\n"
"ds_read_b64_tr_b16 a[106:107], v12 offset:57344\n"
"v_mfma_f32_16x16x32_bf16 v[204:207], a[84:87], a[52:55], v[204:207]\n"
"ds_read_b64_tr_b16 a[108:109], v13 offset:49152\n"
"s_add_u32 m0, s74, 69632\n"
"v_mfma_f32_16x16x32_bf16 v[200:203], a[84:87], a[48:51], v[200:203]\n"
"ds_read_b64_tr_b16 a[110:111], v13 offset:57344\n"
"buffer_load_dwordx4 v68, s[40:43], s79 offen offset:0 lds\n"
"v_mfma_f32_16x16x32_bf16 v[208:211], a[88:91], a[48:51], v[208:211]\n"
"ds_read_b64_tr_b16 a[112:113], v12 offset:49216\n"
"v_mfma_f32_16x16x32_bf16 v[212:215], a[88:91], a[52:55], v[212:215]\n"
"ds_read_b64_tr_b16 a[114:115], v12 offset:57408\n"
"v_mfma_f32_16x16x32_bf16 v[220:223], a[92:95], a[52:55], v[220:223]\n"
"ds_read_b64_tr_b16 a[116:117], v13 offset:49216\n"
"v_mfma_f32_16x16x32_bf16 v[216:219], a[92:95], a[48:51], v[216:219]\n"
"ds_read_b64_tr_b16 a[118:119], v13 offset:57408\n"
"s_waitcnt lgkmcnt(4)\n"
"v_mfma_f32_16x16x32_bf16 v[224:227], a[96:99], a[48:51], v[224:227]\n"
"ds_read_b64_tr_b16 a[120:121], v12 offset:49280\n"
"buffer_load_dwordx4 v69, s[40:43], s79 offen offset:1024 lds\n"
"v_mfma_f32_16x16x32_bf16 v[228:231], a[96:99], a[52:55], v[228:231]\n"
"ds_read_b64_tr_b16 a[122:123], v12 offset:57472\n"
"v_mfma_f32_16x16x32_bf16 v[236:239], a[100:103], a[52:55], v[236:239]\n"
"ds_read_b64_tr_b16 a[124:125], v13 offset:49280\n"
"v_mfma_f32_16x16x32_bf16 v[232:235], a[100:103], a[48:51], v[232:235]\n"
"ds_read_b64_tr_b16 a[126:127], v13 offset:57472\n"
"v_mfma_f32_16x16x32_bf16 v[128:131], a[104:107], a[56:59], v[128:131]\n"
"ds_read_b64_tr_b16 a[64:65], v12 offset:49344\n"
"buffer_load_dwordx4 v70, s[40:43], s79 offen offset:2048 lds\n"
"v_mfma_f32_16x16x32_bf16 v[132:135], a[104:107], a[60:63], v[132:135]\n"
"ds_read_b64_tr_b16 a[66:67], v12 offset:57536\n"
"v_mfma_f32_16x16x32_bf16 v[140:143], a[108:111], a[60:63], v[140:143]\n"
"ds_read_b64_tr_b16 a[68:69], v13 offset:49344\n"
"v_mfma_f32_16x16x32_bf16 v[136:139], a[108:111], a[56:59], v[136:139]\n"
"ds_read_b64_tr_b16 a[70:71], v13 offset:57536\n"
"s_waitcnt lgkmcnt(4)\n"
"v_mfma_f32_16x16x32_bf16 v[144:147], a[112:115], a[56:59], v[144:147]\n"
"ds_read_b64_tr_b16 a[72:73], v12 offset:49408\n"
"v_mfma_f32_16x16x32_bf16 v[148:151], a[112:115], a[60:63], v[148:151]\n"
"ds_read_b64_tr_b16 a[74:75], v12 offset:57600\n"
"buffer_load_dwordx4 v71, s[40:43], s79 offen offset:3072 lds\n"
"v_mfma_f32_16x16x32_bf16 v[156:159], a[116:119], a[60:63], v[156:159]\n"
"ds_read_b64_tr_b16 a[76:77], v13 offset:49408\n"
"v_readlane_b32 s79, v20, 11\n"
"v_mfma_f32_16x16x32_bf16 v[152:155], a[116:119], a[56:59], v[152:155]\n"
"ds_read_b64_tr_b16 a[78:79], v13 offset:57600\n"
"v_mfma_f32_16x16x32_bf16 v[160:163], a[120:123], a[56:59], v[160:163]\n"
"ds_read_b64_tr_b16 a[80:81], v12 offset:49472\n"
"v_mfma_f32_16x16x32_bf16 v[164:167], a[120:123], a[60:63], v[164:167]\n"
"ds_read_b64_tr_b16 a[82:83], v12 offset:57664\n"
"buffer_load_dwordx4 a[32:35], v8, s[52:55], s82 offen offset:0 sc1\n"
"v_mfma_f32_16x16x32_bf16 v[172:175], a[124:127], a[60:63], v[172:175]\n"
"ds_read_b64_tr_b16 a[84:85], v13 offset:49472\n"
"v_mfma_f32_16x16x32_bf16 v[168:171], a[124:127], a[56:59], v[168:171]\n"
"ds_read_b64_tr_b16 a[86:87], v13 offset:57664\n"
"s_waitcnt lgkmcnt(4)\n"
"v_mfma_f32_16x16x32_bf16 v[176:179], a[64:67], a[56:59], v[176:179]\n"
"ds_read_b64_tr_b16 a[88:89], v12 offset:49536\n"
"v_mfma_f32_16x16x32_bf16 v[180:183], a[64:67], a[60:63], v[180:183]\n"
"ds_read_b64_tr_b16 a[90:91], v12 offset:57728\n"
"v_add_u32 v15, 147456, v15\n"
"v_lshlrev_b32 v123, 16, v123\n"
"v_mfma_f32_16x16x32_bf16 v[188:191], a[68:71], a[60:63], v[188:191]\n"
"ds_read_b64_tr_b16 a[92:93], v13 offset:49536\n"
"ds_write_b32 v15, v123 offset:1024\n"
"v_add_f32 v104, v104, v105\n"
"v_mfma_f32_16x16x32_bf16 v[184:187], a[68:71], a[56:59], v[184:187]\n"
"ds_read_b64_tr_b16 a[94:95], v13 offset:57728\n"
"v_add_f32 v104, v104, v106\n"
"v_add_f32 v104, v104, v107\n"
"v_mfma_f32_16x16x32_bf16 v[192:195], a[72:75], a[56:59], v[192:195]\n"
"ds_read_b64_tr_b16 a[96:97], v10 offset:0\n"
"v_add_f32 v104, v104, v108\n"
"v_add_f32 v104, v104, v109\n"
"v_mfma_f32_16x16x32_bf16 v[196:199], a[72:75], a[60:63], v[196:199]\n"
"ds_read_b64_tr_b16 a[98:99], v10 offset:8192\n"
"v_add_f32 v104, v104, v110\n"
"v_mfma_f32_16x16x32_bf16 v[204:207], a[76:79], a[60:63], v[204:207]\n"
"ds_read_b64_tr_b16 a[100:101], v11 offset:0\n"
"v_add_f32 v104, v104, v111\n"
"v_mfma_f32_16x16x32_bf16 v[200:203], a[76:79], a[56:59], v[200:203]\n"
"ds_read_b64_tr_b16 a[102:103], v11 offset:8192\n"
"s_waitcnt lgkmcnt(4)\n"
"v_mfma_f32_16x16x32_bf16 v[208:211], a[80:83], a[56:59], v[208:211]\n"
"ds_read_b64_tr_b16 a[104:105], v10 offset:64\n"
"v_add_f32 v104, v104, v112\n"
"v_mfma_f32_16x16x32_bf16 v[212:215], a[80:83], a[60:63], v[212:215]\n"
"ds_read_b64_tr_b16 a[106:107], v10 offset:8256\n"
"s_waitcnt vmcnt(32)\n"
"v_add_f32 v104, v104, v113\n"
"v_mfma_f32_16x16x32_bf16 v[220:223], a[84:87], a[60:63], v[220:223]\n"
"ds_read_b64_tr_b16 a[108:109], v11 offset:64\n"
"s_waitcnt vmcnt(30)\n"
"v_add_f32 v104, v104, v114\n"
"v_mfma_f32_16x16x32_bf16 v[216:219], a[84:87], a[56:59], v[216:219]\n"
"ds_read_b64_tr_b16 a[110:111], v11 offset:8256\n"
"s_waitcnt vmcnt(29)\n"
"v_add_f32 v104, v104, v115\n"
"v_mfma_f32_16x16x32_bf16 v[224:227], a[88:91], a[56:59], v[224:227]\n"
"ds_read_b64_tr_b16 a[112:113], v10 offset:128\n"
"s_waitcnt vmcnt(27)\n"
"v_add_f32 v104, v104, v116\n"
"v_mfma_f32_16x16x32_bf16 v[228:231], a[88:91], a[60:63], v[228:231]\n"
"ds_read_b64_tr_b16 a[114:115], v10 offset:8320\n"
"s_waitcnt vmcnt(25)\n"
"v_add_f32 v104, v104, v117\n"
"v_mfma_f32_16x16x32_bf16 v[236:239], a[92:95], a[60:63], v[236:239]\n"
"ds_read_b64_tr_b16 a[116:117], v11 offset:128\n"
"s_waitcnt vmcnt(24)\n"
"v_add_f32 v104, v104, v118\n"
"v_mfma_f32_16x16x32_bf16 v[232:235], a[92:95], a[56:59], v[232:235]\n"
"ds_read_b64_tr_b16 a[118:119], v11 offset:8320\n"
"s_waitcnt vmcnt(22)\n"
"v_add_f32 v104, v104, v119\n"
"ds_write_b32 v15, v104\n"
"buffer_load_dwordx4 a[36:39], v8, s[52:55], s82 offen offset:1024 sc1\n"
"buffer_load_dwordx4 a[40:43], v8, s[52:55], s82 offen offset:2048 sc1\n"
"buffer_load_dwordx4 a[44:47], v8, s[52:55], s82 offen offset:3072 sc1\n"
"s_waitcnt vmcnt(17) lgkmcnt(0)\n"
"s_barrier\n"
"v_mfma_f32_16x16x32_bf16 v[128:131], a[96:99], a[0:3], v[128:131]\n"
"ds_read_b64_tr_b16 a[120:121], v10 offset:192\n"
"v_readlane_b32 s82, v20, 27\n"
"v_mfma_f32_16x16x32_bf16 v[132:135], a[96:99], a[4:7], v[132:135]\n"
"ds_read_b64_tr_b16 a[122:123], v10 offset:8384\n"
"s_add_u32 m0, s74, 98304\n"
"v_mfma_f32_16x16x32_bf16 v[140:143], a[100:103], a[4:7], v[140:143]\n"
"ds_read_b64_tr_b16 a[124:125], v11 offset:192\n"
"buffer_load_dwordx4 v64, s[40:43], s79 offen offset:0 lds\n"
"v_mfma_f32_16x16x32_bf16 v[136:139], a[100:103], a[0:3], v[136:139]\n"
"ds_read_b64_tr_b16 a[126:127], v11 offset:8384\n"
"v_mfma_f32_16x16x32_bf16 v[144:147], a[104:107], a[0:3], v[144:147]\n"
"ds_read_b64_tr_b16 a[64:65], v10 offset:256\n"
"v_mfma_f32_16x16x32_bf16 v[148:151], a[104:107], a[4:7], v[148:151]\n"
"ds_read_b64_tr_b16 a[66:67], v10 offset:8448\n"
"buffer_load_dwordx4 v65, s[40:43], s79 offen offset:1024 lds\n"
"v_mfma_f32_16x16x32_bf16 v[156:159], a[108:111], a[4:7], v[156:159]\n"
"ds_read_b64_tr_b16 a[68:69], v11 offset:256\n"
"v_mfma_f32_16x16x32_bf16 v[152:155], a[108:111], a[0:3], v[152:155]\n"
"ds_read_b64_tr_b16 a[70:71], v11 offset:8448\n"
"s_waitcnt lgkmcnt(4)\n"
"v_mfma_f32_16x16x32_bf16 v[160:163], a[112:115], a[0:3], v[160:163]\n"
"ds_read_b64_tr_b16 a[72:73], v10 offset:320\n"
"v_mfma_f32_16x16x32_bf16 v[164:167], a[112:115], a[4:7], v[164:167]\n"
"ds_read_b64_tr_b16 a[74:75], v10 offset:8512\n"
"v_mfma_f32_16x16x32_bf16 v[172:175], a[116:119], a[4:7], v[172:175]\n"
"ds_read_b64_tr_b16 a[76:77], v11 offset:320\n"
"buffer_load_dwordx4 v66, s[40:43], s79 offen offset:2048 lds\n"
"v_mfma_f32_16x16x32_bf16 v[168:171], a[116:119], a[0:3], v[168:171]\n"
"ds_read_b64_tr_b16 a[78:79], v11 offset:8512\n"
"v_mfma_f32_16x16x32_bf16 v[176:179], a[120:123], a[0:3], v[176:179]\n"
"ds_read_b64_tr_b16 a[80:81], v10 offset:384\n"
"v_mfma_f32_16x16x32_bf16 v[180:183], a[120:123], a[4:7], v[180:183]\n"
"ds_read_b64_tr_b16 a[82:83], v10 offset:8576\n"
"v_mfma_f32_16x16x32_bf16 v[188:191], a[124:127], a[4:7], v[188:191]\n"
"ds_read_b64_tr_b16 a[84:85], v11 offset:384\n"
"buffer_load_dwordx4 v67, s[40:43], s79 offen offset:3072 lds\n"
"v_mfma_f32_16x16x32_bf16 v[184:187], a[124:127], a[0:3], v[184:187]\n"
"ds_read_b64_tr_b16 a[86:87], v11 offset:8576\n"
"s_waitcnt lgkmcnt(4)\n"
"v_mfma_f32_16x16x32_bf16 v[192:195], a[64:67], a[0:3], v[192:195]\n"
"ds_read_b64_tr_b16 a[88:89], v10 offset:16384\n"
"v_mfma_f32_16x16x32_bf16 v[196:199], a[64:67], a[4:7], v[196:199]\n"
"ds_read_b64_tr_b16 a[90:91], v10 offset:24576\n"
"v_mfma_f32_16x16x32_bf16 v[204:207], a[68:71], a[4:7], v[204:207]\n"
"ds_read_b64_tr_b16 a[92:93], v11 offset:16384\n"
"s_add_u32 m0, s74, 102400\n"
"v_mfma_f32_16x16x32_bf16 v[200:203], a[68:71], a[0:3], v[200:203]\n"
"ds_read_b64_tr_b16 a[94:95], v11 offset:24576\n"
"buffer_load_dwordx4 v68, s[40:43], s79 offen offset:0 lds\n"
"v_mfma_f32_16x16x32_bf16 v[208:211], a[72:75], a[0:3], v[208:211]\n"
"ds_read_b64_tr_b16 a[96:97], v10 offset:16448\n"
"v_mfma_f32_16x16x32_bf16 v[212:215], a[72:75], a[4:7], v[212:215]\n"
"ds_read_b64_tr_b16 a[98:99], v10 offset:24640\n"
"v_mfma_f32_16x16x32_bf16 v[220:223], a[76:79], a[4:7], v[220:223]\n"
"ds_read_b64_tr_b16 a[100:101], v11 offset:16448\n"
"v_mfma_f32_16x16x32_bf16 v[216:219], a[76:79], a[0:3], v[216:219]\n"
"ds_read_b64_tr_b16 a[102:103], v11 offset:24640\n"
"s_waitcnt lgkmcnt(4)\n"
"v_mfma_f32_16x16x32_bf16 v[224:227], a[80:83], a[0:3], v[224:227]\n"
"ds_read_b64_tr_b16 a[104:105], v10 offset:16512\n"
"buffer_load_dwordx4 v69, s[40:43], s79 offen offset:1024 lds\n"
"v_mfma_f32_16x16x32_bf16 v[228:231], a[80:83], a[4:7], v[228:231]\n"
"ds_read_b64_tr_b16 a[106:107], v10 offset:24704\n"
"v_mfma_f32_16x16x32_bf16 v[236:239], a[84:87], a[4:7], v[236:239]\n"
"ds_read_b64_tr_b16 a[108:109], v11 offset:16512\n"
"v_mfma_f32_16x16x32_bf16 v[232:235], a[84:87], a[0:3], v[232:235]\n"
"ds_read_b64_tr_b16 a[110:111], v11 offset:24704\n"
"buffer_load_dwordx2 v[26:27], v31, s[36:39], s86 offen offset:0 sc1\n"
"v_mfma_f32_16x16x32_bf16 v[128:131], a[88:91], a[8:11], v[128:131]\n"
"ds_read_b64_tr_b16 a[112:113], v10 offset:16576\n"
"buffer_load_dwordx4 v70, s[40:43], s79 offen offset:2048 lds\n"
"v_mfma_f32_16x16x32_bf16 v[132:135], a[88:91], a[12:15], v[132:135]\n"
"ds_read_b64_tr_b16 a[114:115], v10 offset:24768\n"
"v_mfma_f32_16x16x32_bf16 v[140:143], a[92:95], a[12:15], v[140:143]\n"
"ds_read_b64_tr_b16 a[116:117], v11 offset:16576\n"
"v_mfma_f32_16x16x32_bf16 v[136:139], a[92:95], a[8:11], v[136:139]\n"
"ds_read_b64_tr_b16 a[118:119], v11 offset:24768\n"
"s_waitcnt lgkmcnt(4)\n"
"v_mfma_f32_16x16x32_bf16 v[144:147], a[96:99], a[8:11], v[144:147]\n"
"ds_read_b64_tr_b16 a[120:121], v10 offset:16640\n"
"v_mfma_f32_16x16x32_bf16 v[148:151], a[96:99], a[12:15], v[148:151]\n"
"ds_read_b64_tr_b16 a[122:123], v10 offset:24832\n"
"buffer_load_dwordx4 v71, s[40:43], s79 offen offset:3072 lds\n"
"v_mfma_f32_16x16x32_bf16 v[156:159], a[100:103], a[12:15], v[156:159]\n"
"ds_read_b64_tr_b16 a[124:125], v11 offset:16640\n"
"v_readlane_b32 s79, v20, 12\n"
"v_mfma_f32_16x16x32_bf16 v[152:155], a[100:103], a[8:11], v[152:155]\n"
"ds_read_b64_tr_b16 a[126:127], v11 offset:24832\n"
"v_mfma_f32_16x16x32_bf16 v[160:163], a[104:107], a[8:11], v[160:163]\n"
"ds_read_b64_tr_b16 a[64:65], v10 offset:16704\n"
"v_mfma_f32_16x16x32_bf16 v[164:167], a[104:107], a[12:15], v[164:167]\n"
"ds_read_b64_tr_b16 a[66:67], v10 offset:24896\n"
"buffer_load_dwordx4 a[48:51], v8, s[52:55], s82 offen offset:0 sc1\n"
"v_mfma_f32_16x16x32_bf16 v[172:175], a[108:111], a[12:15], v[172:175]\n"
"ds_read_b64_tr_b16 a[68:69], v11 offset:16704\n"
"v_mfma_f32_16x16x32_bf16 v[168:171], a[108:111], a[8:11], v[168:171]\n"
"ds_read_b64_tr_b16 a[70:71], v11 offset:24896\n"
"s_waitcnt lgkmcnt(4)\n"
"v_mfma_f32_16x16x32_bf16 v[176:179], a[112:115], a[8:11], v[176:179]\n"
"ds_read_b64_tr_b16 a[72:73], v10 offset:16768\n"
"v_mfma_f32_16x16x32_bf16 v[180:183], a[112:115], a[12:15], v[180:183]\n"
"ds_read_b64_tr_b16 a[74:75], v10 offset:24960\n"
"v_mfma_f32_16x16x32_bf16 v[188:191], a[116:119], a[12:15], v[188:191]\n"
"ds_read_b64_tr_b16 a[76:77], v11 offset:16768\n"
"buffer_load_dwordx4 a[52:55], v8, s[52:55], s82 offen offset:1024 sc1\n"
"v_mfma_f32_16x16x32_bf16 v[184:187], a[116:119], a[8:11], v[184:187]\n"
"ds_read_b64_tr_b16 a[78:79], v11 offset:24960\n"
"v_mfma_f32_16x16x32_bf16 v[192:195], a[120:123], a[8:11], v[192:195]\n"
"ds_read_b64_tr_b16 a[80:81], v10 offset:32768\n"
"v_mfma_f32_16x16x32_bf16 v[196:199], a[120:123], a[12:15], v[196:199]\n"
"ds_read_b64_tr_b16 a[82:83], v10 offset:40960\n"
"v_mfma_f32_16x16x32_bf16 v[204:207], a[124:127], a[12:15], v[204:207]\n"
"ds_read_b64_tr_b16 a[84:85], v11 offset:32768\n"
"buffer_load_dwordx4 a[56:59], v8, s[52:55], s82 offen offset:2048 sc1\n"
"v_mfma_f32_16x16x32_bf16 v[200:203], a[124:127], a[8:11], v[200:203]\n"
"ds_read_b64_tr_b16 a[86:87], v11 offset:40960\n"
"s_waitcnt lgkmcnt(4)\n"
"v_mfma_f32_16x16x32_bf16 v[208:211], a[64:67], a[8:11], v[208:211]\n"
"ds_read_b64_tr_b16 a[88:89], v10 offset:32832\n"
"v_mfma_f32_16x16x32_bf16 v[212:215], a[64:67], a[12:15], v[212:215]\n"
"ds_read_b64_tr_b16 a[90:91], v10 offset:41024\n"
"v_mfma_f32_16x16x32_bf16 v[220:223], a[68:71], a[12:15], v[220:223]\n"
"ds_read_b64_tr_b16 a[92:93], v11 offset:32832\n"
"v_mfma_f32_16x16x32_bf16 v[216:219], a[68:71], a[8:11], v[216:219]\n"
"ds_read_b64_tr_b16 a[94:95], v11 offset:41024\n"
"buffer_load_dwordx4 a[60:63], v8, s[52:55], s82 offen offset:3072 sc1\n"
"v_mfma_f32_16x16x32_bf16 v[224:227], a[72:75], a[8:11], v[224:227]\n"
"ds_read_b64_tr_b16 a[96:97], v10 offset:32896\n"
"v_readlane_b32 s82, v20, 28\n"
"v_mfma_f32_16x16x32_bf16 v[228:231], a[72:75], a[12:15], v[228:231]\n"
"ds_read_b64_tr_b16 a[98:99], v10 offset:41088\n"
"v_mfma_f32_16x16x32_bf16 v[236:239], a[76:79], a[12:15], v[236:239]\n"
"ds_read_b64_tr_b16 a[100:101], v11 offset:32896\n"
"v_mfma_f32_16x16x32_bf16 v[232:235], a[76:79], a[8:11], v[232:235]\n"
"ds_read_b64_tr_b16 a[102:103], v11 offset:41088\n"
"s_waitcnt vmcnt(17)\n"
"s_barrier\n"
"s_waitcnt lgkmcnt(4)\n"
"v_mfma_f32_16x16x32_bf16 v[128:131], a[80:83], a[16:19], v[128:131]\n"
"ds_read_b64_tr_b16 a[104:105], v10 offset:32960\n"
"s_add_u32 m0, s74, 0\n"
"v_mfma_f32_16x16x32_bf16 v[132:135], a[80:83], a[20:23], v[132:135]\n"
"ds_read_b64_tr_b16 a[106:107], v10 offset:41152\n"
"buffer_load_dwordx4 v64, s[40:43], s79 offen offset:0 lds\n"
"v_mfma_f32_16x16x32_bf16 v[140:143], a[84:87], a[20:23], v[140:143]\n"
"ds_read_b64_tr_b16 a[108:109], v11 offset:32960\n"
"v_mfma_f32_16x16x32_bf16 v[136:139], a[84:87], a[16:19], v[136:139]\n"
"ds_read_b64_tr_b16 a[110:111], v11 offset:41152\n"
"v_mfma_f32_16x16x32_bf16 v[144:147], a[88:91], a[16:19], v[144:147]\n"
"ds_read_b64_tr_b16 a[112:113], v10 offset:33024\n"
"v_mfma_f32_16x16x32_bf16 v[148:151], a[88:91], a[20:23], v[148:151]\n"
"ds_read_b64_tr_b16 a[114:115], v10 offset:41216\n"
"buffer_load_dwordx4 v65, s[40:43], s79 offen offset:1024 lds\n"
"v_mfma_f32_16x16x32_bf16 v[156:159], a[92:95], a[20:23], v[156:159]\n"
"ds_read_b64_tr_b16 a[116:117], v11 offset:33024\n"
"v_mfma_f32_16x16x32_bf16 v[152:155], a[92:95], a[16:19], v[152:155]\n"
"ds_read_b64_tr_b16 a[118:119], v11 offset:41216\n"
"s_waitcnt lgkmcnt(4)\n"
"v_mfma_f32_16x16x32_bf16 v[160:163], a[96:99], a[16:19], v[160:163]\n"
"ds_read_b64_tr_b16 a[120:121], v10 offset:33088\n"
"v_mfma_f32_16x16x32_bf16 v[164:167], a[96:99], a[20:23], v[164:167]\n"
"ds_read_b64_tr_b16 a[122:123], v10 offset:41280\n"
"v_mfma_f32_16x16x32_bf16 v[172:175], a[100:103], a[20:23], v[172:175]\n"
"ds_read_b64_tr_b16 a[124:125], v11 offset:33088\n"
"buffer_load_dwordx4 v66, s[40:43], s79 offen offset:2048 lds\n"
"v_mfma_f32_16x16x32_bf16 v[168:171], a[100:103], a[16:19], v[168:171]\n"
"ds_read_b64_tr_b16 a[126:127], v11 offset:41280\n"
"v_mfma_f32_16x16x32_bf16 v[176:179], a[104:107], a[16:19], v[176:179]\n"
"ds_read_b64_tr_b16 a[64:65], v10 offset:33152\n"
"v_mfma_f32_16x16x32_bf16 v[180:183], a[104:107], a[20:23], v[180:183]\n"
"ds_read_b64_tr_b16 a[66:67], v10 offset:41344\n"
"v_mfma_f32_16x16x32_bf16 v[188:191], a[108:111], a[20:23], v[188:191]\n"
"ds_read_b64_tr_b16 a[68:69], v11 offset:33152\n"
"buffer_load_dwordx4 v67, s[40:43], s79 offen offset:3072 lds\n"
"v_mfma_f32_16x16x32_bf16 v[184:187], a[108:111], a[16:19], v[184:187]\n"
"ds_read_b64_tr_b16 a[70:71], v11 offset:41344\n"
"s_waitcnt lgkmcnt(4)\n"
"v_mfma_f32_16x16x32_bf16 v[192:195], a[112:115], a[16:19], v[192:195]\n"
"ds_read_b64_tr_b16 a[72:73], v10 offset:49152\n"
"v_mfma_f32_16x16x32_bf16 v[196:199], a[112:115], a[20:23], v[196:199]\n"
"ds_read_b64_tr_b16 a[74:75], v10 offset:57344\n"
"v_mfma_f32_16x16x32_bf16 v[204:207], a[116:119], a[20:23], v[204:207]\n"
"ds_read_b64_tr_b16 a[76:77], v11 offset:49152\n"
"s_add_u32 m0, s74, 4096\n"
"v_mfma_f32_16x16x32_bf16 v[200:203], a[116:119], a[16:19], v[200:203]\n"
"ds_read_b64_tr_b16 a[78:79], v11 offset:57344\n"
"buffer_load_dwordx4 v68, s[40:43], s79 offen offset:0 lds\n"
"v_mfma_f32_16x16x32_bf16 v[208:211], a[120:123], a[16:19], v[208:211]\n"
"ds_read_b64_tr_b16 a[80:81], v10 offset:49216\n"
"v_mfma_f32_16x16x32_bf16 v[212:215], a[120:123], a[20:23], v[212:215]\n"
"ds_read_b64_tr_b16 a[82:83], v10 offset:57408\n"
"v_mfma_f32_16x16x32_bf16 v[220:223], a[124:127], a[20:23], v[220:223]\n"
"ds_read_b64_tr_b16 a[84:85], v11 offset:49216\n"
"v_mfma_f32_16x16x32_bf16 v[216:219], a[124:127], a[16:19], v[216:219]\n"
"ds_read_b64_tr_b16 a[86:87], v11 offset:57408\n"
"s_waitcnt lgkmcnt(4)\n"
"v_mfma_f32_16x16x32_bf16 v[224:227], a[64:67], a[16:19], v[224:227]\n"
"ds_read_b64_tr_b16 a[88:89], v10 offset:49280\n"
"buffer_load_dwordx4 v69, s[40:43], s79 offen offset:1024 lds\n"
"v_mfma_f32_16x16x32_bf16 v[228:231], a[64:67], a[20:23], v[228:231]\n"
"ds_read_b64_tr_b16 a[90:91], v10 offset:57472\n"
"v_mfma_f32_16x16x32_bf16 v[236:239], a[68:71], a[20:23], v[236:239]\n"
"ds_read_b64_tr_b16 a[92:93], v11 offset:49280\n"
"v_mfma_f32_16x16x32_bf16 v[232:235], a[68:71], a[16:19], v[232:235]\n"
"ds_read_b64_tr_b16 a[94:95], v11 offset:57472\n"
"v_mfma_f32_16x16x32_bf16 v[128:131], a[72:75], a[24:27], v[128:131]\n"
"ds_read_b64_tr_b16 a[96:97], v10 offset:49344\n"
"buffer_load_dwordx4 v70, s[40:43], s79 offen offset:2048 lds\n"
"v_mfma_f32_16x16x32_bf16 v[132:135], a[72:75], a[28:31], v[132:135]\n"
"ds_read_b64_tr_b16 a[98:99], v10 offset:57536\n"
"v_mfma_f32_16x16x32_bf16 v[140:143], a[76:79], a[28:31], v[140:143]\n"
"ds_read_b64_tr_b16 a[100:101], v11 offset:49344\n"
"v_mfma_f32_16x16x32_bf16 v[136:139], a[76:79], a[24:27], v[136:139]\n"
"ds_read_b64_tr_b16 a[102:103], v11 offset:57536\n"
"s_waitcnt lgkmcnt(4)\n"
"v_mfma_f32_16x16x32_bf16 v[144:147], a[80:83], a[24:27], v[144:147]\n"
"ds_read_b64_tr_b16 a[104:105], v10 offset:49408\n"
"v_mfma_f32_16x16x32_bf16 v[148:151], a[80:83], a[28:31], v[148:151]\n"
"ds_read_b64_tr_b16 a[106:107], v10 offset:57600\n"
"buffer_load_dwordx4 v71, s[40:43], s79 offen offset:3072 lds\n"
"v_mfma_f32_16x16x32_bf16 v[156:159], a[84:87], a[28:31], v[156:159]\n"
"ds_read_b64_tr_b16 a[108:109], v11 offset:49408\n"
"v_readlane_b32 s79, v20, 13\n"
"v_mfma_f32_16x16x32_bf16 v[152:155], a[84:87], a[24:27], v[152:155]\n"
"ds_read_b64_tr_b16 a[110:111], v11 offset:57600\n"
"v_mfma_f32_16x16x32_bf16 v[160:163], a[88:91], a[24:27], v[160:163]\n"
"ds_read_b64_tr_b16 a[112:113], v10 offset:49472\n"
"v_mfma_f32_16x16x32_bf16 v[164:167], a[88:91], a[28:31], v[164:167]\n"
"ds_read_b64_tr_b16 a[114:115], v10 offset:57664\n"
"buffer_load_dwordx4 a[0:3], v8, s[52:55], s82 offen offset:0 sc1\n"
"v_mfma_f32_16x16x32_bf16 v[172:175], a[92:95], a[28:31], v[172:175]\n"
"ds_read_b64_tr_b16 a[116:117], v11 offset:49472\n"
"v_mfma_f32_16x16x32_bf16 v[168:171], a[92:95], a[24:27], v[168:171]\n"
"ds_read_b64_tr_b16 a[118:119], v11 offset:57664\n"
"s_waitcnt lgkmcnt(4)\n"
"v_mfma_f32_16x16x32_bf16 v[176:179], a[96:99], a[24:27], v[176:179]\n"
"ds_read_b64_tr_b16 a[120:121], v10 offset:49536\n"
"v_mfma_f32_16x16x32_bf16 v[180:183], a[96:99], a[28:31], v[180:183]\n"
"ds_read_b64_tr_b16 a[122:123], v10 offset:57728\n"
"v_mfma_f32_16x16x32_bf16 v[188:191], a[100:103], a[28:31], v[188:191]\n"
"ds_read_b64_tr_b16 a[124:125], v11 offset:49536\n"
"buffer_load_dwordx4 a[4:7], v8, s[52:55], s82 offen offset:1024 sc1\n"
"v_mfma_f32_16x16x32_bf16 v[184:187], a[100:103], a[24:27], v[184:187]\n"
"ds_read_b64_tr_b16 a[126:127], v11 offset:57728\n"
"v_mfma_f32_16x16x32_bf16 v[192:195], a[104:107], a[24:27], v[192:195]\n"
"ds_read_b64_tr_b16 a[64:65], v12 offset:0\n"
"v_mfma_f32_16x16x32_bf16 v[196:199], a[104:107], a[28:31], v[196:199]\n"
"ds_read_b64_tr_b16 a[66:67], v12 offset:8192\n"
"v_mfma_f32_16x16x32_bf16 v[204:207], a[108:111], a[28:31], v[204:207]\n"
"ds_read_b64_tr_b16 a[68:69], v13 offset:0\n"
"buffer_load_dwordx4 a[8:11], v8, s[52:55], s82 offen offset:2048 sc1\n"
"v_mfma_f32_16x16x32_bf16 v[200:203], a[108:111], a[24:27], v[200:203]\n"
"ds_read_b64_tr_b16 a[70:71], v13 offset:8192\n"
"s_waitcnt lgkmcnt(4)\n"
"v_mfma_f32_16x16x32_bf16 v[208:211], a[112:115], a[24:27], v[208:211]\n"
"ds_read_b64_tr_b16 a[72:73], v12 offset:64\n"
"v_mfma_f32_16x16x32_bf16 v[212:215], a[112:115], a[28:31], v[212:215]\n"
"ds_read_b64_tr_b16 a[74:75], v12 offset:8256\n"
"v_mfma_f32_16x16x32_bf16 v[220:223], a[116:119], a[28:31], v[220:223]\n"
"ds_read_b64_tr_b16 a[76:77], v13 offset:64\n"
"v_mfma_f32_16x16x32_bf16 v[216:219], a[116:119], a[24:27], v[216:219]\n"
"ds_read_b64_tr_b16 a[78:79], v13 offset:8256\n"
"buffer_load_dwordx4 a[12:15], v8, s[52:55], s82 offen offset:3072 sc1\n"
"v_mfma_f32_16x16x32_bf16 v[224:227], a[120:123], a[24:27], v[224:227]\n"
"ds_read_b64_tr_b16 a[80:81], v12 offset:128\n"
"v_readlane_b32 s82, v20, 29\n"
"v_mfma_f32_16x16x32_bf16 v[228:231], a[120:123], a[28:31], v[228:231]\n"
"ds_read_b64_tr_b16 a[82:83], v12 offset:8320\n"
"v_mfma_f32_16x16x32_bf16 v[236:239], a[124:127], a[28:31], v[236:239]\n"
"ds_read_b64_tr_b16 a[84:85], v13 offset:128\n"
"v_mfma_f32_16x16x32_bf16 v[232:235], a[124:127], a[24:27], v[232:235]\n"
"ds_read_b64_tr_b16 a[86:87], v13 offset:8320\n"
"s_waitcnt vmcnt(16)\n"
"s_barrier\n"
"s_waitcnt lgkmcnt(4)\n"
"v_mfma_f32_16x16x32_bf16 v[128:131], a[64:67], a[32:35], v[128:131]\n"
"ds_read_b64_tr_b16 a[88:89], v12 offset:192\n"
"s_add_u32 m0, s74, 32768\n"
"v_mfma_f32_16x16x32_bf16 v[132:135], a[64:67], a[36:39], v[132:135]\n"
"ds_read_b64_tr_b16 a[90:91], v12 offset:8384\n"
"buffer_load_dwordx4 v64, s[40:43], s79 offen offset:0 lds\n"
"v_mfma_f32_16x16x32_bf16 v[140:143], a[68:71], a[36:39], v[140:143]\n"
"ds_read_b64_tr_b16 a[92:93], v13 offset:192\n"
"v_mfma_f32_16x16x32_bf16 v[136:139], a[68:71], a[32:35], v[136:139]\n"
"ds_read_b64_tr_b16 a[94:95], v13 offset:8384\n"
"v_cmp_ne_u64 vcc, s[70:71], v[26:27]\n"
"s_cbranch_vccnz .Lpslow8_%=\n"
".Lpok8_%=:\n"
"v_lshrrev_b32 v100, 5, v1\n"
"v_lshl_add_u32 v23, v100, 11, v23\n"
"v_mfma_f32_16x16x32_bf16 v[144:147], a[72:75], a[32:35], v[144:147]\n"
"ds_read_b64_tr_b16 a[96:97], v12 offset:256\n"
"v_mfma_f32_16x16x32_bf16 v[148:151], a[72:75], a[36:39], v[148:151]\n"
"ds_read_b64_tr_b16 a[98:99], v12 offset:8448\n"
"buffer_load_dwordx4 v65, s[40:43], s79 offen offset:1024 lds\n"
"v_mfma_f32_16x16x32_bf16 v[156:159], a[76:79], a[36:39], v[156:159]\n"
"ds_read_b64_tr_b16 a[100:101], v13 offset:256\n"
"v_mfma_f32_16x16x32_bf16 v[152:155], a[76:79], a[32:35], v[152:155]\n"
"ds_read_b64_tr_b16 a[102:103], v13 offset:8448\n"
"s_waitcnt lgkmcnt(4)\n"
"v_mfma_f32_16x16x32_bf16 v[160:163], a[80:83], a[32:35], v[160:163]\n"
"ds_read_b64_tr_b16 a[104:105], v12 offset:320\n"
"v_mfma_f32_16x16x32_bf16 v[164:167], a[80:83], a[36:39], v[164:167]\n"
"ds_read_b64_tr_b16 a[106:107], v12 offset:8512\n"
"buffer_load_dwordx2 v[104:105], v23, s[36:39], s80 offen offset:0 sc1\n"
"v_mfma_f32_16x16x32_bf16 v[172:175], a[84:87], a[36:39], v[172:175]\n"
"ds_read_b64_tr_b16 a[108:109], v13 offset:320\n"
"buffer_load_dwordx4 v66, s[40:43], s79 offen offset:2048 lds\n"
"v_mfma_f32_16x16x32_bf16 v[168:171], a[84:87], a[32:35], v[168:171]\n"
"ds_read_b64_tr_b16 a[110:111], v13 offset:8512\n"
"v_mfma_f32_16x16x32_bf16 v[176:179], a[88:91], a[32:35], v[176:179]\n"
"ds_read_b64_tr_b16 a[112:113], v12 offset:384\n"
"v_mfma_f32_16x16x32_bf16 v[180:183], a[88:91], a[36:39], v[180:183]\n"
"ds_read_b64_tr_b16 a[114:115], v12 offset:8576\n"
"v_mfma_f32_16x16x32_bf16 v[188:191], a[92:95], a[36:39], v[188:191]\n"
"ds_read_b64_tr_b16 a[116:117], v13 offset:384\n"
"buffer_load_dwordx4 v67, s[40:43], s79 offen offset:3072 lds\n"
"v_mfma_f32_16x16x32_bf16 v[184:187], a[92:95], a[32:35], v[184:187]\n"
"ds_read_b64_tr_b16 a[118:119], v13 offset:8576\n"
"s_waitcnt lgkmcnt(4)\n"
"v_mfma_f32_16x16x32_bf16 v[192:195], a[96:99], a[32:35], v[192:195]\n"
"ds_read_b64_tr_b16 a[120:121], v12 offset:16384\n"
"buffer_load_dwordx2 v[106:107], v23, s[36:39], s80 offen offset:256 sc1\n"
"v_mfma_f32_16x16x32_bf16 v[196:199], a[96:99], a[36:39], v[196:199]\n"
"ds_read_b64_tr_b16 a[122:123], v12 offset:24576\n"
"v_mfma_f32_16x16x32_bf16 v[204:207], a[100:103], a[36:39], v[204:207]\n"
"ds_read_b64_tr_b16 a[124:125], v13 offset:16384\n"
"s_add_u32 m0, s74, 36864\n"
"v_mfma_f32_16x16x32_bf16 v[200:203], a[100:103], a[32:35], v[200:203]\n"
"ds_read_b64_tr_b16 a[126:127], v13 offset:24576\n"
"buffer_load_dwordx4 v68, s[40:43], s79 offen offset:0 lds\n"
"v_mfma_f32_16x16x32_bf16 v[208:211], a[104:107], a[32:35], v[208:211]\n"
"ds_read_b64_tr_b16 a[64:65], v12 offset:16448\n"
"v_mfma_f32_16x16x32_bf16 v[212:215], a[104:107], a[36:39], v[212:215]\n"
"ds_read_b64_tr_b16 a[66:67], v12 offset:24640\n"
"buffer_load_dwordx2 v[108:109], v23, s[36:39], s80 offen offset:512 sc1\n"
"v_mfma_f32_16x16x32_bf16 v[220:223], a[108:111], a[36:39], v[220:223]\n"
"ds_read_b64_tr_b16 a[68:69], v13 offset:16448\n"
"v_mfma_f32_16x16x32_bf16 v[216:219], a[108:111], a[32:35], v[216:219]\n"
"ds_read_b64_tr_b16 a[70:71], v13 offset:24640\n"
"s_waitcnt lgkmcnt(4)\n"
"v_mfma_f32_16x16x32_bf16 v[224:227], a[112:115], a[32:35], v[224:227]\n"
"ds_read_b64_tr_b16 a[72:73], v12 offset:16512\n"
"buffer_load_dwordx4 v69, s[40:43], s79 offen offset:1024 lds\n"
"v_mfma_f32_16x16x32_bf16 v[228:231], a[112:115], a[36:39], v[228:231]\n"
"ds_read_b64_tr_b16 a[74:75], v12 offset:24704\n"
"v_mfma_f32_16x16x32_bf16 v[236:239], a[116:119], a[36:39], v[236:239]\n"
"ds_read_b64_tr_b16 a[76:77], v13 offset:16512\n"
"v_mfma_f32_16x16x32_bf16 v[232:235], a[116:119], a[32:35], v[232:235]\n"
"ds_read_b64_tr_b16 a[78:79], v13 offset:24704\n"
"buffer_load_dwordx2 v[110:111], v23, s[36:39], s80 offen offset:768 sc1\n"
"v_mfma_f32_16x16x32_bf16 v[128:131], a[120:123], a[40:43], v[128:131]\n"
"ds_read_b64_tr_b16 a[80:81], v12 offset:16576\n"
"buffer_load_dwordx4 v70, s[40:43], s79 offen offset:2048 lds\n"
"v_mfma_f32_16x16x32_bf16 v[132:135], a[120:123], a[44:47], v[132:135]\n"
"ds_read_b64_tr_b16 a[82:83], v12 offset:24768\n"
"v_mfma_f32_16x16x32_bf16 v[140:143], a[124:127], a[44:47], v[140:143]\n"
"ds_read_b64_tr_b16 a[84:85], v13 offset:16576\n"
"v_mfma_f32_16x16x32_bf16 v[136:139], a[124:127], a[40:43], v[136:139]\n"
"ds_read_b64_tr_b16 a[86:87], v13 offset:24768\n"
"s_waitcnt lgkmcnt(4)\n"
"v_mfma_f32_16x16x32_bf16 v[144:147], a[64:67], a[40:43], v[144:147]\n"
"ds_read_b64_tr_b16 a[88:89], v12 offset:16640\n"
"v_mfma_f32_16x16x32_bf16 v[148:151], a[64:67], a[44:47], v[148:151]\n"
"ds_read_b64_tr_b16 a[90:91], v12 offset:24832\n"
"buffer_load_dwordx4 v71, s[40:43], s79 offen offset:3072 lds\n"
"v_mfma_f32_16x16x32_bf16 v[156:159], a[68:71], a[44:47], v[156:159]\n"
"ds_read_b64_tr_b16 a[92:93], v13 offset:16640\n"
"v_readlane_b32 s79, v20, 14\n"
"v_mfma_f32_16x16x32_bf16 v[152:155], a[68:71], a[40:43], v[152:155]\n"
"ds_read_b64_tr_b16 a[94:95], v13 offset:24832\n"
"buffer_load_dwordx2 v[112:113], v23, s[36:39], s80 offen offset:1024 sc1\n"
"v_mfma_f32_16x16x32_bf16 v[160:163], a[72:75], a[40:43], v[160:163]\n"
"ds_read_b64_tr_b16 a[96:97], v12 offset:16704\n"
"v_mfma_f32_16x16x32_bf16 v[164:167], a[72:75], a[44:47], v[164:167]\n"
"ds_read_b64_tr_b16 a[98:99], v12 offset:24896\n"
"buffer_load_dwordx4 a[16:19], v8, s[52:55], s82 offen offset:0 sc1\n"
"v_mfma_f32_16x16x32_bf16 v[172:175], a[76:79], a[44:47], v[172:175]\n"
"ds_read_b64_tr_b16 a[100:101], v13 offset:16704\n"
"v_mfma_f32_16x16x32_bf16 v[168:171], a[76:79], a[40:43], v[168:171]\n"
"ds_read_b64_tr_b16 a[102:103], v13 offset:24896\n"
"s_waitcnt lgkmcnt(4)\n"
"v_mfma_f32_16x16x32_bf16 v[176:179], a[80:83], a[40:43], v[176:179]\n"
"ds_read_b64_tr_b16 a[104:105], v12 offset:16768\n"
"buffer_load_dwordx2 v[114:115], v23, s[36:39], s80 offen offset:1280 sc1\n"
"v_mfma_f32_16x16x32_bf16 v[180:183], a[80:83], a[44:47], v[180:183]\n"
"ds_read_b64_tr_b16 a[106:107], v12 offset:24960\n"
"v_mfma_f32_16x16x32_bf16 v[188:191], a[84:87], a[44:47], v[188:191]\n"
"ds_read_b64_tr_b16 a[108:109], v13 offset:16768\n"
"buffer_load_dwordx4 a[20:23], v8, s[52:55], s82 offen offset:1024 sc1\n"
"v_mfma_f32_16x16x32_bf16 v[184:187], a[84:87], a[40:43], v[184:187]\n"
"ds_read_b64_tr_b16 a[110:111], v13 offset:24960\n"
"v_mfma_f32_16x16x32_bf16 v[192:195], a[88:91], a[40:43], v[192:195]\n"
"ds_read_b64_tr_b16 a[112:113], v12 offset:32768\n"
"v_mfma_f32_16x16x32_bf16 v[196:199], a[88:91], a[44:47], v[196:199]\n"
"ds_read_b64_tr_b16 a[114:115], v12 offset:40960\n"
"buffer_load_dwordx2 v[116:117], v23, s[36:39], s80 offen offset:1536 sc1\n"
"v_mfma_f32_16x16x32_bf16 v[204:207], a[92:95], a[44:47], v[204:207]\n"
"ds_read_b64_tr_b16 a[116:117], v13 offset:32768\n"
"buffer_load_dwordx4 a[24:27], v8, s[52:55], s82 offen offset:2048 sc1\n"
"v_mfma_f32_16x16x32_bf16 v[200:203], a[92:95], a[40:43], v[200:203]\n"
"ds_read_b64_tr_b16 a[118:119], v13 offset:40960\n"
"s_waitcnt lgkmcnt(4)\n"
"v_mfma_f32_16x16x32_bf16 v[208:211], a[96:99], a[40:43], v[208:211]\n"
"ds_read_b64_tr_b16 a[120:121], v12 offset:32832\n"
"v_mfma_f32_16x16x32_bf16 v[212:215], a[96:99], a[44:47], v[212:215]\n"
"ds_read_b64_tr_b16 a[122:123], v12 offset:41024\n"
"v_mfma_f32_16x16x32_bf16 v[220:223], a[100:103], a[44:47], v[220:223]\n"
"ds_read_b64_tr_b16 a[124:125], v13 offset:32832\n"
"v_mfma_f32_16x16x32_bf16 v[216:219], a[100:103], a[40:43], v[216:219]\n"
"ds_read_b64_tr_b16 a[126:127], v13 offset:41024\n"
"buffer_load_dwordx4 a[28:31], v8, s[52:55], s82 offen offset:3072 sc1\n"
"v_mfma_f32_16x16x32_bf16 v[224:227], a[104:107], a[40:43], v[224:227]\n"
"ds_read_b64_tr_b16 a[64:65], v12 offset:32896\n"
"v_readlane_b32 s82, v20, 30\n"
"v_mfma_f32_16x16x32_bf16 v[228:231], a[104:107], a[44:47], v[228:231]\n"
"ds_read_b64_tr_b16 a[66:67], v12 offset:41088\n"
"buffer_load_dwordx2 v[118:119], v23, s[36:39], s80 offen offset:1792 sc1\n"
"v_mfma_f32_16x16x32_bf16 v[236:239], a[108:111], a[44:47], v[236:239]\n"
"ds_read_b64_tr_b16 a[68:69], v13 offset:32896\n"
"v_mfma_f32_16x16x32_bf16 v[232:235], a[108:111], a[40:43], v[232:235]\n"
"ds_read_b64_tr_b16 a[70:71], v13 offset:41088\n"
"s_waitcnt vmcnt(24)\n"
"s_barrier\n"
"s_waitcnt lgkmcnt(4)\n"
"v_mfma_f32_16x16x32_bf16 v[128:131], a[112:115], a[48:51], v[128:131]\n"
"ds_read_b64_tr_b16 a[72:73], v12 offset:32960\n"
"s_add_u32 m0, s74, 65536\n"
"v_mfma_f32_16x16x32_bf16 v[132:135], a[112:115], a[52:55], v[132:135]\n"
"ds_read_b64_tr_b16 a[74:75], v12 offset:41152\n"
"buffer_load_dwordx4 v64, s[40:43], s79 offen offset:0 lds\n"
"v_mfma_f32_16x16x32_bf16 v[140:143], a[116:119], a[52:55], v[140:143]\n"
"ds_read_b64_tr_b16 a[76:77], v13 offset:32960\n"
"v_mfma_f32_16x16x32_bf16 v[136:139], a[116:119], a[48:51], v[136:139]\n"
"ds_read_b64_tr_b16 a[78:79], v13 offset:41152\n"
"v_mfma_f32_16x16x32_bf16 v[144:147], a[120:123], a[48:51], v[144:147]\n"
"ds_read_b64_tr_b16 a[80:81], v12 offset:33024\n"
"v_mfma_f32_16x16x32_bf16 v[148:151], a[120:123], a[52:55], v[148:151]\n"
"ds_read_b64_tr_b16 a[82:83], v12 offset:41216\n"
"buffer_load_dwordx4 v65, s[40:43], s79 offen offset:1024 lds\n"
"v_mfma_f32_16x16x32_bf16 v[156:159], a[124:127], a[52:55], v[156:159]\n"
"ds_read_b64_tr_b16 a[84:85], v13 offset:33024\n"
"v_mfma_f32_16x16x32_bf16 v[152:155], a[124:127], a[48:51], v[152:155]\n"
"ds_read_b64_tr_b16 a[86:87], v13 offset:41216\n"
"s_waitcnt lgkmcnt(4)\n"
"v_mfma_f32_16x16x32_bf16 v[160:163], a[64:67], a[48:51], v[160:163]\n"
"ds_read_b64_tr_b16 a[88:89], v12 offset:33088\n"
"v_mfma_f32_16x16x32_bf16 v[164:167], a[64:67], a[52:55], v[164:167]\n"
"ds_read_b64_tr_b16 a[90:91], v12 offset:41280\n"
"v_mfma_f32_16x16x32_bf16 v[172:175], a[68:71], a[52:55], v[172:175]\n"
"ds_read_b64_tr_b16 a[92:93], v13 offset:33088\n"
"buffer_load_dwordx4 v66, s[40:43], s79 offen offset:2048 lds\n"
"v_mfma_f32_16x16x32_bf16 v[168:171], a[68:71], a[48:51], v[168:171]\n"
"ds_read_b64_tr_b16 a[94:95], v13 offset:41280\n"
"v_mfma_f32_16x16x32_bf16 v[176:179], a[72:75], a[48:51], v[176:179]\n"
"ds_read_b64_tr_b16 a[96:97], v12 offset:33152\n"
"v_mfma_f32_16x16x32_bf16 v[180:183], a[72:75], a[52:55], v[180:183]\n"
"ds_read_b64_tr_b16 a[98:99], v12 offset:41344\n"
"v_mfma_f32_16x16x32_bf16 v[188:191], a[76:79], a[52:55], v[188:191]\n"
"ds_read_b64_tr_b16 a[100:101], v13 offset:33152\n"
"buffer_load_dwordx4 v67, s[40:43], s79 offen offset:3072 lds\n"
"v_mfma_f32_16x16x32_bf16 v[184:187], a[76:79], a[48:51], v[184:187]\n"
"ds_read_b64_tr_b16 a[102:103], v13 offset:41344\n"
"s_waitcnt lgkmcnt(4)\n"
"v_mfma_f32_16x16x32_bf16 v[192:195], a[80:83], a[48:51], v[192:195]\n"
"ds_read_b64_tr_b16 a[104:105], v12 offset:49152\n"
"v_mfma_f32_16x16x32_bf16 v[196:199], a[80:83], a[52:55], v[196:199]\n"
"ds_read_b64_tr_b16 a[106:107], v12 offset:57344\n"
"v_mfma_f32_16x16x32_bf16 v[204:207], a[84:87], a[52:55], v[204:207]\n"
"ds_read_b64_tr_b16 a[108:109], v13 offset:49152\n"
"s_add_u32 m0, s74, 69632\n"
"v_mfma_f32_16x16x32_bf16 v[200:203], a[84:87], a[48:51], v[200:203]\n"
"ds_read_b64_tr_b16 a[110:111], v13 offset:57344\n"
"buffer_load_dwordx4 v68, s[40:43], s79 offen offset:0 lds\n"
"v_mfma_f32_16x16x32_bf16 v[208:211], a[88:91], a[48:51], v[208:211]\n"
"ds_read_b64_tr_b16 a[112:113], v12 offset:49216\n"
"v_mfma_f32_16x16x32_bf16 v[212:215], a[88:91], a[52:55], v[212:215]\n"
"ds_read_b64_tr_b16 a[114:115], v12 offset:57408\n"
"v_mfma_f32_16x16x32_bf16 v[220:223], a[92:95], a[52:55], v[220:223]\n"
"ds_read_b64_tr_b16 a[116:117], v13 offset:49216\n"
"v_mfma_f32_16x16x32_bf16 v[216:219], a[92:95], a[48:51], v[216:219]\n"
"ds_read_b64_tr_b16 a[118:119], v13 offset:57408\n"
"s_waitcnt lgkmcnt(4)\n"
"v_mfma_f32_16x16x32_bf16 v[224:227], a[96:99], a[48:51], v[224:227]\n"
"ds_read_b64_tr_b16 a[120:121], v12 offset:49280\n"
"buffer_load_dwordx4 v69, s[40:43], s79 offen offset:1024 lds\n"
"v_mfma_f32_16x16x32_bf16 v[228:231], a[96:99], a[52:55], v[228:231]\n"
"ds_read_b64_tr_b16 a[122:123], v12 offset:57472\n"
"v_mfma_f32_16x16x32_bf16 v[236:239], a[100:103], a[52:55], v[236:239]\n"
"ds_read_b64_tr_b16 a[124:125], v13 offset:49280\n"
"v_mfma_f32_16x16x32_bf16 v[232:235], a[100:103], a[48:51], v[232:235]\n"
"ds_read_b64_tr_b16 a[126:127], v13 offset:57472\n"
"v_mfma_f32_16x16x32_bf16 v[128:131], a[104:107], a[56:59], v[128:131]\n"
"ds_read_b64_tr_b16 a[64:65], v12 offset:49344\n"
"buffer_load_dwordx4 v70, s[40:43], s79 offen offset:2048 lds\n"
"v_mfma_f32_16x16x32_bf16 v[132:135], a[104:107], a[60:63], v[132:135]\n"
"ds_read_b64_tr_b16 a[66:67], v12 offset:57536\n"
"v_mfma_f32_16x16x32_bf16 v[140:143], a[108:111], a[60:63], v[140:143]\n"
"ds_read_b64_tr_b16 a[68:69], v13 offset:49344\n"
"v_mfma_f32_16x16x32_bf16 v[136:139], a[108:111], a[56:59], v[136:139]\n"
"ds_read_b64_tr_b16 a[70:71], v13 offset:57536\n"
"s_waitcnt lgkmcnt(4)\n"
"v_mfma_f32_16x16x32_bf16 v[144:147], a[112:115], a[56:59], v[144:147]\n"
"ds_read_b64_tr_b16 a[72:73], v12 offset:49408\n"
"v_mfma_f32_16x16x32_bf16 v[148:151], a[112:115], a[60:63], v[148:151]\n"
"ds_read_b64_tr_b16 a[74:75], v12 offset:57600\n"
"buffer_load_dwordx4 v71, s[40:43], s79 offen offset:3072 lds\n"
"v_mfma_f32_16x16x32_bf16 v[156:159], a[116:119], a[60:63], v[156:159]\n"
"ds_read_b64_tr_b16 a[76:77], v13 offset:49408\n"
"v_readlane_b32 s79, v20, 15\n"
"v_mfma_f32_16x16x32_bf16 v[152:155], a[116:119], a[56:59], v[152:155]\n"
"ds_read_b64_tr_b16 a[78:79], v13 offset:57600\n"
"v_mfma_f32_16x16x32_bf16 v[160:163], a[120:123], a[56:59], v[160:163]\n"
"ds_read_b64_tr_b16 a[80:81], v12 offset:49472\n"
"v_mfma_f32_16x16x32_bf16 v[164:167], a[120:123], a[60:63], v[164:167]\n"
"ds_read_b64_tr_b16 a[82:83], v12 offset:57664\n"
"buffer_load_dwordx4 a[32:35], v8, s[52:55], s82 offen offset:0 sc1\n"
"v_mfma_f32_16x16x32_bf16 v[172:175], a[124:127], a[60:63], v[172:175]\n"
"ds_read_b64_tr_b16 a[84:85], v13 offset:49472\n"
"v_mfma_f32_16x16x32_bf16 v[168:171], a[124:127], a[56:59], v[168:171]\n"
"ds_read_b64_tr_b16 a[86:87], v13 offset:57664\n"
"s_waitcnt lgkmcnt(4)\n"
"v_mfma_f32_16x16x32_bf16 v[176:179], a[64:67], a[56:59], v[176:179]\n"
"ds_read_b64_tr_b16 a[88:89], v12 offset:49536\n"
"v_mfma_f32_16x16x32_bf16 v[180:183], a[64:67], a[60:63], v[180:183]\n"
"ds_read_b64_tr_b16 a[90:91], v12 offset:57728\n"
"v_mfma_f32_16x16x32_bf16 v[188:191], a[68:71], a[60:63], v[188:191]\n"
"ds_read_b64_tr_b16 a[92:93], v13 offset:49536\n"
"buffer_load_dwordx4 a[36:39], v8, s[52:55], s82 offen offset:1024 sc1\n"
"v_mfma_f32_16x16x32_bf16 v[184:187], a[68:71], a[56:59], v[184:187]\n"
"ds_read_b64_tr_b16 a[94:95], v13 offset:57728\n"
"v_mfma_f32_16x16x32_bf16 v[192:195], a[72:75], a[56:59], v[192:195]\n"
"ds_read_b64_tr_b16 a[96:97], v10 offset:0\n"
"v_mfma_f32_16x16x32_bf16 v[196:199], a[72:75], a[60:63], v[196:199]\n"
"ds_read_b64_tr_b16 a[98:99], v10 offset:8192\n"
"v_mfma_f32_16x16x32_bf16 v[204:207], a[76:79], a[60:63], v[204:207]\n"
"ds_read_b64_tr_b16 a[100:101], v11 offset:0\n"
"buffer_load_dwordx4 a[40:43], v8, s[52:55], s82 offen offset:2048 sc1\n"
"v_mfma_f32_16x16x32_bf16 v[200:203], a[76:79], a[56:59], v[200:203]\n"
"ds_read_b64_tr_b16 a[102:103], v11 offset:8192\n"
"s_waitcnt lgkmcnt(4)\n"
"v_mfma_f32_16x16x32_bf16 v[208:211], a[80:83], a[56:59], v[208:211]\n"
"ds_read_b64_tr_b16 a[104:105], v10 offset:64\n"
"v_mfma_f32_16x16x32_bf16 v[212:215], a[80:83], a[60:63], v[212:215]\n"
"ds_read_b64_tr_b16 a[106:107], v10 offset:8256\n"
"v_mfma_f32_16x16x32_bf16 v[220:223], a[84:87], a[60:63], v[220:223]\n"
"ds_read_b64_tr_b16 a[108:109], v11 offset:64\n"
"v_mfma_f32_16x16x32_bf16 v[216:219], a[84:87], a[56:59], v[216:219]\n"
"ds_read_b64_tr_b16 a[110:111], v11 offset:8256\n"
"buffer_load_dwordx4 a[44:47], v8, s[52:55], s82 offen offset:3072 sc1\n"
"v_mfma_f32_16x16x32_bf16 v[224:227], a[88:91], a[56:59], v[224:227]\n"
"ds_read_b64_tr_b16 a[112:113], v10 offset:128\n"
"v_readlane_b32 s82, v20, 31\n"
"v_mfma_f32_16x16x32_bf16 v[228:231], a[88:91], a[60:63], v[228:231]\n"
"ds_read_b64_tr_b16 a[114:115], v10 offset:8320\n"
"v_mfma_f32_16x16x32_bf16 v[236:239], a[92:95], a[60:63], v[236:239]\n"
"ds_read_b64_tr_b16 a[116:117], v11 offset:128\n"
"v_mfma_f32_16x16x32_bf16 v[232:235], a[92:95], a[56:59], v[232:235]\n"
"ds_read_b64_tr_b16 a[118:119], v11 offset:8320\n"
"s_waitcnt vmcnt(20)\n"
"s_barrier\n"
"s_waitcnt lgkmcnt(4)\n"
"v_mfma_f32_16x16x32_bf16 v[128:131], a[96:99], a[0:3], v[128:131]\n"
"ds_read_b64_tr_b16 a[120:121], v10 offset:192\n"
"s_add_u32 m0, s74, 98304\n"
"v_mfma_f32_16x16x32_bf16 v[132:135], a[96:99], a[4:7], v[132:135]\n"
"ds_read_b64_tr_b16 a[122:123], v10 offset:8384\n"
"buffer_load_dwordx4 v64, s[40:43], s79 offen offset:0 lds\n"
"v_mfma_f32_16x16x32_bf16 v[140:143], a[100:103], a[4:7], v[140:143]\n"
"ds_read_b64_tr_b16 a[124:125], v11 offset:192\n"
"v_mfma_f32_16x16x32_bf16 v[136:139], a[100:103], a[0:3], v[136:139]\n"
"ds_read_b64_tr_b16 a[126:127], v11 offset:8384\n"
"v_mfma_f32_16x16x32_bf16 v[144:147], a[104:107], a[0:3], v[144:147]\n"
"ds_read_b64_tr_b16 a[64:65], v10 offset:256\n"
"v_mfma_f32_16x16x32_bf16 v[148:151], a[104:107], a[4:7], v[148:151]\n"
"ds_read_b64_tr_b16 a[66:67], v10 offset:8448\n"
"buffer_load_dwordx4 v65, s[40:43], s79 offen offset:1024 lds\n"
"v_mfma_f32_16x16x32_bf16 v[156:159], a[108:111], a[4:7], v[156:159]\n"
"ds_read_b64_tr_b16 a[68:69], v11 offset:256\n"
"v_mfma_f32_16x16x32_bf16 v[152:155], a[108:111], a[0:3], v[152:155]\n"
"ds_read_b64_tr_b16 a[70:71], v11 offset:8448\n"
"s_waitcnt lgkmcnt(4)\n"
"v_mfma_f32_16x16x32_bf16 v[160:163], a[112:115], a[0:3], v[160:163]\n"
"ds_read_b64_tr_b16 a[72:73], v10 offset:320\n"
"v_mfma_f32_16x16x32_bf16 v[164:167], a[112:115], a[4:7], v[164:167]\n"
"ds_read_b64_tr_b16 a[74:75], v10 offset:8512\n"
"v_mfma_f32_16x16x32_bf16 v[172:175], a[116:119], a[4:7], v[172:175]\n"
"ds_read_b64_tr_b16 a[76:77], v11 offset:320\n"
"buffer_load_dwordx4 v66, s[40:43], s79 offen offset:2048 lds\n"
"v_mfma_f32_16x16x32_bf16 v[168:171], a[116:119], a[0:3], v[168:171]\n"
"ds_read_b64_tr_b16 a[78:79], v11 offset:8512\n"
"v_mfma_f32_16x16x32_bf16 v[176:179], a[120:123], a[0:3], v[176:179]\n"
"ds_read_b64_tr_b16 a[80:81], v10 offset:384\n"
"v_mfma_f32_16x16x32_bf16 v[180:183], a[120:123], a[4:7], v[180:183]\n"
"ds_read_b64_tr_b16 a[82:83], v10 offset:8576\n"
"v_mfma_f32_16x16x32_bf16 v[188:191], a[124:127], a[4:7], v[188:191]\n"
"ds_read_b64_tr_b16 a[84:85], v11 offset:384\n"
"buffer_load_dwordx4 v67, s[40:43], s79 offen offset:3072 lds\n"
"v_mfma_f32_16x16x32_bf16 v[184:187], a[124:127], a[0:3], v[184:187]\n"
"ds_read_b64_tr_b16 a[86:87], v11 offset:8576\n"
"s_waitcnt lgkmcnt(4)\n"
"v_mfma_f32_16x16x32_bf16 v[192:195], a[64:67], a[0:3], v[192:195]\n"
"ds_read_b64_tr_b16 a[88:89], v10 offset:16384\n"
"v_mfma_f32_16x16x32_bf16 v[196:199], a[64:67], a[4:7], v[196:199]\n"
"ds_read_b64_tr_b16 a[90:91], v10 offset:24576\n"
"v_mfma_f32_16x16x32_bf16 v[204:207], a[68:71], a[4:7], v[204:207]\n"
"ds_read_b64_tr_b16 a[92:93], v11 offset:16384\n"
"s_add_u32 m0, s74, 102400\n"
"v_mfma_f32_16x16x32_bf16 v[200:203], a[68:71], a[0:3], v[200:203]\n"
"ds_read_b64_tr_b16 a[94:95], v11 offset:24576\n"
"buffer_load_dwordx4 v68, s[40:43], s79 offen offset:0 lds\n"
"v_mfma_f32_16x16x32_bf16 v[208:211], a[72:75], a[0:3], v[208:211]\n"
"ds_read_b64_tr_b16 a[96:97], v10 offset:16448\n"
"v_mfma_f32_16x16x32_bf16 v[212:215], a[72:75], a[4:7], v[212:215]\n"
"ds_read_b64_tr_b16 a[98:99], v10 offset:24640\n"
"v_mfma_f32_16x16x32_bf16 v[220:223], a[76:79], a[4:7], v[220:223]\n"
"ds_read_b64_tr_b16 a[100:101], v11 offset:16448\n"
"v_mfma_f32_16x16x32_bf16 v[216:219], a[76:79], a[0:3], v[216:219]\n"
"ds_read_b64_tr_b16 a[102:103], v11 offset:24640\n"
"s_waitcnt lgkmcnt(4)\n"
"v_mfma_f32_16x16x32_bf16 v[224:227], a[80:83], a[0:3], v[224:227]\n"
"ds_read_b64_tr_b16 a[104:105], v10 offset:16512\n"
"buffer_load_dwordx4 v69, s[40:43], s79 offen offset:1024 lds\n"
"v_mfma_f32_16x16x32_bf16 v[228:231], a[80:83], a[4:7], v[228:231]\n"
"ds_read_b64_tr_b16 a[106:107], v10 offset:24704\n"
"v_mfma_f32_16x16x32_bf16 v[236:239], a[84:87], a[4:7], v[236:239]\n"
"ds_read_b64_tr_b16 a[108:109], v11 offset:16512\n"
"v_mfma_f32_16x16x32_bf16 v[232:235], a[84:87], a[0:3], v[232:235]\n"
"ds_read_b64_tr_b16 a[110:111], v11 offset:24704\n"
"v_add_f32 v104, v104, v106\n"
"v_add_f32 v105, v105, v107\n"
"v_mfma_f32_16x16x32_bf16 v[128:131], a[88:91], a[8:11], v[128:131]\n"
"ds_read_b64_tr_b16 a[112:113], v10 offset:16576\n"
"v_add_f32 v104, v104, v108\n"
"v_add_f32 v105, v105, v109\n"
"v_mfma_f32_16x16x32_bf16 v[132:135], a[88:91], a[12:15], v[132:135]\n"
"ds_read_b64_tr_b16 a[114:115], v10 offset:24768\n"
"v_add_f32 v104, v104, v110\n"
"v_add_f32 v105, v105, v111\n"
"v_mfma_f32_16x16x32_bf16 v[140:143], a[92:95], a[12:15], v[140:143]\n"
"ds_read_b64_tr_b16 a[116:117], v11 offset:16576\n"
"s_waitcnt vmcnt(25)\n"
"v_add_f32 v104, v104, v112\n"
"v_add_f32 v105, v105, v113\n"
"v_mfma_f32_16x16x32_bf16 v[136:139], a[92:95], a[8:11], v[136:139]\n"
"ds_read_b64_tr_b16 a[118:119], v11 offset:24768\n"
"s_waitcnt vmcnt(23)\n"
"v_add_f32 v104, v104, v114\n"
"s_waitcnt lgkmcnt(4)\n"
"v_mfma_f32_16x16x32_bf16 v[144:147], a[96:99], a[8:11], v[144:147]\n"
"ds_read_b64_tr_b16 a[120:121], v10 offset:16640\n"
"v_add_f32 v105, v105, v115\n"
"s_waitcnt vmcnt(21)\n"
"v_add_f32 v104, v104, v116\n"
"v_mfma_f32_16x16x32_bf16 v[148:151], a[96:99], a[12:15], v[148:151]\n"
"ds_read_b64_tr_b16 a[122:123], v10 offset:24832\n"
"v_add_f32 v105, v105, v117\n"
"s_waitcnt vmcnt(18)\n"
"v_add_f32 v104, v104, v118\n"
"v_mfma_f32_16x16x32_bf16 v[156:159], a[100:103], a[12:15], v[156:159]\n"
"ds_read_b64_tr_b16 a[124:125], v11 offset:16640\n"
"v_add_f32 v105, v105, v119\n"
"v_mov_b32 v106, v104\n"
"v_mfma_f32_16x16x32_bf16 v[152:155], a[100:103], a[8:11], v[152:155]\n"
"ds_read_b64_tr_b16 a[126:127], v11 offset:24832\n"
"v_mov_b32 v107, v105\n"
"v_permlane32_swap_b32 v104, v106\n"
"v_mfma_f32_16x16x32_bf16 v[160:163], a[104:107], a[8:11], v[160:163]\n"
"ds_read_b64_tr_b16 a[64:65], v10 offset:16704\n"
"v_permlane32_swap_b32 v105, v107\n"
"v_add_f32 v104, v104, v106\n"
"v_mfma_f32_16x16x32_bf16 v[164:167], a[104:107], a[12:15], v[164:167]\n"
"ds_read_b64_tr_b16 a[66:67], v10 offset:24896\n"
"v_add_f32 v105, v105, v107\n"
"v_mul_f32 v104, 0x3a800000, v104\n"
"v_mfma_f32_16x16x32_bf16 v[172:175], a[108:111], a[12:15], v[172:175]\n"
"ds_read_b64_tr_b16 a[68:69], v11 offset:16704\n"
"v_mul_f32 v105, 0x3a800000, v105\n"
"v_fma_f32 v105, -v104, v104, v105\n"
"v_mfma_f32_16x16x32_bf16 v[168:171], a[108:111], a[8:11], v[168:171]\n"
"ds_read_b64_tr_b16 a[70:71], v11 offset:24896\n"
"v_max_f32 v105, 0, v105\n"
"s_waitcnt lgkmcnt(4)\n"
"v_mfma_f32_16x16x32_bf16 v[176:179], a[112:115], a[8:11], v[176:179]\n"
"ds_read_b64_tr_b16 a[72:73], v10 offset:16768\n"
"v_add_f32 v105, 0x3727c5ac, v105\n"
"v_rsq_f32 v88, v105\n"
"v_mfma_f32_16x16x32_bf16 v[180:183], a[112:115], a[12:15], v[180:183]\n"
"ds_read_b64_tr_b16 a[74:75], v10 offset:24960\n"
"v_mul_f32 v92, -v104, v88\n"
"v_mfma_f32_16x16x32_bf16 v[188:191], a[116:119], a[12:15], v[188:191]\n"
"ds_read_b64_tr_b16 a[76:77], v11 offset:16768\n"
"v_mov_b32 v90, v88\n"
"v_mfma_f32_16x16x32_bf16 v[184:187], a[116:119], a[8:11], v[184:187]\n"
"ds_read_b64_tr_b16 a[78:79], v11 offset:24960\n"
"v_mov_b32 v94, v92\n"
"v_mfma_f32_16x16x32_bf16 v[192:195], a[120:123], a[8:11], v[192:195]\n"
"ds_read_b64_tr_b16 a[80:81], v10 offset:32768\n"
"v_permlane16_swap_b32 v88, v90\n"
"v_mfma_f32_16x16x32_bf16 v[196:199], a[120:123], a[12:15], v[196:199]\n"
"ds_read_b64_tr_b16 a[82:83], v10 offset:40960\n"
"v_permlane16_swap_b32 v92, v94\n"
"v_mfma_f32_16x16x32_bf16 v[204:207], a[124:127], a[12:15], v[204:207]\n"
"ds_read_b64_tr_b16 a[84:85], v11 offset:32768\n"
"v_mov_b32 v89, v88\n"
"v_mfma_f32_16x16x32_bf16 v[200:203], a[124:127], a[8:11], v[200:203]\n"
"ds_read_b64_tr_b16 a[86:87], v11 offset:40960\n"
"s_waitcnt lgkmcnt(4)\n"
"v_mfma_f32_16x16x32_bf16 v[208:211], a[64:67], a[8:11], v[208:211]\n"
"ds_read_b64_tr_b16 a[88:89], v10 offset:32832\n"
"v_mov_b32 v91, v90\n"
"v_mfma_f32_16x16x32_bf16 v[212:215], a[64:67], a[12:15], v[212:215]\n"
"ds_read_b64_tr_b16 a[90:91], v10 offset:41024\n"
"v_mov_b32 v93, v92\n"
"v_mfma_f32_16x16x32_bf16 v[220:223], a[68:71], a[12:15], v[220:223]\n"
"ds_read_b64_tr_b16 a[92:93], v11 offset:32832\n"
"v_mov_b32 v95, v94\n"
"v_mfma_f32_16x16x32_bf16 v[216:219], a[68:71], a[8:11], v[216:219]\n"
"ds_read_b64_tr_b16 a[94:95], v11 offset:41024\n"
"buffer_load_dwordx4 v70, s[40:43], s79 offen offset:2048 lds\n"
"v_mfma_f32_16x16x32_bf16 v[224:227], a[72:75], a[8:11], v[224:227]\n"
"ds_read_b64_tr_b16 a[96:97], v10 offset:32896\n"
"buffer_load_dwordx4 v71, s[40:43], s79 offen offset:3072 lds\n"
"v_mfma_f32_16x16x32_bf16 v[228:231], a[72:75], a[12:15], v[228:231]\n"
"ds_read_b64_tr_b16 a[98:99], v10 offset:41088\n"
"buffer_load_dwordx4 a[48:51], v8, s[52:55], s82 offen offset:0 sc1\n"
"v_mfma_f32_16x16x32_bf16 v[236:239], a[76:79], a[12:15], v[236:239]\n"
"ds_read_b64_tr_b16 a[100:101], v11 offset:32896\n"
"buffer_load_dwordx4 a[52:55], v8, s[52:55], s82 offen offset:1024 sc1\n"
"v_mfma_f32_16x16x32_bf16 v[232:235], a[76:79], a[8:11], v[232:235]\n"
"ds_read_b64_tr_b16 a[102:103], v11 offset:41088\n"
"buffer_load_dwordx4 a[56:59], v8, s[52:55], s82 offen offset:2048 sc1\n"
"buffer_load_dwordx4 a[60:63], v8, s[52:55], s82 offen offset:3072 sc1\n"
"s_waitcnt vmcnt(16)\n"
"s_barrier\n"
"s_waitcnt lgkmcnt(4)\n"
"v_mfma_f32_16x16x32_bf16 v[128:131], a[80:83], a[16:19], v[128:131]\n"
"ds_read_b64_tr_b16 a[104:105], v10 offset:32960\n"
"v_readlane_b32 s79, v21, 0\n"
"v_mfma_f32_16x16x32_bf16 v[132:135], a[80:83], a[20:23], v[132:135]\n"
"ds_read_b64_tr_b16 a[106:107], v10 offset:41152\n"
"s_add_u32 m0, s74, 0\n"
"v_mfma_f32_16x16x32_bf16 v[140:143], a[84:87], a[20:23], v[140:143]\n"
"ds_read_b64_tr_b16 a[108:109], v11 offset:32960\n"
"buffer_load_dwordx4 v4, s[4:7], s79 offen offset:0 lds\n"
"v_mfma_f32_16x16x32_bf16 v[136:139], a[84:87], a[16:19], v[136:139]\n"
"ds_read_b64_tr_b16 a[110:111], v11 offset:41152\n"
"v_mfma_f32_16x16x32_bf16 v[144:147], a[88:91], a[16:19], v[144:147]\n"
"ds_read_b64_tr_b16 a[112:113], v10 offset:33024\n"
"v_mfma_f32_16x16x32_bf16 v[148:151], a[88:91], a[20:23], v[148:151]\n"
"ds_read_b64_tr_b16 a[114:115], v10 offset:41216\n"
"buffer_load_dwordx4 v5, s[4:7], s79 offen offset:1024 lds\n"
"v_mfma_f32_16x16x32_bf16 v[156:159], a[92:95], a[20:23], v[156:159]\n"
"ds_read_b64_tr_b16 a[116:117], v11 offset:33024\n"
"v_mfma_f32_16x16x32_bf16 v[152:155], a[92:95], a[16:19], v[152:155]\n"
"ds_read_b64_tr_b16 a[118:119], v11 offset:41216\n"
"s_waitcnt lgkmcnt(4)\n"
"v_mfma_f32_16x16x32_bf16 v[160:163], a[96:99], a[16:19], v[160:163]\n"
"ds_read_b64_tr_b16 a[120:121], v10 offset:33088\n"
"v_mfma_f32_16x16x32_bf16 v[164:167], a[96:99], a[20:23], v[164:167]\n"
"ds_read_b64_tr_b16 a[122:123], v10 offset:41280\n"
"v_mfma_f32_16x16x32_bf16 v[172:175], a[100:103], a[20:23], v[172:175]\n"
"ds_read_b64_tr_b16 a[124:125], v11 offset:33088\n"
"buffer_load_dwordx4 v6, s[4:7], s79 offen offset:2048 lds\n"
"v_mfma_f32_16x16x32_bf16 v[168:171], a[100:103], a[16:19], v[168:171]\n"
"ds_read_b64_tr_b16 a[126:127], v11 offset:41280\n"
"v_mfma_f32_16x16x32_bf16 v[176:179], a[104:107], a[16:19], v[176:179]\n"
"ds_read_b64_tr_b16 a[64:65], v10 offset:33152\n"
"v_mfma_f32_16x16x32_bf16 v[180:183], a[104:107], a[20:23], v[180:183]\n"
"ds_read_b64_tr_b16 a[66:67], v10 offset:41344\n"
"v_mfma_f32_16x16x32_bf16 v[188:191], a[108:111], a[20:23], v[188:191]\n"
"ds_read_b64_tr_b16 a[68:69], v11 offset:33152\n"
"buffer_load_dwordx4 v7, s[4:7], s79 offen offset:3072 lds\n"
"v_mfma_f32_16x16x32_bf16 v[184:187], a[108:111], a[16:19], v[184:187]\n"
"ds_read_b64_tr_b16 a[70:71], v11 offset:41344\n"
"s_waitcnt lgkmcnt(4)\n"
"v_mfma_f32_16x16x32_bf16 v[192:195], a[112:115], a[16:19], v[192:195]\n"
"ds_read_b64_tr_b16 a[72:73], v10 offset:49152\n"
"v_mfma_f32_16x16x32_bf16 v[196:199], a[112:115], a[20:23], v[196:199]\n"
"ds_read_b64_tr_b16 a[74:75], v10 offset:57344\n"
"v_mfma_f32_16x16x32_bf16 v[204:207], a[116:119], a[20:23], v[204:207]\n"
"ds_read_b64_tr_b16 a[76:77], v11 offset:49152\n"
"s_add_u32 m0, s74, 4096\n"
"v_mfma_f32_16x16x32_bf16 v[200:203], a[116:119], a[16:19], v[200:203]\n"
"ds_read_b64_tr_b16 a[78:79], v11 offset:57344\n"
"buffer_load_dwordx4 v16, s[4:7], s79 offen offset:0 lds\n"
"v_mfma_f32_16x16x32_bf16 v[208:211], a[120:123], a[16:19], v[208:211]\n"
"ds_read_b64_tr_b16 a[80:81], v10 offset:49216\n"
"v_mfma_f32_16x16x32_bf16 v[212:215], a[120:123], a[20:23], v[212:215]\n"
"ds_read_b64_tr_b16 a[82:83], v10 offset:57408\n"
"v_mfma_f32_16x16x32_bf16 v[220:223], a[124:127], a[20:23], v[220:223]\n"
"ds_read_b64_tr_b16 a[84:85], v11 offset:49216\n"
"v_mfma_f32_16x16x32_bf16 v[216:219], a[124:127], a[16:19], v[216:219]\n"
"ds_read_b64_tr_b16 a[86:87], v11 offset:57408\n"
"s_waitcnt lgkmcnt(4)\n"
"v_mfma_f32_16x16x32_bf16 v[224:227], a[64:67], a[16:19], v[224:227]\n"
"ds_read_b64_tr_b16 a[88:89], v10 offset:49280\n"
"buffer_load_dwordx4 v17, s[4:7], s79 offen offset:1024 lds\n"
"v_mfma_f32_16x16x32_bf16 v[228:231], a[64:67], a[20:23], v[228:231]\n"
"ds_read_b64_tr_b16 a[90:91], v10 offset:57472\n"
"v_mfma_f32_16x16x32_bf16 v[236:239], a[68:71], a[20:23], v[236:239]\n"
"ds_read_b64_tr_b16 a[92:93], v11 offset:49280\n"
"v_mfma_f32_16x16x32_bf16 v[232:235], a[68:71], a[16:19], v[232:235]\n"
"ds_read_b64_tr_b16 a[94:95], v11 offset:57472\n"
"v_mfma_f32_16x16x32_bf16 v[128:131], a[72:75], a[24:27], v[128:131]\n"
"ds_read_b64_tr_b16 a[96:97], v10 offset:49344\n"
"buffer_load_dwordx4 v18, s[4:7], s79 offen offset:2048 lds\n"
"v_mfma_f32_16x16x32_bf16 v[132:135], a[72:75], a[28:31], v[132:135]\n"
"ds_read_b64_tr_b16 a[98:99], v10 offset:57536\n"
"v_mfma_f32_16x16x32_bf16 v[140:143], a[76:79], a[28:31], v[140:143]\n"
"ds_read_b64_tr_b16 a[100:101], v11 offset:49344\n"
"v_mfma_f32_16x16x32_bf16 v[136:139], a[76:79], a[24:27], v[136:139]\n"
"ds_read_b64_tr_b16 a[102:103], v11 offset:57536\n"
"s_waitcnt lgkmcnt(4)\n"
"v_mfma_f32_16x16x32_bf16 v[144:147], a[80:83], a[24:27], v[144:147]\n"
"ds_read_b64_tr_b16 a[104:105], v10 offset:49408\n"
"v_mfma_f32_16x16x32_bf16 v[148:151], a[80:83], a[28:31], v[148:151]\n"
"ds_read_b64_tr_b16 a[106:107], v10 offset:57600\n"
"buffer_load_dwordx4 v19, s[4:7], s79 offen offset:3072 lds\n"
"v_mfma_f32_16x16x32_bf16 v[156:159], a[84:87], a[28:31], v[156:159]\n"
"ds_read_b64_tr_b16 a[108:109], v11 offset:49408\n"
"v_readlane_b32 s79, v21, 1\n"
"v_mfma_f32_16x16x32_bf16 v[152:155], a[84:87], a[24:27], v[152:155]\n"
"ds_read_b64_tr_b16 a[110:111], v11 offset:57600\n"
"v_mfma_f32_16x16x32_bf16 v[160:163], a[88:91], a[24:27], v[160:163]\n"
"ds_read_b64_tr_b16 a[112:113], v10 offset:49472\n"
"s_lshr_b32 s77, s66, 1\n"
"v_mfma_f32_16x16x32_bf16 v[164:167], a[88:91], a[28:31], v[164:167]\n"
"ds_read_b64_tr_b16 a[114:115], v10 offset:57664\n"
"s_and_b32 s78, s66, 1\n"
"v_mfma_f32_16x16x32_bf16 v[172:175], a[92:95], a[28:31], v[172:175]\n"
"ds_read_b64_tr_b16 a[116:117], v11 offset:49472\n"
"s_lshl_b32 s80, s67, 18\n"
"v_mfma_f32_16x16x32_bf16 v[168:171], a[92:95], a[24:27], v[168:171]\n"
"ds_read_b64_tr_b16 a[118:119], v11 offset:57664\n"
"s_waitcnt lgkmcnt(4)\n"
"v_mfma_f32_16x16x32_bf16 v[176:179], a[96:99], a[24:27], v[176:179]\n"
"ds_read_b64_tr_b16 a[120:121], v10 offset:49536\n"
"s_lshl_b32 s76, s77, 17\n"
"v_mfma_f32_16x16x32_bf16 v[180:183], a[96:99], a[28:31], v[180:183]\n"
"ds_read_b64_tr_b16 a[122:123], v10 offset:57728\n"
"s_add_u32 s80, s80, s76\n"
"v_mfma_f32_16x16x32_bf16 v[188:191], a[100:103], a[28:31], v[188:191]\n"
"ds_read_b64_tr_b16 a[124:125], v11 offset:49536\n"
"s_lshl_b32 s76, s78, 11\n"
"v_mfma_f32_16x16x32_bf16 v[184:187], a[100:103], a[24:27], v[184:187]\n"
"ds_read_b64_tr_b16 a[126:127], v11 offset:57728\n"
"s_add_u32 s80, s80, s76\n"
"v_mfma_f32_16x16x32_bf16 v[192:195], a[104:107], a[24:27], v[192:195]\n"
"ds_read_b64_tr_b16 a[64:65], v12 offset:0\n"
"s_add_u32 s96, s80, 65536\n"
"v_mfma_f32_16x16x32_bf16 v[196:199], a[104:107], a[28:31], v[196:199]\n"
"ds_read_b64_tr_b16 a[66:67], v12 offset:8192\n"
"buffer_load_dwordx4 a[176:179], v8, s[52:55], s80 offen offset:0 sc1\n"
"v_mfma_f32_16x16x32_bf16 v[204:207], a[108:111], a[28:31], v[204:207]\n"
"ds_read_b64_tr_b16 a[68:69], v13 offset:0\n"
"buffer_load_dwordx4 a[180:183], v8, s[52:55], s80 offen offset:1024 sc1\n"
"v_mfma_f32_16x16x32_bf16 v[200:203], a[108:111], a[24:27], v[200:203]\n"
"ds_read_b64_tr_b16 a[70:71], v13 offset:8192\n"
"s_waitcnt lgkmcnt(4)\n"
"v_mfma_f32_16x16x32_bf16 v[208:211], a[112:115], a[24:27], v[208:211]\n"
"ds_read_b64_tr_b16 a[72:73], v12 offset:16384\n"
"buffer_load_dwordx4 a[184:187], v8, s[52:55], s96 offen offset:0 sc1\n"
"v_mfma_f32_16x16x32_bf16 v[212:215], a[112:115], a[28:31], v[212:215]\n"
"ds_read_b64_tr_b16 a[74:75], v12 offset:24576\n"
"v_mfma_f32_16x16x32_bf16 v[220:223], a[116:119], a[28:31], v[220:223]\n"
"ds_read_b64_tr_b16 a[76:77], v13 offset:16384\n"
"v_mfma_f32_16x16x32_bf16 v[216:219], a[116:119], a[24:27], v[216:219]\n"
"ds_read_b64_tr_b16 a[78:79], v13 offset:24576\n"
"buffer_load_dwordx4 a[188:191], v8, s[52:55], s96 offen offset:1024 sc1\n"
"v_mfma_f32_16x16x32_bf16 v[224:227], a[120:123], a[24:27], v[224:227]\n"
"v_mfma_f32_16x16x32_bf16 v[228:231], a[120:123], a[28:31], v[228:231]\n"
"v_mfma_f32_16x16x32_bf16 v[236:239], a[124:127], a[28:31], v[236:239]\n"
"v_mfma_f32_16x16x32_bf16 v[232:235], a[124:127], a[24:27], v[232:235]\n"
"buffer_load_dwordx2 v[96:97], v22, s[44:47], 0 offen offset:0\n"
"buffer_load_dwordx2 v[98:99], v22, s[44:47], 0 offen offset:32\n"
"buffer_load_dwordx2 v[100:101], v22, s[44:47], 0 offen offset:64\n"
"buffer_load_dwordx2 v[102:103], v22, s[44:47], 0 offen offset:96\n"
"s_waitcnt vmcnt(20)\n"
"s_barrier\n"
"s_waitcnt lgkmcnt(0)\n"
"v_mfma_f32_16x16x32_bf16 v[128:131], a[64:67], a[32:35], v[128:131]\n"
"ds_read_b64_tr_b16 a[80:81], v12 offset:32768\n"
"ds_read_b64_tr_b16 a[82:83], v12 offset:40960\n"
"v_mfma_f32_16x16x32_bf16 v[132:135], a[64:67], a[36:39], v[132:135]\n"
"ds_read_b64_tr_b16 a[84:85], v13 offset:32768\n"
"ds_read_b64_tr_b16 a[86:87], v13 offset:40960\n"
"v_mfma_f32_16x16x32_bf16 v[140:143], a[68:71], a[36:39], v[140:143]\n"
"ds_read_b64_tr_b16 a[88:89], v12 offset:49152\n"
"ds_read_b64_tr_b16 a[90:91], v12 offset:57344\n"
"v_mfma_f32_16x16x32_bf16 v[136:139], a[68:71], a[32:35], v[136:139]\n"
"ds_read_b64_tr_b16 a[92:93], v13 offset:49152\n"
"s_add_u32 m0, s74, 32768\n"
"v_mfma_f32_16x16x32_bf16 v[128:131], a[72:75], a[40:43], v[128:131]\n"
"ds_read_b64_tr_b16 a[94:95], v13 offset:57344\n"
"buffer_load_dwordx4 v4, s[4:7], s79 offen offset:0 lds\n"
"v_mfma_f32_16x16x32_bf16 v[132:135], a[72:75], a[44:47], v[132:135]\n"
"ds_read_b64_tr_b16 a[96:97], v12 offset:64\n"
"buffer_load_dwordx4 v5, s[4:7], s79 offen offset:1024 lds\n"
"v_mfma_f32_16x16x32_bf16 v[140:143], a[76:79], a[44:47], v[140:143]\n"
"ds_read_b64_tr_b16 a[98:99], v12 offset:8256\n"
"buffer_load_dwordx4 v6, s[4:7], s79 offen offset:2048 lds\n"
"v_mfma_f32_16x16x32_bf16 v[136:139], a[76:79], a[40:43], v[136:139]\n"
"ds_read_b64_tr_b16 a[100:101], v13 offset:64\n"
"s_waitcnt lgkmcnt(3)\n"
"s_waitcnt vmcnt(22)\n"
"v_mfma_f32_16x16x32_bf16 v[128:131], a[80:83], a[48:51], v[128:131]\n"
"ds_read_b64_tr_b16 a[102:103], v13 offset:8256\n"
"buffer_load_dwordx4 v7, s[4:7], s79 offen offset:3072 lds\n"
"s_waitcnt vmcnt(22)\n"
"v_mfma_f32_16x16x32_bf16 v[132:135], a[80:83], a[52:55], v[132:135]\n"
"ds_read_b64_tr_b16 a[104:105], v12 offset:16448\n"
"s_add_u32 m0, s74, 36864\n"
"v_mfma_f32_16x16x32_bf16 v[140:143], a[84:87], a[52:55], v[140:143]\n"
"ds_read_b64_tr_b16 a[106:107], v12 offset:24640\n"
"buffer_load_dwordx4 v16, s[4:7], s79 offen offset:0 lds\n"
"v_mfma_f32_16x16x32_bf16 v[136:139], a[84:87], a[48:51], v[136:139]\n"
"ds_read_b64_tr_b16 a[108:109], v13 offset:16448\n"
"buffer_load_dwordx4 v17, s[4:7], s79 offen offset:1024 lds\n"
"s_waitcnt vmcnt(23)\n"
"v_mfma_f32_16x16x32_bf16 v[128:131], a[88:91], a[56:59], v[128:131]\n"
"ds_read_b64_tr_b16 a[110:111], v13 offset:24640\n"
"buffer_load_dwordx4 v18, s[4:7], s79 offen offset:2048 lds\n"
"s_waitcnt vmcnt(23)\n"
"v_mfma_f32_16x16x32_bf16 v[132:135], a[88:91], a[60:63], v[132:135]\n"
"ds_read_b64_tr_b16 a[112:113], v12 offset:32832\n"
"v_mfma_f32_16x16x32_bf16 v[140:143], a[92:95], a[60:63], v[140:143]\n"
"ds_read_b64_tr_b16 a[114:115], v12 offset:41024\n"
"buffer_load_dwordx4 v19, s[4:7], s79 offen offset:3072 lds\n"
"v_mfma_f32_16x16x32_bf16 v[136:139], a[92:95], a[56:59], v[136:139]\n"
"ds_read_b64_tr_b16 a[116:117], v13 offset:32832\n"
"s_waitcnt lgkmcnt(3)\n"
"v_mfma_f32_16x16x32_bf16 v[144:147], a[96:99], a[32:35], v[144:147]\n"
"ds_read_b64_tr_b16 a[118:119], v13 offset:41024\n"
"v_readlane_b32 s79, v21, 2\n"
"v_mfma_f32_16x16x32_bf16 v[148:151], a[96:99], a[36:39], v[148:151]\n"
"ds_read_b64_tr_b16 a[120:121], v12 offset:49216\n"
"ds_read_b128 v[112:115], v9 offset:0\n"
"v_mfma_f32_16x16x32_bf16 v[156:159], a[100:103], a[36:39], v[156:159]\n"
"ds_read_b64_tr_b16 a[122:123], v12 offset:57408\n"
"ds_read_b128 v[116:119], v9 offset:1024\n"
"v_mfma_f32_16x16x32_bf16 v[152:155], a[100:103], a[32:35], v[152:155]\n"
"ds_read_b64_tr_b16 a[124:125], v13 offset:49216\n"
"ds_read_b128 v[120:123], v9 offset:64\n"
"v_mfma_f32_16x16x32_bf16 v[144:147], a[104:107], a[40:43], v[144:147]\n"
"ds_read_b64_tr_b16 a[126:127], v13 offset:57408\n"
"ds_read_b128 v[124:127], v9 offset:1088\n"
"v_mfma_f32_16x16x32_bf16 v[148:151], a[104:107], a[44:47], v[148:151]\n"
"ds_read_b64_tr_b16 a[64:65], v12 offset:128\n"
"v_mfma_f32_16x16x32_bf16 v[156:159], a[108:111], a[44:47], v[156:159]\n"
"ds_read_b64_tr_b16 a[66:67], v12 offset:8320\n"
"v_mfma_f32_16x16x32_bf16 v[152:155], a[108:111], a[40:43], v[152:155]\n"
"ds_read_b64_tr_b16 a[68:69], v13 offset:128\n"
"s_waitcnt lgkmcnt(4)\n"
"v_mfma_f32_16x16x32_bf16 v[144:147], a[112:115], a[48:51], v[144:147]\n"
"ds_read_b64_tr_b16 a[70:71], v13 offset:8320\n"
"v_mfma_f32_16x16x32_bf16 v[148:151], a[112:115], a[52:55], v[148:151]\n"
"ds_read_b64_tr_b16 a[72:73], v12 offset:16512\n"
"v_mfma_f32_16x16x32_bf16 v[156:159], a[116:119], a[52:55], v[156:159]\n"
"ds_read_b64_tr_b16 a[74:75], v12 offset:24704\n"
"v_mfma_f32_16x16x32_bf16 v[152:155], a[116:119], a[48:51], v[152:155]\n"
"ds_read_b64_tr_b16 a[76:77], v13 offset:16512\n"
"ds_read_b128 v[240:243], v9 offset:128\n"
"v_mfma_f32_16x16x32_bf16 v[144:147], a[120:123], a[56:59], v[144:147]\n"
"ds_read_b64_tr_b16 a[78:79], v13 offset:24704\n"
"ds_read_b128 v[244:247], v9 offset:1152\n"
"v_mfma_f32_16x16x32_bf16 v[148:151], a[120:123], a[60:63], v[148:151]\n"
"ds_read_b64_tr_b16 a[80:81], v12 offset:32896\n"
"ds_read_b128 v[248:251], v9 offset:192\n"
"v_mfma_f32_16x16x32_bf16 v[156:159], a[124:127], a[60:63], v[156:159]\n"
"ds_read_b64_tr_b16 a[82:83], v12 offset:41088\n"
"ds_read_b128 v[252:255], v9 offset:1216\n"
"v_mfma_f32_16x16x32_bf16 v[152:155], a[124:127], a[56:59], v[152:155]\n"
"s_waitcnt lgkmcnt(14)\n"
"ds_read_b64_tr_b16 a[84:85], v13 offset:32896\n"
"v_pk_fma_f32 v[104:105], v[112:113], v[92:93], v[116:117]\n"
"v_pk_fma_f32 v[106:107], v[114:115], v[92:93], v[118:119]\n"
"v_pk_fma_f32 v[128:129], v[128:129], v[88:89], v[104:105]\n"
"v_pk_fma_f32 v[130:131], v[130:131], v[88:89], v[106:107]\n"
"v_max_f32 v128, 0, v128\n"
"v_max_f32 v129, 0, v129\n"
"v_max_f32 v130, 0, v130\n"
"v_max_f32 v131, 0, v131\n"
"v_pk_mul_f32 v[128:129], v[128:129], v[128:129]\n"
"v_pk_mul_f32 v[130:131], v[130:131], v[130:131]\n"
"v_cvt_pk_bf16_f32 v32, v128, v129\n"
"v_cvt_pk_bf16_f32 v33, v130, v131\n"
"v_pk_fma_f32 v[108:109], v[112:113], v[94:95], v[116:117]\n"
"v_pk_fma_f32 v[110:111], v[114:115], v[94:95], v[118:119]\n"
"v_pk_fma_f32 v[132:133], v[132:133], v[90:91], v[108:109]\n"
"v_pk_fma_f32 v[134:135], v[134:135], v[90:91], v[110:111]\n"
"v_max_f32 v132, 0, v132\n"
"v_max_f32 v133, 0, v133\n"
"v_max_f32 v134, 0, v134\n"
"v_max_f32 v135, 0, v135\n"
"v_pk_mul_f32 v[132:133], v[132:133], v[132:133]\n"
"v_pk_mul_f32 v[134:135], v[134:135], v[134:135]\n"
"v_cvt_pk_bf16_f32 v36, v132, v133\n"
"v_cvt_pk_bf16_f32 v37, v134, v135\n"
"v_pk_fma_f32 v[104:105], v[120:121], v[92:93], v[124:125]\n"
"v_pk_fma_f32 v[106:107], v[122:123], v[92:93], v[126:127]\n"
"v_pk_fma_f32 v[136:137], v[136:137], v[88:89], v[104:105]\n"
"v_pk_fma_f32 v[138:139], v[138:139], v[88:89], v[106:107]\n"
"v_max_f32 v136, 0, v136\n"
"v_max_f32 v137, 0, v137\n"
"v_max_f32 v138, 0, v138\n"
"v_max_f32 v139, 0, v139\n"
"v_pk_mul_f32 v[136:137], v[136:137], v[136:137]\n"
"v_pk_mul_f32 v[138:139], v[138:139], v[138:139]\n"
"v_cvt_pk_bf16_f32 v34, v136, v137\n"
"v_cvt_pk_bf16_f32 v35, v138, v139\n"
"v_pk_fma_f32 v[108:109], v[120:121], v[94:95], v[124:125]\n"
"v_pk_fma_f32 v[110:111], v[122:123], v[94:95], v[126:127]\n"
"v_pk_fma_f32 v[140:141], v[140:141], v[90:91], v[108:109]\n"
"v_pk_fma_f32 v[142:143], v[142:143], v[90:91], v[110:111]\n"
"v_max_f32 v140, 0, v140\n"
"v_max_f32 v141, 0, v141\n"
"v_max_f32 v142, 0, v142\n"
"v_max_f32 v143, 0, v143\n"
"v_pk_mul_f32 v[140:141], v[140:141], v[140:141]\n"
"v_pk_mul_f32 v[142:143], v[142:143], v[142:143]\n"
"v_cvt_pk_bf16_f32 v38, v140, v141\n"
"v_cvt_pk_bf16_f32 v39, v142, v143\n"
"buffer_store_dwordx4 v[32:35], v8, s[36:39], s93 offen offset:0 sc1\n"
"buffer_store_dwordx4 v[36:39], v8, s[36:39], s93 offen offset:1024 sc1\n"
"s_waitcnt lgkmcnt(6)\n"
"v_mfma_f32_16x16x32_bf16 v[160:163], a[64:67], a[32:35], v[160:163]\n"
"ds_read_b64_tr_b16 a[86:87], v13 offset:41088\n"
"v_mfma_f32_16x16x32_bf16 v[164:167], a[64:67], a[36:39], v[164:167]\n"
"ds_read_b64_tr_b16 a[88:89], v12 offset:49280\n"
"v_mfma_f32_16x16x32_bf16 v[172:175], a[68:71], a[36:39], v[172:175]\n"
"ds_read_b64_tr_b16 a[90:91], v12 offset:57472\n"
"v_mfma_f32_16x16x32_bf16 v[168:171], a[68:71], a[32:35], v[168:171]\n"
"ds_read_b64_tr_b16 a[92:93], v13 offset:49280\n"
"v_mfma_f32_16x16x32_bf16 v[160:163], a[72:75], a[40:43], v[160:163]\n"
"ds_read_b64_tr_b16 a[94:95], v13 offset:57472\n"
"v_mfma_f32_16x16x32_bf16 v[164:167], a[72:75], a[44:47], v[164:167]\n"
"ds_read_b64_tr_b16 a[96:97], v12 offset:192\n"
"v_mfma_f32_16x16x32_bf16 v[172:175], a[76:79], a[44:47], v[172:175]\n"
"ds_read_b64_tr_b16 a[98:99], v12 offset:8384\n"
"v_mfma_f32_16x16x32_bf16 v[168:171], a[76:79], a[40:43], v[168:171]\n"
"ds_read_b64_tr_b16 a[100:101], v13 offset:192\n"
"s_waitcnt lgkmcnt(3)\n"
"v_mfma_f32_16x16x32_bf16 v[160:163], a[80:83], a[48:51], v[160:163]\n"
"ds_read_b64_tr_b16 a[102:103], v13 offset:8384\n"
"v_mfma_f32_16x16x32_bf16 v[164:167], a[80:83], a[52:55], v[164:167]\n"
"ds_read_b64_tr_b16 a[104:105], v12 offset:16576\n"
"v_mfma_f32_16x16x32_bf16 v[172:175], a[84:87], a[52:55], v[172:175]\n"
"ds_read_b64_tr_b16 a[106:107], v12 offset:24768\n"
"v_mfma_f32_16x16x32_bf16 v[168:171], a[84:87], a[48:51], v[168:171]\n"
"ds_read_b64_tr_b16 a[108:109], v13 offset:16576\n"
"ds_read_b128 v[112:115], v9 offset:256\n"
"v_mfma_f32_16x16x32_bf16 v[160:163], a[88:91], a[56:59], v[160:163]\n"
"ds_read_b64_tr_b16 a[110:111], v13 offset:24768\n"
"ds_read_b128 v[116:119], v9 offset:1280\n"
"v_mfma_f32_16x16x32_bf16 v[164:167], a[88:91], a[60:63], v[164:167]\n"
"ds_read_b64_tr_b16 a[112:113], v12 offset:32960\n"
"ds_read_b128 v[120:123], v9 offset:320\n"
"v_mfma_f32_16x16x32_bf16 v[172:175], a[92:95], a[60:63], v[172:175]\n"
"ds_read_b64_tr_b16 a[114:115], v12 offset:41152\n"
"ds_read_b128 v[124:127], v9 offset:1344\n"
"v_mfma_f32_16x16x32_bf16 v[168:171], a[92:95], a[56:59], v[168:171]\n"
"ds_read_b64_tr_b16 a[116:117], v13 offset:32960\n"
"v_pk_fma_f32 v[104:105], v[240:241], v[92:93], v[244:245]\n"
"v_pk_fma_f32 v[106:107], v[242:243], v[92:93], v[246:247]\n"
"v_pk_fma_f32 v[144:145], v[144:145], v[88:89], v[104:105]\n"
"v_pk_fma_f32 v[146:147], v[146:147], v[88:89], v[106:107]\n"
"v_max_f32 v144, 0, v144\n"
"v_max_f32 v145, 0, v145\n"
"v_max_f32 v146, 0, v146\n"
"v_max_f32 v147, 0, v147\n"
"v_pk_mul_f32 v[144:145], v[144:145], v[144:145]\n"
"v_pk_mul_f32 v[146:147], v[146:147], v[146:147]\n"
"v_cvt_pk_bf16_f32 v40, v144, v145\n"
"v_cvt_pk_bf16_f32 v41, v146, v147\n"
"v_pk_fma_f32 v[108:109], v[240:241], v[94:95], v[244:245]\n"
"v_pk_fma_f32 v[110:111], v[242:243], v[94:95], v[246:247]\n"
"v_pk_fma_f32 v[148:149], v[148:149], v[90:91], v[108:109]\n"
"v_pk_fma_f32 v[150:151], v[150:151], v[90:91], v[110:111]\n"
"v_max_f32 v148, 0, v148\n"
"v_max_f32 v149, 0, v149\n"
"v_max_f32 v150, 0, v150\n"
"v_max_f32 v151, 0, v151\n"
"v_pk_mul_f32 v[148:149], v[148:149], v[148:149]\n"
"v_pk_mul_f32 v[150:151], v[150:151], v[150:151]\n"
"v_cvt_pk_bf16_f32 v44, v148, v149\n"
"v_cvt_pk_bf16_f32 v45, v150, v151\n"
"v_pk_fma_f32 v[104:105], v[248:249], v[92:93], v[252:253]\n"
"v_pk_fma_f32 v[106:107], v[250:251], v[92:93], v[254:255]\n"
"v_pk_fma_f32 v[152:153], v[152:153], v[88:89], v[104:105]\n"
"v_pk_fma_f32 v[154:155], v[154:155], v[88:89], v[106:107]\n"
"v_max_f32 v152, 0, v152\n"
"v_max_f32 v153, 0, v153\n"
"v_max_f32 v154, 0, v154\n"
"v_max_f32 v155, 0, v155\n"
"v_pk_mul_f32 v[152:153], v[152:153], v[152:153]\n"
"v_pk_mul_f32 v[154:155], v[154:155], v[154:155]\n"
"v_cvt_pk_bf16_f32 v42, v152, v153\n"
"v_cvt_pk_bf16_f32 v43, v154, v155\n"
"v_pk_fma_f32 v[108:109], v[248:249], v[94:95], v[252:253]\n"
"v_pk_fma_f32 v[110:111], v[250:251], v[94:95], v[254:255]\n"
"v_pk_fma_f32 v[156:157], v[156:157], v[90:91], v[108:109]\n"
"v_pk_fma_f32 v[158:159], v[158:159], v[90:91], v[110:111]\n"
"v_max_f32 v156, 0, v156\n"
"v_max_f32 v157, 0, v157\n"
"v_max_f32 v158, 0, v158\n"
"v_max_f32 v159, 0, v159\n"
"v_pk_mul_f32 v[156:157], v[156:157], v[156:157]\n"
"v_pk_mul_f32 v[158:159], v[158:159], v[158:159]\n"
"v_cvt_pk_bf16_f32 v46, v156, v157\n"
"v_cvt_pk_bf16_f32 v47, v158, v159\n"
"buffer_store_dwordx4 v[40:43], v8, s[36:39], s93 offen offset:2048 sc1\n"
"buffer_store_dwordx4 v[44:47], v8, s[36:39], s93 offen offset:3072 sc1\n"
"s_add_u32 s93, s93, 4096\n"
"s_waitcnt lgkmcnt(6)\n"
"v_mfma_f32_16x16x32_bf16 v[176:179], a[96:99], a[32:35], v[176:179]\n"
"ds_read_b64_tr_b16 a[118:119], v13 offset:41152\n"
"v_mfma_f32_16x16x32_bf16 v[180:183], a[96:99], a[36:39], v[180:183]\n"
"ds_read_b64_tr_b16 a[120:121], v12 offset:49344\n"
"v_mfma_f32_16x16x32_bf16 v[188:191], a[100:103], a[36:39], v[188:191]\n"
"ds_read_b64_tr_b16 a[122:123], v12 offset:57536\n"
"v_mfma_f32_16x16x32_bf16 v[184:187], a[100:103], a[32:35], v[184:187]\n"
"ds_read_b64_tr_b16 a[124:125], v13 offset:49344\n"
"v_mfma_f32_16x16x32_bf16 v[176:179], a[104:107], a[40:43], v[176:179]\n"
"ds_read_b64_tr_b16 a[126:127], v13 offset:57536\n"
"v_mfma_f32_16x16x32_bf16 v[180:183], a[104:107], a[44:47], v[180:183]\n"
"ds_read_b64_tr_b16 a[64:65], v12 offset:256\n"
"v_mfma_f32_16x16x32_bf16 v[188:191], a[108:111], a[44:47], v[188:191]\n"
"ds_read_b64_tr_b16 a[66:67], v12 offset:8448\n"
"v_mfma_f32_16x16x32_bf16 v[184:187], a[108:111], a[40:43], v[184:187]\n"
"ds_read_b64_tr_b16 a[68:69], v13 offset:256\n"
"s_waitcnt lgkmcnt(3)\n"
"v_mfma_f32_16x16x32_bf16 v[176:179], a[112:115], a[48:51], v[176:179]\n"
"ds_read_b64_tr_b16 a[70:71], v13 offset:8448\n"
"v_mfma_f32_16x16x32_bf16 v[180:183], a[112:115], a[52:55], v[180:183]\n"
"ds_read_b64_tr_b16 a[72:73], v12 offset:16640\n"
"v_mfma_f32_16x16x32_bf16 v[188:191], a[116:119], a[52:55], v[188:191]\n"
"ds_read_b64_tr_b16 a[74:75], v12 offset:24832\n"
"v_mfma_f32_16x16x32_bf16 v[184:187], a[116:119], a[48:51], v[184:187]\n"
"ds_read_b64_tr_b16 a[76:77], v13 offset:16640\n"
"ds_read_b128 v[240:243], v9 offset:384\n"
"v_mfma_f32_16x16x32_bf16 v[176:179], a[120:123], a[56:59], v[176:179]\n"
"ds_read_b64_tr_b16 a[78:79], v13 offset:24832\n"
"ds_read_b128 v[244:247], v9 offset:1408\n"
"v_mfma_f32_16x16x32_bf16 v[180:183], a[120:123], a[60:63], v[180:183]\n"
"ds_read_b64_tr_b16 a[80:81], v12 offset:33024\n"
"ds_read_b128 v[248:251], v9 offset:448\n"
"v_mfma_f32_16x16x32_bf16 v[188:191], a[124:127], a[60:63], v[188:191]\n"
"ds_read_b64_tr_b16 a[82:83], v12 offset:41216\n"
"ds_read_b128 v[252:255], v9 offset:1472\n"
"v_mfma_f32_16x16x32_bf16 v[184:187], a[124:127], a[56:59], v[184:187]\n"
"ds_read_b64_tr_b16 a[84:85], v13 offset:33024\n"
"v_pk_fma_f32 v[104:105], v[112:113], v[92:93], v[116:117]\n"
"v_pk_fma_f32 v[106:107], v[114:115], v[92:93], v[118:119]\n"
"v_pk_fma_f32 v[160:161], v[160:161], v[88:89], v[104:105]\n"
"v_pk_fma_f32 v[162:163], v[162:163], v[88:89], v[106:107]\n"
"v_max_f32 v160, 0, v160\n"
"v_max_f32 v161, 0, v161\n"
"v_max_f32 v162, 0, v162\n"
"v_max_f32 v163, 0, v163\n"
"v_pk_mul_f32 v[160:161], v[160:161], v[160:161]\n"
"v_pk_mul_f32 v[162:163], v[162:163], v[162:163]\n"
"v_cvt_pk_bf16_f32 v48, v160, v161\n"
"v_cvt_pk_bf16_f32 v49, v162, v163\n"
"v_pk_fma_f32 v[108:109], v[112:113], v[94:95], v[116:117]\n"
"v_pk_fma_f32 v[110:111], v[114:115], v[94:95], v[118:119]\n"
"v_pk_fma_f32 v[164:165], v[164:165], v[90:91], v[108:109]\n"
"v_pk_fma_f32 v[166:167], v[166:167], v[90:91], v[110:111]\n"
"v_max_f32 v164, 0, v164\n"
"v_max_f32 v165, 0, v165\n"
"v_max_f32 v166, 0, v166\n"
"v_max_f32 v167, 0, v167\n"
"v_pk_mul_f32 v[164:165], v[164:165], v[164:165]\n"
"v_pk_mul_f32 v[166:167], v[166:167], v[166:167]\n"
"v_cvt_pk_bf16_f32 v52, v164, v165\n"
"v_cvt_pk_bf16_f32 v53, v166, v167\n"
"v_pk_fma_f32 v[104:105], v[120:121], v[92:93], v[124:125]\n"
"v_pk_fma_f32 v[106:107], v[122:123], v[92:93], v[126:127]\n"
"v_pk_fma_f32 v[168:169], v[168:169], v[88:89], v[104:105]\n"
"v_pk_fma_f32 v[170:171], v[170:171], v[88:89], v[106:107]\n"
"v_max_f32 v168, 0, v168\n"
"v_max_f32 v169, 0, v169\n"
"v_max_f32 v170, 0, v170\n"
"v_max_f32 v171, 0, v171\n"
"v_pk_mul_f32 v[168:169], v[168:169], v[168:169]\n"
"v_pk_mul_f32 v[170:171], v[170:171], v[170:171]\n"
"v_cvt_pk_bf16_f32 v50, v168, v169\n"
"v_cvt_pk_bf16_f32 v51, v170, v171\n"
"v_pk_fma_f32 v[108:109], v[120:121], v[94:95], v[124:125]\n"
"v_pk_fma_f32 v[110:111], v[122:123], v[94:95], v[126:127]\n"
"v_pk_fma_f32 v[172:173], v[172:173], v[90:91], v[108:109]\n"
"v_pk_fma_f32 v[174:175], v[174:175], v[90:91], v[110:111]\n"
"v_max_f32 v172, 0, v172\n"
"v_max_f32 v173, 0, v173\n"
"v_max_f32 v174, 0, v174\n"
"v_max_f32 v175, 0, v175\n"
"v_pk_mul_f32 v[172:173], v[172:173], v[172:173]\n"
"v_pk_mul_f32 v[174:175], v[174:175], v[174:175]\n"
"v_cvt_pk_bf16_f32 v54, v172, v173\n"
"v_cvt_pk_bf16_f32 v55, v174, v175\n"
"buffer_store_dwordx4 v[48:51], v8, s[36:39], s93 offen offset:0 sc1\n"
"buffer_store_dwordx4 v[52:55], v8, s[36:39], s93 offen offset:1024 sc1\n"
"s_waitcnt lgkmcnt(6)\n"
"v_mfma_f32_16x16x32_bf16 v[192:195], a[64:67], a[32:35], v[192:195]\n"
"ds_read_b64_tr_b16 a[86:87], v13 offset:41216\n"
"v_mfma_f32_16x16x32_bf16 v[196:199], a[64:67], a[36:39], v[196:199]\n"
"ds_read_b64_tr_b16 a[88:89], v12 offset:49408\n"
"v_mfma_f32_16x16x32_bf16 v[204:207], a[68:71], a[36:39], v[204:207]\n"
"ds_read_b64_tr_b16 a[90:91], v12 offset:57600\n"
"v_mfma_f32_16x16x32_bf16 v[200:203], a[68:71], a[32:35], v[200:203]\n"
"ds_read_b64_tr_b16 a[92:93], v13 offset:49408\n"
"v_mfma_f32_16x16x32_bf16 v[192:195], a[72:75], a[40:43], v[192:195]\n"
"ds_read_b64_tr_b16 a[94:95], v13 offset:57600\n"
"v_mfma_f32_16x16x32_bf16 v[196:199], a[72:75], a[44:47], v[196:199]\n"
"ds_read_b64_tr_b16 a[96:97], v12 offset:320\n"
"v_mfma_f32_16x16x32_bf16 v[204:207], a[76:79], a[44:47], v[204:207]\n"
"ds_read_b64_tr_b16 a[98:99], v12 offset:8512\n"
"v_mfma_f32_16x16x32_bf16 v[200:203], a[76:79], a[40:43], v[200:203]\n"
"ds_read_b64_tr_b16 a[100:101], v13 offset:320\n"
"s_waitcnt lgkmcnt(3)\n"
"v_mfma_f32_16x16x32_bf16 v[192:195], a[80:83], a[48:51], v[192:195]\n"
"ds_read_b64_tr_b16 a[102:103], v13 offset:8512\n"
"v_mfma_f32_16x16x32_bf16 v[196:199], a[80:83], a[52:55], v[196:199]\n"
"ds_read_b64_tr_b16 a[104:105], v12 offset:16704\n"
"v_mfma_f32_16x16x32_bf16 v[204:207], a[84:87], a[52:55], v[204:207]\n"
"ds_read_b64_tr_b16 a[106:107], v12 offset:24896\n"
"v_mfma_f32_16x16x32_bf16 v[200:203], a[84:87], a[48:51], v[200:203]\n"
"ds_read_b64_tr_b16 a[108:109], v13 offset:16704\n"
"ds_read_b128 v[112:115], v9 offset:512\n"
"v_mfma_f32_16x16x32_bf16 v[192:195], a[88:91], a[56:59], v[192:195]\n"
"ds_read_b64_tr_b16 a[110:111], v13 offset:24896\n"
"ds_read_b128 v[116:119], v9 offset:1536\n"
"v_mfma_f32_16x16x32_bf16 v[196:199], a[88:91], a[60:63], v[196:199]\n"
"ds_read_b64_tr_b16 a[112:113], v12 offset:33088\n"
"ds_read_b128 v[120:123], v9 offset:576\n"
"v_mfma_f32_16x16x32_bf16 v[204:207], a[92:95], a[60:63], v[204:207]\n"
"ds_read_b64_tr_b16 a[114:115], v12 offset:41280\n"
"ds_read_b128 v[124:127], v9 offset:1600\n"
"v_mfma_f32_16x16x32_bf16 v[200:203], a[92:95], a[56:59], v[200:203]\n"
"ds_read_b64_tr_b16 a[116:117], v13 offset:33088\n"
"v_pk_fma_f32 v[104:105], v[240:241], v[92:93], v[244:245]\n"
"v_pk_fma_f32 v[106:107], v[242:243], v[92:93], v[246:247]\n"
"v_pk_fma_f32 v[176:177], v[176:177], v[88:89], v[104:105]\n"
"v_pk_fma_f32 v[178:179], v[178:179], v[88:89], v[106:107]\n"
"v_max_f32 v176, 0, v176\n"
"v_max_f32 v177, 0, v177\n"
"v_max_f32 v178, 0, v178\n"
"v_max_f32 v179, 0, v179\n"
"v_pk_mul_f32 v[176:177], v[176:177], v[176:177]\n"
"v_pk_mul_f32 v[178:179], v[178:179], v[178:179]\n"
"v_cvt_pk_bf16_f32 v56, v176, v177\n"
"v_cvt_pk_bf16_f32 v57, v178, v179\n"
"v_pk_fma_f32 v[108:109], v[240:241], v[94:95], v[244:245]\n"
"v_pk_fma_f32 v[110:111], v[242:243], v[94:95], v[246:247]\n"
"v_pk_fma_f32 v[180:181], v[180:181], v[90:91], v[108:109]\n"
"v_pk_fma_f32 v[182:183], v[182:183], v[90:91], v[110:111]\n"
"v_max_f32 v180, 0, v180\n"
"v_max_f32 v181, 0, v181\n"
"v_max_f32 v182, 0, v182\n"
"v_max_f32 v183, 0, v183\n"
"v_pk_mul_f32 v[180:181], v[180:181], v[180:181]\n"
"v_pk_mul_f32 v[182:183], v[182:183], v[182:183]\n"
"v_cvt_pk_bf16_f32 v60, v180, v181\n"
"v_cvt_pk_bf16_f32 v61, v182, v183\n"
"v_pk_fma_f32 v[104:105], v[248:249], v[92:93], v[252:253]\n"
"v_pk_fma_f32 v[106:107], v[250:251], v[92:93], v[254:255]\n"
"v_pk_fma_f32 v[184:185], v[184:185], v[88:89], v[104:105]\n"
"v_pk_fma_f32 v[186:187], v[186:187], v[88:89], v[106:107]\n"
"v_max_f32 v184, 0, v184\n"
"v_max_f32 v185, 0, v185\n"
"v_max_f32 v186, 0, v186\n"
"v_max_f32 v187, 0, v187\n"
"v_pk_mul_f32 v[184:185], v[184:185], v[184:185]\n"
"v_pk_mul_f32 v[186:187], v[186:187], v[186:187]\n"
"v_cvt_pk_bf16_f32 v58, v184, v185\n"
"v_cvt_pk_bf16_f32 v59, v186, v187\n"
"v_pk_fma_f32 v[108:109], v[248:249], v[94:95], v[252:253]\n"
"v_pk_fma_f32 v[110:111], v[250:251], v[94:95], v[254:255]\n"
"v_pk_fma_f32 v[188:189], v[188:189], v[90:91], v[108:109]\n"
"v_pk_fma_f32 v[190:191], v[190:191], v[90:91], v[110:111]\n"
"v_max_f32 v188, 0, v188\n"
"v_max_f32 v189, 0, v189\n"
"v_max_f32 v190, 0, v190\n"
"v_max_f32 v191, 0, v191\n"
"v_pk_mul_f32 v[188:189], v[188:189], v[188:189]\n"
"v_pk_mul_f32 v[190:191], v[190:191], v[190:191]\n"
"v_cvt_pk_bf16_f32 v62, v188, v189\n"
"v_cvt_pk_bf16_f32 v63, v190, v191\n"
"buffer_store_dwordx4 v[56:59], v8, s[36:39], s93 offen offset:2048 sc1\n"
"buffer_store_dwordx4 v[60:63], v8, s[36:39], s93 offen offset:3072 sc1\n"
"s_add_u32 s93, s93, 4096\n"
"s_waitcnt lgkmcnt(6)\n"
"v_mfma_f32_16x16x32_bf16 v[208:211], a[96:99], a[32:35], v[208:211]\n"
"ds_read_b64_tr_b16 a[118:119], v13 offset:41280\n"
"v_mfma_f32_16x16x32_bf16 v[212:215], a[96:99], a[36:39], v[212:215]\n"
"ds_read_b64_tr_b16 a[120:121], v12 offset:49472\n"
"v_mfma_f32_16x16x32_bf16 v[220:223], a[100:103], a[36:39], v[220:223]\n"
"ds_read_b64_tr_b16 a[122:123], v12 offset:57664\n"
"v_mfma_f32_16x16x32_bf16 v[216:219], a[100:103], a[32:35], v[216:219]\n"
"ds_read_b64_tr_b16 a[124:125], v13 offset:49472\n"
"v_mfma_f32_16x16x32_bf16 v[208:211], a[104:107], a[40:43], v[208:211]\n"
"ds_read_b64_tr_b16 a[126:127], v13 offset:57664\n"
"v_mfma_f32_16x16x32_bf16 v[212:215], a[104:107], a[44:47], v[212:215]\n"
"ds_read_b64_tr_b16 a[64:65], v12 offset:384\n"
"v_mfma_f32_16x16x32_bf16 v[220:223], a[108:111], a[44:47], v[220:223]\n"
"ds_read_b64_tr_b16 a[66:67], v12 offset:8576\n"
"v_mfma_f32_16x16x32_bf16 v[216:219], a[108:111], a[40:43], v[216:219]\n"
"ds_read_b64_tr_b16 a[68:69], v13 offset:384\n"
"s_waitcnt lgkmcnt(3)\n"
"v_mfma_f32_16x16x32_bf16 v[208:211], a[112:115], a[48:51], v[208:211]\n"
"ds_read_b64_tr_b16 a[70:71], v13 offset:8576\n"
"v_mfma_f32_16x16x32_bf16 v[212:215], a[112:115], a[52:55], v[212:215]\n"
"ds_read_b64_tr_b16 a[72:73], v12 offset:16768\n"
"v_mfma_f32_16x16x32_bf16 v[220:223], a[116:119], a[52:55], v[220:223]\n"
"ds_read_b64_tr_b16 a[74:75], v12 offset:24960\n"
"v_mfma_f32_16x16x32_bf16 v[216:219], a[116:119], a[48:51], v[216:219]\n"
"ds_read_b64_tr_b16 a[76:77], v13 offset:16768\n"
"ds_read_b128 v[240:243], v9 offset:640\n"
"v_mfma_f32_16x16x32_bf16 v[208:211], a[120:123], a[56:59], v[208:211]\n"
"ds_read_b64_tr_b16 a[78:79], v13 offset:24960\n"
"ds_read_b128 v[244:247], v9 offset:1664\n"
"v_mfma_f32_16x16x32_bf16 v[212:215], a[120:123], a[60:63], v[212:215]\n"
"ds_read_b64_tr_b16 a[80:81], v12 offset:33152\n"
"ds_read_b128 v[248:251], v9 offset:704\n"
"v_mfma_f32_16x16x32_bf16 v[220:223], a[124:127], a[60:63], v[220:223]\n"
"ds_read_b64_tr_b16 a[82:83], v12 offset:41344\n"
"ds_read_b128 v[252:255], v9 offset:1728\n"
"v_mfma_f32_16x16x32_bf16 v[216:219], a[124:127], a[56:59], v[216:219]\n"
"ds_read_b64_tr_b16 a[84:85], v13 offset:33152\n"
"v_pk_fma_f32 v[104:105], v[112:113], v[92:93], v[116:117]\n"
"v_pk_fma_f32 v[106:107], v[114:115], v[92:93], v[118:119]\n"
"v_pk_fma_f32 v[192:193], v[192:193], v[88:89], v[104:105]\n"
"v_pk_fma_f32 v[194:195], v[194:195], v[88:89], v[106:107]\n"
"v_max_f32 v192, 0, v192\n"
"v_max_f32 v193, 0, v193\n"
"v_max_f32 v194, 0, v194\n"
"v_max_f32 v195, 0, v195\n"
"v_pk_mul_f32 v[192:193], v[192:193], v[192:193]\n"
"v_pk_mul_f32 v[194:195], v[194:195], v[194:195]\n"
"v_cvt_pk_bf16_f32 v64, v192, v193\n"
"v_cvt_pk_bf16_f32 v65, v194, v195\n"
"v_pk_fma_f32 v[108:109], v[112:113], v[94:95], v[116:117]\n"
"v_pk_fma_f32 v[110:111], v[114:115], v[94:95], v[118:119]\n"
"v_pk_fma_f32 v[196:197], v[196:197], v[90:91], v[108:109]\n"
"v_pk_fma_f32 v[198:199], v[198:199], v[90:91], v[110:111]\n"
"v_max_f32 v196, 0, v196\n"
"v_max_f32 v197, 0, v197\n"
"v_max_f32 v198, 0, v198\n"
"v_max_f32 v199, 0, v199\n"
"v_pk_mul_f32 v[196:197], v[196:197], v[196:197]\n"
"v_pk_mul_f32 v[198:199], v[198:199], v[198:199]\n"
"v_cvt_pk_bf16_f32 v68, v196, v197\n"
"v_cvt_pk_bf16_f32 v69, v198, v199\n"
"v_pk_fma_f32 v[104:105], v[120:121], v[92:93], v[124:125]\n"
"v_pk_fma_f32 v[106:107], v[122:123], v[92:93], v[126:127]\n"
"v_pk_fma_f32 v[200:201], v[200:201], v[88:89], v[104:105]\n"
"v_pk_fma_f32 v[202:203], v[202:203], v[88:89], v[106:107]\n"
"v_max_f32 v200, 0, v200\n"
"v_max_f32 v201, 0, v201\n"
"v_max_f32 v202, 0, v202\n"
"v_max_f32 v203, 0, v203\n"
"v_pk_mul_f32 v[200:201], v[200:201], v[200:201]\n"
"v_pk_mul_f32 v[202:203], v[202:203], v[202:203]\n"
"v_cvt_pk_bf16_f32 v66, v200, v201\n"
"v_cvt_pk_bf16_f32 v67, v202, v203\n"
"v_pk_fma_f32 v[108:109], v[120:121], v[94:95], v[124:125]\n"
"v_pk_fma_f32 v[110:111], v[122:123], v[94:95], v[126:127]\n"
"v_pk_fma_f32 v[204:205], v[204:205], v[90:91], v[108:109]\n"
"v_pk_fma_f32 v[206:207], v[206:207], v[90:91], v[110:111]\n"
"v_max_f32 v204, 0, v204\n"
"v_max_f32 v205, 0, v205\n"
"v_max_f32 v206, 0, v206\n"
"v_max_f32 v207, 0, v207\n"
"v_pk_mul_f32 v[204:205], v[204:205], v[204:205]\n"
"v_pk_mul_f32 v[206:207], v[206:207], v[206:207]\n"
"v_cvt_pk_bf16_f32 v70, v204, v205\n"
"v_cvt_pk_bf16_f32 v71, v206, v207\n"
"buffer_store_dwordx4 v[64:67], v8, s[36:39], s93 offen offset:0 sc1\n"
"buffer_store_dwordx4 v[68:71], v8, s[36:39], s93 offen offset:1024 sc1\n"
"s_waitcnt lgkmcnt(6)\n"
"v_mfma_f32_16x16x32_bf16 v[224:227], a[64:67], a[32:35], v[224:227]\n"
"ds_read_b64_tr_b16 a[86:87], v13 offset:41344\n"
"v_mfma_f32_16x16x32_bf16 v[228:231], a[64:67], a[36:39], v[228:231]\n"
"ds_read_b64_tr_b16 a[88:89], v12 offset:49536\n"
"v_mfma_f32_16x16x32_bf16 v[236:239], a[68:71], a[36:39], v[236:239]\n"
"ds_read_b64_tr_b16 a[90:91], v12 offset:57728\n"
"v_mfma_f32_16x16x32_bf16 v[232:235], a[68:71], a[32:35], v[232:235]\n"
"ds_read_b64_tr_b16 a[92:93], v13 offset:49536\n"
"v_mfma_f32_16x16x32_bf16 v[224:227], a[72:75], a[40:43], v[224:227]\n"
"ds_read_b64_tr_b16 a[94:95], v13 offset:57728\n"
"v_mfma_f32_16x16x32_bf16 v[228:231], a[72:75], a[44:47], v[228:231]\n"
"v_mfma_f32_16x16x32_bf16 v[236:239], a[76:79], a[44:47], v[236:239]\n"
"v_mfma_f32_16x16x32_bf16 v[232:235], a[76:79], a[40:43], v[232:235]\n"
"s_waitcnt lgkmcnt(0)\n"
"v_mfma_f32_16x16x32_bf16 v[224:227], a[80:83], a[48:51], v[224:227]\n"
"v_mfma_f32_16x16x32_bf16 v[228:231], a[80:83], a[52:55], v[228:231]\n"
"v_mfma_f32_16x16x32_bf16 v[236:239], a[84:87], a[52:55], v[236:239]\n"
"v_mfma_f32_16x16x32_bf16 v[232:235], a[84:87], a[48:51], v[232:235]\n"
"ds_read_b128 v[112:115], v9 offset:768\n"
"ds_read_b128 v[116:119], v9 offset:1792\n"
"v_mfma_f32_16x16x32_bf16 v[224:227], a[88:91], a[56:59], v[224:227]\n"
"ds_read_b128 v[120:123], v9 offset:832\n"
"ds_read_b128 v[124:127], v9 offset:1856\n"
"v_mfma_f32_16x16x32_bf16 v[228:231], a[88:91], a[60:63], v[228:231]\n"
"v_mfma_f32_16x16x32_bf16 v[236:239], a[92:95], a[60:63], v[236:239]\n"
"v_mfma_f32_16x16x32_bf16 v[232:235], a[92:95], a[56:59], v[232:235]\n"
"v_pk_fma_f32 v[104:105], v[240:241], v[92:93], v[244:245]\n"
"v_pk_fma_f32 v[106:107], v[242:243], v[92:93], v[246:247]\n"
"v_pk_fma_f32 v[208:209], v[208:209], v[88:89], v[104:105]\n"
"v_pk_fma_f32 v[210:211], v[210:211], v[88:89], v[106:107]\n"
"v_max_f32 v208, 0, v208\n"
"v_max_f32 v209, 0, v209\n"
"v_max_f32 v210, 0, v210\n"
"v_max_f32 v211, 0, v211\n"
"v_pk_mul_f32 v[208:209], v[208:209], v[208:209]\n"
"v_pk_mul_f32 v[210:211], v[210:211], v[210:211]\n"
"v_cvt_pk_bf16_f32 v72, v208, v209\n"
"v_cvt_pk_bf16_f32 v73, v210, v211\n"
"v_pk_fma_f32 v[108:109], v[240:241], v[94:95], v[244:245]\n"
"v_pk_fma_f32 v[110:111], v[242:243], v[94:95], v[246:247]\n"
"v_pk_fma_f32 v[212:213], v[212:213], v[90:91], v[108:109]\n"
"v_pk_fma_f32 v[214:215], v[214:215], v[90:91], v[110:111]\n"
"v_max_f32 v212, 0, v212\n"
"v_max_f32 v213, 0, v213\n"
"v_max_f32 v214, 0, v214\n"
"v_max_f32 v215, 0, v215\n"
"v_pk_mul_f32 v[212:213], v[212:213], v[212:213]\n"
"v_pk_mul_f32 v[214:215], v[214:215], v[214:215]\n"
"v_cvt_pk_bf16_f32 v76, v212, v213\n"
"v_cvt_pk_bf16_f32 v77, v214, v215\n"
"v_pk_fma_f32 v[104:105], v[248:249], v[92:93], v[252:253]\n"
"v_pk_fma_f32 v[106:107], v[250:251], v[92:93], v[254:255]\n"
"v_pk_fma_f32 v[216:217], v[216:217], v[88:89], v[104:105]\n"
"v_pk_fma_f32 v[218:219], v[218:219], v[88:89], v[106:107]\n"
"v_max_f32 v216, 0, v216\n"
"v_max_f32 v217, 0, v217\n"
"v_max_f32 v218, 0, v218\n"
"v_max_f32 v219, 0, v219\n"
"v_pk_mul_f32 v[216:217], v[216:217], v[216:217]\n"
"v_pk_mul_f32 v[218:219], v[218:219], v[218:219]\n"
"v_cvt_pk_bf16_f32 v74, v216, v217\n"
"v_cvt_pk_bf16_f32 v75, v218, v219\n"
"v_pk_fma_f32 v[108:109], v[248:249], v[94:95], v[252:253]\n"
"v_pk_fma_f32 v[110:111], v[250:251], v[94:95], v[254:255]\n"
"v_pk_fma_f32 v[220:221], v[220:221], v[90:91], v[108:109]\n"
"v_pk_fma_f32 v[222:223], v[222:223], v[90:91], v[110:111]\n"
"v_max_f32 v220, 0, v220\n"
"v_max_f32 v221, 0, v221\n"
"v_max_f32 v222, 0, v222\n"
"v_max_f32 v223, 0, v223\n"
"v_pk_mul_f32 v[220:221], v[220:221], v[220:221]\n"
"v_pk_mul_f32 v[222:223], v[222:223], v[222:223]\n"
"v_cvt_pk_bf16_f32 v78, v220, v221\n"
"v_cvt_pk_bf16_f32 v79, v222, v223\n"
"buffer_store_dwordx4 v[72:75], v8, s[36:39], s93 offen offset:2048 sc1\n"
"buffer_store_dwordx4 v[76:79], v8, s[36:39], s93 offen offset:3072 sc1\n"
"s_add_u32 s93, s93, 4096\n"
"s_waitcnt lgkmcnt(2)\n"
"v_pk_fma_f32 v[104:105], v[112:113], v[92:93], v[116:117]\n"
"v_pk_fma_f32 v[106:107], v[114:115], v[92:93], v[118:119]\n"
"v_pk_fma_f32 v[224:225], v[224:225], v[88:89], v[104:105]\n"
"v_pk_fma_f32 v[226:227], v[226:227], v[88:89], v[106:107]\n"
"v_max_f32 v224, 0, v224\n"
"v_max_f32 v225, 0, v225\n"
"v_max_f32 v226, 0, v226\n"
"v_max_f32 v227, 0, v227\n"
"v_pk_mul_f32 v[224:225], v[224:225], v[224:225]\n"
"v_pk_mul_f32 v[226:227], v[226:227], v[226:227]\n"
"v_cvt_pk_bf16_f32 v80, v224, v225\n"
"v_cvt_pk_bf16_f32 v81, v226, v227\n"
"v_pk_fma_f32 v[108:109], v[112:113], v[94:95], v[116:117]\n"
"v_pk_fma_f32 v[110:111], v[114:115], v[94:95], v[118:119]\n"
"v_pk_fma_f32 v[228:229], v[228:229], v[90:91], v[108:109]\n"
"v_pk_fma_f32 v[230:231], v[230:231], v[90:91], v[110:111]\n"
"v_max_f32 v228, 0, v228\n"
"v_max_f32 v229, 0, v229\n"
"v_max_f32 v230, 0, v230\n"
"v_max_f32 v231, 0, v231\n"
"v_pk_mul_f32 v[228:229], v[228:229], v[228:229]\n"
"v_pk_mul_f32 v[230:231], v[230:231], v[230:231]\n"
"v_cvt_pk_bf16_f32 v84, v228, v229\n"
"v_cvt_pk_bf16_f32 v85, v230, v231\n"
"s_waitcnt lgkmcnt(0)\n"
"v_pk_fma_f32 v[104:105], v[120:121], v[92:93], v[124:125]\n"
"v_pk_fma_f32 v[106:107], v[122:123], v[92:93], v[126:127]\n"
"v_pk_fma_f32 v[232:233], v[232:233], v[88:89], v[104:105]\n"
"v_pk_fma_f32 v[234:235], v[234:235], v[88:89], v[106:107]\n"
"v_max_f32 v232, 0, v232\n"
"v_max_f32 v233, 0, v233\n"
"v_max_f32 v234, 0, v234\n"
"v_max_f32 v235, 0, v235\n"
"v_pk_mul_f32 v[232:233], v[232:233], v[232:233]\n"
"v_pk_mul_f32 v[234:235], v[234:235], v[234:235]\n"
"v_cvt_pk_bf16_f32 v82, v232, v233\n"
"v_cvt_pk_bf16_f32 v83, v234, v235\n"
"v_pk_fma_f32 v[108:109], v[120:121], v[94:95], v[124:125]\n"
"v_pk_fma_f32 v[110:111], v[122:123], v[94:95], v[126:127]\n"
"v_pk_fma_f32 v[236:237], v[236:237], v[90:91], v[108:109]\n"
"v_pk_fma_f32 v[238:239], v[238:239], v[90:91], v[110:111]\n"
"v_max_f32 v236, 0, v236\n"
"v_max_f32 v237, 0, v237\n"
"v_max_f32 v238, 0, v238\n"
"v_max_f32 v239, 0, v239\n"
"v_pk_mul_f32 v[236:237], v[236:237], v[236:237]\n"
"v_pk_mul_f32 v[238:239], v[238:239], v[238:239]\n"
"v_cvt_pk_bf16_f32 v86, v236, v237\n"
"v_cvt_pk_bf16_f32 v87, v238, v239\n"
"buffer_store_dwordx4 v[80:83], v8, s[36:39], s93 offen offset:0 sc1\n"
"buffer_store_dwordx4 v[84:87], v8, s[36:39], s93 offen offset:1024 sc1\n"
"s_waitcnt vmcnt(25)\n"
"v_lshlrev_b32 v104, 16, v96\n"
"v_and_b32 v105, 0xffff0000, v96\n"
"v_lshlrev_b32 v106, 16, v97\n"
"v_and_b32 v107, 0xffff0000, v97\n"
"s_waitcnt vmcnt(24)\n"
"v_lshlrev_b32 v108, 16, v98\n"
"v_and_b32 v109, 0xffff0000, v98\n"
"v_lshlrev_b32 v110, 16, v99\n"
"v_and_b32 v111, 0xffff0000, v99\n"
"s_waitcnt vmcnt(23)\n"
"v_lshlrev_b32 v112, 16, v100\n"
"v_and_b32 v113, 0xffff0000, v100\n"
"v_lshlrev_b32 v114, 16, v101\n"
"v_and_b32 v115, 0xffff0000, v101\n"
"s_waitcnt vmcnt(22)\n"
"v_lshlrev_b32 v116, 16, v102\n"
"v_and_b32 v117, 0xffff0000, v102\n"
"v_lshlrev_b32 v118, 16, v103\n"
"v_and_b32 v119, 0xffff0000, v103\n"
"v_mfma_f32_16x16x32_bf16 v[128:131], a[210:213], a[176:179], v[104:107]\n"
"v_mfma_f32_16x16x32_bf16 v[132:135], a[210:213], a[180:183], v[104:107]\n"
"v_mfma_f32_16x16x32_bf16 v[136:139], a[210:213], a[184:187], v[108:111]\n"
"v_mfma_f32_16x16x32_bf16 v[140:143], a[210:213], a[188:191], v[108:111]\n"
"v_mfma_f32_16x16x32_bf16 v[144:147], a[208:211], a[176:179], v[112:115]\n"
"v_mfma_f32_16x16x32_bf16 v[148:151], a[208:211], a[180:183], v[112:115]\n"
"v_mfma_f32_16x16x32_bf16 v[152:155], a[208:211], a[184:187], v[116:119]\n"
"v_mfma_f32_16x16x32_bf16 v[156:159], a[208:211], a[188:191], v[116:119]\n"
"v_lshrrev_b32 v100, 2, v1\n"
"v_and_b32 v101, 3, v1\n"
"v_lshlrev_b32 v101, 3, v101\n"
"v_lshl_add_u32 v101, v100, 9, v101\n"
"v_and_b32 v100, 7, v100\n"
"v_lshl_add_u32 v112, v100, 5, v101\n"
"v_xor_b32 v113, 32, v112\n"
"v_xor_b32 v114, 64, v112\n"
"v_xor_b32 v115, 96, v112\n"
"v_xor_b32 v116, 128, v112\n"
"v_xor_b32 v117, 160, v112\n"
"v_xor_b32 v118, 192, v112\n"
"v_xor_b32 v119, 224, v112\n"
"v_add_u32 v120, 0x10000, v112\n"
"v_add_u32 v121, 0x10000, v113\n"
"v_add_u32 v122, 0x10000, v114\n"
"v_add_u32 v123, 0x10000, v115\n"
"v_add_u32 v124, 0x10000, v116\n"
"v_add_u32 v125, 0x10000, v117\n"
"v_add_u32 v126, 0x10000, v118\n"
"v_add_u32 v127, 0x10000, v119\n"
"s_lshl_b32 s77, s63, 9\n"
"s_lshl_b32 s76, s31, 7\n"
"s_add_u32 s77, s77, s76\n"
"s_add_u32 s77, s77, 36864\n"
"s_lshl3_add_u32 s85, s65, s77\n"
"s_lshl_b32 s86, s66, 5\n"
"s_add_u32 s86, s86, s77\n"
"v_lshrrev_b32 v31, 4, v1\n"
"v_lshlrev_b32 v31, 3, v31\n"
"s_mul_i32 s75, s63, 917504\n"
"s_mul_i32 s76, s31, 229376\n"
"s_add_u32 s75, s75, s76\n"
"s_mul_i32 s76, s66, 57344\n"
"s_add_u32 s75, s75, s76\n"
"s_add_u32 s75, s75, 26214400\n"
"s_add_u32 s52, s28, s75\n"
"s_addc_u32 s53, s29, 0\n"
"s_sub_u32 s54, 68157440, s75\n"
"v_readlane_b32 s82, v21, 23\n"
"s_waitcnt vmcnt(14)\n"
"s_barrier\n"
"ds_read_b64_tr_b16 a[96:97], v112 offset:0\n"
"ds_read_b64_tr_b16 a[98:99], v112 offset:8192\n"
"ds_read_b64_tr_b16 a[100:101], v113 offset:0\n"
"ds_read_b64_tr_b16 a[102:103], v113 offset:8192\n"
"ds_read_b64_tr_b16 a[104:105], v114 offset:0\n"
"ds_read_b64_tr_b16 a[106:107], v114 offset:8192\n"
"ds_read_b64_tr_b16 a[108:109], v115 offset:0\n"
"ds_read_b64_tr_b16 a[110:111], v115 offset:8192\n"
"ds_read_b64_tr_b16 a[112:113], v116 offset:0\n"
"ds_read_b64_tr_b16 a[114:115], v116 offset:8192\n"
"ds_read_b64_tr_b16 a[116:117], v117 offset:0\n"
"ds_read_b64_tr_b16 a[118:119], v117 offset:8192\n"
"ds_read_b64_tr_b16 a[120:121], v118 offset:0\n"
"ds_read_b64_tr_b16 a[122:123], v118 offset:8192\n"
"s_waitcnt lgkmcnt(6)\n"
"v_mfma_f32_16x16x32_bf16 v[128:131], a[96:99], v[32:35], v[128:131]\n"
"ds_read_b64_tr_b16 a[124:125], v119 offset:0\n"
"s_add_u32 m0, s74, 65536\n"
"v_mfma_f32_16x16x32_bf16 v[132:135], a[96:99], v[36:39], v[132:135]\n"
"ds_read_b64_tr_b16 a[126:127], v119 offset:8192\n"
"buffer_load_dwordx4 v4, s[4:7], s79 offen offset:0 lds\n"
"v_mfma_f32_16x16x32_bf16 v[140:143], a[100:103], v[36:39], v[140:143]\n"
"ds_read_b64_tr_b16 a[64:65], v112 offset:256\n"
"v_mfma_f32_16x16x32_bf16 v[136:139], a[100:103], v[32:35], v[136:139]\n"
"ds_read_b64_tr_b16 a[66:67], v112 offset:8448\n"
"v_mfma_f32_16x16x32_bf16 v[144:147], a[104:107], v[32:35], v[144:147]\n"
"ds_read_b64_tr_b16 a[68:69], v113 offset:256\n"
"v_mfma_f32_16x16x32_bf16 v[148:151], a[104:107], v[36:39], v[148:151]\n"
"ds_read_b64_tr_b16 a[70:71], v113 offset:8448\n"
"v_mfma_f32_16x16x32_bf16 v[156:159], a[108:111], v[36:39], v[156:159]\n"
"ds_read_b64_tr_b16 a[72:73], v114 offset:256\n"
"buffer_load_dwordx4 v5, s[4:7], s79 offen offset:1024 lds\n"
"v_mfma_f32_16x16x32_bf16 v[152:155], a[108:111], v[32:35], v[152:155]\n"
"ds_read_b64_tr_b16 a[74:75], v114 offset:8448\n"
"s_waitcnt lgkmcnt(6)\n"
"v_mfma_f32_16x16x32_bf16 v[160:163], a[112:115], v[32:35], 0\n"
"ds_read_b64_tr_b16 a[76:77], v115 offset:256\n"
"v_mfma_f32_16x16x32_bf16 v[164:167], a[112:115], v[36:39], 0\n"
"ds_read_b64_tr_b16 a[78:79], v115 offset:8448\n"
"v_mfma_f32_16x16x32_bf16 v[172:175], a[116:119], v[36:39], 0\n"
"ds_read_b64_tr_b16 a[80:81], v116 offset:256\n"
"v_mfma_f32_16x16x32_bf16 v[168:171], a[116:119], v[32:35], 0\n"
"ds_read_b64_tr_b16 a[82:83], v116 offset:8448\n"
"buffer_load_dwordx4 v6, s[4:7], s79 offen offset:2048 lds\n"
"v_mfma_f32_16x16x32_bf16 v[176:179], a[120:123], v[32:35], 0\n"
"ds_read_b64_tr_b16 a[84:85], v117 offset:256\n"
"v_mfma_f32_16x16x32_bf16 v[180:183], a[120:123], v[36:39], 0\n"
"ds_read_b64_tr_b16 a[86:87], v117 offset:8448\n"
"v_mfma_f32_16x16x32_bf16 v[188:191], a[124:127], v[36:39], 0\n"
"ds_read_b64_tr_b16 a[88:89], v118 offset:256\n"
"v_mfma_f32_16x16x32_bf16 v[184:187], a[124:127], v[32:35], 0\n"
"ds_read_b64_tr_b16 a[90:91], v118 offset:8448\n"
"s_waitcnt lgkmcnt(6)\n"
"v_mfma_f32_16x16x32_bf16 v[192:195], a[64:67], v[32:35], 0\n"
"ds_read_b64_tr_b16 a[92:93], v119 offset:256\n"
"buffer_load_dwordx4 v7, s[4:7], s79 offen offset:3072 lds\n"
"v_mfma_f32_16x16x32_bf16 v[196:199], a[64:67], v[36:39], 0\n"
"ds_read_b64_tr_b16 a[94:95], v119 offset:8448\n"
"v_mfma_f32_16x16x32_bf16 v[204:207], a[68:71], v[36:39], 0\n"
"ds_read_b64_tr_b16 a[96:97], v112 offset:16384\n"
"v_mfma_f32_16x16x32_bf16 v[200:203], a[68:71], v[32:35], 0\n"
"ds_read_b64_tr_b16 a[98:99], v112 offset:24576\n"
"v_mfma_f32_16x16x32_bf16 v[208:211], a[72:75], v[32:35], 0\n"
"ds_read_b64_tr_b16 a[100:101], v113 offset:16384\n"
"v_mfma_f32_16x16x32_bf16 v[212:215], a[72:75], v[36:39], 0\n"
"ds_read_b64_tr_b16 a[102:103], v113 offset:24576\n"
"s_add_u32 m0, s74, 69632\n"
"v_mfma_f32_16x16x32_bf16 v[220:223], a[76:79], v[36:39], 0\n"
"ds_read_b64_tr_b16 a[104:105], v114 offset:16384\n"
"buffer_load_dwordx4 v16, s[4:7], s79 offen offset:0 lds\n"
"v_mfma_f32_16x16x32_bf16 v[216:219], a[76:79], v[32:35], 0\n"
"ds_read_b64_tr_b16 a[106:107], v114 offset:24576\n"
"s_waitcnt lgkmcnt(6)\n"
"v_mfma_f32_16x16x32_bf16 v[224:227], a[80:83], v[32:35], 0\n"
"ds_read_b64_tr_b16 a[108:109], v115 offset:16384\n"
"v_mfma_f32_16x16x32_bf16 v[228:231], a[80:83], v[36:39], 0\n"
"ds_read_b64_tr_b16 a[110:111], v115 offset:24576\n"
"v_mfma_f32_16x16x32_bf16 v[236:239], a[84:87], v[36:39], 0\n"
"ds_read_b64_tr_b16 a[112:113], v116 offset:16384\n"
"v_mfma_f32_16x16x32_bf16 v[232:235], a[84:87], v[32:35], 0\n"
"ds_read_b64_tr_b16 a[114:115], v116 offset:24576\n"
"buffer_load_dwordx4 v17, s[4:7], s79 offen offset:1024 lds\n"
"v_mfma_f32_16x16x32_bf16 v[240:243], a[88:91], v[32:35], 0\n"
"ds_read_b64_tr_b16 a[116:117], v117 offset:16384\n"
"v_mfma_f32_16x16x32_bf16 v[244:247], a[88:91], v[36:39], 0\n"
"ds_read_b64_tr_b16 a[118:119], v117 offset:24576\n"
"v_mfma_f32_16x16x32_bf16 v[252:255], a[92:95], v[36:39], 0\n"
"ds_read_b64_tr_b16 a[120:121], v118 offset:16384\n"
"v_mfma_f32_16x16x32_bf16 v[248:251], a[92:95], v[32:35], 0\n"
"ds_read_b64_tr_b16 a[122:123], v118 offset:24576\n"
"s_waitcnt lgkmcnt(6)\n"
"v_mfma_f32_16x16x32_bf16 v[128:131], a[96:99], v[40:43], v[128:131]\n"
"ds_read_b64_tr_b16 a[124:125], v119 offset:16384\n"
"buffer_load_dwordx4 v18, s[4:7], s79 offen offset:2048 lds\n"
"v_mfma_f32_16x16x32_bf16 v[132:135], a[96:99], v[44:47], v[132:135]\n"
"ds_read_b64_tr_b16 a[126:127], v119 offset:24576\n"
"v_mfma_f32_16x16x32_bf16 v[140:143], a[100:103], v[44:47], v[140:143]\n"
"ds_read_b64_tr_b16 a[64:65], v112 offset:16640\n"
"v_mfma_f32_16x16x32_bf16 v[136:139], a[100:103], v[40:43], v[136:139]\n"
"ds_read_b64_tr_b16 a[66:67], v112 offset:24832\n"
"v_mfma_f32_16x16x32_bf16 v[144:147], a[104:107], v[40:43], v[144:147]\n"
"ds_read_b64_tr_b16 a[68:69], v113 offset:16640\n"
"v_mfma_f32_16x16x32_bf16 v[148:151], a[104:107], v[44:47], v[148:151]\n"
"ds_read_b64_tr_b16 a[70:71], v113 offset:24832\n"
"buffer_load_dwordx4 v19, s[4:7], s79 offen offset:3072 lds\n"
"v_mfma_f32_16x16x32_bf16 v[156:159], a[108:111], v[44:47], v[156:159]\n"
"ds_read_b64_tr_b16 a[72:73], v114 offset:16640\n"
"v_readlane_b32 s79, v21, 3\n"
"v_mfma_f32_16x16x32_bf16 v[152:155], a[108:111], v[40:43], v[152:155]\n"
"ds_read_b64_tr_b16 a[74:75], v114 offset:24832\n"
"s_waitcnt lgkmcnt(6)\n"
"v_mfma_f32_16x16x32_bf16 v[160:163], a[112:115], v[40:43], v[160:163]\n"
"ds_read_b64_tr_b16 a[76:77], v115 offset:16640\n"
"v_mfma_f32_16x16x32_bf16 v[164:167], a[112:115], v[44:47], v[164:167]\n"
"ds_read_b64_tr_b16 a[78:79], v115 offset:24832\n"
"v_mfma_f32_16x16x32_bf16 v[172:175], a[116:119], v[44:47], v[172:175]\n"
"ds_read_b64_tr_b16 a[80:81], v116 offset:16640\n"
"s_add_u32 m0, s74, 98304\n"
"v_mfma_f32_16x16x32_bf16 v[168:171], a[116:119], v[40:43], v[168:171]\n"
"ds_read_b64_tr_b16 a[82:83], v116 offset:24832\n"
"buffer_load_dwordx4 v4, s[4:7], s79 offen offset:0 lds\n"
"v_mfma_f32_16x16x32_bf16 v[176:179], a[120:123], v[40:43], v[176:179]\n"
"ds_read_b64_tr_b16 a[84:85], v117 offset:16640\n"
"v_mfma_f32_16x16x32_bf16 v[180:183], a[120:123], v[44:47], v[180:183]\n"
"ds_read_b64_tr_b16 a[86:87], v117 offset:24832\n"
"v_mfma_f32_16x16x32_bf16 v[188:191], a[124:127], v[44:47], v[188:191]\n"
"ds_read_b64_tr_b16 a[88:89], v118 offset:16640\n"
"v_mfma_f32_16x16x32_bf16 v[184:187], a[124:127], v[40:43], v[184:187]\n"
"ds_read_b64_tr_b16 a[90:91], v118 offset:24832\n"
"s_waitcnt lgkmcnt(6)\n"
"v_mfma_f32_16x16x32_bf16 v[192:195], a[64:67], v[40:43], v[192:195]\n"
"ds_read_b64_tr_b16 a[92:93], v119 offset:16640\n"
"buffer_load_dwordx4 v5, s[4:7], s79 offen offset:1024 lds\n"
"v_mfma_f32_16x16x32_bf16 v[196:199], a[64:67], v[44:47], v[196:199]\n"
"ds_read_b64_tr_b16 a[94:95], v119 offset:24832\n"
"v_mfma_f32_16x16x32_bf16 v[204:207], a[68:71], v[44:47], v[204:207]\n"
"ds_read_b64_tr_b16 a[96:97], v112 offset:32768\n"
"v_mfma_f32_16x16x32_bf16 v[200:203], a[68:71], v[40:43], v[200:203]\n"
"ds_read_b64_tr_b16 a[98:99], v112 offset:40960\n"
"v_mfma_f32_16x16x32_bf16 v[208:211], a[72:75], v[40:43], v[208:211]\n"
"ds_read_b64_tr_b16 a[100:101], v113 offset:32768\n"
"v_mfma_f32_16x16x32_bf16 v[212:215], a[72:75], v[44:47], v[212:215]\n"
"ds_read_b64_tr_b16 a[102:103], v113 offset:40960\n"
"buffer_load_dwordx4 v6, s[4:7], s79 offen offset:2048 lds\n"
"v_mfma_f32_16x16x32_bf16 v[220:223], a[76:79], v[44:47], v[220:223]\n"
"ds_read_b64_tr_b16 a[104:105], v114 offset:32768\n"
"v_mfma_f32_16x16x32_bf16 v[216:219], a[76:79], v[40:43], v[216:219]\n"
"ds_read_b64_tr_b16 a[106:107], v114 offset:40960\n"
"s_waitcnt lgkmcnt(6)\n"
"v_mfma_f32_16x16x32_bf16 v[224:227], a[80:83], v[40:43], v[224:227]\n"
"ds_read_b64_tr_b16 a[108:109], v115 offset:32768\n"
"v_mfma_f32_16x16x32_bf16 v[228:231], a[80:83], v[44:47], v[228:231]\n"
"ds_read_b64_tr_b16 a[110:111], v115 offset:40960\n"
"v_mfma_f32_16x16x32_bf16 v[236:239], a[84:87], v[44:47], v[236:239]\n"
"ds_read_b64_tr_b16 a[112:113], v116 offset:32768\n"
"buffer_load_dwordx4 v7, s[4:7], s79 offen offset:3072 lds\n"
"v_mfma_f32_16x16x32_bf16 v[232:235], a[84:87], v[40:43], v[232:235]\n"
"ds_read_b64_tr_b16 a[114:115], v116 offset:40960\n"
"v_mfma_f32_16x16x32_bf16 v[240:243], a[88:91], v[40:43], v[240:243]\n"
"ds_read_b64_tr_b16 a[116:117], v117 offset:32768\n"
"v_mfma_f32_16x16x32_bf16 v[244:247], a[88:91], v[44:47], v[244:247]\n"
"ds_read_b64_tr_b16 a[118:119], v117 offset:40960\n"
"v_mfma_f32_16x16x32_bf16 v[252:255], a[92:95], v[44:47], v[252:255]\n"
"ds_read_b64_tr_b16 a[120:121], v118 offset:32768\n"
"v_mfma_f32_16x16x32_bf16 v[248:251], a[92:95], v[40:43], v[248:251]\n"
"ds_read_b64_tr_b16 a[122:123], v118 offset:40960\n"
"s_barrier\n"
"s_waitcnt lgkmcnt(6)\n"
"v_mfma_f32_16x16x32_bf16 v[128:131], a[96:99], v[48:51], v[128:131]\n"
"ds_read_b64_tr_b16 a[124:125], v119 offset:32768\n"
"s_add_u32 m0, s74, 102400\n"
"v_mfma_f32_16x16x32_bf16 v[132:135], a[96:99], v[52:55], v[132:135]\n"
"ds_read_b64_tr_b16 a[126:127], v119 offset:40960\n"
"buffer_load_dwordx4 v16, s[4:7], s79 offen offset:0 lds\n"
"v_mfma_f32_16x16x32_bf16 v[140:143], a[100:103], v[52:55], v[140:143]\n"
"ds_read_b64_tr_b16 a[64:65], v112 offset:33024\n"
"v_mfma_f32_16x16x32_bf16 v[136:139], a[100:103], v[48:51], v[136:139]\n"
"ds_read_b64_tr_b16 a[66:67], v112 offset:41216\n"
"v_mfma_f32_16x16x32_bf16 v[144:147], a[104:107], v[48:51], v[144:147]\n"
"ds_read_b64_tr_b16 a[68:69], v113 offset:33024\n"
"v_mfma_f32_16x16x32_bf16 v[148:151], a[104:107], v[52:55], v[148:151]\n"
"ds_read_b64_tr_b16 a[70:71], v113 offset:41216\n"
"v_mfma_f32_16x16x32_bf16 v[156:159], a[108:111], v[52:55], v[156:159]\n"
"ds_read_b64_tr_b16 a[72:73], v114 offset:33024\n"
"buffer_load_dwordx4 v17, s[4:7], s79 offen offset:1024 lds\n"
"v_mfma_f32_16x16x32_bf16 v[152:155], a[108:111], v[48:51], v[152:155]\n"
"ds_read_b64_tr_b16 a[74:75], v114 offset:41216\n"
"s_waitcnt lgkmcnt(6)\n"
"v_mfma_f32_16x16x32_bf16 v[160:163], a[112:115], v[48:51], v[160:163]\n"
"ds_read_b64_tr_b16 a[76:77], v115 offset:33024\n"
"v_mfma_f32_16x16x32_bf16 v[164:167], a[112:115], v[52:55], v[164:167]\n"
"ds_read_b64_tr_b16 a[78:79], v115 offset:41216\n"
"v_mfma_f32_16x16x32_bf16 v[172:175], a[116:119], v[52:55], v[172:175]\n"
"ds_read_b64_tr_b16 a[80:81], v116 offset:33024\n"
"v_mfma_f32_16x16x32_bf16 v[168:171], a[116:119], v[48:51], v[168:171]\n"
"ds_read_b64_tr_b16 a[82:83], v116 offset:41216\n"
"buffer_load_dwordx4 v18, s[4:7], s79 offen offset:2048 lds\n"
"v_mfma_f32_16x16x32_bf16 v[176:179], a[120:123], v[48:51], v[176:179]\n"
"ds_read_b64_tr_b16 a[84:85], v117 offset:33024\n"
"v_mfma_f32_16x16x32_bf16 v[180:183], a[120:123], v[52:55], v[180:183]\n"
"ds_read_b64_tr_b16 a[86:87], v117 offset:41216\n"
"v_mfma_f32_16x16x32_bf16 v[188:191], a[124:127], v[52:55], v[188:191]\n"
"ds_read_b64_tr_b16 a[88:89], v118 offset:33024\n"
"v_mfma_f32_16x16x32_bf16 v[184:187], a[124:127], v[48:51], v[184:187]\n"
"ds_read_b64_tr_b16 a[90:91], v118 offset:41216\n"
"s_waitcnt lgkmcnt(6)\n"
"v_mfma_f32_16x16x32_bf16 v[192:195], a[64:67], v[48:51], v[192:195]\n"
"ds_read_b64_tr_b16 a[92:93], v119 offset:33024\n"
"buffer_load_dwordx4 v19, s[4:7], s79 offen offset:3072 lds\n"
"v_mfma_f32_16x16x32_bf16 v[196:199], a[64:67], v[52:55], v[196:199]\n"
"ds_read_b64_tr_b16 a[94:95], v119 offset:41216\n"
"v_readlane_b32 s79, v21, 4\n"
"v_mfma_f32_16x16x32_bf16 v[204:207], a[68:71], v[52:55], v[204:207]\n"
"ds_read_b64_tr_b16 a[96:97], v112 offset:49152\n"
"v_mfma_f32_16x16x32_bf16 v[200:203], a[68:71], v[48:51], v[200:203]\n"
"ds_read_b64_tr_b16 a[98:99], v112 offset:57344\n"
"s_waitcnt vmcnt(16)\n"
"buffer_store_dwordx2 v[28:29], v30, s[36:39], s85 offen offset:0 sc1\n"
"v_mfma_f32_16x16x32_bf16 v[208:211], a[72:75], v[48:51], v[208:211]\n"
"ds_read_b64_tr_b16 a[100:101], v113 offset:49152\n"
"v_mfma_f32_16x16x32_bf16 v[212:215], a[72:75], v[52:55], v[212:215]\n"
"ds_read_b64_tr_b16 a[102:103], v113 offset:57344\n"
"s_add_u32 m0, s74, 0\n"
"v_mfma_f32_16x16x32_bf16 v[220:223], a[76:79], v[52:55], v[220:223]\n"
"ds_read_b64_tr_b16 a[104:105], v114 offset:49152\n"
"buffer_load_dwordx4 v4, s[4:7], s79 offen offset:0 lds\n"
"v_mfma_f32_16x16x32_bf16 v[216:219], a[76:79], v[48:51], v[216:219]\n"
"ds_read_b64_tr_b16 a[106:107], v114 offset:57344\n"
"s_waitcnt lgkmcnt(6)\n"
"v_mfma_f32_16x16x32_bf16 v[224:227], a[80:83], v[48:51], v[224:227]\n"
"ds_read_b64_tr_b16 a[108:109], v115 offset:49152\n"
"v_mfma_f32_16x16x32_bf16 v[228:231], a[80:83], v[52:55], v[228:231]\n"
"ds_read_b64_tr_b16 a[110:111], v115 offset:57344\n"
"v_mfma_f32_16x16x32_bf16 v[236:239], a[84:87], v[52:55], v[236:239]\n"
"ds_read_b64_tr_b16 a[112:113], v116 offset:49152\n"
"v_mfma_f32_16x16x32_bf16 v[232:235], a[84:87], v[48:51], v[232:235]\n"
"ds_read_b64_tr_b16 a[114:115], v116 offset:57344\n"
"buffer_load_dwordx4 v5, s[4:7], s79 offen offset:1024 lds\n"
"v_mfma_f32_16x16x32_bf16 v[240:243], a[88:91], v[48:51], v[240:243]\n"
"ds_read_b64_tr_b16 a[116:117], v117 offset:49152\n"
"v_mfma_f32_16x16x32_bf16 v[244:247], a[88:91], v[52:55], v[244:247]\n"
"ds_read_b64_tr_b16 a[118:119], v117 offset:57344\n"
"v_mfma_f32_16x16x32_bf16 v[252:255], a[92:95], v[52:55], v[252:255]\n"
"ds_read_b64_tr_b16 a[120:121], v118 offset:49152\n"
"v_mfma_f32_16x16x32_bf16 v[248:251], a[92:95], v[48:51], v[248:251]\n"
"ds_read_b64_tr_b16 a[122:123], v118 offset:57344\n"
"s_waitcnt lgkmcnt(6)\n"
"v_mfma_f32_16x16x32_bf16 v[128:131], a[96:99], v[56:59], v[128:131]\n"
"ds_read_b64_tr_b16 a[124:125], v119 offset:49152\n"
"buffer_load_dwordx4 v6, s[4:7], s79 offen offset:2048 lds\n"
"v_mfma_f32_16x16x32_bf16 v[132:135], a[96:99], v[60:63], v[132:135]\n"
"ds_read_b64_tr_b16 a[126:127], v119 offset:57344\n"
"v_mfma_f32_16x16x32_bf16 v[140:143], a[100:103], v[60:63], v[140:143]\n"
"ds_read_b64_tr_b16 a[64:65], v112 offset:49408\n"
"v_mfma_f32_16x16x32_bf16 v[136:139], a[100:103], v[56:59], v[136:139]\n"
"ds_read_b64_tr_b16 a[66:67], v112 offset:57600\n"
"v_mfma_f32_16x16x32_bf16 v[144:147], a[104:107], v[56:59], v[144:147]\n"
"ds_read_b64_tr_b16 a[68:69], v113 offset:49408\n"
"v_mfma_f32_16x16x32_bf16 v[148:151], a[104:107], v[60:63], v[148:151]\n"
"ds_read_b64_tr_b16 a[70:71], v113 offset:57600\n"
"buffer_load_dwordx4 v7, s[4:7], s79 offen offset:3072 lds\n"
"v_mfma_f32_16x16x32_bf16 v[156:159], a[108:111], v[60:63], v[156:159]\n"
"ds_read_b64_tr_b16 a[72:73], v114 offset:49408\n"
"v_mfma_f32_16x16x32_bf16 v[152:155], a[108:111], v[56:59], v[152:155]\n"
"ds_read_b64_tr_b16 a[74:75], v114 offset:57600\n"
"s_waitcnt lgkmcnt(6)\n"
"v_mfma_f32_16x16x32_bf16 v[160:163], a[112:115], v[56:59], v[160:163]\n"
"ds_read_b64_tr_b16 a[76:77], v115 offset:49408\n"
"v_mfma_f32_16x16x32_bf16 v[164:167], a[112:115], v[60:63], v[164:167]\n"
"ds_read_b64_tr_b16 a[78:79], v115 offset:57600\n"
"v_mfma_f32_16x16x32_bf16 v[172:175], a[116:119], v[60:63], v[172:175]\n"
"ds_read_b64_tr_b16 a[80:81], v116 offset:49408\n"
"s_add_u32 m0, s74, 4096\n"
"v_mfma_f32_16x16x32_bf16 v[168:171], a[116:119], v[56:59], v[168:171]\n"
"ds_read_b64_tr_b16 a[82:83], v116 offset:57600\n"
"buffer_load_dwordx4 v16, s[4:7], s79 offen offset:0 lds\n"
"v_mfma_f32_16x16x32_bf16 v[176:179], a[120:123], v[56:59], v[176:179]\n"
"ds_read_b64_tr_b16 a[84:85], v117 offset:49408\n"
"v_mfma_f32_16x16x32_bf16 v[180:183], a[120:123], v[60:63], v[180:183]\n"
"ds_read_b64_tr_b16 a[86:87], v117 offset:57600\n"
"v_mfma_f32_16x16x32_bf16 v[188:191], a[124:127], v[60:63], v[188:191]\n"
"ds_read_b64_tr_b16 a[88:89], v118 offset:49408\n"
"v_mfma_f32_16x16x32_bf16 v[184:187], a[124:127], v[56:59], v[184:187]\n"
"ds_read_b64_tr_b16 a[90:91], v118 offset:57600\n"
"s_waitcnt lgkmcnt(6)\n"
"v_mfma_f32_16x16x32_bf16 v[192:195], a[64:67], v[56:59], v[192:195]\n"
"ds_read_b64_tr_b16 a[92:93], v119 offset:49408\n"
"buffer_load_dwordx4 v17, s[4:7], s79 offen offset:1024 lds\n"
"v_mfma_f32_16x16x32_bf16 v[196:199], a[64:67], v[60:63], v[196:199]\n"
"ds_read_b64_tr_b16 a[94:95], v119 offset:57600\n"
"s_waitcnt vmcnt(15)\n"
"s_barrier\n"
"v_mfma_f32_16x16x32_bf16 v[204:207], a[68:71], v[60:63], v[204:207]\n"
"ds_read_b64_tr_b16 a[96:97], v120 offset:0\n"
"v_mfma_f32_16x16x32_bf16 v[200:203], a[68:71], v[56:59], v[200:203]\n"
"ds_read_b64_tr_b16 a[98:99], v120 offset:8192\n"
"v_mfma_f32_16x16x32_bf16 v[208:211], a[72:75], v[56:59], v[208:211]\n"
"ds_read_b64_tr_b16 a[100:101], v121 offset:0\n"
"v_mfma_f32_16x16x32_bf16 v[212:215], a[72:75], v[60:63], v[212:215]\n"
"ds_read_b64_tr_b16 a[102:103], v121 offset:8192\n"
"buffer_load_dwordx4 v18, s[4:7], s79 offen offset:2048 lds\n"
"v_mfma_f32_16x16x32_bf16 v[220:223], a[76:79], v[60:63], v[220:223]\n"
"ds_read_b64_tr_b16 a[104:105], v122 offset:0\n"
"v_mfma_f32_16x16x32_bf16 v[216:219], a[76:79], v[56:59], v[216:219]\n"
"ds_read_b64_tr_b16 a[106:107], v122 offset:8192\n"
"s_waitcnt lgkmcnt(6)\n"
"v_mfma_f32_16x16x32_bf16 v[224:227], a[80:83], v[56:59], v[224:227]\n"
"ds_read_b64_tr_b16 a[108:109], v123 offset:0\n"
"v_mfma_f32_16x16x32_bf16 v[228:231], a[80:83], v[60:63], v[228:231]\n"
"ds_read_b64_tr_b16 a[110:111], v123 offset:8192\n"
"v_mfma_f32_16x16x32_bf16 v[236:239], a[84:87], v[60:63], v[236:239]\n"
"ds_read_b64_tr_b16 a[112:113], v124 offset:0\n"
"buffer_load_dwordx4 v19, s[4:7], s79 offen offset:3072 lds\n"
"v_mfma_f32_16x16x32_bf16 v[232:235], a[84:87], v[56:59], v[232:235]\n"
"ds_read_b64_tr_b16 a[114:115], v124 offset:8192\n"
"v_readlane_b32 s79, v21, 5\n"
"v_mfma_f32_16x16x32_bf16 v[240:243], a[88:91], v[56:59], v[240:243]\n"
"ds_read_b64_tr_b16 a[116:117], v125 offset:0\n"
"v_mfma_f32_16x16x32_bf16 v[244:247], a[88:91], v[60:63], v[244:247]\n"
"ds_read_b64_tr_b16 a[118:119], v125 offset:8192\n"
"v_mfma_f32_16x16x32_bf16 v[252:255], a[92:95], v[60:63], v[252:255]\n"
"ds_read_b64_tr_b16 a[120:121], v126 offset:0\n"
"v_mfma_f32_16x16x32_bf16 v[248:251], a[92:95], v[56:59], v[248:251]\n"
"ds_read_b64_tr_b16 a[122:123], v126 offset:8192\n"
"s_waitcnt vmcnt(9)\n"
"s_barrier\n"
"s_waitcnt lgkmcnt(6)\n"
"v_mfma_f32_16x16x32_bf16 v[128:131], a[96:99], v[64:67], v[128:131]\n"
"ds_read_b64_tr_b16 a[124:125], v127 offset:0\n"
"s_add_u32 m0, s74, 32768\n"
"v_mfma_f32_16x16x32_bf16 v[132:135], a[96:99], v[68:71], v[132:135]\n"
"ds_read_b64_tr_b16 a[126:127], v127 offset:8192\n"
"buffer_load_dwordx4 v4, s[4:7], s79 offen offset:0 lds\n"
"v_mfma_f32_16x16x32_bf16 v[140:143], a[100:103], v[68:71], v[140:143]\n"
"ds_read_b64_tr_b16 a[64:65], v120 offset:256\n"
"v_mfma_f32_16x16x32_bf16 v[136:139], a[100:103], v[64:67], v[136:139]\n"
"ds_read_b64_tr_b16 a[66:67], v120 offset:8448\n"
"v_mfma_f32_16x16x32_bf16 v[144:147], a[104:107], v[64:67], v[144:147]\n"
"ds_read_b64_tr_b16 a[68:69], v121 offset:256\n"
"v_mfma_f32_16x16x32_bf16 v[148:151], a[104:107], v[68:71], v[148:151]\n"
"ds_read_b64_tr_b16 a[70:71], v121 offset:8448\n"
"v_mfma_f32_16x16x32_bf16 v[156:159], a[108:111], v[68:71], v[156:159]\n"
"ds_read_b64_tr_b16 a[72:73], v122 offset:256\n"
"v_mfma_f32_16x16x32_bf16 v[152:155], a[108:111], v[64:67], v[152:155]\n"
"ds_read_b64_tr_b16 a[74:75], v122 offset:8448\n"
"s_waitcnt lgkmcnt(6)\n"
"v_mfma_f32_16x16x32_bf16 v[160:163], a[112:115], v[64:67], v[160:163]\n"
"ds_read_b64_tr_b16 a[76:77], v123 offset:256\n"
"buffer_load_dwordx4 v5, s[4:7], s79 offen offset:1024 lds\n"
"v_mfma_f32_16x16x32_bf16 v[164:167], a[112:115], v[68:71], v[164:167]\n"
"ds_read_b64_tr_b16 a[78:79], v123 offset:8448\n"
"v_mfma_f32_16x16x32_bf16 v[172:175], a[116:119], v[68:71], v[172:175]\n"
"ds_read_b64_tr_b16 a[80:81], v124 offset:256\n"
"v_mfma_f32_16x16x32_bf16 v[168:171], a[116:119], v[64:67], v[168:171]\n"
"ds_read_b64_tr_b16 a[82:83], v124 offset:8448\n"
"v_mfma_f32_16x16x32_bf16 v[176:179], a[120:123], v[64:67], v[176:179]\n"
"ds_read_b64_tr_b16 a[84:85], v125 offset:256\n"
"v_mfma_f32_16x16x32_bf16 v[180:183], a[120:123], v[68:71], v[180:183]\n"
"ds_read_b64_tr_b16 a[86:87], v125 offset:8448\n"
"v_mfma_f32_16x16x32_bf16 v[188:191], a[124:127], v[68:71], v[188:191]\n"
"ds_read_b64_tr_b16 a[88:89], v126 offset:256\n"
"buffer_load_dwordx4 v6, s[4:7], s79 offen offset:2048 lds\n"
"v_mfma_f32_16x16x32_bf16 v[184:187], a[124:127], v[64:67], v[184:187]\n"
"ds_read_b64_tr_b16 a[90:91], v126 offset:8448\n"
"s_waitcnt lgkmcnt(6)\n"
"v_mfma_f32_16x16x32_bf16 v[192:195], a[64:67], v[64:67], v[192:195]\n"
"ds_read_b64_tr_b16 a[92:93], v127 offset:256\n"
"v_mfma_f32_16x16x32_bf16 v[196:199], a[64:67], v[68:71], v[196:199]\n"
"ds_read_b64_tr_b16 a[94:95], v127 offset:8448\n"
"v_mfma_f32_16x16x32_bf16 v[204:207], a[68:71], v[68:71], v[204:207]\n"
"ds_read_b64_tr_b16 a[96:97], v120 offset:16384\n"
"v_mfma_f32_16x16x32_bf16 v[200:203], a[68:71], v[64:67], v[200:203]\n"
"ds_read_b64_tr_b16 a[98:99], v120 offset:24576\n"
"v_mfma_f32_16x16x32_bf16 v[208:211], a[72:75], v[64:67], v[208:211]\n"
"ds_read_b64_tr_b16 a[100:101], v121 offset:16384\n"
"v_mfma_f32_16x16x32_bf16 v[212:215], a[72:75], v[68:71], v[212:215]\n"
"ds_read_b64_tr_b16 a[102:103], v121 offset:24576\n"
"buffer_load_dwordx4 v7, s[4:7], s79 offen offset:3072 lds\n"
"v_mfma_f32_16x16x32_bf16 v[220:223], a[76:79], v[68:71], v[220:223]\n"
"ds_read_b64_tr_b16 a[104:105], v122 offset:16384\n"
"v_mfma_f32_16x16x32_bf16 v[216:219], a[76:79], v[64:67], v[216:219]\n"
"ds_read_b64_tr_b16 a[106:107], v122 offset:24576\n"
"s_waitcnt lgkmcnt(6)\n"
"v_mfma_f32_16x16x32_bf16 v[224:227], a[80:83], v[64:67], v[224:227]\n"
"ds_read_b64_tr_b16 a[108:109], v123 offset:16384\n"
"v_mfma_f32_16x16x32_bf16 v[228:231], a[80:83], v[68:71], v[228:231]\n"
"ds_read_b64_tr_b16 a[110:111], v123 offset:24576\n"
"v_mfma_f32_16x16x32_bf16 v[236:239], a[84:87], v[68:71], v[236:239]\n"
"ds_read_b64_tr_b16 a[112:113], v124 offset:16384\n"
"v_mfma_f32_16x16x32_bf16 v[232:235], a[84:87], v[64:67], v[232:235]\n"
"ds_read_b64_tr_b16 a[114:115], v124 offset:24576\n"
"s_add_u32 m0, s74, 36864\n"
"v_mfma_f32_16x16x32_bf16 v[240:243], a[88:91], v[64:67], v[240:243]\n"
"ds_read_b64_tr_b16 a[116:117], v125 offset:16384\n"
"buffer_load_dwordx2 v[26:27], v31, s[36:39], s86 offen offset:0 sc1\n"
"v_mfma_f32_16x16x32_bf16 v[244:247], a[88:91], v[68:71], v[244:247]\n"
"ds_read_b64_tr_b16 a[118:119], v125 offset:24576\n"
"buffer_load_dwordx4 v16, s[4:7], s79 offen offset:0 lds\n"
"v_mfma_f32_16x16x32_bf16 v[252:255], a[92:95], v[68:71], v[252:255]\n"
"ds_read_b64_tr_b16 a[120:121], v126 offset:16384\n"
"v_mfma_f32_16x16x32_bf16 v[248:251], a[92:95], v[64:67], v[248:251]\n"
"ds_read_b64_tr_b16 a[122:123], v126 offset:24576\n"
"s_waitcnt lgkmcnt(6)\n"
"v_mfma_f32_16x16x32_bf16 v[128:131], a[96:99], v[72:75], v[128:131]\n"
"ds_read_b64_tr_b16 a[124:125], v127 offset:16384\n"
"v_mfma_f32_16x16x32_bf16 v[132:135], a[96:99], v[76:79], v[132:135]\n"
"ds_read_b64_tr_b16 a[126:127], v127 offset:24576\n"
"v_mfma_f32_16x16x32_bf16 v[140:143], a[100:103], v[76:79], v[140:143]\n"
"ds_read_b64_tr_b16 a[64:65], v120 offset:16640\n"
"v_mfma_f32_16x16x32_bf16 v[136:139], a[100:103], v[72:75], v[136:139]\n"
"ds_read_b64_tr_b16 a[66:67], v120 offset:24832\n"
"buffer_load_dwordx4 v17, s[4:7], s79 offen offset:1024 lds\n"
"v_mfma_f32_16x16x32_bf16 v[144:147], a[104:107], v[72:75], v[144:147]\n"
"ds_read_b64_tr_b16 a[68:69], v121 offset:16640\n"
"v_mfma_f32_16x16x32_bf16 v[148:151], a[104:107], v[76:79], v[148:151]\n"
"ds_read_b64_tr_b16 a[70:71], v121 offset:24832\n"
"v_mfma_f32_16x16x32_bf16 v[156:159], a[108:111], v[76:79], v[156:159]\n"
"ds_read_b64_tr_b16 a[72:73], v122 offset:16640\n"
"v_mfma_f32_16x16x32_bf16 v[152:155], a[108:111], v[72:75], v[152:155]\n"
"ds_read_b64_tr_b16 a[74:75], v122 offset:24832\n"
"s_waitcnt lgkmcnt(6)\n"
"v_mfma_f32_16x16x32_bf16 v[160:163], a[112:115], v[72:75], v[160:163]\n"
"ds_read_b64_tr_b16 a[76:77], v123 offset:16640\n"
"v_mfma_f32_16x16x32_bf16 v[164:167], a[112:115], v[76:79], v[164:167]\n"
"ds_read_b64_tr_b16 a[78:79], v123 offset:24832\n"
"v_mfma_f32_16x16x32_bf16 v[172:175], a[116:119], v[76:79], v[172:175]\n"
"ds_read_b64_tr_b16 a[80:81], v124 offset:16640\n"
"buffer_load_dwordx4 v18, s[4:7], s79 offen offset:2048 lds\n"
"v_mfma_f32_16x16x32_bf16 v[168:171], a[116:119], v[72:75], v[168:171]\n"
"ds_read_b64_tr_b16 a[82:83], v124 offset:24832\n"
"v_mfma_f32_16x16x32_bf16 v[176:179], a[120:123], v[72:75], v[176:179]\n"
"ds_read_b64_tr_b16 a[84:85], v125 offset:16640\n"
"v_mfma_f32_16x16x32_bf16 v[180:183], a[120:123], v[76:79], v[180:183]\n"
"ds_read_b64_tr_b16 a[86:87], v125 offset:24832\n"
"v_mfma_f32_16x16x32_bf16 v[188:191], a[124:127], v[76:79], v[188:191]\n"
"ds_read_b64_tr_b16 a[88:89], v126 offset:16640\n"
"v_mfma_f32_16x16x32_bf16 v[184:187], a[124:127], v[72:75], v[184:187]\n"
"ds_read_b64_tr_b16 a[90:91], v126 offset:24832\n"
"s_waitcnt lgkmcnt(6)\n"
"v_mfma_f32_16x16x32_bf16 v[192:195], a[64:67], v[72:75], v[192:195]\n"
"ds_read_b64_tr_b16 a[92:93], v127 offset:16640\n"
"v_mfma_f32_16x16x32_bf16 v[196:199], a[64:67], v[76:79], v[196:199]\n"
"ds_read_b64_tr_b16 a[94:95], v127 offset:24832\n"
"buffer_load_dwordx4 v19, s[4:7], s79 offen offset:3072 lds\n"
"v_mfma_f32_16x16x32_bf16 v[204:207], a[68:71], v[76:79], v[204:207]\n"
"ds_read_b64_tr_b16 a[96:97], v120 offset:32768\n"
"v_readlane_b32 s79, v21, 6\n"
"v_mfma_f32_16x16x32_bf16 v[200:203], a[68:71], v[72:75], v[200:203]\n"
"ds_read_b64_tr_b16 a[98:99], v120 offset:40960\n"
"v_mfma_f32_16x16x32_bf16 v[208:211], a[72:75], v[72:75], v[208:211]\n"
"ds_read_b64_tr_b16 a[100:101], v121 offset:32768\n"
"v_mfma_f32_16x16x32_bf16 v[212:215], a[72:75], v[76:79], v[212:215]\n"
"ds_read_b64_tr_b16 a[102:103], v121 offset:40960\n"
"v_mfma_f32_16x16x32_bf16 v[220:223], a[76:79], v[76:79], v[220:223]\n"
"ds_read_b64_tr_b16 a[104:105], v122 offset:32768\n"
"v_mfma_f32_16x16x32_bf16 v[216:219], a[76:79], v[72:75], v[216:219]\n"
"ds_read_b64_tr_b16 a[106:107], v122 offset:40960\n"
"s_waitcnt vmcnt(4)\n"
"v_cmp_ne_u64 vcc, s[70:71], v[26:27]\n"
"s_cbranch_vccnz .Lpslow9_%=\n"
".Lpok9_%=:\n"
"s_waitcnt lgkmcnt(6)\n"
"v_mfma_f32_16x16x32_bf16 v[224:227], a[80:83], v[72:75], v[224:227]\n"
"ds_read_b64_tr_b16 a[108:109], v123 offset:32768\n"
"v_mfma_f32_16x16x32_bf16 v[228:231], a[80:83], v[76:79], v[228:231]\n"
"ds_read_b64_tr_b16 a[110:111], v123 offset:40960\n"
"buffer_load_dwordx4 a[56:59], v8, s[52:55], s82 offen offset:0 sc1\n"
"v_mfma_f32_16x16x32_bf16 v[236:239], a[84:87], v[76:79], v[236:239]\n"
"ds_read_b64_tr_b16 a[112:113], v124 offset:32768\n"
"buffer_load_dwordx4 a[60:63], v8, s[52:55], s82 offen offset:1024 sc1\n"
"v_mfma_f32_16x16x32_bf16 v[232:235], a[84:87], v[72:75], v[232:235]\n"
"ds_read_b64_tr_b16 a[114:115], v124 offset:40960\n"
"v_readlane_b32 s82, v21, 24\n"
"v_mfma_f32_16x16x32_bf16 v[240:243], a[88:91], v[72:75], v[240:243]\n"
"ds_read_b64_tr_b16 a[116:117], v125 offset:32768\n"
"s_nop 2\n"
"buffer_load_dwordx4 a[0:3], v8, s[52:55], s82 offen offset:0 sc1\n"
"v_mfma_f32_16x16x32_bf16 v[244:247], a[88:91], v[76:79], v[244:247]\n"
"ds_read_b64_tr_b16 a[118:119], v125 offset:40960\n"
"buffer_load_dwordx4 a[4:7], v8, s[52:55], s82 offen offset:1024 sc1\n"
"v_mfma_f32_16x16x32_bf16 v[252:255], a[92:95], v[76:79], v[252:255]\n"
"ds_read_b64_tr_b16 a[120:121], v126 offset:32768\n"
"v_readlane_b32 s82, v21, 25\n"
"v_mfma_f32_16x16x32_bf16 v[248:251], a[92:95], v[72:75], v[248:251]\n"
"ds_read_b64_tr_b16 a[122:123], v126 offset:40960\n"
"s_nop 2\n"
"buffer_load_dwordx4 a[8:11], v8, s[52:55], s82 offen offset:0 sc1\n"
"buffer_load_dwordx4 a[12:15], v8, s[52:55], s82 offen offset:1024 sc1\n"
"s_barrier\n"
"s_waitcnt lgkmcnt(6)\n"
"v_mfma_f32_16x16x32_bf16 v[128:131], a[96:99], v[80:83], v[128:131]\n"
"ds_read_b64_tr_b16 a[124:125], v127 offset:32768\n"
"v_readlane_b32 s82, v21, 26\n"
"v_mfma_f32_16x16x32_bf16 v[132:135], a[96:99], v[84:87], v[132:135]\n"
"ds_read_b64_tr_b16 a[126:127], v127 offset:40960\n"
"s_nop 2\n"
"buffer_load_dwordx4 a[16:19], v8, s[52:55], s82 offen offset:0 sc1\n"
"v_mfma_f32_16x16x32_bf16 v[140:143], a[100:103], v[84:87], v[140:143]\n"
"ds_read_b64_tr_b16 a[64:65], v120 offset:33024\n"
"v_mfma_f32_16x16x32_bf16 v[136:139], a[100:103], v[80:83], v[136:139]\n"
"ds_read_b64_tr_b16 a[66:67], v120 offset:41216\n"
"v_mfma_f32_16x16x32_bf16 v[144:147], a[104:107], v[80:83], v[144:147]\n"
"ds_read_b64_tr_b16 a[68:69], v121 offset:33024\n"
"buffer_load_dwordx4 a[20:23], v8, s[52:55], s82 offen offset:1024 sc1\n"
"v_mfma_f32_16x16x32_bf16 v[148:151], a[104:107], v[84:87], v[148:151]\n"
"ds_read_b64_tr_b16 a[70:71], v121 offset:41216\n"
"v_readlane_b32 s82, v21, 27\n"
"v_mfma_f32_16x16x32_bf16 v[156:159], a[108:111], v[84:87], v[156:159]\n"
"ds_read_b64_tr_b16 a[72:73], v122 offset:33024\n"
"v_mfma_f32_16x16x32_bf16 v[152:155], a[108:111], v[80:83], v[152:155]\n"
"ds_read_b64_tr_b16 a[74:75], v122 offset:41216\n"
"s_waitcnt lgkmcnt(6)\n"
"v_mfma_f32_16x16x32_bf16 v[160:163], a[112:115], v[80:83], v[160:163]\n"
"ds_read_b64_tr_b16 a[76:77], v123 offset:33024\n"
"buffer_load_dwordx4 a[24:27], v8, s[52:55], s82 offen offset:0 sc1\n"
"v_mfma_f32_16x16x32_bf16 v[164:167], a[112:115], v[84:87], v[164:167]\n"
"ds_read_b64_tr_b16 a[78:79], v123 offset:41216\n"
"v_mfma_f32_16x16x32_bf16 v[172:175], a[116:119], v[84:87], v[172:175]\n"
"ds_read_b64_tr_b16 a[80:81], v124 offset:33024\n"
"v_mfma_f32_16x16x32_bf16 v[168:171], a[116:119], v[80:83], v[168:171]\n"
"ds_read_b64_tr_b16 a[82:83], v124 offset:41216\n"
"v_mfma_f32_16x16x32_bf16 v[176:179], a[120:123], v[80:83], v[176:179]\n"
"ds_read_b64_tr_b16 a[84:85], v125 offset:33024\n"
"buffer_load_dwordx4 a[28:31], v8, s[52:55], s82 offen offset:1024 sc1\n"
"v_mfma_f32_16x16x32_bf16 v[180:183], a[120:123], v[84:87], v[180:183]\n"
"ds_read_b64_tr_b16 a[86:87], v125 offset:41216\n"
"v_readlane_b32 s82, v21, 28\n"
"v_mfma_f32_16x16x32_bf16 v[188:191], a[124:127], v[84:87], v[188:191]\n"
"ds_read_b64_tr_b16 a[88:89], v126 offset:33024\n"
"v_mfma_f32_16x16x32_bf16 v[184:187], a[124:127], v[80:83], v[184:187]\n"
"ds_read_b64_tr_b16 a[90:91], v126 offset:41216\n"
"s_waitcnt lgkmcnt(6)\n"
"v_mfma_f32_16x16x32_bf16 v[192:195], a[64:67], v[80:83], v[192:195]\n"
"ds_read_b64_tr_b16 a[92:93], v127 offset:33024\n"
"s_add_u32 m0, s74, 65536\n"
"v_mfma_f32_16x16x32_bf16 v[196:199], a[64:67], v[84:87], v[196:199]\n"
"ds_read_b64_tr_b16 a[94:95], v127 offset:41216\n"
"buffer_load_dwordx4 v4, s[4:7], s79 offen offset:0 lds\n"
"v_mfma_f32_16x16x32_bf16 v[204:207], a[68:71], v[84:87], v[204:207]\n"
"ds_read_b64_tr_b16 a[96:97], v120 offset:49152\n"
"v_mfma_f32_16x16x32_bf16 v[200:203], a[68:71], v[80:83], v[200:203]\n"
"ds_read_b64_tr_b16 a[98:99], v120 offset:57344\n"
"v_mfma_f32_16x16x32_bf16 v[208:211], a[72:75], v[80:83], v[208:211]\n"
"ds_read_b64_tr_b16 a[100:101], v121 offset:49152\n"
"buffer_load_dwordx4 v5, s[4:7], s79 offen offset:1024 lds\n"
"v_mfma_f32_16x16x32_bf16 v[212:215], a[72:75], v[84:87], v[212:215]\n"
"ds_read_b64_tr_b16 a[102:103], v121 offset:57344\n"
"v_mfma_f32_16x16x32_bf16 v[220:223], a[76:79], v[84:87], v[220:223]\n"
"ds_read_b64_tr_b16 a[104:105], v122 offset:49152\n"
"v_mfma_f32_16x16x32_bf16 v[216:219], a[76:79], v[80:83], v[216:219]\n"
"ds_read_b64_tr_b16 a[106:107], v122 offset:57344\n"
"s_waitcnt lgkmcnt(6)\n"
"v_mfma_f32_16x16x32_bf16 v[224:227], a[80:83], v[80:83], v[224:227]\n"
"ds_read_b64_tr_b16 a[108:109], v123 offset:49152\n"
"buffer_load_dwordx4 v6, s[4:7], s79 offen offset:2048 lds\n"
"v_mfma_f32_16x16x32_bf16 v[228:231], a[80:83], v[84:87], v[228:231]\n"
"ds_read_b64_tr_b16 a[110:111], v123 offset:57344\n"
"v_mfma_f32_16x16x32_bf16 v[236:239], a[84:87], v[84:87], v[236:239]\n"
"ds_read_b64_tr_b16 a[112:113], v124 offset:49152\n"
"v_mfma_f32_16x16x32_bf16 v[232:235], a[84:87], v[80:83], v[232:235]\n"
"ds_read_b64_tr_b16 a[114:115], v124 offset:57344\n"
"v_mfma_f32_16x16x32_bf16 v[240:243], a[88:91], v[80:83], v[240:243]\n"
"ds_read_b64_tr_b16 a[116:117], v125 offset:49152\n"
"buffer_load_dwordx4 v7, s[4:7], s79 offen offset:3072 lds\n"
"v_mfma_f32_16x16x32_bf16 v[244:247], a[88:91], v[84:87], v[244:247]\n"
"ds_read_b64_tr_b16 a[118:119], v125 offset:57344\n"
"v_mfma_f32_16x16x32_bf16 v[252:255], a[92:95], v[84:87], v[252:255]\n"
"ds_read_b64_tr_b16 a[120:121], v126 offset:49152\n"
"v_mfma_f32_16x16x32_bf16 v[248:251], a[92:95], v[80:83], v[248:251]\n"
"ds_read_b64_tr_b16 a[122:123], v126 offset:57344\n"
"s_waitcnt lgkmcnt(6)\n"
"s_waitcnt vmcnt(13)\n"
"v_mfma_f32_16x16x32_bf16 v[128:131], a[96:99], a[56:59], v[128:131]\n"
"ds_read_b64_tr_b16 a[124:125], v127 offset:49152\n"
"s_add_u32 m0, s74, 69632\n"
"s_waitcnt vmcnt(12)\n"
"v_mfma_f32_16x16x32_bf16 v[132:135], a[96:99], a[60:63], v[132:135]\n"
"ds_read_b64_tr_b16 a[126:127], v127 offset:57344\n"
"buffer_load_dwordx4 v16, s[4:7], s79 offen offset:0 lds\n"
"v_mfma_f32_16x16x32_bf16 v[140:143], a[100:103], a[60:63], v[140:143]\n"
"ds_read_b64_tr_b16 a[64:65], v120 offset:49408\n"
"v_mfma_f32_16x16x32_bf16 v[136:139], a[100:103], a[56:59], v[136:139]\n"
"ds_read_b64_tr_b16 a[66:67], v120 offset:57600\n"
"v_mfma_f32_16x16x32_bf16 v[144:147], a[104:107], a[56:59], v[144:147]\n"
"ds_read_b64_tr_b16 a[68:69], v121 offset:49408\n"
"buffer_load_dwordx4 v17, s[4:7], s79 offen offset:1024 lds\n"
"v_mfma_f32_16x16x32_bf16 v[148:151], a[104:107], a[60:63], v[148:151]\n"
"ds_read_b64_tr_b16 a[70:71], v121 offset:57600\n"
"v_mfma_f32_16x16x32_bf16 v[156:159], a[108:111], a[60:63], v[156:159]\n"
"ds_read_b64_tr_b16 a[72:73], v122 offset:49408\n"
"v_mfma_f32_16x16x32_bf16 v[152:155], a[108:111], a[56:59], v[152:155]\n"
"ds_read_b64_tr_b16 a[74:75], v122 offset:57600\n"
"s_waitcnt lgkmcnt(6)\n"
"v_mfma_f32_16x16x32_bf16 v[160:163], a[112:115], a[56:59], v[160:163]\n"
"ds_read_b64_tr_b16 a[76:77], v123 offset:49408\n"
"buffer_load_dwordx4 v18, s[4:7], s79 offen offset:2048 lds\n"
"v_mfma_f32_16x16x32_bf16 v[164:167], a[112:115], a[60:63], v[164:167]\n"
"ds_read_b64_tr_b16 a[78:79], v123 offset:57600\n"
"v_mfma_f32_16x16x32_bf16 v[172:175], a[116:119], a[60:63], v[172:175]\n"
"ds_read_b64_tr_b16 a[80:81], v124 offset:49408\n"
"v_mfma_f32_16x16x32_bf16 v[168:171], a[116:119], a[56:59], v[168:171]\n"
"ds_read_b64_tr_b16 a[82:83], v124 offset:57600\n"
"v_mfma_f32_16x16x32_bf16 v[176:179], a[120:123], a[56:59], v[176:179]\n"
"ds_read_b64_tr_b16 a[84:85], v125 offset:49408\n"
"buffer_load_dwordx4 v19, s[4:7], s79 offen offset:3072 lds\n"
"v_mfma_f32_16x16x32_bf16 v[180:183], a[120:123], a[60:63], v[180:183]\n"
"ds_read_b64_tr_b16 a[86:87], v125 offset:57600\n"
"v_readlane_b32 s79, v21, 7\n"
"v_mfma_f32_16x16x32_bf16 v[188:191], a[124:127], a[60:63], v[188:191]\n"
"ds_read_b64_tr_b16 a[88:89], v126 offset:49408\n"
"v_mfma_f32_16x16x32_bf16 v[184:187], a[124:127], a[56:59], v[184:187]\n"
"ds_read_b64_tr_b16 a[90:91], v126 offset:57600\n"
"s_waitcnt lgkmcnt(6)\n"
"v_mfma_f32_16x16x32_bf16 v[192:195], a[64:67], a[56:59], v[192:195]\n"
"ds_read_b64_tr_b16 a[92:93], v127 offset:49408\n"
"buffer_load_dwordx4 a[32:35], v8, s[52:55], s82 offen offset:0 sc1\n"
"v_mfma_f32_16x16x32_bf16 v[196:199], a[64:67], a[60:63], v[196:199]\n"
"ds_read_b64_tr_b16 a[94:95], v127 offset:57600\n"
"v_mfma_f32_16x16x32_bf16 v[204:207], a[68:71], a[60:63], v[204:207]\n"
"ds_read_b64_tr_b16 a[96:97], v112 offset:0\n"
"v_mfma_f32_16x16x32_bf16 v[200:203], a[68:71], a[56:59], v[200:203]\n"
"ds_read_b64_tr_b16 a[98:99], v112 offset:8192\n"
"v_mfma_f32_16x16x32_bf16 v[208:211], a[72:75], a[56:59], v[208:211]\n"
"ds_read_b64_tr_b16 a[100:101], v113 offset:0\n"
"buffer_load_dwordx4 a[36:39], v8, s[52:55], s82 offen offset:1024 sc1\n"
"v_mfma_f32_16x16x32_bf16 v[212:215], a[72:75], a[60:63], v[212:215]\n"
"ds_read_b64_tr_b16 a[102:103], v113 offset:8192\n"
"v_readlane_b32 s82, v21, 29\n"
"v_mfma_f32_16x16x32_bf16 v[220:223], a[76:79], a[60:63], v[220:223]\n"
"ds_read_b64_tr_b16 a[104:105], v114 offset:0\n"
"v_mfma_f32_16x16x32_bf16 v[216:219], a[76:79], a[56:59], v[216:219]\n"
"ds_read_b64_tr_b16 a[106:107], v114 offset:8192\n"
"s_waitcnt lgkmcnt(6)\n"
"v_mfma_f32_16x16x32_bf16 v[224:227], a[80:83], a[56:59], v[224:227]\n"
"ds_read_b64_tr_b16 a[108:109], v115 offset:0\n"
"buffer_load_dwordx4 a[40:43], v8, s[52:55], s82 offen offset:0 sc1\n"
"v_mfma_f32_16x16x32_bf16 v[228:231], a[80:83], a[60:63], v[228:231]\n"
"ds_read_b64_tr_b16 a[110:111], v115 offset:8192\n"
"v_mfma_f32_16x16x32_bf16 v[236:239], a[84:87], a[60:63], v[236:239]\n"
"ds_read_b64_tr_b16 a[112:113], v116 offset:0\n"
"v_mfma_f32_16x16x32_bf16 v[232:235], a[84:87], a[56:59], v[232:235]\n"
"ds_read_b64_tr_b16 a[114:115], v116 offset:8192\n"
"v_mfma_f32_16x16x32_bf16 v[240:243], a[88:91], a[56:59], v[240:243]\n"
"ds_read_b64_tr_b16 a[116:117], v117 offset:0\n"
"buffer_load_dwordx4 a[44:47], v8, s[52:55], s82 offen offset:1024 sc1\n"
"v_mfma_f32_16x16x32_bf16 v[244:247], a[88:91], a[60:63], v[244:247]\n"
"ds_read_b64_tr_b16 a[118:119], v117 offset:8192\n"
"v_readlane_b32 s82, v21, 30\n"
"v_mfma_f32_16x16x32_bf16 v[252:255], a[92:95], a[60:63], v[252:255]\n"
"ds_read_b64_tr_b16 a[120:121], v118 offset:0\n"
"v_mfma_f32_16x16x32_bf16 v[248:251], a[92:95], a[56:59], v[248:251]\n"
"ds_read_b64_tr_b16 a[122:123], v118 offset:8192\n"
"s_barrier\n"
"s_waitcnt lgkmcnt(6)\n"
"s_waitcnt vmcnt(19)\n"
"v_mfma_f32_16x16x32_bf16 v[128:131], a[96:99], a[0:3], v[128:131]\n"
"ds_read_b64_tr_b16 a[124:125], v119 offset:0\n"
"s_add_u32 m0, s74, 98304\n"
"s_waitcnt vmcnt(18)\n"
"v_mfma_f32_16x16x32_bf16 v[132:135], a[96:99], a[4:7], v[132:135]\n"
"ds_read_b64_tr_b16 a[126:127], v119 offset:8192\n"
"buffer_load_dwordx4 v4, s[4:7], s79 offen offset:0 lds\n"
"v_mfma_f32_16x16x32_bf16 v[140:143], a[100:103], a[4:7], v[140:143]\n"
"ds_read_b64_tr_b16 a[64:65], v112 offset:256\n"
"v_mfma_f32_16x16x32_bf16 v[136:139], a[100:103], a[0:3], v[136:139]\n"
"ds_read_b64_tr_b16 a[66:67], v112 offset:8448\n"
"v_mfma_f32_16x16x32_bf16 v[144:147], a[104:107], a[0:3], v[144:147]\n"
"ds_read_b64_tr_b16 a[68:69], v113 offset:256\n"
"v_mfma_f32_16x16x32_bf16 v[148:151], a[104:107], a[4:7], v[148:151]\n"
"ds_read_b64_tr_b16 a[70:71], v113 offset:8448\n"
"v_mfma_f32_16x16x32_bf16 v[156:159], a[108:111], a[4:7], v[156:159]\n"
"ds_read_b64_tr_b16 a[72:73], v114 offset:256\n"
"buffer_load_dwordx4 v5, s[4:7], s79 offen offset:1024 lds\n"
"v_mfma_f32_16x16x32_bf16 v[152:155], a[108:111], a[0:3], v[152:155]\n"
"ds_read_b64_tr_b16 a[74:75], v114 offset:8448\n"
"s_waitcnt lgkmcnt(6)\n"
"v_mfma_f32_16x16x32_bf16 v[160:163], a[112:115], a[0:3], v[160:163]\n"
"ds_read_b64_tr_b16 a[76:77], v115 offset:256\n"
"v_mfma_f32_16x16x32_bf16 v[164:167], a[112:115], a[4:7], v[164:167]\n"
"ds_read_b64_tr_b16 a[78:79], v115 offset:8448\n"
"v_mfma_f32_16x16x32_bf16 v[172:175], a[116:119], a[4:7], v[172:175]\n"
"ds_read_b64_tr_b16 a[80:81], v116 offset:256\n"
"v_mfma_f32_16x16x32_bf16 v[168:171], a[116:119], a[0:3], v[168:171]\n"
"ds_read_b64_tr_b16 a[82:83], v116 offset:8448\n"
"buffer_load_dwordx4 v6, s[4:7], s79 offen offset:2048 lds\n"
"v_mfma_f32_16x16x32_bf16 v[176:179], a[120:123], a[0:3], v[176:179]\n"
"ds_read_b64_tr_b16 a[84:85], v117 offset:256\n"
"v_mfma_f32_16x16x32_bf16 v[180:183], a[120:123], a[4:7], v[180:183]\n"
"ds_read_b64_tr_b16 a[86:87], v117 offset:8448\n"
"v_mfma_f32_16x16x32_bf16 v[188:191], a[124:127], a[4:7], v[188:191]\n"
"ds_read_b64_tr_b16 a[88:89], v118 offset:256\n"
"v_mfma_f32_16x16x32_bf16 v[184:187], a[124:127], a[0:3], v[184:187]\n"
"ds_read_b64_tr_b16 a[90:91], v118 offset:8448\n"
"s_waitcnt lgkmcnt(6)\n"
"v_mfma_f32_16x16x32_bf16 v[192:195], a[64:67], a[0:3], v[192:195]\n"
"ds_read_b64_tr_b16 a[92:93], v119 offset:256\n"
"buffer_load_dwordx4 v7, s[4:7], s79 offen offset:3072 lds\n"
"v_mfma_f32_16x16x32_bf16 v[196:199], a[64:67], a[4:7], v[196:199]\n"
"ds_read_b64_tr_b16 a[94:95], v119 offset:8448\n"
"v_mfma_f32_16x16x32_bf16 v[204:207], a[68:71], a[4:7], v[204:207]\n"
"ds_read_b64_tr_b16 a[96:97], v112 offset:16384\n"
"v_mfma_f32_16x16x32_bf16 v[200:203], a[68:71], a[0:3], v[200:203]\n"
"ds_read_b64_tr_b16 a[98:99], v112 offset:24576\n"
"v_mfma_f32_16x16x32_bf16 v[208:211], a[72:75], a[0:3], v[208:211]\n"
"ds_read_b64_tr_b16 a[100:101], v113 offset:16384\n"
"v_mfma_f32_16x16x32_bf16 v[212:215], a[72:75], a[4:7], v[212:215]\n"
"ds_read_b64_tr_b16 a[102:103], v113 offset:24576\n"
"s_add_u32 m0, s74, 102400\n"
"v_mfma_f32_16x16x32_bf16 v[220:223], a[76:79], a[4:7], v[220:223]\n"
"ds_read_b64_tr_b16 a[104:105], v114 offset:16384\n"
"buffer_load_dwordx4 v16, s[4:7], s79 offen offset:0 lds\n"
"v_mfma_f32_16x16x32_bf16 v[216:219], a[76:79], a[0:3], v[216:219]\n"
"ds_read_b64_tr_b16 a[106:107], v114 offset:24576\n"
"s_waitcnt lgkmcnt(6)\n"
"v_mfma_f32_16x16x32_bf16 v[224:227], a[80:83], a[0:3], v[224:227]\n"
"ds_read_b64_tr_b16 a[108:109], v115 offset:16384\n"
"v_mfma_f32_16x16x32_bf16 v[228:231], a[80:83], a[4:7], v[228:231]\n"
"ds_read_b64_tr_b16 a[110:111], v115 offset:24576\n"
"v_mfma_f32_16x16x32_bf16 v[236:239], a[84:87], a[4:7], v[236:239]\n"
"ds_read_b64_tr_b16 a[112:113], v116 offset:16384\n"
"v_mfma_f32_16x16x32_bf16 v[232:235], a[84:87], a[0:3], v[232:235]\n"
"ds_read_b64_tr_b16 a[114:115], v116 offset:24576\n"
"buffer_load_dwordx4 v17, s[4:7], s79 offen offset:1024 lds\n"
"v_mfma_f32_16x16x32_bf16 v[240:243], a[88:91], a[0:3], v[240:243]\n"
"ds_read_b64_tr_b16 a[116:117], v117 offset:16384\n"
"v_mfma_f32_16x16x32_bf16 v[244:247], a[88:91], a[4:7], v[244:247]\n"
"ds_read_b64_tr_b16 a[118:119], v117 offset:24576\n"
"v_mfma_f32_16x16x32_bf16 v[252:255], a[92:95], a[4:7], v[252:255]\n"
"ds_read_b64_tr_b16 a[120:121], v118 offset:16384\n"
"v_mfma_f32_16x16x32_bf16 v[248:251], a[92:95], a[0:3], v[248:251]\n"
"ds_read_b64_tr_b16 a[122:123], v118 offset:24576\n"
"s_waitcnt lgkmcnt(6)\n"
"s_waitcnt vmcnt(23)\n"
"v_mfma_f32_16x16x32_bf16 v[128:131], a[96:99], a[8:11], v[128:131]\n"
"ds_read_b64_tr_b16 a[124:125], v119 offset:16384\n"
"buffer_load_dwordx4 v18, s[4:7], s79 offen offset:2048 lds\n"
"s_waitcnt vmcnt(23)\n"
"v_mfma_f32_16x16x32_bf16 v[132:135], a[96:99], a[12:15], v[132:135]\n"
"ds_read_b64_tr_b16 a[126:127], v119 offset:24576\n"
"v_mfma_f32_16x16x32_bf16 v[140:143], a[100:103], a[12:15], v[140:143]\n"
"ds_read_b64_tr_b16 a[64:65], v112 offset:16640\n"
"v_mfma_f32_16x16x32_bf16 v[136:139], a[100:103], a[8:11], v[136:139]\n"
"ds_read_b64_tr_b16 a[66:67], v112 offset:24832\n"
"v_mfma_f32_16x16x32_bf16 v[144:147], a[104:107], a[8:11], v[144:147]\n"
"ds_read_b64_tr_b16 a[68:69], v113 offset:16640\n"
"v_mfma_f32_16x16x32_bf16 v[148:151], a[104:107], a[12:15], v[148:151]\n"
"ds_read_b64_tr_b16 a[70:71], v113 offset:24832\n"
"buffer_load_dwordx4 v19, s[4:7], s79 offen offset:3072 lds\n"
"v_mfma_f32_16x16x32_bf16 v[156:159], a[108:111], a[12:15], v[156:159]\n"
"ds_read_b64_tr_b16 a[72:73], v114 offset:16640\n"
"v_readlane_b32 s79, v21, 8\n"
"v_mfma_f32_16x16x32_bf16 v[152:155], a[108:111], a[8:11], v[152:155]\n"
"ds_read_b64_tr_b16 a[74:75], v114 offset:24832\n"
"s_waitcnt lgkmcnt(6)\n"
"v_mfma_f32_16x16x32_bf16 v[160:163], a[112:115], a[8:11], v[160:163]\n"
"ds_read_b64_tr_b16 a[76:77], v115 offset:16640\n"
"v_mfma_f32_16x16x32_bf16 v[164:167], a[112:115], a[12:15], v[164:167]\n"
"ds_read_b64_tr_b16 a[78:79], v115 offset:24832\n"
"v_mfma_f32_16x16x32_bf16 v[172:175], a[116:119], a[12:15], v[172:175]\n"
"ds_read_b64_tr_b16 a[80:81], v116 offset:16640\n"
"v_mfma_f32_16x16x32_bf16 v[168:171], a[116:119], a[8:11], v[168:171]\n"
"ds_read_b64_tr_b16 a[82:83], v116 offset:24832\n"
"buffer_load_dwordx4 a[48:51], v8, s[52:55], s82 offen offset:0 sc1\n"
"v_mfma_f32_16x16x32_bf16 v[176:179], a[120:123], a[8:11], v[176:179]\n"
"ds_read_b64_tr_b16 a[84:85], v117 offset:16640\n"
"v_mfma_f32_16x16x32_bf16 v[180:183], a[120:123], a[12:15], v[180:183]\n"
"ds_read_b64_tr_b16 a[86:87], v117 offset:24832\n"
"v_mfma_f32_16x16x32_bf16 v[188:191], a[124:127], a[12:15], v[188:191]\n"
"ds_read_b64_tr_b16 a[88:89], v118 offset:16640\n"
"v_mfma_f32_16x16x32_bf16 v[184:187], a[124:127], a[8:11], v[184:187]\n"
"ds_read_b64_tr_b16 a[90:91], v118 offset:24832\n"
"s_waitcnt lgkmcnt(6)\n"
"v_mfma_f32_16x16x32_bf16 v[192:195], a[64:67], a[8:11], v[192:195]\n"
"ds_read_b64_tr_b16 a[92:93], v119 offset:16640\n"
"buffer_load_dwordx4 a[52:55], v8, s[52:55], s82 offen offset:1024 sc1\n"
"v_mfma_f32_16x16x32_bf16 v[196:199], a[64:67], a[12:15], v[196:199]\n"
"ds_read_b64_tr_b16 a[94:95], v119 offset:24832\n"
"v_readlane_b32 s82, v21, 31\n"
"v_mfma_f32_16x16x32_bf16 v[204:207], a[68:71], a[12:15], v[204:207]\n"
"ds_read_b64_tr_b16 a[96:97], v112 offset:32768\n"
"v_mfma_f32_16x16x32_bf16 v[200:203], a[68:71], a[8:11], v[200:203]\n"
"ds_read_b64_tr_b16 a[98:99], v112 offset:40960\n"
"v_mfma_f32_16x16x32_bf16 v[208:211], a[72:75], a[8:11], v[208:211]\n"
"ds_read_b64_tr_b16 a[100:101], v113 offset:32768\n"
"v_mfma_f32_16x16x32_bf16 v[212:215], a[72:75], a[12:15], v[212:215]\n"
"ds_read_b64_tr_b16 a[102:103], v113 offset:40960\n"
"buffer_load_dwordx4 a[56:59], v8, s[52:55], s82 offen offset:0 sc1\n"
"v_mfma_f32_16x16x32_bf16 v[220:223], a[76:79], a[12:15], v[220:223]\n"
"ds_read_b64_tr_b16 a[104:105], v114 offset:32768\n"
"v_mfma_f32_16x16x32_bf16 v[216:219], a[76:79], a[8:11], v[216:219]\n"
"ds_read_b64_tr_b16 a[106:107], v114 offset:40960\n"
"s_waitcnt lgkmcnt(6)\n"
"v_mfma_f32_16x16x32_bf16 v[224:227], a[80:83], a[8:11], v[224:227]\n"
"ds_read_b64_tr_b16 a[108:109], v115 offset:32768\n"
"v_mfma_f32_16x16x32_bf16 v[228:231], a[80:83], a[12:15], v[228:231]\n"
"ds_read_b64_tr_b16 a[110:111], v115 offset:40960\n"
"v_mfma_f32_16x16x32_bf16 v[236:239], a[84:87], a[12:15], v[236:239]\n"
"ds_read_b64_tr_b16 a[112:113], v116 offset:32768\n"
"buffer_load_dwordx4 a[60:63], v8, s[52:55], s82 offen offset:1024 sc1\n"
"v_mfma_f32_16x16x32_bf16 v[232:235], a[84:87], a[8:11], v[232:235]\n"
"ds_read_b64_tr_b16 a[114:115], v116 offset:40960\n"
"v_readlane_b32 s82, v21, 32\n"
"v_mfma_f32_16x16x32_bf16 v[240:243], a[88:91], a[8:11], v[240:243]\n"
"ds_read_b64_tr_b16 a[116:117], v117 offset:32768\n"
"v_mfma_f32_16x16x32_bf16 v[244:247], a[88:91], a[12:15], v[244:247]\n"
"ds_read_b64_tr_b16 a[118:119], v117 offset:40960\n"
"v_mfma_f32_16x16x32_bf16 v[252:255], a[92:95], a[12:15], v[252:255]\n"
"ds_read_b64_tr_b16 a[120:121], v118 offset:32768\n"
"v_mfma_f32_16x16x32_bf16 v[248:251], a[92:95], a[8:11], v[248:251]\n"
"ds_read_b64_tr_b16 a[122:123], v118 offset:40960\n"
"s_waitcnt vmcnt(16)\n"
"s_barrier\n"
"s_waitcnt lgkmcnt(6)\n"
"v_mfma_f32_16x16x32_bf16 v[128:131], a[96:99], a[16:19], v[128:131]\n"
"ds_read_b64_tr_b16 a[124:125], v119 offset:32768\n"
"s_add_u32 m0, s74, 0\n"
"v_mfma_f32_16x16x32_bf16 v[132:135], a[96:99], a[20:23], v[132:135]\n"
"ds_read_b64_tr_b16 a[126:127], v119 offset:40960\n"
"buffer_load_dwordx4 v4, s[4:7], s79 offen offset:0 lds\n"
"v_mfma_f32_16x16x32_bf16 v[140:143], a[100:103], a[20:23], v[140:143]\n"
"ds_read_b64_tr_b16 a[64:65], v112 offset:33024\n"
"v_mfma_f32_16x16x32_bf16 v[136:139], a[100:103], a[16:19], v[136:139]\n"
"ds_read_b64_tr_b16 a[66:67], v112 offset:41216\n"
"v_mfma_f32_16x16x32_bf16 v[144:147], a[104:107], a[16:19], v[144:147]\n"
"ds_read_b64_tr_b16 a[68:69], v113 offset:33024\n"
"v_mfma_f32_16x16x32_bf16 v[148:151], a[104:107], a[20:23], v[148:151]\n"
"ds_read_b64_tr_b16 a[70:71], v113 offset:41216\n"
"v_mfma_f32_16x16x32_bf16 v[156:159], a[108:111], a[20:23], v[156:159]\n"
"ds_read_b64_tr_b16 a[72:73], v114 offset:33024\n"
"buffer_load_dwordx4 v5, s[4:7], s79 offen offset:1024 lds\n"
"v_mfma_f32_16x16x32_bf16 v[152:155], a[108:111], a[16:19], v[152:155]\n"
"ds_read_b64_tr_b16 a[74:75], v114 offset:41216\n"
"s_waitcnt lgkmcnt(6)\n"
"v_mfma_f32_16x16x32_bf16 v[160:163], a[112:115], a[16:19], v[160:163]\n"
"ds_read_b64_tr_b16 a[76:77], v115 offset:33024\n"
"v_mfma_f32_16x16x32_bf16 v[164:167], a[112:115], a[20:23], v[164:167]\n"
"ds_read_b64_tr_b16 a[78:79], v115 offset:41216\n"
"v_mfma_f32_16x16x32_bf16 v[172:175], a[116:119], a[20:23], v[172:175]\n"
"ds_read_b64_tr_b16 a[80:81], v116 offset:33024\n"
"v_mfma_f32_16x16x32_bf16 v[168:171], a[116:119], a[16:19], v[168:171]\n"
"ds_read_b64_tr_b16 a[82:83], v116 offset:41216\n"
"buffer_load_dwordx4 v6, s[4:7], s79 offen offset:2048 lds\n"
"v_mfma_f32_16x16x32_bf16 v[176:179], a[120:123], a[16:19], v[176:179]\n"
"ds_read_b64_tr_b16 a[84:85], v117 offset:33024\n"
"v_mfma_f32_16x16x32_bf16 v[180:183], a[120:123], a[20:23], v[180:183]\n"
"ds_read_b64_tr_b16 a[86:87], v117 offset:41216\n"
"v_mfma_f32_16x16x32_bf16 v[188:191], a[124:127], a[20:23], v[188:191]\n"
"ds_read_b64_tr_b16 a[88:89], v118 offset:33024\n"
"v_mfma_f32_16x16x32_bf16 v[184:187], a[124:127], a[16:19], v[184:187]\n"
"ds_read_b64_tr_b16 a[90:91], v118 offset:41216\n"
"s_waitcnt lgkmcnt(6)\n"
"v_mfma_f32_16x16x32_bf16 v[192:195], a[64:67], a[16:19], v[192:195]\n"
"ds_read_b64_tr_b16 a[92:93], v119 offset:33024\n"
"buffer_load_dwordx4 v7, s[4:7], s79 offen offset:3072 lds\n"
"v_mfma_f32_16x16x32_bf16 v[196:199], a[64:67], a[20:23], v[196:199]\n"
"ds_read_b64_tr_b16 a[94:95], v119 offset:41216\n"
"v_mfma_f32_16x16x32_bf16 v[204:207], a[68:71], a[20:23], v[204:207]\n"
"ds_read_b64_tr_b16 a[96:97], v112 offset:49152\n"
"v_mfma_f32_16x16x32_bf16 v[200:203], a[68:71], a[16:19], v[200:203]\n"
"ds_read_b64_tr_b16 a[98:99], v112 offset:57344\n"
"v_mfma_f32_16x16x32_bf16 v[208:211], a[72:75], a[16:19], v[208:211]\n"
"ds_read_b64_tr_b16 a[100:101], v113 offset:49152\n"
"v_mfma_f32_16x16x32_bf16 v[212:215], a[72:75], a[20:23], v[212:215]\n"
"ds_read_b64_tr_b16 a[102:103], v113 offset:57344\n"
"s_add_u32 m0, s74, 4096\n"
"v_mfma_f32_16x16x32_bf16 v[220:223], a[76:79], a[20:23], v[220:223]\n"
"ds_read_b64_tr_b16 a[104:105], v114 offset:49152\n"
"buffer_load_dwordx4 v16, s[4:7], s79 offen offset:0 lds\n"
"v_mfma_f32_16x16x32_bf16 v[216:219], a[76:79], a[16:19], v[216:219]\n"
"ds_read_b64_tr_b16 a[106:107], v114 offset:57344\n"
"s_waitcnt lgkmcnt(6)\n"
"v_mfma_f32_16x16x32_bf16 v[224:227], a[80:83], a[16:19], v[224:227]\n"
"ds_read_b64_tr_b16 a[108:109], v115 offset:49152\n"
"v_mfma_f32_16x16x32_bf16 v[228:231], a[80:83], a[20:23], v[228:231]\n"
"ds_read_b64_tr_b16 a[110:111], v115 offset:57344\n"
"v_mfma_f32_16x16x32_bf16 v[236:239], a[84:87], a[20:23], v[236:239]\n"
"ds_read_b64_tr_b16 a[112:113], v116 offset:49152\n"
"v_mfma_f32_16x16x32_bf16 v[232:235], a[84:87], a[16:19], v[232:235]\n"
"ds_read_b64_tr_b16 a[114:115], v116 offset:57344\n"
"buffer_load_dwordx4 v17, s[4:7], s79 offen offset:1024 lds\n"
"v_mfma_f32_16x16x32_bf16 v[240:243], a[88:91], a[16:19], v[240:243]\n"
"ds_read_b64_tr_b16 a[116:117], v117 offset:49152\n"
"v_mfma_f32_16x16x32_bf16 v[244:247], a[88:91], a[20:23], v[244:247]\n"
"ds_read_b64_tr_b16 a[118:119], v117 offset:57344\n"
"v_mfma_f32_16x16x32_bf16 v[252:255], a[92:95], a[20:23], v[252:255]\n"
"ds_read_b64_tr_b16 a[120:121], v118 offset:49152\n"
"v_mfma_f32_16x16x32_bf16 v[248:251], a[92:95], a[16:19], v[248:251]\n"
"ds_read_b64_tr_b16 a[122:123], v118 offset:57344\n"
"s_waitcnt lgkmcnt(6)\n"
"v_mfma_f32_16x16x32_bf16 v[128:131], a[96:99], a[24:27], v[128:131]\n"
"ds_read_b64_tr_b16 a[124:125], v119 offset:49152\n"
"buffer_load_dwordx4 v18, s[4:7], s79 offen offset:2048 lds\n"
"v_mfma_f32_16x16x32_bf16 v[132:135], a[96:99], a[28:31], v[132:135]\n"
"ds_read_b64_tr_b16 a[126:127], v119 offset:57344\n"
"v_mfma_f32_16x16x32_bf16 v[140:143], a[100:103], a[28:31], v[140:143]\n"
"ds_read_b64_tr_b16 a[64:65], v112 offset:49408\n"
"v_mfma_f32_16x16x32_bf16 v[136:139], a[100:103], a[24:27], v[136:139]\n"
"ds_read_b64_tr_b16 a[66:67], v112 offset:57600\n"
"v_mfma_f32_16x16x32_bf16 v[144:147], a[104:107], a[24:27], v[144:147]\n"
"ds_read_b64_tr_b16 a[68:69], v113 offset:49408\n"
"v_mfma_f32_16x16x32_bf16 v[148:151], a[104:107], a[28:31], v[148:151]\n"
"ds_read_b64_tr_b16 a[70:71], v113 offset:57600\n"
"buffer_load_dwordx4 v19, s[4:7], s79 offen offset:3072 lds\n"
"v_mfma_f32_16x16x32_bf16 v[156:159], a[108:111], a[28:31], v[156:159]\n"
"ds_read_b64_tr_b16 a[72:73], v114 offset:49408\n"
"v_readlane_b32 s79, v21, 9\n"
"v_mfma_f32_16x16x32_bf16 v[152:155], a[108:111], a[24:27], v[152:155]\n"
"ds_read_b64_tr_b16 a[74:75], v114 offset:57600\n"
"s_waitcnt lgkmcnt(6)\n"
"v_mfma_f32_16x16x32_bf16 v[160:163], a[112:115], a[24:27], v[160:163]\n"
"ds_read_b64_tr_b16 a[76:77], v115 offset:49408\n"
"v_mfma_f32_16x16x32_bf16 v[164:167], a[112:115], a[28:31], v[164:167]\n"
"ds_read_b64_tr_b16 a[78:79], v115 offset:57600\n"
"v_mfma_f32_16x16x32_bf16 v[172:175], a[116:119], a[28:31], v[172:175]\n"
"ds_read_b64_tr_b16 a[80:81], v116 offset:49408\n"
"v_mfma_f32_16x16x32_bf16 v[168:171], a[116:119], a[24:27], v[168:171]\n"
"ds_read_b64_tr_b16 a[82:83], v116 offset:57600\n"
"buffer_load_dwordx4 a[0:3], v8, s[52:55], s82 offen offset:0 sc1\n"
"v_mfma_f32_16x16x32_bf16 v[176:179], a[120:123], a[24:27], v[176:179]\n"
"ds_read_b64_tr_b16 a[84:85], v117 offset:49408\n"
"v_mfma_f32_16x16x32_bf16 v[180:183], a[120:123], a[28:31], v[180:183]\n"
"ds_read_b64_tr_b16 a[86:87], v117 offset:57600\n"
"v_mfma_f32_16x16x32_bf16 v[188:191], a[124:127], a[28:31], v[188:191]\n"
"ds_read_b64_tr_b16 a[88:89], v118 offset:49408\n"
"v_mfma_f32_16x16x32_bf16 v[184:187], a[124:127], a[24:27], v[184:187]\n"
"ds_read_b64_tr_b16 a[90:91], v118 offset:57600\n"
"s_waitcnt lgkmcnt(6)\n"
"v_mfma_f32_16x16x32_bf16 v[192:195], a[64:67], a[24:27], v[192:195]\n"
"ds_read_b64_tr_b16 a[92:93], v119 offset:49408\n"
"buffer_load_dwordx4 a[4:7], v8, s[52:55], s82 offen offset:1024 sc1\n"
"v_mfma_f32_16x16x32_bf16 v[196:199], a[64:67], a[28:31], v[196:199]\n"
"ds_read_b64_tr_b16 a[94:95], v119 offset:57600\n"
"v_readlane_b32 s82, v21, 33\n"
"v_mfma_f32_16x16x32_bf16 v[204:207], a[68:71], a[28:31], v[204:207]\n"
"ds_read_b64_tr_b16 a[96:97], v120 offset:0\n"
"v_mfma_f32_16x16x32_bf16 v[200:203], a[68:71], a[24:27], v[200:203]\n"
"ds_read_b64_tr_b16 a[98:99], v120 offset:8192\n"
"v_mfma_f32_16x16x32_bf16 v[208:211], a[72:75], a[24:27], v[208:211]\n"
"ds_read_b64_tr_b16 a[100:101], v121 offset:0\n"
"v_mfma_f32_16x16x32_bf16 v[212:215], a[72:75], a[28:31], v[212:215]\n"
"ds_read_b64_tr_b16 a[102:103], v121 offset:8192\n"
"buffer_load_dwordx4 a[8:11], v8, s[52:55], s82 offen offset:0 sc1\n"
"v_mfma_f32_16x16x32_bf16 v[220:223], a[76:79], a[28:31], v[220:223]\n"
"ds_read_b64_tr_b16 a[104:105], v122 offset:0\n"
"v_mfma_f32_16x16x32_bf16 v[216:219], a[76:79], a[24:27], v[216:219]\n"
"ds_read_b64_tr_b16 a[106:107], v122 offset:8192\n"
"s_waitcnt lgkmcnt(6)\n"
"v_mfma_f32_16x16x32_bf16 v[224:227], a[80:83], a[24:27], v[224:227]\n"
"ds_read_b64_tr_b16 a[108:109], v123 offset:0\n"
"v_mfma_f32_16x16x32_bf16 v[228:231], a[80:83], a[28:31], v[228:231]\n"
"ds_read_b64_tr_b16 a[110:111], v123 offset:8192\n"
"v_mfma_f32_16x16x32_bf16 v[236:239], a[84:87], a[28:31], v[236:239]\n"
"ds_read_b64_tr_b16 a[112:113], v124 offset:0\n"
"buffer_load_dwordx4 a[12:15], v8, s[52:55], s82 offen offset:1024 sc1\n"
"v_mfma_f32_16x16x32_bf16 v[232:235], a[84:87], a[24:27], v[232:235]\n"
"ds_read_b64_tr_b16 a[114:115], v124 offset:8192\n"
"v_readlane_b32 s82, v21, 34\n"
"v_mfma_f32_16x16x32_bf16 v[240:243], a[88:91], a[24:27], v[240:243]\n"
"ds_read_b64_tr_b16 a[116:117], v125 offset:0\n"
"v_mfma_f32_16x16x32_bf16 v[244:247], a[88:91], a[28:31], v[244:247]\n"
"ds_read_b64_tr_b16 a[118:119], v125 offset:8192\n"
"v_mfma_f32_16x16x32_bf16 v[252:255], a[92:95], a[28:31], v[252:255]\n"
"ds_read_b64_tr_b16 a[120:121], v126 offset:0\n"
"v_mfma_f32_16x16x32_bf16 v[248:251], a[92:95], a[24:27], v[248:251]\n"
"ds_read_b64_tr_b16 a[122:123], v126 offset:8192\n"
"s_waitcnt vmcnt(16)\n"
"s_barrier\n"
"s_waitcnt lgkmcnt(6)\n"
"v_mfma_f32_16x16x32_bf16 v[128:131], a[96:99], a[32:35], v[128:131]\n"
"ds_read_b64_tr_b16 a[124:125], v127 offset:0\n"
"s_add_u32 m0, s74, 32768\n"
"v_mfma_f32_16x16x32_bf16 v[132:135], a[96:99], a[36:39], v[132:135]\n"
"ds_read_b64_tr_b16 a[126:127], v127 offset:8192\n"
"buffer_load_dwordx4 v4, s[4:7], s79 offen offset:0 lds\n"
"v_mfma_f32_16x16x32_bf16 v[140:143], a[100:103], a[36:39], v[140:143]\n"
"ds_read_b64_tr_b16 a[64:65], v120 offset:256\n"
"v_mfma_f32_16x16x32_bf16 v[136:139], a[100:103], a[32:35], v[136:139]\n"
"ds_read_b64_tr_b16 a[66:67], v120 offset:8448\n"
"v_mfma_f32_16x16x32_bf16 v[144:147], a[104:107], a[32:35], v[144:147]\n"
"ds_read_b64_tr_b16 a[68:69], v121 offset:256\n"
"v_mfma_f32_16x16x32_bf16 v[148:151], a[104:107], a[36:39], v[148:151]\n"
"ds_read_b64_tr_b16 a[70:71], v121 offset:8448\n"
"v_mfma_f32_16x16x32_bf16 v[156:159], a[108:111], a[36:39], v[156:159]\n"
"ds_read_b64_tr_b16 a[72:73], v122 offset:256\n"
"buffer_load_dwordx4 v5, s[4:7], s79 offen offset:1024 lds\n"
"v_mfma_f32_16x16x32_bf16 v[152:155], a[108:111], a[32:35], v[152:155]\n"
"ds_read_b64_tr_b16 a[74:75], v122 offset:8448\n"
"s_waitcnt lgkmcnt(6)\n"
"v_mfma_f32_16x16x32_bf16 v[160:163], a[112:115], a[32:35], v[160:163]\n"
"ds_read_b64_tr_b16 a[76:77], v123 offset:256\n"
"v_mfma_f32_16x16x32_bf16 v[164:167], a[112:115], a[36:39], v[164:167]\n"
"ds_read_b64_tr_b16 a[78:79], v123 offset:8448\n"
"v_mfma_f32_16x16x32_bf16 v[172:175], a[116:119], a[36:39], v[172:175]\n"
"ds_read_b64_tr_b16 a[80:81], v124 offset:256\n"
"v_mfma_f32_16x16x32_bf16 v[168:171], a[116:119], a[32:35], v[168:171]\n"
"ds_read_b64_tr_b16 a[82:83], v124 offset:8448\n"
"buffer_load_dwordx4 v6, s[4:7], s79 offen offset:2048 lds\n"
"v_mfma_f32_16x16x32_bf16 v[176:179], a[120:123], a[32:35], v[176:179]\n"
"ds_read_b64_tr_b16 a[84:85], v125 offset:256\n"
"v_mfma_f32_16x16x32_bf16 v[180:183], a[120:123], a[36:39], v[180:183]\n"
"ds_read_b64_tr_b16 a[86:87], v125 offset:8448\n"
"v_mfma_f32_16x16x32_bf16 v[188:191], a[124:127], a[36:39], v[188:191]\n"
"ds_read_b64_tr_b16 a[88:89], v126 offset:256\n"
"v_mfma_f32_16x16x32_bf16 v[184:187], a[124:127], a[32:35], v[184:187]\n"
"ds_read_b64_tr_b16 a[90:91], v126 offset:8448\n"
"s_waitcnt lgkmcnt(6)\n"
"v_mfma_f32_16x16x32_bf16 v[192:195], a[64:67], a[32:35], v[192:195]\n"
"ds_read_b64_tr_b16 a[92:93], v127 offset:256\n"
"buffer_load_dwordx4 v7, s[4:7], s79 offen offset:3072 lds\n"
"v_mfma_f32_16x16x32_bf16 v[196:199], a[64:67], a[36:39], v[196:199]\n"
"ds_read_b64_tr_b16 a[94:95], v127 offset:8448\n"
"v_mfma_f32_16x16x32_bf16 v[204:207], a[68:71], a[36:39], v[204:207]\n"
"ds_read_b64_tr_b16 a[96:97], v120 offset:16384\n"
"v_mfma_f32_16x16x32_bf16 v[200:203], a[68:71], a[32:35], v[200:203]\n"
"ds_read_b64_tr_b16 a[98:99], v120 offset:24576\n"
"v_mfma_f32_16x16x32_bf16 v[208:211], a[72:75], a[32:35], v[208:211]\n"
"ds_read_b64_tr_b16 a[100:101], v121 offset:16384\n"
"v_mfma_f32_16x16x32_bf16 v[212:215], a[72:75], a[36:39], v[212:215]\n"
"ds_read_b64_tr_b16 a[102:103], v121 offset:24576\n"
"s_add_u32 m0, s74, 36864\n"
"v_mfma_f32_16x16x32_bf16 v[220:223], a[76:79], a[36:39], v[220:223]\n"
"ds_read_b64_tr_b16 a[104:105], v122 offset:16384\n"
"buffer_load_dwordx4 v16, s[4:7], s79 offen offset:0 lds\n"
"v_mfma_f32_16x16x32_bf16 v[216:219], a[76:79], a[32:35], v[216:219]\n"
"ds_read_b64_tr_b16 a[106:107], v122 offset:24576\n"
"s_waitcnt lgkmcnt(6)\n"
"v_mfma_f32_16x16x32_bf16 v[224:227], a[80:83], a[32:35], v[224:227]\n"
"ds_read_b64_tr_b16 a[108:109], v123 offset:16384\n"
"v_mfma_f32_16x16x32_bf16 v[228:231], a[80:83], a[36:39], v[228:231]\n"
"ds_read_b64_tr_b16 a[110:111], v123 offset:24576\n"
"v_mfma_f32_16x16x32_bf16 v[236:239], a[84:87], a[36:39], v[236:239]\n"
"ds_read_b64_tr_b16 a[112:113], v124 offset:16384\n"
"v_mfma_f32_16x16x32_bf16 v[232:235], a[84:87], a[32:35], v[232:235]\n"
"ds_read_b64_tr_b16 a[114:115], v124 offset:24576\n"
"buffer_load_dwordx4 v17, s[4:7], s79 offen offset:1024 lds\n"
"v_mfma_f32_16x16x32_bf16 v[240:243], a[88:91], a[32:35], v[240:243]\n"
"ds_read_b64_tr_b16 a[116:117], v125 offset:16384\n"
"v_mfma_f32_16x16x32_bf16 v[244:247], a[88:91], a[36:39], v[244:247]\n"
"ds_read_b64_tr_b16 a[118:119], v125 offset:24576\n"
"v_mfma_f32_16x16x32_bf16 v[252:255], a[92:95], a[36:39], v[252:255]\n"
"ds_read_b64_tr_b16 a[120:121], v126 offset:16384\n"
"v_mfma_f32_16x16x32_bf16 v[248:251], a[92:95], a[32:35], v[248:251]\n"
"ds_read_b64_tr_b16 a[122:123], v126 offset:24576\n"
"s_waitcnt lgkmcnt(6)\n"
"v_mfma_f32_16x16x32_bf16 v[128:131], a[96:99], a[40:43], v[128:131]\n"
"ds_read_b64_tr_b16 a[124:125], v127 offset:16384\n"
"buffer_load_dwordx4 v18, s[4:7], s79 offen offset:2048 lds\n"
"v_mfma_f32_16x16x32_bf16 v[132:135], a[96:99], a[44:47], v[132:135]\n"
"ds_read_b64_tr_b16 a[126:127], v127 offset:24576\n"
"v_mfma_f32_16x16x32_bf16 v[140:143], a[100:103], a[44:47], v[140:143]\n"
"ds_read_b64_tr_b16 a[64:65], v120 offset:16640\n"
"v_mfma_f32_16x16x32_bf16 v[136:139], a[100:103], a[40:43], v[136:139]\n"
"ds_read_b64_tr_b16 a[66:67], v120 offset:24832\n"
"v_mfma_f32_16x16x32_bf16 v[144:147], a[104:107], a[40:43], v[144:147]\n"
"ds_read_b64_tr_b16 a[68:69], v121 offset:16640\n"
"v_mfma_f32_16x16x32_bf16 v[148:151], a[104:107], a[44:47], v[148:151]\n"
"ds_read_b64_tr_b16 a[70:71], v121 offset:24832\n"
"buffer_load_dwordx4 v19, s[4:7], s79 offen offset:3072 lds\n"
"v_mfma_f32_16x16x32_bf16 v[156:159], a[108:111], a[44:47], v[156:159]\n"
"ds_read_b64_tr_b16 a[72:73], v122 offset:16640\n"
"v_readlane_b32 s79, v21, 10\n"
"v_mfma_f32_16x16x32_bf16 v[152:155], a[108:111], a[40:43], v[152:155]\n"
"ds_read_b64_tr_b16 a[74:75], v122 offset:24832\n"
"s_waitcnt lgkmcnt(6)\n"
"v_mfma_f32_16x16x32_bf16 v[160:163], a[112:115], a[40:43], v[160:163]\n"
"ds_read_b64_tr_b16 a[76:77], v123 offset:16640\n"
"v_mfma_f32_16x16x32_bf16 v[164:167], a[112:115], a[44:47], v[164:167]\n"
"ds_read_b64_tr_b16 a[78:79], v123 offset:24832\n"
"v_mfma_f32_16x16x32_bf16 v[172:175], a[116:119], a[44:47], v[172:175]\n"
"ds_read_b64_tr_b16 a[80:81], v124 offset:16640\n"
"v_mfma_f32_16x16x32_bf16 v[168:171], a[116:119], a[40:43], v[168:171]\n"
"ds_read_b64_tr_b16 a[82:83], v124 offset:24832\n"
"buffer_load_dwordx4 a[16:19], v8, s[52:55], s82 offen offset:0 sc1\n"
"v_mfma_f32_16x16x32_bf16 v[176:179], a[120:123], a[40:43], v[176:179]\n"
"ds_read_b64_tr_b16 a[84:85], v125 offset:16640\n"
"v_mfma_f32_16x16x32_bf16 v[180:183], a[120:123], a[44:47], v[180:183]\n"
"ds_read_b64_tr_b16 a[86:87], v125 offset:24832\n"
"v_mfma_f32_16x16x32_bf16 v[188:191], a[124:127], a[44:47], v[188:191]\n"
"ds_read_b64_tr_b16 a[88:89], v126 offset:16640\n"
"v_mfma_f32_16x16x32_bf16 v[184:187], a[124:127], a[40:43], v[184:187]\n"
"ds_read_b64_tr_b16 a[90:91], v126 offset:24832\n"
"s_waitcnt lgkmcnt(6)\n"
"v_mfma_f32_16x16x32_bf16 v[192:195], a[64:67], a[40:43], v[192:195]\n"
"ds_read_b64_tr_b16 a[92:93], v127 offset:16640\n"
"buffer_load_dwordx4 a[20:23], v8, s[52:55], s82 offen offset:1024 sc1\n"
"v_mfma_f32_16x16x32_bf16 v[196:199], a[64:67], a[44:47], v[196:199]\n"
"ds_read_b64_tr_b16 a[94:95], v127 offset:24832\n"
"v_readlane_b32 s82, v21, 35\n"
"v_mfma_f32_16x16x32_bf16 v[204:207], a[68:71], a[44:47], v[204:207]\n"
"ds_read_b64_tr_b16 a[96:97], v120 offset:32768\n"
"v_mfma_f32_16x16x32_bf16 v[200:203], a[68:71], a[40:43], v[200:203]\n"
"ds_read_b64_tr_b16 a[98:99], v120 offset:40960\n"
"v_mfma_f32_16x16x32_bf16 v[208:211], a[72:75], a[40:43], v[208:211]\n"
"ds_read_b64_tr_b16 a[100:101], v121 offset:32768\n"
"v_mfma_f32_16x16x32_bf16 v[212:215], a[72:75], a[44:47], v[212:215]\n"
"ds_read_b64_tr_b16 a[102:103], v121 offset:40960\n"
"buffer_load_dwordx4 a[24:27], v8, s[52:55], s82 offen offset:0 sc1\n"
"v_mfma_f32_16x16x32_bf16 v[220:223], a[76:79], a[44:47], v[220:223]\n"
"ds_read_b64_tr_b16 a[104:105], v122 offset:32768\n"
"v_mfma_f32_16x16x32_bf16 v[216:219], a[76:79], a[40:43], v[216:219]\n"
"ds_read_b64_tr_b16 a[106:107], v122 offset:40960\n"
"s_waitcnt lgkmcnt(6)\n"
"v_mfma_f32_16x16x32_bf16 v[224:227], a[80:83], a[40:43], v[224:227]\n"
"ds_read_b64_tr_b16 a[108:109], v123 offset:32768\n"
"v_mfma_f32_16x16x32_bf16 v[228:231], a[80:83], a[44:47], v[228:231]\n"
"ds_read_b64_tr_b16 a[110:111], v123 offset:40960\n"
"v_mfma_f32_16x16x32_bf16 v[236:239], a[84:87], a[44:47], v[236:239]\n"
"ds_read_b64_tr_b16 a[112:113], v124 offset:32768\n"
"buffer_load_dwordx4 a[28:31], v8, s[52:55], s82 offen offset:1024 sc1\n"
"v_mfma_f32_16x16x32_bf16 v[232:235], a[84:87], a[40:43], v[232:235]\n"
"ds_read_b64_tr_b16 a[114:115], v124 offset:40960\n"
"v_readlane_b32 s82, v21, 36\n"
"v_mfma_f32_16x16x32_bf16 v[240:243], a[88:91], a[40:43], v[240:243]\n"
"ds_read_b64_tr_b16 a[116:117], v125 offset:32768\n"
"v_mfma_f32_16x16x32_bf16 v[244:247], a[88:91], a[44:47], v[244:247]\n"
"ds_read_b64_tr_b16 a[118:119], v125 offset:40960\n"
"v_mfma_f32_16x16x32_bf16 v[252:255], a[92:95], a[44:47], v[252:255]\n"
"ds_read_b64_tr_b16 a[120:121], v126 offset:32768\n"
"v_mfma_f32_16x16x32_bf16 v[248:251], a[92:95], a[40:43], v[248:251]\n"
"ds_read_b64_tr_b16 a[122:123], v126 offset:40960\n"
"s_waitcnt vmcnt(16)\n"
"s_barrier\n"
"s_waitcnt lgkmcnt(6)\n"
"v_mfma_f32_16x16x32_bf16 v[128:131], a[96:99], a[48:51], v[128:131]\n"
"ds_read_b64_tr_b16 a[124:125], v127 offset:32768\n"
"s_add_u32 m0, s74, 65536\n"
"v_mfma_f32_16x16x32_bf16 v[132:135], a[96:99], a[52:55], v[132:135]\n"
"ds_read_b64_tr_b16 a[126:127], v127 offset:40960\n"
"buffer_load_dwordx4 v4, s[4:7], s79 offen offset:0 lds\n"
"v_mfma_f32_16x16x32_bf16 v[140:143], a[100:103], a[52:55], v[140:143]\n"
"ds_read_b64_tr_b16 a[64:65], v120 offset:33024\n"
"v_mfma_f32_16x16x32_bf16 v[136:139], a[100:103], a[48:51], v[136:139]\n"
"ds_read_b64_tr_b16 a[66:67], v120 offset:41216\n"
"v_mfma_f32_16x16x32_bf16 v[144:147], a[104:107], a[48:51], v[144:147]\n"
"ds_read_b64_tr_b16 a[68:69], v121 offset:33024\n"
"v_mfma_f32_16x16x32_bf16 v[148:151], a[104:107], a[52:55], v[148:151]\n"
"ds_read_b64_tr_b16 a[70:71], v121 offset:41216\n"
"v_mfma_f32_16x16x32_bf16 v[156:159], a[108:111], a[52:55], v[156:159]\n"
"ds_read_b64_tr_b16 a[72:73], v122 offset:33024\n"
"buffer_load_dwordx4 v5, s[4:7], s79 offen offset:1024 lds\n"
"v_mfma_f32_16x16x32_bf16 v[152:155], a[108:111], a[48:51], v[152:155]\n"
"ds_read_b64_tr_b16 a[74:75], v122 offset:41216\n"
"s_waitcnt lgkmcnt(6)\n"
"v_mfma_f32_16x16x32_bf16 v[160:163], a[112:115], a[48:51], v[160:163]\n"
"ds_read_b64_tr_b16 a[76:77], v123 offset:33024\n"
"v_mfma_f32_16x16x32_bf16 v[164:167], a[112:115], a[52:55], v[164:167]\n"
"ds_read_b64_tr_b16 a[78:79], v123 offset:41216\n"
"v_mfma_f32_16x16x32_bf16 v[172:175], a[116:119], a[52:55], v[172:175]\n"
"ds_read_b64_tr_b16 a[80:81], v124 offset:33024\n"
"v_mfma_f32_16x16x32_bf16 v[168:171], a[116:119], a[48:51], v[168:171]\n"
"ds_read_b64_tr_b16 a[82:83], v124 offset:41216\n"
"buffer_load_dwordx4 v6, s[4:7], s79 offen offset:2048 lds\n"
"v_mfma_f32_16x16x32_bf16 v[176:179], a[120:123], a[48:51], v[176:179]\n"
"ds_read_b64_tr_b16 a[84:85], v125 offset:33024\n"
"v_mfma_f32_16x16x32_bf16 v[180:183], a[120:123], a[52:55], v[180:183]\n"
"ds_read_b64_tr_b16 a[86:87], v125 offset:41216\n"
"v_mfma_f32_16x16x32_bf16 v[188:191], a[124:127], a[52:55], v[188:191]\n"
"ds_read_b64_tr_b16 a[88:89], v126 offset:33024\n"
"v_mfma_f32_16x16x32_bf16 v[184:187], a[124:127], a[48:51], v[184:187]\n"
"ds_read_b64_tr_b16 a[90:91], v126 offset:41216\n"
"s_waitcnt lgkmcnt(6)\n"
"v_mfma_f32_16x16x32_bf16 v[192:195], a[64:67], a[48:51], v[192:195]\n"
"ds_read_b64_tr_b16 a[92:93], v127 offset:33024\n"
"buffer_load_dwordx4 v7, s[4:7], s79 offen offset:3072 lds\n"
"v_mfma_f32_16x16x32_bf16 v[196:199], a[64:67], a[52:55], v[196:199]\n"
"ds_read_b64_tr_b16 a[94:95], v127 offset:41216\n"
"v_mfma_f32_16x16x32_bf16 v[204:207], a[68:71], a[52:55], v[204:207]\n"
"ds_read_b64_tr_b16 a[96:97], v120 offset:49152\n"
"v_mfma_f32_16x16x32_bf16 v[200:203], a[68:71], a[48:51], v[200:203]\n"
"ds_read_b64_tr_b16 a[98:99], v120 offset:57344\n"
"v_mfma_f32_16x16x32_bf16 v[208:211], a[72:75], a[48:51], v[208:211]\n"
"ds_read_b64_tr_b16 a[100:101], v121 offset:49152\n"
"v_mfma_f32_16x16x32_bf16 v[212:215], a[72:75], a[52:55], v[212:215]\n"
"ds_read_b64_tr_b16 a[102:103], v121 offset:57344\n"
"s_add_u32 m0, s74, 69632\n"
"v_mfma_f32_16x16x32_bf16 v[220:223], a[76:79], a[52:55], v[220:223]\n"
"ds_read_b64_tr_b16 a[104:105], v122 offset:49152\n"
"buffer_load_dwordx4 v16, s[4:7], s79 offen offset:0 lds\n"
"v_mfma_f32_16x16x32_bf16 v[216:219], a[76:79], a[48:51], v[216:219]\n"
"ds_read_b64_tr_b16 a[106:107], v122 offset:57344\n"
"s_waitcnt lgkmcnt(6)\n"
"v_mfma_f32_16x16x32_bf16 v[224:227], a[80:83], a[48:51], v[224:227]\n"
"ds_read_b64_tr_b16 a[108:109], v123 offset:49152\n"
"v_mfma_f32_16x16x32_bf16 v[228:231], a[80:83], a[52:55], v[228:231]\n"
"ds_read_b64_tr_b16 a[110:111], v123 offset:57344\n"
"v_mfma_f32_16x16x32_bf16 v[236:239], a[84:87], a[52:55], v[236:239]\n"
"ds_read_b64_tr_b16 a[112:113], v124 offset:49152\n"
"v_mfma_f32_16x16x32_bf16 v[232:235], a[84:87], a[48:51], v[232:235]\n"
"ds_read_b64_tr_b16 a[114:115], v124 offset:57344\n"
"buffer_load_dwordx4 v17, s[4:7], s79 offen offset:1024 lds\n"
"v_mfma_f32_16x16x32_bf16 v[240:243], a[88:91], a[48:51], v[240:243]\n"
"ds_read_b64_tr_b16 a[116:117], v125 offset:49152\n"
"v_mfma_f32_16x16x32_bf16 v[244:247], a[88:91], a[52:55], v[244:247]\n"
"ds_read_b64_tr_b16 a[118:119], v125 offset:57344\n"
"v_mfma_f32_16x16x32_bf16 v[252:255], a[92:95], a[52:55], v[252:255]\n"
"ds_read_b64_tr_b16 a[120:121], v126 offset:49152\n"
"v_mfma_f32_16x16x32_bf16 v[248:251], a[92:95], a[48:51], v[248:251]\n"
"ds_read_b64_tr_b16 a[122:123], v126 offset:57344\n"
"s_waitcnt lgkmcnt(6)\n"
"v_mfma_f32_16x16x32_bf16 v[128:131], a[96:99], a[56:59], v[128:131]\n"
"ds_read_b64_tr_b16 a[124:125], v127 offset:49152\n"
"buffer_load_dwordx4 v18, s[4:7], s79 offen offset:2048 lds\n"
"v_mfma_f32_16x16x32_bf16 v[132:135], a[96:99], a[60:63], v[132:135]\n"
"ds_read_b64_tr_b16 a[126:127], v127 offset:57344\n"
"v_mfma_f32_16x16x32_bf16 v[140:143], a[100:103], a[60:63], v[140:143]\n"
"ds_read_b64_tr_b16 a[64:65], v120 offset:49408\n"
"v_mfma_f32_16x16x32_bf16 v[136:139], a[100:103], a[56:59], v[136:139]\n"
"ds_read_b64_tr_b16 a[66:67], v120 offset:57600\n"
"v_mfma_f32_16x16x32_bf16 v[144:147], a[104:107], a[56:59], v[144:147]\n"
"ds_read_b64_tr_b16 a[68:69], v121 offset:49408\n"
"v_mfma_f32_16x16x32_bf16 v[148:151], a[104:107], a[60:63], v[148:151]\n"
"ds_read_b64_tr_b16 a[70:71], v121 offset:57600\n"
"buffer_load_dwordx4 v19, s[4:7], s79 offen offset:3072 lds\n"
"v_mfma_f32_16x16x32_bf16 v[156:159], a[108:111], a[60:63], v[156:159]\n"
"ds_read_b64_tr_b16 a[72:73], v122 offset:49408\n"
"v_readlane_b32 s79, v21, 11\n"
"v_mfma_f32_16x16x32_bf16 v[152:155], a[108:111], a[56:59], v[152:155]\n"
"ds_read_b64_tr_b16 a[74:75], v122 offset:57600\n"
"s_waitcnt lgkmcnt(6)\n"
"v_mfma_f32_16x16x32_bf16 v[160:163], a[112:115], a[56:59], v[160:163]\n"
"ds_read_b64_tr_b16 a[76:77], v123 offset:49408\n"
"v_mfma_f32_16x16x32_bf16 v[164:167], a[112:115], a[60:63], v[164:167]\n"
"ds_read_b64_tr_b16 a[78:79], v123 offset:57600\n"
"v_mfma_f32_16x16x32_bf16 v[172:175], a[116:119], a[60:63], v[172:175]\n"
"ds_read_b64_tr_b16 a[80:81], v124 offset:49408\n"
"v_mfma_f32_16x16x32_bf16 v[168:171], a[116:119], a[56:59], v[168:171]\n"
"ds_read_b64_tr_b16 a[82:83], v124 offset:57600\n"
"buffer_load_dwordx4 a[32:35], v8, s[52:55], s82 offen offset:0 sc1\n"
"v_mfma_f32_16x16x32_bf16 v[176:179], a[120:123], a[56:59], v[176:179]\n"
"ds_read_b64_tr_b16 a[84:85], v125 offset:49408\n"
"v_mfma_f32_16x16x32_bf16 v[180:183], a[120:123], a[60:63], v[180:183]\n"
"ds_read_b64_tr_b16 a[86:87], v125 offset:57600\n"
"v_mfma_f32_16x16x32_bf16 v[188:191], a[124:127], a[60:63], v[188:191]\n"
"ds_read_b64_tr_b16 a[88:89], v126 offset:49408\n"
"v_mfma_f32_16x16x32_bf16 v[184:187], a[124:127], a[56:59], v[184:187]\n"
"ds_read_b64_tr_b16 a[90:91], v126 offset:57600\n"
"s_waitcnt lgkmcnt(6)\n"
"v_mfma_f32_16x16x32_bf16 v[192:195], a[64:67], a[56:59], v[192:195]\n"
"ds_read_b64_tr_b16 a[92:93], v127 offset:49408\n"
"buffer_load_dwordx4 a[36:39], v8, s[52:55], s82 offen offset:1024 sc1\n"
"v_mfma_f32_16x16x32_bf16 v[196:199], a[64:67], a[60:63], v[196:199]\n"
"ds_read_b64_tr_b16 a[94:95], v127 offset:57600\n"
"v_readlane_b32 s82, v21, 37\n"
"v_mfma_f32_16x16x32_bf16 v[204:207], a[68:71], a[60:63], v[204:207]\n"
"ds_read_b64_tr_b16 a[96:97], v112 offset:0\n"
"v_mfma_f32_16x16x32_bf16 v[200:203], a[68:71], a[56:59], v[200:203]\n"
"ds_read_b64_tr_b16 a[98:99], v112 offset:8192\n"
"v_mfma_f32_16x16x32_bf16 v[208:211], a[72:75], a[56:59], v[208:211]\n"
"ds_read_b64_tr_b16 a[100:101], v113 offset:0\n"
"v_mfma_f32_16x16x32_bf16 v[212:215], a[72:75], a[60:63], v[212:215]\n"
"ds_read_b64_tr_b16 a[102:103], v113 offset:8192\n"
"buffer_load_dwordx4 a[40:43], v8, s[52:55], s82 offen offset:0 sc1\n"
"v_mfma_f32_16x16x32_bf16 v[220:223], a[76:79], a[60:63], v[220:223]\n"
"ds_read_b64_tr_b16 a[104:105], v114 offset:0\n"
"v_mfma_f32_16x16x32_bf16 v[216:219], a[76:79], a[56:59], v[216:219]\n"
"ds_read_b64_tr_b16 a[106:107], v114 offset:8192\n"
"s_waitcnt lgkmcnt(6)\n"
"v_mfma_f32_16x16x32_bf16 v[224:227], a[80:83], a[56:59], v[224:227]\n"
"ds_read_b64_tr_b16 a[108:109], v115 offset:0\n"
"v_mfma_f32_16x16x32_bf16 v[228:231], a[80:83], a[60:63], v[228:231]\n"
"ds_read_b64_tr_b16 a[110:111], v115 offset:8192\n"
"v_mfma_f32_16x16x32_bf16 v[236:239], a[84:87], a[60:63], v[236:239]\n"
"ds_read_b64_tr_b16 a[112:113], v116 offset:0\n"
"buffer_load_dwordx4 a[44:47], v8, s[52:55], s82 offen offset:1024 sc1\n"
"v_mfma_f32_16x16x32_bf16 v[232:235], a[84:87], a[56:59], v[232:235]\n"
"ds_read_b64_tr_b16 a[114:115], v116 offset:8192\n"
"v_readlane_b32 s82, v21, 38\n"
"v_mfma_f32_16x16x32_bf16 v[240:243], a[88:91], a[56:59], v[240:243]\n"
"ds_read_b64_tr_b16 a[116:117], v117 offset:0\n"
"v_mfma_f32_16x16x32_bf16 v[244:247], a[88:91], a[60:63], v[244:247]\n"
"ds_read_b64_tr_b16 a[118:119], v117 offset:8192\n"
"v_mfma_f32_16x16x32_bf16 v[252:255], a[92:95], a[60:63], v[252:255]\n"
"ds_read_b64_tr_b16 a[120:121], v118 offset:0\n"
"v_mfma_f32_16x16x32_bf16 v[248:251], a[92:95], a[56:59], v[248:251]\n"
"ds_read_b64_tr_b16 a[122:123], v118 offset:8192\n"
"s_waitcnt vmcnt(16)\n"
"s_barrier\n"
"s_waitcnt lgkmcnt(6)\n"
"v_mfma_f32_16x16x32_bf16 v[128:131], a[96:99], a[0:3], v[128:131]\n"
"ds_read_b64_tr_b16 a[124:125], v119 offset:0\n"
"s_add_u32 m0, s74, 98304\n"
"v_mfma_f32_16x16x32_bf16 v[132:135], a[96:99], a[4:7], v[132:135]\n"
"ds_read_b64_tr_b16 a[126:127], v119 offset:8192\n"
"buffer_load_dwordx4 v4, s[4:7], s79 offen offset:0 lds\n"
"v_mfma_f32_16x16x32_bf16 v[140:143], a[100:103], a[4:7], v[140:143]\n"
"ds_read_b64_tr_b16 a[64:65], v112 offset:256\n"
"v_mfma_f32_16x16x32_bf16 v[136:139], a[100:103], a[0:3], v[136:139]\n"
"ds_read_b64_tr_b16 a[66:67], v112 offset:8448\n"
"v_mfma_f32_16x16x32_bf16 v[144:147], a[104:107], a[0:3], v[144:147]\n"
"ds_read_b64_tr_b16 a[68:69], v113 offset:256\n"
"v_mfma_f32_16x16x32_bf16 v[148:151], a[104:107], a[4:7], v[148:151]\n"
"ds_read_b64_tr_b16 a[70:71], v113 offset:8448\n"
"v_mfma_f32_16x16x32_bf16 v[156:159], a[108:111], a[4:7], v[156:159]\n"
"ds_read_b64_tr_b16 a[72:73], v114 offset:256\n"
"buffer_load_dwordx4 v5, s[4:7], s79 offen offset:1024 lds\n"
"v_mfma_f32_16x16x32_bf16 v[152:155], a[108:111], a[0:3], v[152:155]\n"
"ds_read_b64_tr_b16 a[74:75], v114 offset:8448\n"
"s_waitcnt lgkmcnt(6)\n"
"v_mfma_f32_16x16x32_bf16 v[160:163], a[112:115], a[0:3], v[160:163]\n"
"ds_read_b64_tr_b16 a[76:77], v115 offset:256\n"
"v_mfma_f32_16x16x32_bf16 v[164:167], a[112:115], a[4:7], v[164:167]\n"
"ds_read_b64_tr_b16 a[78:79], v115 offset:8448\n"
"v_mfma_f32_16x16x32_bf16 v[172:175], a[116:119], a[4:7], v[172:175]\n"
"ds_read_b64_tr_b16 a[80:81], v116 offset:256\n"
"v_mfma_f32_16x16x32_bf16 v[168:171], a[116:119], a[0:3], v[168:171]\n"
"ds_read_b64_tr_b16 a[82:83], v116 offset:8448\n"
"buffer_load_dwordx4 v6, s[4:7], s79 offen offset:2048 lds\n"
"v_mfma_f32_16x16x32_bf16 v[176:179], a[120:123], a[0:3], v[176:179]\n"
"ds_read_b64_tr_b16 a[84:85], v117 offset:256\n"
"v_mfma_f32_16x16x32_bf16 v[180:183], a[120:123], a[4:7], v[180:183]\n"
"ds_read_b64_tr_b16 a[86:87], v117 offset:8448\n"
"v_mfma_f32_16x16x32_bf16 v[188:191], a[124:127], a[4:7], v[188:191]\n"
"ds_read_b64_tr_b16 a[88:89], v118 offset:256\n"
"v_mfma_f32_16x16x32_bf16 v[184:187], a[124:127], a[0:3], v[184:187]\n"
"ds_read_b64_tr_b16 a[90:91], v118 offset:8448\n"
"s_waitcnt lgkmcnt(6)\n"
"v_mfma_f32_16x16x32_bf16 v[192:195], a[64:67], a[0:3], v[192:195]\n"
"ds_read_b64_tr_b16 a[92:93], v119 offset:256\n"
"buffer_load_dwordx4 v7, s[4:7], s79 offen offset:3072 lds\n"
"v_mfma_f32_16x16x32_bf16 v[196:199], a[64:67], a[4:7], v[196:199]\n"
"ds_read_b64_tr_b16 a[94:95], v119 offset:8448\n"
"v_mfma_f32_16x16x32_bf16 v[204:207], a[68:71], a[4:7], v[204:207]\n"
"ds_read_b64_tr_b16 a[96:97], v112 offset:16384\n"
"v_mfma_f32_16x16x32_bf16 v[200:203], a[68:71], a[0:3], v[200:203]\n"
"ds_read_b64_tr_b16 a[98:99], v112 offset:24576\n"
"v_mfma_f32_16x16x32_bf16 v[208:211], a[72:75], a[0:3], v[208:211]\n"
"ds_read_b64_tr_b16 a[100:101], v113 offset:16384\n"
"v_mfma_f32_16x16x32_bf16 v[212:215], a[72:75], a[4:7], v[212:215]\n"
"ds_read_b64_tr_b16 a[102:103], v113 offset:24576\n"
"s_add_u32 m0, s74, 102400\n"
"v_mfma_f32_16x16x32_bf16 v[220:223], a[76:79], a[4:7], v[220:223]\n"
"ds_read_b64_tr_b16 a[104:105], v114 offset:16384\n"
"buffer_load_dwordx4 v16, s[4:7], s79 offen offset:0 lds\n"
"v_mfma_f32_16x16x32_bf16 v[216:219], a[76:79], a[0:3], v[216:219]\n"
"ds_read_b64_tr_b16 a[106:107], v114 offset:24576\n"
"s_waitcnt lgkmcnt(6)\n"
"v_mfma_f32_16x16x32_bf16 v[224:227], a[80:83], a[0:3], v[224:227]\n"
"ds_read_b64_tr_b16 a[108:109], v115 offset:16384\n"
"v_mfma_f32_16x16x32_bf16 v[228:231], a[80:83], a[4:7], v[228:231]\n"
"ds_read_b64_tr_b16 a[110:111], v115 offset:24576\n"
"v_mfma_f32_16x16x32_bf16 v[236:239], a[84:87], a[4:7], v[236:239]\n"
"ds_read_b64_tr_b16 a[112:113], v116 offset:16384\n"
"v_mfma_f32_16x16x32_bf16 v[232:235], a[84:87], a[0:3], v[232:235]\n"
"ds_read_b64_tr_b16 a[114:115], v116 offset:24576\n"
"buffer_load_dwordx4 v17, s[4:7], s79 offen offset:1024 lds\n"
"v_mfma_f32_16x16x32_bf16 v[240:243], a[88:91], a[0:3], v[240:243]\n"
"ds_read_b64_tr_b16 a[116:117], v117 offset:16384\n"
"v_mfma_f32_16x16x32_bf16 v[244:247], a[88:91], a[4:7], v[244:247]\n"
"ds_read_b64_tr_b16 a[118:119], v117 offset:24576\n"
"v_mfma_f32_16x16x32_bf16 v[252:255], a[92:95], a[4:7], v[252:255]\n"
"ds_read_b64_tr_b16 a[120:121], v118 offset:16384\n"
"v_mfma_f32_16x16x32_bf16 v[248:251], a[92:95], a[0:3], v[248:251]\n"
"ds_read_b64_tr_b16 a[122:123], v118 offset:24576\n"
"s_waitcnt lgkmcnt(6)\n"
"v_mfma_f32_16x16x32_bf16 v[128:131], a[96:99], a[8:11], v[128:131]\n"
"ds_read_b64_tr_b16 a[124:125], v119 offset:16384\n"
"buffer_load_dwordx4 v18, s[4:7], s79 offen offset:2048 lds\n"
"v_mfma_f32_16x16x32_bf16 v[132:135], a[96:99], a[12:15], v[132:135]\n"
"ds_read_b64_tr_b16 a[126:127], v119 offset:24576\n"
"v_mfma_f32_16x16x32_bf16 v[140:143], a[100:103], a[12:15], v[140:143]\n"
"ds_read_b64_tr_b16 a[64:65], v112 offset:16640\n"
"v_mfma_f32_16x16x32_bf16 v[136:139], a[100:103], a[8:11], v[136:139]\n"
"ds_read_b64_tr_b16 a[66:67], v112 offset:24832\n"
"v_mfma_f32_16x16x32_bf16 v[144:147], a[104:107], a[8:11], v[144:147]\n"
"ds_read_b64_tr_b16 a[68:69], v113 offset:16640\n"
"v_mfma_f32_16x16x32_bf16 v[148:151], a[104:107], a[12:15], v[148:151]\n"
"ds_read_b64_tr_b16 a[70:71], v113 offset:24832\n"
"buffer_load_dwordx4 v19, s[4:7], s79 offen offset:3072 lds\n"
"v_mfma_f32_16x16x32_bf16 v[156:159], a[108:111], a[12:15], v[156:159]\n"
"ds_read_b64_tr_b16 a[72:73], v114 offset:16640\n"
"v_readlane_b32 s79, v21, 12\n"
"v_mfma_f32_16x16x32_bf16 v[152:155], a[108:111], a[8:11], v[152:155]\n"
"ds_read_b64_tr_b16 a[74:75], v114 offset:24832\n"
"s_waitcnt lgkmcnt(6)\n"
"v_mfma_f32_16x16x32_bf16 v[160:163], a[112:115], a[8:11], v[160:163]\n"
"ds_read_b64_tr_b16 a[76:77], v115 offset:16640\n"
"v_mfma_f32_16x16x32_bf16 v[164:167], a[112:115], a[12:15], v[164:167]\n"
"ds_read_b64_tr_b16 a[78:79], v115 offset:24832\n"
"v_mfma_f32_16x16x32_bf16 v[172:175], a[116:119], a[12:15], v[172:175]\n"
"ds_read_b64_tr_b16 a[80:81], v116 offset:16640\n"
"v_mfma_f32_16x16x32_bf16 v[168:171], a[116:119], a[8:11], v[168:171]\n"
"ds_read_b64_tr_b16 a[82:83], v116 offset:24832\n"
"buffer_load_dwordx4 a[48:51], v8, s[52:55], s82 offen offset:0 sc1\n"
"v_mfma_f32_16x16x32_bf16 v[176:179], a[120:123], a[8:11], v[176:179]\n"
"ds_read_b64_tr_b16 a[84:85], v117 offset:16640\n"
"v_mfma_f32_16x16x32_bf16 v[180:183], a[120:123], a[12:15], v[180:183]\n"
"ds_read_b64_tr_b16 a[86:87], v117 offset:24832\n"
"v_mfma_f32_16x16x32_bf16 v[188:191], a[124:127], a[12:15], v[188:191]\n"
"ds_read_b64_tr_b16 a[88:89], v118 offset:16640\n"
"v_mfma_f32_16x16x32_bf16 v[184:187], a[124:127], a[8:11], v[184:187]\n"
"ds_read_b64_tr_b16 a[90:91], v118 offset:24832\n"
"s_waitcnt lgkmcnt(6)\n"
"v_mfma_f32_16x16x32_bf16 v[192:195], a[64:67], a[8:11], v[192:195]\n"
"ds_read_b64_tr_b16 a[92:93], v119 offset:16640\n"
"buffer_load_dwordx4 a[52:55], v8, s[52:55], s82 offen offset:1024 sc1\n"
"v_mfma_f32_16x16x32_bf16 v[196:199], a[64:67], a[12:15], v[196:199]\n"
"ds_read_b64_tr_b16 a[94:95], v119 offset:24832\n"
"v_readlane_b32 s82, v21, 39\n"
"v_mfma_f32_16x16x32_bf16 v[204:207], a[68:71], a[12:15], v[204:207]\n"
"ds_read_b64_tr_b16 a[96:97], v112 offset:32768\n"
"v_mfma_f32_16x16x32_bf16 v[200:203], a[68:71], a[8:11], v[200:203]\n"
"ds_read_b64_tr_b16 a[98:99], v112 offset:40960\n"
"v_mfma_f32_16x16x32_bf16 v[208:211], a[72:75], a[8:11], v[208:211]\n"
"ds_read_b64_tr_b16 a[100:101], v113 offset:32768\n"
"v_mfma_f32_16x16x32_bf16 v[212:215], a[72:75], a[12:15], v[212:215]\n"
"ds_read_b64_tr_b16 a[102:103], v113 offset:40960\n"
"buffer_load_dwordx4 a[56:59], v8, s[52:55], s82 offen offset:0 sc1\n"
"v_mfma_f32_16x16x32_bf16 v[220:223], a[76:79], a[12:15], v[220:223]\n"
"ds_read_b64_tr_b16 a[104:105], v114 offset:32768\n"
"v_mfma_f32_16x16x32_bf16 v[216:219], a[76:79], a[8:11], v[216:219]\n"
"ds_read_b64_tr_b16 a[106:107], v114 offset:40960\n"
"s_waitcnt lgkmcnt(6)\n"
"v_mfma_f32_16x16x32_bf16 v[224:227], a[80:83], a[8:11], v[224:227]\n"
"ds_read_b64_tr_b16 a[108:109], v115 offset:32768\n"
"v_mfma_f32_16x16x32_bf16 v[228:231], a[80:83], a[12:15], v[228:231]\n"
"ds_read_b64_tr_b16 a[110:111], v115 offset:40960\n"
"v_mfma_f32_16x16x32_bf16 v[236:239], a[84:87], a[12:15], v[236:239]\n"
"ds_read_b64_tr_b16 a[112:113], v116 offset:32768\n"
"buffer_load_dwordx4 a[60:63], v8, s[52:55], s82 offen offset:1024 sc1\n"
"v_mfma_f32_16x16x32_bf16 v[232:235], a[84:87], a[8:11], v[232:235]\n"
"ds_read_b64_tr_b16 a[114:115], v116 offset:40960\n"
"v_readlane_b32 s82, v21, 40\n"
"v_mfma_f32_16x16x32_bf16 v[240:243], a[88:91], a[8:11], v[240:243]\n"
"ds_read_b64_tr_b16 a[116:117], v117 offset:32768\n"
"v_mfma_f32_16x16x32_bf16 v[244:247], a[88:91], a[12:15], v[244:247]\n"
"ds_read_b64_tr_b16 a[118:119], v117 offset:40960\n"
"v_mfma_f32_16x16x32_bf16 v[252:255], a[92:95], a[12:15], v[252:255]\n"
"ds_read_b64_tr_b16 a[120:121], v118 offset:32768\n"
"v_mfma_f32_16x16x32_bf16 v[248:251], a[92:95], a[8:11], v[248:251]\n"
"ds_read_b64_tr_b16 a[122:123], v118 offset:40960\n"
"s_waitcnt vmcnt(16)\n"
"s_barrier\n"
"s_waitcnt lgkmcnt(6)\n"
"v_mfma_f32_16x16x32_bf16 v[128:131], a[96:99], a[16:19], v[128:131]\n"
"ds_read_b64_tr_b16 a[124:125], v119 offset:32768\n"
"s_add_u32 m0, s74, 0\n"
"v_mfma_f32_16x16x32_bf16 v[132:135], a[96:99], a[20:23], v[132:135]\n"
"ds_read_b64_tr_b16 a[126:127], v119 offset:40960\n"
"buffer_load_dwordx4 v4, s[4:7], s79 offen offset:0 lds\n"
"v_mfma_f32_16x16x32_bf16 v[140:143], a[100:103], a[20:23], v[140:143]\n"
"ds_read_b64_tr_b16 a[64:65], v112 offset:33024\n"
"v_mfma_f32_16x16x32_bf16 v[136:139], a[100:103], a[16:19], v[136:139]\n"
"ds_read_b64_tr_b16 a[66:67], v112 offset:41216\n"
"v_mfma_f32_16x16x32_bf16 v[144:147], a[104:107], a[16:19], v[144:147]\n"
"ds_read_b64_tr_b16 a[68:69], v113 offset:33024\n"
"v_mfma_f32_16x16x32_bf16 v[148:151], a[104:107], a[20:23], v[148:151]\n"
"ds_read_b64_tr_b16 a[70:71], v113 offset:41216\n"
"v_mfma_f32_16x16x32_bf16 v[156:159], a[108:111], a[20:23], v[156:159]\n"
"ds_read_b64_tr_b16 a[72:73], v114 offset:33024\n"
"buffer_load_dwordx4 v5, s[4:7], s79 offen offset:1024 lds\n"
"v_mfma_f32_16x16x32_bf16 v[152:155], a[108:111], a[16:19], v[152:155]\n"
"ds_read_b64_tr_b16 a[74:75], v114 offset:41216\n"
"s_waitcnt lgkmcnt(6)\n"
"v_mfma_f32_16x16x32_bf16 v[160:163], a[112:115], a[16:19], v[160:163]\n"
"ds_read_b64_tr_b16 a[76:77], v115 offset:33024\n"
"v_mfma_f32_16x16x32_bf16 v[164:167], a[112:115], a[20:23], v[164:167]\n"
"ds_read_b64_tr_b16 a[78:79], v115 offset:41216\n"
"v_mfma_f32_16x16x32_bf16 v[172:175], a[116:119], a[20:23], v[172:175]\n"
"ds_read_b64_tr_b16 a[80:81], v116 offset:33024\n"
"v_mfma_f32_16x16x32_bf16 v[168:171], a[116:119], a[16:19], v[168:171]\n"
"ds_read_b64_tr_b16 a[82:83], v116 offset:41216\n"
"buffer_load_dwordx4 v6, s[4:7], s79 offen offset:2048 lds\n"
"v_mfma_f32_16x16x32_bf16 v[176:179], a[120:123], a[16:19], v[176:179]\n"
"ds_read_b64_tr_b16 a[84:85], v117 offset:33024\n"
"v_mfma_f32_16x16x32_bf16 v[180:183], a[120:123], a[20:23], v[180:183]\n"
"ds_read_b64_tr_b16 a[86:87], v117 offset:41216\n"
"v_mfma_f32_16x16x32_bf16 v[188:191], a[124:127], a[20:23], v[188:191]\n"
"ds_read_b64_tr_b16 a[88:89], v118 offset:33024\n"
"v_mfma_f32_16x16x32_bf16 v[184:187], a[124:127], a[16:19], v[184:187]\n"
"ds_read_b64_tr_b16 a[90:91], v118 offset:41216\n"
"s_waitcnt lgkmcnt(6)\n"
"v_mfma_f32_16x16x32_bf16 v[192:195], a[64:67], a[16:19], v[192:195]\n"
"ds_read_b64_tr_b16 a[92:93], v119 offset:33024\n"
"buffer_load_dwordx4 v7, s[4:7], s79 offen offset:3072 lds\n"
"v_mfma_f32_16x16x32_bf16 v[196:199], a[64:67], a[20:23], v[196:199]\n"
"ds_read_b64_tr_b16 a[94:95], v119 offset:41216\n"
"v_mfma_f32_16x16x32_bf16 v[204:207], a[68:71], a[20:23], v[204:207]\n"
"ds_read_b64_tr_b16 a[96:97], v112 offset:49152\n"
"v_mfma_f32_16x16x32_bf16 v[200:203], a[68:71], a[16:19], v[200:203]\n"
"ds_read_b64_tr_b16 a[98:99], v112 offset:57344\n"
"v_mfma_f32_16x16x32_bf16 v[208:211], a[72:75], a[16:19], v[208:211]\n"
"ds_read_b64_tr_b16 a[100:101], v113 offset:49152\n"
"v_mfma_f32_16x16x32_bf16 v[212:215], a[72:75], a[20:23], v[212:215]\n"
"ds_read_b64_tr_b16 a[102:103], v113 offset:57344\n"
"s_add_u32 m0, s74, 4096\n"
"v_mfma_f32_16x16x32_bf16 v[220:223], a[76:79], a[20:23], v[220:223]\n"
"ds_read_b64_tr_b16 a[104:105], v114 offset:49152\n"
"buffer_load_dwordx4 v16, s[4:7], s79 offen offset:0 lds\n"
"v_mfma_f32_16x16x32_bf16 v[216:219], a[76:79], a[16:19], v[216:219]\n"
"ds_read_b64_tr_b16 a[106:107], v114 offset:57344\n"
"s_waitcnt lgkmcnt(6)\n"
"v_mfma_f32_16x16x32_bf16 v[224:227], a[80:83], a[16:19], v[224:227]\n"
"ds_read_b64_tr_b16 a[108:109], v115 offset:49152\n"
"v_mfma_f32_16x16x32_bf16 v[228:231], a[80:83], a[20:23], v[228:231]\n"
"ds_read_b64_tr_b16 a[110:111], v115 offset:57344\n"
"v_mfma_f32_16x16x32_bf16 v[236:239], a[84:87], a[20:23], v[236:239]\n"
"ds_read_b64_tr_b16 a[112:113], v116 offset:49152\n"
"v_mfma_f32_16x16x32_bf16 v[232:235], a[84:87], a[16:19], v[232:235]\n"
"ds_read_b64_tr_b16 a[114:115], v116 offset:57344\n"
"buffer_load_dwordx4 v17, s[4:7], s79 offen offset:1024 lds\n"
"v_mfma_f32_16x16x32_bf16 v[240:243], a[88:91], a[16:19], v[240:243]\n"
"ds_read_b64_tr_b16 a[116:117], v117 offset:49152\n"
"v_mfma_f32_16x16x32_bf16 v[244:247], a[88:91], a[20:23], v[244:247]\n"
"ds_read_b64_tr_b16 a[118:119], v117 offset:57344\n"
"v_mfma_f32_16x16x32_bf16 v[252:255], a[92:95], a[20:23], v[252:255]\n"
"ds_read_b64_tr_b16 a[120:121], v118 offset:49152\n"
"v_mfma_f32_16x16x32_bf16 v[248:251], a[92:95], a[16:19], v[248:251]\n"
"ds_read_b64_tr_b16 a[122:123], v118 offset:57344\n"
"s_waitcnt lgkmcnt(6)\n"
"v_mfma_f32_16x16x32_bf16 v[128:131], a[96:99], a[24:27], v[128:131]\n"
"ds_read_b64_tr_b16 a[124:125], v119 offset:49152\n"
"buffer_load_dwordx4 v18, s[4:7], s79 offen offset:2048 lds\n"
"v_mfma_f32_16x16x32_bf16 v[132:135], a[96:99], a[28:31], v[132:135]\n"
"ds_read_b64_tr_b16 a[126:127], v119 offset:57344\n"
"v_mfma_f32_16x16x32_bf16 v[140:143], a[100:103], a[28:31], v[140:143]\n"
"ds_read_b64_tr_b16 a[64:65], v112 offset:49408\n"
"v_mfma_f32_16x16x32_bf16 v[136:139], a[100:103], a[24:27], v[136:139]\n"
"ds_read_b64_tr_b16 a[66:67], v112 offset:57600\n"
"v_mfma_f32_16x16x32_bf16 v[144:147], a[104:107], a[24:27], v[144:147]\n"
"ds_read_b64_tr_b16 a[68:69], v113 offset:49408\n"
"v_mfma_f32_16x16x32_bf16 v[148:151], a[104:107], a[28:31], v[148:151]\n"
"ds_read_b64_tr_b16 a[70:71], v113 offset:57600\n"
"buffer_load_dwordx4 v19, s[4:7], s79 offen offset:3072 lds\n"
"v_mfma_f32_16x16x32_bf16 v[156:159], a[108:111], a[28:31], v[156:159]\n"
"ds_read_b64_tr_b16 a[72:73], v114 offset:49408\n"
"v_readlane_b32 s79, v21, 13\n"
"v_mfma_f32_16x16x32_bf16 v[152:155], a[108:111], a[24:27], v[152:155]\n"
"ds_read_b64_tr_b16 a[74:75], v114 offset:57600\n"
"s_waitcnt lgkmcnt(6)\n"
"v_mfma_f32_16x16x32_bf16 v[160:163], a[112:115], a[24:27], v[160:163]\n"
"ds_read_b64_tr_b16 a[76:77], v115 offset:49408\n"
"v_mfma_f32_16x16x32_bf16 v[164:167], a[112:115], a[28:31], v[164:167]\n"
"ds_read_b64_tr_b16 a[78:79], v115 offset:57600\n"
"v_mfma_f32_16x16x32_bf16 v[172:175], a[116:119], a[28:31], v[172:175]\n"
"ds_read_b64_tr_b16 a[80:81], v116 offset:49408\n"
"v_mfma_f32_16x16x32_bf16 v[168:171], a[116:119], a[24:27], v[168:171]\n"
"ds_read_b64_tr_b16 a[82:83], v116 offset:57600\n"
"buffer_load_dwordx4 a[0:3], v8, s[52:55], s82 offen offset:0 sc1\n"
"v_mfma_f32_16x16x32_bf16 v[176:179], a[120:123], a[24:27], v[176:179]\n"
"ds_read_b64_tr_b16 a[84:85], v117 offset:49408\n"
"v_mfma_f32_16x16x32_bf16 v[180:183], a[120:123], a[28:31], v[180:183]\n"
"ds_read_b64_tr_b16 a[86:87], v117 offset:57600\n"
"v_mfma_f32_16x16x32_bf16 v[188:191], a[124:127], a[28:31], v[188:191]\n"
"ds_read_b64_tr_b16 a[88:89], v118 offset:49408\n"
"v_mfma_f32_16x16x32_bf16 v[184:187], a[124:127], a[24:27], v[184:187]\n"
"ds_read_b64_tr_b16 a[90:91], v118 offset:57600\n"
"s_waitcnt lgkmcnt(6)\n"
"v_mfma_f32_16x16x32_bf16 v[192:195], a[64:67], a[24:27], v[192:195]\n"
"ds_read_b64_tr_b16 a[92:93], v119 offset:49408\n"
"buffer_load_dwordx4 a[4:7], v8, s[52:55], s82 offen offset:1024 sc1\n"
"v_mfma_f32_16x16x32_bf16 v[196:199], a[64:67], a[28:31], v[196:199]\n"
"ds_read_b64_tr_b16 a[94:95], v119 offset:57600\n"
"v_readlane_b32 s82, v21, 41\n"
"v_mfma_f32_16x16x32_bf16 v[204:207], a[68:71], a[28:31], v[204:207]\n"
"ds_read_b64_tr_b16 a[96:97], v120 offset:0\n"
"v_mfma_f32_16x16x32_bf16 v[200:203], a[68:71], a[24:27], v[200:203]\n"
"ds_read_b64_tr_b16 a[98:99], v120 offset:8192\n"
"v_mfma_f32_16x16x32_bf16 v[208:211], a[72:75], a[24:27], v[208:211]\n"
"ds_read_b64_tr_b16 a[100:101], v121 offset:0\n"
"v_mfma_f32_16x16x32_bf16 v[212:215], a[72:75], a[28:31], v[212:215]\n"
"ds_read_b64_tr_b16 a[102:103], v121 offset:8192\n"
"buffer_load_dwordx4 a[8:11], v8, s[52:55], s82 offen offset:0 sc1\n"
"v_mfma_f32_16x16x32_bf16 v[220:223], a[76:79], a[28:31], v[220:223]\n"
"ds_read_b64_tr_b16 a[104:105], v122 offset:0\n"
"v_mfma_f32_16x16x32_bf16 v[216:219], a[76:79], a[24:27], v[216:219]\n"
"ds_read_b64_tr_b16 a[106:107], v122 offset:8192\n"
"s_waitcnt lgkmcnt(6)\n"
"v_mfma_f32_16x16x32_bf16 v[224:227], a[80:83], a[24:27], v[224:227]\n"
"ds_read_b64_tr_b16 a[108:109], v123 offset:0\n"
"v_mfma_f32_16x16x32_bf16 v[228:231], a[80:83], a[28:31], v[228:231]\n"
"ds_read_b64_tr_b16 a[110:111], v123 offset:8192\n"
"v_mfma_f32_16x16x32_bf16 v[236:239], a[84:87], a[28:31], v[236:239]\n"
"ds_read_b64_tr_b16 a[112:113], v124 offset:0\n"
"buffer_load_dwordx4 a[12:15], v8, s[52:55], s82 offen offset:1024 sc1\n"
"v_mfma_f32_16x16x32_bf16 v[232:235], a[84:87], a[24:27], v[232:235]\n"
"ds_read_b64_tr_b16 a[114:115], v124 offset:8192\n"
"v_readlane_b32 s82, v21, 42\n"
"v_mfma_f32_16x16x32_bf16 v[240:243], a[88:91], a[24:27], v[240:243]\n"
"ds_read_b64_tr_b16 a[116:117], v125 offset:0\n"
"v_mfma_f32_16x16x32_bf16 v[244:247], a[88:91], a[28:31], v[244:247]\n"
"ds_read_b64_tr_b16 a[118:119], v125 offset:8192\n"
"v_mfma_f32_16x16x32_bf16 v[252:255], a[92:95], a[28:31], v[252:255]\n"
"ds_read_b64_tr_b16 a[120:121], v126 offset:0\n"
"v_mfma_f32_16x16x32_bf16 v[248:251], a[92:95], a[24:27], v[248:251]\n"
"ds_read_b64_tr_b16 a[122:123], v126 offset:8192\n"
"s_waitcnt vmcnt(16)\n"
"s_barrier\n"
"s_waitcnt lgkmcnt(6)\n"
"v_mfma_f32_16x16x32_bf16 v[128:131], a[96:99], a[32:35], v[128:131]\n"
"ds_read_b64_tr_b16 a[124:125], v127 offset:0\n"
"s_add_u32 m0, s74, 32768\n"
"v_mfma_f32_16x16x32_bf16 v[132:135], a[96:99], a[36:39], v[132:135]\n"
"ds_read_b64_tr_b16 a[126:127], v127 offset:8192\n"
"buffer_load_dwordx4 v4, s[4:7], s79 offen offset:0 lds\n"
"v_mfma_f32_16x16x32_bf16 v[140:143], a[100:103], a[36:39], v[140:143]\n"
"ds_read_b64_tr_b16 a[64:65], v120 offset:256\n"
"v_mfma_f32_16x16x32_bf16 v[136:139], a[100:103], a[32:35], v[136:139]\n"
"ds_read_b64_tr_b16 a[66:67], v120 offset:8448\n"
"v_mfma_f32_16x16x32_bf16 v[144:147], a[104:107], a[32:35], v[144:147]\n"
"ds_read_b64_tr_b16 a[68:69], v121 offset:256\n"
"v_mfma_f32_16x16x32_bf16 v[148:151], a[104:107], a[36:39], v[148:151]\n"
"ds_read_b64_tr_b16 a[70:71], v121 offset:8448\n"
"v_mfma_f32_16x16x32_bf16 v[156:159], a[108:111], a[36:39], v[156:159]\n"
"ds_read_b64_tr_b16 a[72:73], v122 offset:256\n"
"buffer_load_dwordx4 v5, s[4:7], s79 offen offset:1024 lds\n"
"v_mfma_f32_16x16x32_bf16 v[152:155], a[108:111], a[32:35], v[152:155]\n"
"ds_read_b64_tr_b16 a[74:75], v122 offset:8448\n"
"s_waitcnt lgkmcnt(6)\n"
"v_mfma_f32_16x16x32_bf16 v[160:163], a[112:115], a[32:35], v[160:163]\n"
"ds_read_b64_tr_b16 a[76:77], v123 offset:256\n"
"v_mfma_f32_16x16x32_bf16 v[164:167], a[112:115], a[36:39], v[164:167]\n"
"ds_read_b64_tr_b16 a[78:79], v123 offset:8448\n"
"v_mfma_f32_16x16x32_bf16 v[172:175], a[116:119], a[36:39], v[172:175]\n"
"ds_read_b64_tr_b16 a[80:81], v124 offset:256\n"
"v_mfma_f32_16x16x32_bf16 v[168:171], a[116:119], a[32:35], v[168:171]\n"
"ds_read_b64_tr_b16 a[82:83], v124 offset:8448\n"
"buffer_load_dwordx4 v6, s[4:7], s79 offen offset:2048 lds\n"
"v_mfma_f32_16x16x32_bf16 v[176:179], a[120:123], a[32:35], v[176:179]\n"
"ds_read_b64_tr_b16 a[84:85], v125 offset:256\n"
"v_mfma_f32_16x16x32_bf16 v[180:183], a[120:123], a[36:39], v[180:183]\n"
"ds_read_b64_tr_b16 a[86:87], v125 offset:8448\n"
"v_mfma_f32_16x16x32_bf16 v[188:191], a[124:127], a[36:39], v[188:191]\n"
"ds_read_b64_tr_b16 a[88:89], v126 offset:256\n"
"v_mfma_f32_16x16x32_bf16 v[184:187], a[124:127], a[32:35], v[184:187]\n"
"ds_read_b64_tr_b16 a[90:91], v126 offset:8448\n"
"s_waitcnt lgkmcnt(6)\n"
"v_mfma_f32_16x16x32_bf16 v[192:195], a[64:67], a[32:35], v[192:195]\n"
"ds_read_b64_tr_b16 a[92:93], v127 offset:256\n"
"buffer_load_dwordx4 v7, s[4:7], s79 offen offset:3072 lds\n"
"v_mfma_f32_16x16x32_bf16 v[196:199], a[64:67], a[36:39], v[196:199]\n"
"ds_read_b64_tr_b16 a[94:95], v127 offset:8448\n"
"v_mfma_f32_16x16x32_bf16 v[204:207], a[68:71], a[36:39], v[204:207]\n"
"ds_read_b64_tr_b16 a[96:97], v120 offset:16384\n"
"v_mfma_f32_16x16x32_bf16 v[200:203], a[68:71], a[32:35], v[200:203]\n"
"ds_read_b64_tr_b16 a[98:99], v120 offset:24576\n"
"v_mfma_f32_16x16x32_bf16 v[208:211], a[72:75], a[32:35], v[208:211]\n"
"ds_read_b64_tr_b16 a[100:101], v121 offset:16384\n"
"v_mfma_f32_16x16x32_bf16 v[212:215], a[72:75], a[36:39], v[212:215]\n"
"ds_read_b64_tr_b16 a[102:103], v121 offset:24576\n"
"s_add_u32 m0, s74, 36864\n"
"v_mfma_f32_16x16x32_bf16 v[220:223], a[76:79], a[36:39], v[220:223]\n"
"ds_read_b64_tr_b16 a[104:105], v122 offset:16384\n"
"buffer_load_dwordx4 v16, s[4:7], s79 offen offset:0 lds\n"
"v_mfma_f32_16x16x32_bf16 v[216:219], a[76:79], a[32:35], v[216:219]\n"
"ds_read_b64_tr_b16 a[106:107], v122 offset:24576\n"
"s_waitcnt lgkmcnt(6)\n"
"v_mfma_f32_16x16x32_bf16 v[224:227], a[80:83], a[32:35], v[224:227]\n"
"ds_read_b64_tr_b16 a[108:109], v123 offset:16384\n"
"v_mfma_f32_16x16x32_bf16 v[228:231], a[80:83], a[36:39], v[228:231]\n"
"ds_read_b64_tr_b16 a[110:111], v123 offset:24576\n"
"v_mfma_f32_16x16x32_bf16 v[236:239], a[84:87], a[36:39], v[236:239]\n"
"ds_read_b64_tr_b16 a[112:113], v124 offset:16384\n"
"v_mfma_f32_16x16x32_bf16 v[232:235], a[84:87], a[32:35], v[232:235]\n"
"ds_read_b64_tr_b16 a[114:115], v124 offset:24576\n"
"buffer_load_dwordx4 v17, s[4:7], s79 offen offset:1024 lds\n"
"v_mfma_f32_16x16x32_bf16 v[240:243], a[88:91], a[32:35], v[240:243]\n"
"ds_read_b64_tr_b16 a[116:117], v125 offset:16384\n"
"v_mfma_f32_16x16x32_bf16 v[244:247], a[88:91], a[36:39], v[244:247]\n"
"ds_read_b64_tr_b16 a[118:119], v125 offset:24576\n"
"v_mfma_f32_16x16x32_bf16 v[252:255], a[92:95], a[36:39], v[252:255]\n"
"ds_read_b64_tr_b16 a[120:121], v126 offset:16384\n"
"v_mfma_f32_16x16x32_bf16 v[248:251], a[92:95], a[32:35], v[248:251]\n"
"ds_read_b64_tr_b16 a[122:123], v126 offset:24576\n"
"s_waitcnt lgkmcnt(6)\n"
"v_mfma_f32_16x16x32_bf16 v[128:131], a[96:99], a[40:43], v[128:131]\n"
"ds_read_b64_tr_b16 a[124:125], v127 offset:16384\n"
"buffer_load_dwordx4 v18, s[4:7], s79 offen offset:2048 lds\n"
"v_mfma_f32_16x16x32_bf16 v[132:135], a[96:99], a[44:47], v[132:135]\n"
"ds_read_b64_tr_b16 a[126:127], v127 offset:24576\n"
"v_mfma_f32_16x16x32_bf16 v[140:143], a[100:103], a[44:47], v[140:143]\n"
"ds_read_b64_tr_b16 a[64:65], v120 offset:16640\n"
"v_mfma_f32_16x16x32_bf16 v[136:139], a[100:103], a[40:43], v[136:139]\n"
"ds_read_b64_tr_b16 a[66:67], v120 offset:24832\n"
"v_mfma_f32_16x16x32_bf16 v[144:147], a[104:107], a[40:43], v[144:147]\n"
"ds_read_b64_tr_b16 a[68:69], v121 offset:16640\n"
"v_mfma_f32_16x16x32_bf16 v[148:151], a[104:107], a[44:47], v[148:151]\n"
"ds_read_b64_tr_b16 a[70:71], v121 offset:24832\n"
"buffer_load_dwordx4 v19, s[4:7], s79 offen offset:3072 lds\n"
"v_mfma_f32_16x16x32_bf16 v[156:159], a[108:111], a[44:47], v[156:159]\n"
"ds_read_b64_tr_b16 a[72:73], v122 offset:16640\n"
"v_mfma_f32_16x16x32_bf16 v[152:155], a[108:111], a[40:43], v[152:155]\n"
"ds_read_b64_tr_b16 a[74:75], v122 offset:24832\n"
"s_waitcnt lgkmcnt(6)\n"
"v_mfma_f32_16x16x32_bf16 v[160:163], a[112:115], a[40:43], v[160:163]\n"
"ds_read_b64_tr_b16 a[76:77], v123 offset:16640\n"
"v_mfma_f32_16x16x32_bf16 v[164:167], a[112:115], a[44:47], v[164:167]\n"
"ds_read_b64_tr_b16 a[78:79], v123 offset:24832\n"
"v_mfma_f32_16x16x32_bf16 v[172:175], a[116:119], a[44:47], v[172:175]\n"
"ds_read_b64_tr_b16 a[80:81], v124 offset:16640\n"
"v_mfma_f32_16x16x32_bf16 v[168:171], a[116:119], a[40:43], v[168:171]\n"
"ds_read_b64_tr_b16 a[82:83], v124 offset:24832\n"
"buffer_load_dwordx4 a[16:19], v8, s[52:55], s82 offen offset:0 sc1\n"
"v_mfma_f32_16x16x32_bf16 v[176:179], a[120:123], a[40:43], v[176:179]\n"
"ds_read_b64_tr_b16 a[84:85], v125 offset:16640\n"
"v_mfma_f32_16x16x32_bf16 v[180:183], a[120:123], a[44:47], v[180:183]\n"
"ds_read_b64_tr_b16 a[86:87], v125 offset:24832\n"
"v_mfma_f32_16x16x32_bf16 v[188:191], a[124:127], a[44:47], v[188:191]\n"
"ds_read_b64_tr_b16 a[88:89], v126 offset:16640\n"
"v_mfma_f32_16x16x32_bf16 v[184:187], a[124:127], a[40:43], v[184:187]\n"
"ds_read_b64_tr_b16 a[90:91], v126 offset:24832\n"
"s_waitcnt lgkmcnt(6)\n"
"v_mfma_f32_16x16x32_bf16 v[192:195], a[64:67], a[40:43], v[192:195]\n"
"ds_read_b64_tr_b16 a[92:93], v127 offset:16640\n"
"buffer_load_dwordx4 a[20:23], v8, s[52:55], s82 offen offset:1024 sc1\n"
"v_mfma_f32_16x16x32_bf16 v[196:199], a[64:67], a[44:47], v[196:199]\n"
"ds_read_b64_tr_b16 a[94:95], v127 offset:24832\n"
"v_readlane_b32 s82, v21, 43\n"
"v_mfma_f32_16x16x32_bf16 v[204:207], a[68:71], a[44:47], v[204:207]\n"
"ds_read_b64_tr_b16 a[96:97], v124 offset:32768\n"
"v_mfma_f32_16x16x32_bf16 v[200:203], a[68:71], a[40:43], v[200:203]\n"
"ds_read_b64_tr_b16 a[98:99], v124 offset:40960\n"
"v_mfma_f32_16x16x32_bf16 v[208:211], a[72:75], a[40:43], v[208:211]\n"
"ds_read_b64_tr_b16 a[100:101], v125 offset:32768\n"
"v_mfma_f32_16x16x32_bf16 v[212:215], a[72:75], a[44:47], v[212:215]\n"
"ds_read_b64_tr_b16 a[102:103], v125 offset:40960\n"
"buffer_load_dwordx4 a[24:27], v8, s[52:55], s82 offen offset:0 sc1\n"
"v_mfma_f32_16x16x32_bf16 v[220:223], a[76:79], a[44:47], v[220:223]\n"
"ds_read_b64_tr_b16 a[104:105], v126 offset:32768\n"
"v_mfma_f32_16x16x32_bf16 v[216:219], a[76:79], a[40:43], v[216:219]\n"
"ds_read_b64_tr_b16 a[106:107], v126 offset:40960\n"
"s_waitcnt lgkmcnt(6)\n"
"v_mfma_f32_16x16x32_bf16 v[224:227], a[80:83], a[40:43], v[224:227]\n"
"ds_read_b64_tr_b16 a[108:109], v127 offset:32768\n"
"v_mfma_f32_16x16x32_bf16 v[228:231], a[80:83], a[44:47], v[228:231]\n"
"ds_read_b64_tr_b16 a[110:111], v127 offset:40960\n"
"v_mfma_f32_16x16x32_bf16 v[236:239], a[84:87], a[44:47], v[236:239]\n"
"ds_read_b64_tr_b16 a[112:113], v124 offset:49152\n"
"buffer_load_dwordx4 a[28:31], v8, s[52:55], s82 offen offset:1024 sc1\n"
"v_mfma_f32_16x16x32_bf16 v[232:235], a[84:87], a[40:43], v[232:235]\n"
"ds_read_b64_tr_b16 a[114:115], v124 offset:57344\n"
"v_mfma_f32_16x16x32_bf16 v[240:243], a[88:91], a[40:43], v[240:243]\n"
"ds_read_b64_tr_b16 a[116:117], v125 offset:49152\n"
"v_mfma_f32_16x16x32_bf16 v[244:247], a[88:91], a[44:47], v[244:247]\n"
"ds_read_b64_tr_b16 a[118:119], v125 offset:57344\n"
"v_mfma_f32_16x16x32_bf16 v[252:255], a[92:95], a[44:47], v[252:255]\n"
"ds_read_b64_tr_b16 a[120:121], v126 offset:49152\n"
"v_mfma_f32_16x16x32_bf16 v[248:251], a[92:95], a[40:43], v[248:251]\n"
"ds_read_b64_tr_b16 a[122:123], v126 offset:57344\n"
"s_waitcnt vmcnt(16)\n"
"s_barrier\n"
"s_waitcnt lgkmcnt(6)\n"
"v_mfma_f32_16x16x32_bf16 v[160:163], a[96:99], a[48:51], v[160:163]\n"
"ds_read_b64_tr_b16 a[124:125], v127 offset:49152\n"
"v_mfma_f32_16x16x32_bf16 v[164:167], a[96:99], a[52:55], v[164:167]\n"
"ds_read_b64_tr_b16 a[126:127], v127 offset:57344\n"
"v_mfma_f32_16x16x32_bf16 v[172:175], a[100:103], a[52:55], v[172:175]\n"
"ds_read_b64_tr_b16 a[64:65], v116 offset:0\n"
"v_mfma_f32_16x16x32_bf16 v[168:171], a[100:103], a[48:51], v[168:171]\n"
"ds_read_b64_tr_b16 a[66:67], v116 offset:8192\n"
"v_mfma_f32_16x16x32_bf16 v[176:179], a[104:107], a[48:51], v[176:179]\n"
"ds_read_b64_tr_b16 a[68:69], v117 offset:0\n"
"v_mfma_f32_16x16x32_bf16 v[180:183], a[104:107], a[52:55], v[180:183]\n"
"ds_read_b64_tr_b16 a[70:71], v117 offset:8192\n"
"v_mfma_f32_16x16x32_bf16 v[188:191], a[108:111], a[52:55], v[188:191]\n"
"ds_read_b64_tr_b16 a[72:73], v118 offset:0\n"
"v_mfma_f32_16x16x32_bf16 v[184:187], a[108:111], a[48:51], v[184:187]\n"
"ds_read_b64_tr_b16 a[74:75], v118 offset:8192\n"
"s_waitcnt lgkmcnt(6)\n"
"v_mfma_f32_16x16x32_bf16 v[160:163], a[112:115], a[56:59], v[160:163]\n"
"ds_read_b64_tr_b16 a[76:77], v119 offset:0\n"
"v_mfma_f32_16x16x32_bf16 v[164:167], a[112:115], a[60:63], v[164:167]\n"
"ds_read_b64_tr_b16 a[78:79], v119 offset:8192\n"
"v_mfma_f32_16x16x32_bf16 v[172:175], a[116:119], a[60:63], v[172:175]\n"
"ds_read_b64_tr_b16 a[80:81], v116 offset:16384\n"
"v_mfma_f32_16x16x32_bf16 v[168:171], a[116:119], a[56:59], v[168:171]\n"
"ds_read_b64_tr_b16 a[82:83], v116 offset:24576\n"
"v_mfma_f32_16x16x32_bf16 v[176:179], a[120:123], a[56:59], v[176:179]\n"
"ds_read_b64_tr_b16 a[84:85], v117 offset:16384\n"
"v_mfma_f32_16x16x32_bf16 v[180:183], a[120:123], a[60:63], v[180:183]\n"
"ds_read_b64_tr_b16 a[86:87], v117 offset:24576\n"
"v_mfma_f32_16x16x32_bf16 v[188:191], a[124:127], a[60:63], v[188:191]\n"
"ds_read_b64_tr_b16 a[88:89], v118 offset:16384\n"
"v_mfma_f32_16x16x32_bf16 v[184:187], a[124:127], a[56:59], v[184:187]\n"
"ds_read_b64_tr_b16 a[90:91], v118 offset:24576\n"
"s_waitcnt lgkmcnt(6)\n"
"s_waitcnt vmcnt(15)\n"
"v_mfma_f32_16x16x32_bf16 v[160:163], a[64:67], a[0:3], v[160:163]\n"
"ds_read_b64_tr_b16 a[92:93], v119 offset:16384\n"
"s_waitcnt vmcnt(14)\n"
"v_mfma_f32_16x16x32_bf16 v[164:167], a[64:67], a[4:7], v[164:167]\n"
"ds_read_b64_tr_b16 a[94:95], v119 offset:24576\n"
"v_mfma_f32_16x16x32_bf16 v[172:175], a[68:71], a[4:7], v[172:175]\n"
"v_mfma_f32_16x16x32_bf16 v[168:171], a[68:71], a[0:3], v[168:171]\n"
"v_mfma_f32_16x16x32_bf16 v[176:179], a[72:75], a[0:3], v[176:179]\n"
"v_mfma_f32_16x16x32_bf16 v[180:183], a[72:75], a[4:7], v[180:183]\n"
"v_mfma_f32_16x16x32_bf16 v[188:191], a[76:79], a[4:7], v[188:191]\n"
"v_mfma_f32_16x16x32_bf16 v[184:187], a[76:79], a[0:3], v[184:187]\n"
"s_waitcnt lgkmcnt(0)\n"
"s_waitcnt vmcnt(13)\n"
"v_mfma_f32_16x16x32_bf16 v[160:163], a[80:83], a[8:11], v[160:163]\n"
"s_waitcnt vmcnt(12)\n"
"v_mfma_f32_16x16x32_bf16 v[164:167], a[80:83], a[12:15], v[164:167]\n"
"v_mfma_f32_16x16x32_bf16 v[172:175], a[84:87], a[12:15], v[172:175]\n"
"v_mfma_f32_16x16x32_bf16 v[168:171], a[84:87], a[8:11], v[168:171]\n"
"v_mfma_f32_16x16x32_bf16 v[176:179], a[88:91], a[8:11], v[176:179]\n"
"v_mfma_f32_16x16x32_bf16 v[180:183], a[88:91], a[12:15], v[180:183]\n"
"v_mfma_f32_16x16x32_bf16 v[188:191], a[92:95], a[12:15], v[188:191]\n"
"v_mfma_f32_16x16x32_bf16 v[184:187], a[92:95], a[8:11], v[184:187]\n"
"s_waitcnt vmcnt(4)\n"
"s_barrier\n"
"ds_read_b64_tr_b16 a[96:97], v116 offset:32768\n"
"ds_read_b64_tr_b16 a[98:99], v116 offset:40960\n"
"s_waitcnt lgkmcnt(0)\n"
"s_waitcnt vmcnt(3)\n"
"v_mfma_f32_16x16x32_bf16 v[160:163], a[96:99], a[16:19], v[160:163]\n"
"ds_read_b64_tr_b16 a[100:101], v117 offset:32768\n"
"ds_read_b64_tr_b16 a[102:103], v117 offset:40960\n"
"s_waitcnt vmcnt(2)\n"
"v_mfma_f32_16x16x32_bf16 v[164:167], a[96:99], a[20:23], v[164:167]\n"
"ds_read_b64_tr_b16 a[104:105], v116 offset:49152\n"
"ds_read_b64_tr_b16 a[106:107], v116 offset:57344\n"
"s_waitcnt lgkmcnt(2)\n"
"v_mfma_f32_16x16x32_bf16 v[172:175], a[100:103], a[20:23], v[172:175]\n"
"ds_read_b64_tr_b16 a[108:109], v117 offset:49152\n"
"ds_read_b64_tr_b16 a[110:111], v117 offset:57344\n"
"v_mfma_f32_16x16x32_bf16 v[168:171], a[100:103], a[16:19], v[168:171]\n"
"ds_read_b64_tr_b16 a[112:113], v118 offset:32768\n"
"ds_read_b64_tr_b16 a[114:115], v118 offset:40960\n"
"s_waitcnt vmcnt(1) lgkmcnt(4)\n"
"v_mfma_f32_16x16x32_bf16 v[160:163], a[104:107], a[24:27], v[160:163]\n"
"ds_read_b64_tr_b16 a[116:117], v119 offset:32768\n"
"ds_read_b64_tr_b16 a[118:119], v119 offset:40960\n"
"s_waitcnt vmcnt(0)\n"
"v_mfma_f32_16x16x32_bf16 v[164:167], a[104:107], a[28:31], v[164:167]\n"
"ds_read_b64_tr_b16 a[120:121], v118 offset:49152\n"
"ds_read_b64_tr_b16 a[122:123], v118 offset:57344\n"
"s_waitcnt lgkmcnt(6)\n"
"v_mfma_f32_16x16x32_bf16 v[172:175], a[108:111], a[28:31], v[172:175]\n"
"ds_read_b64_tr_b16 a[124:125], v119 offset:49152\n"
"ds_read_b64_tr_b16 a[126:127], v119 offset:57344\n"
"v_mfma_f32_16x16x32_bf16 v[168:171], a[108:111], a[24:27], v[168:171]\n"
"ds_read_b64_tr_b16 a[64:65], v120 offset:33024\n"
"ds_read_b64_tr_b16 a[66:67], v120 offset:41216\n"
"s_waitcnt lgkmcnt(2)\n"
"v_mfma_f32_16x16x32_bf16 v[176:179], a[112:115], a[16:19], v[176:179]\n"
"ds_read_b64_tr_b16 a[68:69], v121 offset:33024\n"
"ds_read_b64_tr_b16 a[70:71], v121 offset:41216\n"
"v_mfma_f32_16x16x32_bf16 v[180:183], a[112:115], a[20:23], v[180:183]\n"
"ds_read_b64_tr_b16 a[72:73], v122 offset:33024\n"
"v_cvt_pk_bf16_f32 v48, v160, v161\n"
"v_mfma_f32_16x16x32_bf16 v[188:191], a[116:119], a[20:23], v[188:191]\n"
"ds_read_b64_tr_b16 a[74:75], v122 offset:41216\n"
"v_cvt_pk_bf16_f32 v49, v162, v163\n"
"v_mfma_f32_16x16x32_bf16 v[184:187], a[116:119], a[16:19], v[184:187]\n"
"ds_read_b64_tr_b16 a[76:77], v123 offset:33024\n"
"v_cvt_pk_bf16_f32 v50, v168, v169\n"
"v_mfma_f32_16x16x32_bf16 v[176:179], a[120:123], a[24:27], v[176:179]\n"
"ds_read_b64_tr_b16 a[78:79], v123 offset:41216\n"
"v_cvt_pk_bf16_f32 v51, v170, v171\n"
"v_mfma_f32_16x16x32_bf16 v[180:183], a[120:123], a[28:31], v[180:183]\n"
"ds_read_b64_tr_b16 a[80:81], v120 offset:49408\n"
"buffer_store_dwordx4 v[48:51], v8, s[36:39], s90 offen offset:0 sc1\n"
"v_mfma_f32_16x16x32_bf16 v[188:191], a[124:127], a[28:31], v[188:191]\n"
"ds_read_b64_tr_b16 a[82:83], v120 offset:57600\n"
"v_cvt_pk_bf16_f32 v52, v164, v165\n"
"v_mfma_f32_16x16x32_bf16 v[184:187], a[124:127], a[24:27], v[184:187]\n"
"ds_read_b64_tr_b16 a[84:85], v121 offset:49408\n"
"s_waitcnt lgkmcnt(3)\n"
"v_mfma_f32_16x16x32_bf16 v[192:195], a[64:67], a[48:51], v[192:195]\n"
"ds_read_b64_tr_b16 a[86:87], v121 offset:57600\n"
"v_cvt_pk_bf16_f32 v53, v166, v167\n"
"v_mfma_f32_16x16x32_bf16 v[196:199], a[64:67], a[52:55], v[196:199]\n"
"ds_read_b64_tr_b16 a[88:89], v122 offset:49408\n"
"v_cvt_pk_bf16_f32 v54, v172, v173\n"
"v_cvt_pk_bf16_f32 v55, v174, v175\n"
"v_mfma_f32_16x16x32_bf16 v[204:207], a[68:71], a[52:55], v[204:207]\n"
"ds_read_b64_tr_b16 a[90:91], v122 offset:57600\n"
"buffer_store_dwordx4 v[52:55], v8, s[36:39], s90 offen offset:1024 sc1\n"
"v_mfma_f32_16x16x32_bf16 v[200:203], a[68:71], a[48:51], v[200:203]\n"
"ds_read_b64_tr_b16 a[92:93], v123 offset:49408\n"
"v_cvt_pk_bf16_f32 v56, v176, v177\n"
"v_mfma_f32_16x16x32_bf16 v[208:211], a[72:75], a[48:51], v[208:211]\n"
"ds_read_b64_tr_b16 a[94:95], v123 offset:57600\n"
"v_cvt_pk_bf16_f32 v57, v178, v179\n"
"v_mfma_f32_16x16x32_bf16 v[212:215], a[72:75], a[52:55], v[212:215]\n"
"ds_read_b64_tr_b16 a[96:97], v112 offset:256\n"
"v_cvt_pk_bf16_f32 v58, v184, v185\n"
"v_mfma_f32_16x16x32_bf16 v[220:223], a[76:79], a[52:55], v[220:223]\n"
"ds_read_b64_tr_b16 a[98:99], v112 offset:8448\n"
"v_cvt_pk_bf16_f32 v59, v186, v187\n"
"v_mfma_f32_16x16x32_bf16 v[216:219], a[76:79], a[48:51], v[216:219]\n"
"ds_read_b64_tr_b16 a[100:101], v113 offset:256\n"
"s_waitcnt lgkmcnt(3)\n"
"v_mfma_f32_16x16x32_bf16 v[192:195], a[80:83], a[56:59], v[192:195]\n"
"ds_read_b64_tr_b16 a[102:103], v113 offset:8448\n"
"buffer_store_dwordx4 v[56:59], v8, s[36:39], s90 offen offset:2048 sc1\n"
"v_mfma_f32_16x16x32_bf16 v[196:199], a[80:83], a[60:63], v[196:199]\n"
"ds_read_b64_tr_b16 a[104:105], v114 offset:256\n"
"v_cvt_pk_bf16_f32 v60, v180, v181\n"
"v_mfma_f32_16x16x32_bf16 v[204:207], a[84:87], a[60:63], v[204:207]\n"
"ds_read_b64_tr_b16 a[106:107], v114 offset:8448\n"
"v_cvt_pk_bf16_f32 v61, v182, v183\n"
"v_mfma_f32_16x16x32_bf16 v[200:203], a[84:87], a[56:59], v[200:203]\n"
"ds_read_b64_tr_b16 a[108:109], v115 offset:256\n"
"v_cvt_pk_bf16_f32 v62, v188, v189\n"
"v_mfma_f32_16x16x32_bf16 v[208:211], a[88:91], a[56:59], v[208:211]\n"
"ds_read_b64_tr_b16 a[110:111], v115 offset:8448\n"
"v_cvt_pk_bf16_f32 v63, v190, v191\n"
"v_mfma_f32_16x16x32_bf16 v[212:215], a[88:91], a[60:63], v[212:215]\n"
"ds_read_b64_tr_b16 a[112:113], v112 offset:16640\n"
"buffer_store_dwordx4 v[60:63], v8, s[36:39], s90 offen offset:3072 sc1\n"
"v_mfma_f32_16x16x32_bf16 v[220:223], a[92:95], a[60:63], v[220:223]\n"
"ds_read_b64_tr_b16 a[114:115], v112 offset:24832\n"
"v_mfma_f32_16x16x32_bf16 v[216:219], a[92:95], a[56:59], v[216:219]\n"
"ds_read_b64_tr_b16 a[116:117], v113 offset:16640\n"
"s_waitcnt lgkmcnt(3)\n"
"v_mfma_f32_16x16x32_bf16 v[192:195], a[96:99], a[0:3], v[192:195]\n"
"ds_read_b64_tr_b16 a[118:119], v113 offset:24832\n"
"v_mfma_f32_16x16x32_bf16 v[196:199], a[96:99], a[4:7], v[196:199]\n"
"ds_read_b64_tr_b16 a[120:121], v114 offset:16640\n"
"v_mfma_f32_16x16x32_bf16 v[204:207], a[100:103], a[4:7], v[204:207]\n"
"ds_read_b64_tr_b16 a[122:123], v114 offset:24832\n"
"v_mfma_f32_16x16x32_bf16 v[200:203], a[100:103], a[0:3], v[200:203]\n"
"ds_read_b64_tr_b16 a[124:125], v115 offset:16640\n"
"v_mfma_f32_16x16x32_bf16 v[208:211], a[104:107], a[0:3], v[208:211]\n"
"ds_read_b64_tr_b16 a[126:127], v115 offset:24832\n"
"v_mfma_f32_16x16x32_bf16 v[212:215], a[104:107], a[4:7], v[212:215]\n"
"ds_read_b64_tr_b16 a[64:65], v112 offset:33024\n"
"v_mfma_f32_16x16x32_bf16 v[220:223], a[108:111], a[4:7], v[220:223]\n"
"ds_read_b64_tr_b16 a[66:67], v112 offset:41216\n"
"v_mfma_f32_16x16x32_bf16 v[216:219], a[108:111], a[0:3], v[216:219]\n"
"ds_read_b64_tr_b16 a[68:69], v113 offset:33024\n"
"s_waitcnt lgkmcnt(3)\n"
"v_mfma_f32_16x16x32_bf16 v[192:195], a[112:115], a[8:11], v[192:195]\n"
"ds_read_b64_tr_b16 a[70:71], v113 offset:41216\n"
"v_mfma_f32_16x16x32_bf16 v[196:199], a[112:115], a[12:15], v[196:199]\n"
"ds_read_b64_tr_b16 a[72:73], v112 offset:49408\n"
"v_mfma_f32_16x16x32_bf16 v[204:207], a[116:119], a[12:15], v[204:207]\n"
"ds_read_b64_tr_b16 a[74:75], v112 offset:57600\n"
"v_mfma_f32_16x16x32_bf16 v[200:203], a[116:119], a[8:11], v[200:203]\n"
"ds_read_b64_tr_b16 a[76:77], v113 offset:49408\n"
"v_mfma_f32_16x16x32_bf16 v[208:211], a[120:123], a[8:11], v[208:211]\n"
"ds_read_b64_tr_b16 a[78:79], v113 offset:57600\n"
"v_mfma_f32_16x16x32_bf16 v[212:215], a[120:123], a[12:15], v[212:215]\n"
"ds_read_b64_tr_b16 a[80:81], v114 offset:33024\n"
"v_mfma_f32_16x16x32_bf16 v[220:223], a[124:127], a[12:15], v[220:223]\n"
"ds_read_b64_tr_b16 a[82:83], v114 offset:41216\n"
"v_mfma_f32_16x16x32_bf16 v[216:219], a[124:127], a[8:11], v[216:219]\n"
"ds_read_b64_tr_b16 a[84:85], v115 offset:33024\n"
"s_waitcnt lgkmcnt(3)\n"
"v_mfma_f32_16x16x32_bf16 v[192:195], a[64:67], a[16:19], v[192:195]\n"
"ds_read_b64_tr_b16 a[86:87], v115 offset:41216\n"
"v_mfma_f32_16x16x32_bf16 v[196:199], a[64:67], a[20:23], v[196:199]\n"
"ds_read_b64_tr_b16 a[88:89], v114 offset:49408\n"
"v_mfma_f32_16x16x32_bf16 v[204:207], a[68:71], a[20:23], v[204:207]\n"
"ds_read_b64_tr_b16 a[90:91], v114 offset:57600\n"
"v_mfma_f32_16x16x32_bf16 v[200:203], a[68:71], a[16:19], v[200:203]\n"
"ds_read_b64_tr_b16 a[92:93], v115 offset:49408\n"
"v_mfma_f32_16x16x32_bf16 v[192:195], a[72:75], a[24:27], v[192:195]\n"
"ds_read_b64_tr_b16 a[94:95], v115 offset:57600\n"
"v_mfma_f32_16x16x32_bf16 v[196:199], a[72:75], a[28:31], v[196:199]\n"
"ds_read_b64_tr_b16 a[96:97], v124 offset:33024\n"
"v_mfma_f32_16x16x32_bf16 v[204:207], a[76:79], a[28:31], v[204:207]\n"
"ds_read_b64_tr_b16 a[98:99], v124 offset:41216\n"
"v_mfma_f32_16x16x32_bf16 v[200:203], a[76:79], a[24:27], v[200:203]\n"
"ds_read_b64_tr_b16 a[100:101], v125 offset:33024\n"
"s_waitcnt lgkmcnt(3)\n"
"v_mfma_f32_16x16x32_bf16 v[208:211], a[80:83], a[16:19], v[208:211]\n"
"ds_read_b64_tr_b16 a[102:103], v125 offset:41216\n"
"v_mfma_f32_16x16x32_bf16 v[212:215], a[80:83], a[20:23], v[212:215]\n"
"ds_read_b64_tr_b16 a[104:105], v126 offset:33024\n"
"v_cvt_pk_bf16_f32 v32, v192, v193\n"
"v_mfma_f32_16x16x32_bf16 v[220:223], a[84:87], a[20:23], v[220:223]\n"
"ds_read_b64_tr_b16 a[106:107], v126 offset:41216\n"
"v_cvt_pk_bf16_f32 v33, v194, v195\n"
"v_mfma_f32_16x16x32_bf16 v[216:219], a[84:87], a[16:19], v[216:219]\n"
"ds_read_b64_tr_b16 a[108:109], v127 offset:33024\n"
"v_cvt_pk_bf16_f32 v34, v200, v201\n"
"v_mfma_f32_16x16x32_bf16 v[208:211], a[88:91], a[24:27], v[208:211]\n"
"ds_read_b64_tr_b16 a[110:111], v127 offset:41216\n"
"v_cvt_pk_bf16_f32 v35, v202, v203\n"
"v_mfma_f32_16x16x32_bf16 v[212:215], a[88:91], a[28:31], v[212:215]\n"
"ds_read_b64_tr_b16 a[112:113], v124 offset:49408\n"
"buffer_store_dwordx4 v[32:35], v8, s[36:39], s91 offen offset:0 sc1\n"
"v_mfma_f32_16x16x32_bf16 v[220:223], a[92:95], a[28:31], v[220:223]\n"
"ds_read_b64_tr_b16 a[114:115], v124 offset:57600\n"
"v_cvt_pk_bf16_f32 v36, v196, v197\n"
"v_mfma_f32_16x16x32_bf16 v[216:219], a[92:95], a[24:27], v[216:219]\n"
"ds_read_b64_tr_b16 a[116:117], v125 offset:49408\n"
"s_waitcnt lgkmcnt(3)\n"
"v_mfma_f32_16x16x32_bf16 v[224:227], a[96:99], a[48:51], v[224:227]\n"
"ds_read_b64_tr_b16 a[118:119], v125 offset:57600\n"
"v_cvt_pk_bf16_f32 v37, v198, v199\n"
"v_mfma_f32_16x16x32_bf16 v[228:231], a[96:99], a[52:55], v[228:231]\n"
"ds_read_b64_tr_b16 a[120:121], v126 offset:49408\n"
"v_cvt_pk_bf16_f32 v38, v204, v205\n"
"v_cvt_pk_bf16_f32 v39, v206, v207\n"
"v_mfma_f32_16x16x32_bf16 v[236:239], a[100:103], a[52:55], v[236:239]\n"
"ds_read_b64_tr_b16 a[122:123], v126 offset:57600\n"
"buffer_store_dwordx4 v[36:39], v8, s[36:39], s91 offen offset:1024 sc1\n"
"v_mfma_f32_16x16x32_bf16 v[232:235], a[100:103], a[48:51], v[232:235]\n"
"ds_read_b64_tr_b16 a[124:125], v127 offset:49408\n"
"v_cvt_pk_bf16_f32 v40, v208, v209\n"
"v_mfma_f32_16x16x32_bf16 v[240:243], a[104:107], a[48:51], v[240:243]\n"
"ds_read_b64_tr_b16 a[126:127], v127 offset:57600\n"
"v_cvt_pk_bf16_f32 v41, v210, v211\n"
"v_mfma_f32_16x16x32_bf16 v[244:247], a[104:107], a[52:55], v[244:247]\n"
"ds_read_b64_tr_b16 a[64:65], v116 offset:256\n"
"v_cvt_pk_bf16_f32 v42, v216, v217\n"
"v_mfma_f32_16x16x32_bf16 v[252:255], a[108:111], a[52:55], v[252:255]\n"
"ds_read_b64_tr_b16 a[66:67], v116 offset:8448\n"
"v_cvt_pk_bf16_f32 v43, v218, v219\n"
"v_mfma_f32_16x16x32_bf16 v[248:251], a[108:111], a[48:51], v[248:251]\n"
"ds_read_b64_tr_b16 a[68:69], v117 offset:256\n"
"s_waitcnt lgkmcnt(3)\n"
"v_mfma_f32_16x16x32_bf16 v[224:227], a[112:115], a[56:59], v[224:227]\n"
"ds_read_b64_tr_b16 a[70:71], v117 offset:8448\n"
"buffer_store_dwordx4 v[40:43], v8, s[36:39], s91 offen offset:2048 sc1\n"
"v_mfma_f32_16x16x32_bf16 v[228:231], a[112:115], a[60:63], v[228:231]\n"
"ds_read_b64_tr_b16 a[72:73], v118 offset:256\n"
"v_cvt_pk_bf16_f32 v44, v212, v213\n"
"v_mfma_f32_16x16x32_bf16 v[236:239], a[116:119], a[60:63], v[236:239]\n"
"ds_read_b64_tr_b16 a[74:75], v118 offset:8448\n"
"v_cvt_pk_bf16_f32 v45, v214, v215\n"
"v_mfma_f32_16x16x32_bf16 v[232:235], a[116:119], a[56:59], v[232:235]\n"
"ds_read_b64_tr_b16 a[76:77], v119 offset:256\n"
"v_cvt_pk_bf16_f32 v46, v220, v221\n"
"v_mfma_f32_16x16x32_bf16 v[240:243], a[120:123], a[56:59], v[240:243]\n"
"ds_read_b64_tr_b16 a[78:79], v119 offset:8448\n"
"v_cvt_pk_bf16_f32 v47, v222, v223\n"
"v_mfma_f32_16x16x32_bf16 v[244:247], a[120:123], a[60:63], v[244:247]\n"
"ds_read_b64_tr_b16 a[80:81], v116 offset:16640\n"
"buffer_store_dwordx4 v[44:47], v8, s[36:39], s91 offen offset:3072 sc1\n"
"v_mfma_f32_16x16x32_bf16 v[252:255], a[124:127], a[60:63], v[252:255]\n"
"ds_read_b64_tr_b16 a[82:83], v116 offset:24832\n"
"v_mfma_f32_16x16x32_bf16 v[248:251], a[124:127], a[56:59], v[248:251]\n"
"ds_read_b64_tr_b16 a[84:85], v117 offset:16640\n"
"s_waitcnt lgkmcnt(3)\n"
"v_mfma_f32_16x16x32_bf16 v[224:227], a[64:67], a[0:3], v[224:227]\n"
"ds_read_b64_tr_b16 a[86:87], v117 offset:24832\n"
"v_mfma_f32_16x16x32_bf16 v[228:231], a[64:67], a[4:7], v[228:231]\n"
"ds_read_b64_tr_b16 a[88:89], v118 offset:16640\n"
"v_mfma_f32_16x16x32_bf16 v[236:239], a[68:71], a[4:7], v[236:239]\n"
"ds_read_b64_tr_b16 a[90:91], v118 offset:24832\n"
"v_mfma_f32_16x16x32_bf16 v[232:235], a[68:71], a[0:3], v[232:235]\n"
"ds_read_b64_tr_b16 a[92:93], v119 offset:16640\n"
"v_mfma_f32_16x16x32_bf16 v[240:243], a[72:75], a[0:3], v[240:243]\n"
"ds_read_b64_tr_b16 a[94:95], v119 offset:24832\n"
"v_mfma_f32_16x16x32_bf16 v[244:247], a[72:75], a[4:7], v[244:247]\n"
"ds_read_b64_tr_b16 a[96:97], v116 offset:33024\n"
"v_mfma_f32_16x16x32_bf16 v[252:255], a[76:79], a[4:7], v[252:255]\n"
"ds_read_b64_tr_b16 a[98:99], v116 offset:41216\n"
"v_mfma_f32_16x16x32_bf16 v[248:251], a[76:79], a[0:3], v[248:251]\n"
"ds_read_b64_tr_b16 a[100:101], v117 offset:33024\n"
"s_waitcnt lgkmcnt(3)\n"
"v_mfma_f32_16x16x32_bf16 v[224:227], a[80:83], a[8:11], v[224:227]\n"
"ds_read_b64_tr_b16 a[102:103], v117 offset:41216\n"
"v_mfma_f32_16x16x32_bf16 v[228:231], a[80:83], a[12:15], v[228:231]\n"
"ds_read_b64_tr_b16 a[104:105], v116 offset:49408\n"
"v_mfma_f32_16x16x32_bf16 v[236:239], a[84:87], a[12:15], v[236:239]\n"
"ds_read_b64_tr_b16 a[106:107], v116 offset:57600\n"
"v_mfma_f32_16x16x32_bf16 v[232:235], a[84:87], a[8:11], v[232:235]\n"
"ds_read_b64_tr_b16 a[108:109], v117 offset:49408\n"
"v_mfma_f32_16x16x32_bf16 v[240:243], a[88:91], a[8:11], v[240:243]\n"
"ds_read_b64_tr_b16 a[110:111], v117 offset:57600\n"
"v_mfma_f32_16x16x32_bf16 v[244:247], a[88:91], a[12:15], v[244:247]\n"
"ds_read_b64_tr_b16 a[112:113], v118 offset:33024\n"
"v_mfma_f32_16x16x32_bf16 v[252:255], a[92:95], a[12:15], v[252:255]\n"
"ds_read_b64_tr_b16 a[114:115], v118 offset:41216\n"
"v_mfma_f32_16x16x32_bf16 v[248:251], a[92:95], a[8:11], v[248:251]\n"
"ds_read_b64_tr_b16 a[116:117], v119 offset:33024\n"
"s_waitcnt lgkmcnt(3)\n"
"v_mfma_f32_16x16x32_bf16 v[224:227], a[96:99], a[16:19], v[224:227]\n"
"ds_read_b64_tr_b16 a[118:119], v119 offset:41216\n"
"v_mfma_f32_16x16x32_bf16 v[228:231], a[96:99], a[20:23], v[228:231]\n"
"ds_read_b64_tr_b16 a[120:121], v118 offset:49408\n"
"v_mfma_f32_16x16x32_bf16 v[236:239], a[100:103], a[20:23], v[236:239]\n"
"ds_read_b64_tr_b16 a[122:123], v118 offset:57600\n"
"v_mfma_f32_16x16x32_bf16 v[232:235], a[100:103], a[16:19], v[232:235]\n"
"ds_read_b64_tr_b16 a[124:125], v119 offset:49408\n"
"v_mfma_f32_16x16x32_bf16 v[224:227], a[104:107], a[24:27], v[224:227]\n"
"ds_read_b64_tr_b16 a[126:127], v119 offset:57600\n"
"v_mfma_f32_16x16x32_bf16 v[228:231], a[104:107], a[28:31], v[228:231]\n"
"ds_read_b64_tr_b16 a[64:65], v120 offset:32768\n"
"v_mfma_f32_16x16x32_bf16 v[236:239], a[108:111], a[28:31], v[236:239]\n"
"ds_read_b64_tr_b16 a[66:67], v120 offset:40960\n"
"v_mfma_f32_16x16x32_bf16 v[232:235], a[108:111], a[24:27], v[232:235]\n"
"ds_read_b64_tr_b16 a[68:69], v121 offset:32768\n"
"s_waitcnt lgkmcnt(3)\n"
"v_mfma_f32_16x16x32_bf16 v[240:243], a[112:115], a[16:19], v[240:243]\n"
"ds_read_b64_tr_b16 a[70:71], v121 offset:40960\n"
"v_mfma_f32_16x16x32_bf16 v[244:247], a[112:115], a[20:23], v[244:247]\n"
"ds_read_b64_tr_b16 a[72:73], v122 offset:32768\n"
"v_cvt_pk_bf16_f32 v48, v224, v225\n"
"v_cvt_pk_bf16_f32 v49, v226, v227\n"
"v_cvt_pk_bf16_f32 v50, v232, v233\n"
"v_cvt_pk_bf16_f32 v51, v234, v235\n"
"buffer_store_dwordx4 v[48:51], v8, s[36:39], s92 offen offset:0 sc1\n"
"v_cvt_pk_bf16_f32 v52, v228, v229\n"
"s_waitcnt vmcnt(5)\n"
"buffer_store_dwordx2 v[28:29], v30, s[36:39], s87 offen offset:0 sc1\n"
"v_mfma_f32_16x16x32_bf16 v[252:255], a[116:119], a[20:23], v[252:255]\n"
"ds_read_b64_tr_b16 a[74:75], v122 offset:40960\n"
"v_cvt_pk_bf16_f32 v53, v230, v231\n"
"v_mfma_f32_16x16x32_bf16 v[248:251], a[116:119], a[16:19], v[248:251]\n"
"ds_read_b64_tr_b16 a[76:77], v123 offset:32768\n"
"v_cvt_pk_bf16_f32 v54, v236, v237\n"
"v_mfma_f32_16x16x32_bf16 v[240:243], a[120:123], a[24:27], v[240:243]\n"
"ds_read_b64_tr_b16 a[78:79], v123 offset:40960\n"
"v_cvt_pk_bf16_f32 v55, v238, v239\n"
"v_mfma_f32_16x16x32_bf16 v[244:247], a[120:123], a[28:31], v[244:247]\n"
"ds_read_b64_tr_b16 a[80:81], v120 offset:49152\n"
"buffer_store_dwordx4 v[52:55], v8, s[36:39], s92 offen offset:1024 sc1\n"
"v_mfma_f32_16x16x32_bf16 v[252:255], a[124:127], a[28:31], v[252:255]\n"
"ds_read_b64_tr_b16 a[82:83], v120 offset:57344\n"
"v_mfma_f32_16x16x32_bf16 v[248:251], a[124:127], a[24:27], v[248:251]\n"
"ds_read_b64_tr_b16 a[84:85], v121 offset:49152\n"
"s_waitcnt lgkmcnt(3)\n"
"v_mfma_f32_16x16x32_bf16 v[128:131], a[64:67], a[48:51], v[128:131]\n"
"ds_read_b64_tr_b16 a[86:87], v121 offset:57344\n"
"v_mfma_f32_16x16x32_bf16 v[132:135], a[64:67], a[52:55], v[132:135]\n"
"ds_read_b64_tr_b16 a[88:89], v122 offset:49152\n"
"v_cvt_pk_bf16_f32 v56, v240, v241\n"
"v_mfma_f32_16x16x32_bf16 v[140:143], a[68:71], a[52:55], v[140:143]\n"
"ds_read_b64_tr_b16 a[90:91], v122 offset:57344\n"
"v_cvt_pk_bf16_f32 v57, v242, v243\n"
"v_mfma_f32_16x16x32_bf16 v[136:139], a[68:71], a[48:51], v[136:139]\n"
"ds_read_b64_tr_b16 a[92:93], v123 offset:49152\n"
"v_cvt_pk_bf16_f32 v58, v248, v249\n"
"v_mfma_f32_16x16x32_bf16 v[144:147], a[72:75], a[48:51], v[144:147]\n"
"ds_read_b64_tr_b16 a[94:95], v123 offset:57344\n"
"v_cvt_pk_bf16_f32 v59, v250, v251\n"
"v_mfma_f32_16x16x32_bf16 v[148:151], a[72:75], a[52:55], v[148:151]\n"
"ds_read_b64_tr_b16 a[96:97], v112 offset:0\n"
"buffer_store_dwordx4 v[56:59], v8, s[36:39], s92 offen offset:2048 sc1\n"
"v_mfma_f32_16x16x32_bf16 v[156:159], a[76:79], a[52:55], v[156:159]\n"
"ds_read_b64_tr_b16 a[98:99], v112 offset:8192\n"
"v_cvt_pk_bf16_f32 v60, v244, v245\n"
"v_mfma_f32_16x16x32_bf16 v[152:155], a[76:79], a[48:51], v[152:155]\n"
"ds_read_b64_tr_b16 a[100:101], v113 offset:0\n"
"s_waitcnt lgkmcnt(3)\n"
"v_mfma_f32_16x16x32_bf16 v[128:131], a[80:83], a[56:59], v[128:131]\n"
"ds_read_b64_tr_b16 a[102:103], v113 offset:8192\n"
"v_cvt_pk_bf16_f32 v61, v246, v247\n"
"v_mfma_f32_16x16x32_bf16 v[132:135], a[80:83], a[60:63], v[132:135]\n"
"ds_read_b64_tr_b16 a[104:105], v114 offset:0\n"
"v_cvt_pk_bf16_f32 v62, v252, v253\n"
"v_mfma_f32_16x16x32_bf16 v[140:143], a[84:87], a[60:63], v[140:143]\n"
"ds_read_b64_tr_b16 a[106:107], v114 offset:8192\n"
"v_cvt_pk_bf16_f32 v63, v254, v255\n"
"v_mfma_f32_16x16x32_bf16 v[136:139], a[84:87], a[56:59], v[136:139]\n"
"ds_read_b64_tr_b16 a[108:109], v115 offset:0\n"
"buffer_store_dwordx4 v[60:63], v8, s[36:39], s92 offen offset:3072 sc1\n"
"v_mfma_f32_16x16x32_bf16 v[144:147], a[88:91], a[56:59], v[144:147]\n"
"ds_read_b64_tr_b16 a[110:111], v115 offset:8192\n"
"v_mfma_f32_16x16x32_bf16 v[148:151], a[88:91], a[60:63], v[148:151]\n"
"ds_read_b64_tr_b16 a[112:113], v112 offset:16384\n"
"v_mfma_f32_16x16x32_bf16 v[156:159], a[92:95], a[60:63], v[156:159]\n"
"ds_read_b64_tr_b16 a[114:115], v112 offset:24576\n"
"v_mfma_f32_16x16x32_bf16 v[152:155], a[92:95], a[56:59], v[152:155]\n"
"ds_read_b64_tr_b16 a[116:117], v113 offset:16384\n"
"s_waitcnt vmcnt(5)\n"
"buffer_store_dwordx2 v[28:29], v30, s[36:39], s83 offen offset:0 sc1\n"
"s_waitcnt lgkmcnt(3)\n"
"v_mfma_f32_16x16x32_bf16 v[128:131], a[96:99], a[0:3], v[128:131]\n"
"ds_read_b64_tr_b16 a[118:119], v113 offset:24576\n"
"v_mfma_f32_16x16x32_bf16 v[132:135], a[96:99], a[4:7], v[132:135]\n"
"ds_read_b64_tr_b16 a[120:121], v114 offset:16384\n"
"v_mfma_f32_16x16x32_bf16 v[140:143], a[100:103], a[4:7], v[140:143]\n"
"ds_read_b64_tr_b16 a[122:123], v114 offset:24576\n"
"v_mfma_f32_16x16x32_bf16 v[136:139], a[100:103], a[0:3], v[136:139]\n"
"ds_read_b64_tr_b16 a[124:125], v115 offset:16384\n"
"v_mfma_f32_16x16x32_bf16 v[144:147], a[104:107], a[0:3], v[144:147]\n"
"ds_read_b64_tr_b16 a[126:127], v115 offset:24576\n"
"v_mfma_f32_16x16x32_bf16 v[148:151], a[104:107], a[4:7], v[148:151]\n"
"ds_read_b64_tr_b16 a[64:65], v112 offset:32768\n"
"v_mfma_f32_16x16x32_bf16 v[156:159], a[108:111], a[4:7], v[156:159]\n"
"ds_read_b64_tr_b16 a[66:67], v112 offset:40960\n"
"v_mfma_f32_16x16x32_bf16 v[152:155], a[108:111], a[0:3], v[152:155]\n"
"ds_read_b64_tr_b16 a[68:69], v113 offset:32768\n"
"s_waitcnt lgkmcnt(3)\n"
"v_mfma_f32_16x16x32_bf16 v[128:131], a[112:115], a[8:11], v[128:131]\n"
"ds_read_b64_tr_b16 a[70:71], v113 offset:40960\n"
"v_mfma_f32_16x16x32_bf16 v[132:135], a[112:115], a[12:15], v[132:135]\n"
"ds_read_b64_tr_b16 a[72:73], v112 offset:49152\n"
"v_mfma_f32_16x16x32_bf16 v[140:143], a[116:119], a[12:15], v[140:143]\n"
"ds_read_b64_tr_b16 a[74:75], v112 offset:57344\n"
"v_mfma_f32_16x16x32_bf16 v[136:139], a[116:119], a[8:11], v[136:139]\n"
"ds_read_b64_tr_b16 a[76:77], v113 offset:49152\n"
"v_mfma_f32_16x16x32_bf16 v[144:147], a[120:123], a[8:11], v[144:147]\n"
"ds_read_b64_tr_b16 a[78:79], v113 offset:57344\n"
"buffer_load_dwordx2 v[90:91], v30, s[36:39], s69 offen offset:8 sc1\n"
"v_mfma_f32_16x16x32_bf16 v[148:151], a[120:123], a[12:15], v[148:151]\n"
"ds_read_b64_tr_b16 a[80:81], v114 offset:32768\n"
"v_mfma_f32_16x16x32_bf16 v[156:159], a[124:127], a[12:15], v[156:159]\n"
"ds_read_b64_tr_b16 a[82:83], v114 offset:40960\n"
"v_mfma_f32_16x16x32_bf16 v[152:155], a[124:127], a[8:11], v[152:155]\n"
"ds_read_b64_tr_b16 a[84:85], v115 offset:32768\n"
"s_waitcnt lgkmcnt(3)\n"
"v_mfma_f32_16x16x32_bf16 v[128:131], a[64:67], a[16:19], v[128:131]\n"
"ds_read_b64_tr_b16 a[86:87], v115 offset:40960\n"
"v_mfma_f32_16x16x32_bf16 v[132:135], a[64:67], a[20:23], v[132:135]\n"
"ds_read_b64_tr_b16 a[88:89], v114 offset:49152\n"
"v_mfma_f32_16x16x32_bf16 v[140:143], a[68:71], a[20:23], v[140:143]\n"
"ds_read_b64_tr_b16 a[90:91], v114 offset:57344\n"
"v_mfma_f32_16x16x32_bf16 v[136:139], a[68:71], a[16:19], v[136:139]\n"
"ds_read_b64_tr_b16 a[92:93], v115 offset:49152\n"
"v_mfma_f32_16x16x32_bf16 v[128:131], a[72:75], a[24:27], v[128:131]\n"
"ds_read_b64_tr_b16 a[94:95], v115 offset:57344\n"
"v_mfma_f32_16x16x32_bf16 v[132:135], a[72:75], a[28:31], v[132:135]\n"
"v_mfma_f32_16x16x32_bf16 v[140:143], a[76:79], a[28:31], v[140:143]\n"
"v_mfma_f32_16x16x32_bf16 v[136:139], a[76:79], a[24:27], v[136:139]\n"
"s_waitcnt lgkmcnt(0)\n"
"v_mfma_f32_16x16x32_bf16 v[144:147], a[80:83], a[16:19], v[144:147]\n"
"v_mfma_f32_16x16x32_bf16 v[148:151], a[80:83], a[20:23], v[148:151]\n"
"v_mfma_f32_16x16x32_bf16 v[156:159], a[84:87], a[20:23], v[156:159]\n"
"v_mfma_f32_16x16x32_bf16 v[152:155], a[84:87], a[16:19], v[152:155]\n"
"v_mfma_f32_16x16x32_bf16 v[144:147], a[88:91], a[24:27], v[144:147]\n"
"v_mfma_f32_16x16x32_bf16 v[148:151], a[88:91], a[28:31], v[148:151]\n"
"buffer_load_dwordx2 v[92:93], v30, s[36:39], s69 offen offset:16 sc1\n"
"v_mfma_f32_16x16x32_bf16 v[156:159], a[92:95], a[28:31], v[156:159]\n"
"v_mfma_f32_16x16x32_bf16 v[152:155], a[92:95], a[24:27], v[152:155]\n"
"s_waitcnt vmcnt(3)\n"
"buffer_store_dwordx2 v[28:29], v30, s[36:39], s84 offen offset:0 sc1\n"
"s_waitcnt vmcnt(2)\n"
"v_cmp_ne_u64 vcc, s[70:71], v[90:91]\n"
"s_cbranch_vccnz .Lpslow10_%=\n"
".Lpok10_%=:\n"
"s_add_u32 s77, s97, 0\n"
"buffer_load_dwordx4 a[128:131], v8, s[36:39], s77 offen offset:0 sc1\n"
"buffer_load_dwordx4 a[132:135], v8, s[36:39], s77 offen offset:1024 sc1\n"
"buffer_load_dwordx4 a[136:139], v8, s[36:39], s77 offen offset:2048 sc1\n"
"buffer_load_dwordx4 a[140:143], v8, s[36:39], s77 offen offset:3072 sc1\n"
"s_lshl_b32 s75, s63, 18\n"
"s_lshl_b32 s76, s31, 16\n"
"s_add_u32 s75, s75, s76\n"
"s_lshl_b32 s76, s67, 9\n"
"s_add_u32 s75, s75, s76\n"
"s_lshl_b32 s76, s66, 7\n"
"s_add_u32 s75, s75, s76\n"
"s_add_u32 s48, s26, s75\n"
"s_addc_u32 s49, s27, 0\n"
"s_sub_u32 s50, 4194304, s75\n"
"s_lshl_b32 s75, s31, 12\n"
"s_add_u32 s75, s75, 131072\n"
"v_and_b32 v100, 15, v1\n"
"v_lshrrev_b32 v101, 5, v1\n"
"v_and_b32 v102, 7, v100\n"
"v_xor_b32 v101, v101, v102\n"
"v_lshlrev_b32 v101, 4, v101\n"
"v_lshl_add_u32 v101, v100, 7, v101\n"
"v_bfe_u32 v102, v1, 4, 1\n"
"v_lshl_add_u32 v101, v102, 3, v101\n"
"v_add_u32 v104, s75, v101\n"
"v_xor_b32 v105, 32, v104\n"
"v_xor_b32 v106, 64, v104\n"
"v_xor_b32 v107, 96, v104\n"
"v_lshrrev_b32 v100, 3, v1\n"
"v_and_b32 v101, 7, v1\n"
"v_xor_b32 v102, v100, v101\n"
"v_lshlrev_b32 v102, 4, v102\n"
"v_lshl_add_u32 v102, v100, 7, v102\n"
"v_add_u32 v108, s75, v102\n"
"v_lshlrev_b32 v101, 4, v101\n"
"v_lshl_add_u32 v109, v100, 11, v101\n"
"s_sleep 6\n"
"s_waitcnt vmcnt(5)\n"
"v_cmp_ne_u64 vcc, s[70:71], v[92:93]\n"
"s_cbranch_vccnz .Lpslow11_%=\n"
".Lpok11_%=:\n"
"buffer_load_dwordx2 v[94:95], v30, s[36:39], s69 offen offset:24 sc1\n"
"s_add_u32 s77, s97, 32768\n"
"buffer_load_dwordx4 a[144:147], v8, s[36:39], s77 offen offset:0 sc1\n"
"buffer_load_dwordx4 a[148:151], v8, s[36:39], s77 offen offset:1024 sc1\n"
"buffer_load_dwordx4 a[152:155], v8, s[36:39], s77 offen offset:2048 sc1\n"
"buffer_load_dwordx4 a[156:159], v8, s[36:39], s77 offen offset:3072 sc1\n"
"s_waitcnt vmcnt(8)\n"
"v_mfma_f32_16x16x32_bf16 v[128:131], a[210:213], a[128:131], v[128:131]\n"
"v_mfma_f32_16x16x32_bf16 v[136:139], a[208:211], a[128:131], v[136:139]\n"
"s_waitcnt vmcnt(7)\n"
"v_mfma_f32_16x16x32_bf16 v[132:135], a[210:213], a[132:135], v[132:135]\n"
"v_mfma_f32_16x16x32_bf16 v[140:143], a[208:211], a[132:135], v[140:143]\n"
"s_waitcnt vmcnt(6)\n"
"v_mfma_f32_16x16x32_bf16 v[144:147], a[210:213], a[136:139], v[144:147]\n"
"v_mfma_f32_16x16x32_bf16 v[152:155], a[208:211], a[136:139], v[152:155]\n"
"s_waitcnt vmcnt(5)\n"
"v_mfma_f32_16x16x32_bf16 v[148:151], a[210:213], a[140:143], v[148:151]\n"
"v_mfma_f32_16x16x32_bf16 v[156:159], a[208:211], a[140:143], v[156:159]\n"
"s_waitcnt vmcnt(4)\n"
"v_cmp_ne_u64 vcc, s[70:71], v[94:95]\n"
"s_cbranch_vccnz .Lpslow12_%=\n"
".Lpok12_%=:\n"
"s_add_u32 s77, s97, 49152\n"
"buffer_load_dwordx4 a[160:163], v8, s[36:39], s77 offen offset:0 sc1\n"
"buffer_load_dwordx4 a[164:167], v8, s[36:39], s77 offen offset:1024 sc1\n"
"buffer_load_dwordx4 a[168:171], v8, s[36:39], s77 offen offset:2048 sc1\n"
"buffer_load_dwordx4 a[172:175], v8, s[36:39], s77 offen offset:3072 sc1\n"
"s_waitcnt vmcnt(7)\n"
"v_mfma_f32_16x16x32_bf16 v[128:131], a[210:213], a[144:147], v[128:131]\n"
"v_mfma_f32_16x16x32_bf16 v[136:139], a[208:211], a[144:147], v[136:139]\n"
"s_waitcnt vmcnt(6)\n"
"v_mfma_f32_16x16x32_bf16 v[132:135], a[210:213], a[148:151], v[132:135]\n"
"v_mfma_f32_16x16x32_bf16 v[140:143], a[208:211], a[148:151], v[140:143]\n"
"s_waitcnt vmcnt(5)\n"
"v_mfma_f32_16x16x32_bf16 v[144:147], a[210:213], a[152:155], v[144:147]\n"
"v_mfma_f32_16x16x32_bf16 v[152:155], a[208:211], a[152:155], v[152:155]\n"
"s_waitcnt vmcnt(4)\n"
"v_mfma_f32_16x16x32_bf16 v[148:151], a[210:213], a[156:159], v[148:151]\n"
"v_mfma_f32_16x16x32_bf16 v[156:159], a[208:211], a[156:159], v[156:159]\n"
"s_waitcnt vmcnt(3)\n"
"v_mfma_f32_16x16x32_bf16 v[128:131], a[210:213], a[160:163], v[128:131]\n"
"v_mfma_f32_16x16x32_bf16 v[136:139], a[208:211], a[160:163], v[136:139]\n"
"s_waitcnt vmcnt(2)\n"
"v_mfma_f32_16x16x32_bf16 v[132:135], a[210:213], a[164:167], v[132:135]\n"
"v_mfma_f32_16x16x32_bf16 v[140:143], a[208:211], a[164:167], v[140:143]\n"
"s_nop 3\n"
"v_cvt_pk_bf16_f32 v32, v128, v129\n"
"v_cvt_pk_bf16_f32 v33, v130, v131\n"
"ds_write_b64 v104, v[32:33] offset:0\n"
"v_cvt_pk_bf16_f32 v34, v136, v137\n"
"v_cvt_pk_bf16_f32 v35, v138, v139\n"
"ds_write_b64 v105, v[34:35] offset:0\n"
"s_waitcnt vmcnt(1)\n"
"v_mfma_f32_16x16x32_bf16 v[144:147], a[210:213], a[168:171], v[144:147]\n"
"v_mfma_f32_16x16x32_bf16 v[152:155], a[208:211], a[168:171], v[152:155]\n"
"v_cvt_pk_bf16_f32 v40, v132, v133\n"
"v_cvt_pk_bf16_f32 v41, v134, v135\n"
"ds_write_b64 v104, v[40:41] offset:2048\n"
"v_cvt_pk_bf16_f32 v42, v140, v141\n"
"v_cvt_pk_bf16_f32 v43, v142, v143\n"
"ds_write_b64 v105, v[42:43] offset:2048\n"
"s_waitcnt vmcnt(0)\n"
"v_mfma_f32_16x16x32_bf16 v[148:151], a[210:213], a[172:175], v[148:151]\n"
"v_mfma_f32_16x16x32_bf16 v[156:159], a[208:211], a[172:175], v[156:159]\n"
"v_cvt_pk_bf16_f32 v36, v144, v145\n"
"v_cvt_pk_bf16_f32 v37, v146, v147\n"
"ds_write_b64 v106, v[36:37] offset:0\n"
"v_cvt_pk_bf16_f32 v38, v152, v153\n"
"v_cvt_pk_bf16_f32 v39, v154, v155\n"
"ds_write_b64 v107, v[38:39] offset:0\n"
"s_nop 0\n"
"v_cvt_pk_bf16_f32 v44, v148, v149\n"
"v_cvt_pk_bf16_f32 v45, v150, v151\n"
"ds_write_b64 v106, v[44:45] offset:2048\n"
"v_cvt_pk_bf16_f32 v46, v156, v157\n"
"v_cvt_pk_bf16_f32 v47, v158, v159\n"
"ds_write_b64 v107, v[46:47] offset:2048\n"
"s_waitcnt lgkmcnt(2)\n"
"ds_read_b128 v[64:67], v108 offset:0\n"
"ds_read_b128 v[68:71], v108 offset:1024\n"
"s_waitcnt lgkmcnt(2)\n"
"ds_read_b128 v[72:75], v108 offset:2048\n"
"ds_read_b128 v[76:79], v108 offset:3072\n"
"s_mov_b32 s77, 0\n"
"s_waitcnt lgkmcnt(3)\n"
"buffer_store_dwordx4 v[64:67], v109, s[48:51], s77 offen offset:0 nt\n"
"s_add_u32 s77, s77, 16384\n"
"s_waitcnt lgkmcnt(2)\n"
"buffer_store_dwordx4 v[68:71], v109, s[48:51], s77 offen offset:0 nt\n"
"s_add_u32 s77, s77, 16384\n"
"s_waitcnt lgkmcnt(1)\n"
"buffer_store_dwordx4 v[72:75], v109, s[48:51], s77 offen offset:0 nt\n"
"s_add_u32 s77, s77, 16384\n"
"s_waitcnt lgkmcnt(0)\n"
"buffer_store_dwordx4 v[76:79], v109, s[48:51], s77 offen offset:0 nt\n"
"s_or_b32 s75, s30, s31\n"
"s_cmp_lg_u32 s75, 0\n"
"s_cbranch_scc1 .Lcode_end_%=\n"
"buffer_store_dwordx2 v[28:29], v30, s[36:39], 0 offen offset:0 sc1\n"
"s_branch .Lcode_end_%=\n"
".Lpslow0_%=:\n"
"s_mov_b32 s73, 100000\n"
".Lploop0_%=:\n"
"s_cmp_lg_u32 s72, 0\n"
"s_cbranch_scc1 .Lpok0_%=\n"
"s_sleep 4\n"
"buffer_load_dwordx2 v[26:27], v31, s[36:39], 0 offen offset:0 sc1\n"
"s_waitcnt vmcnt(0)\n"
"v_cmp_ne_u64 vcc, s[70:71], v[26:27]\n"
"s_cbranch_vccz .Lpok0_%=\n"
"s_sub_u32 s73, s73, 1\n"
"s_cmp_lg_u32 s73, 0\n"
"s_cbranch_scc1 .Lploop0_%=\n"
"s_mov_b32 s72, 1\n"
"s_branch .Lpok0_%=\n"
".Lpslow1_%=:\n"
"s_mov_b32 s73, 100000\n"
".Lploop1_%=:\n"
"s_cmp_lg_u32 s72, 0\n"
"s_cbranch_scc1 .Lpok1_%=\n"
"s_sleep 4\n"
"buffer_load_dwordx2 v[26:27], v30, s[36:39], s86 offen offset:0 sc1\n"
"s_waitcnt vmcnt(0)\n"
"v_cmp_ne_u64 vcc, s[70:71], v[26:27]\n"
"s_cbranch_vccz .Lpok1_%=\n"
"s_sub_u32 s73, s73, 1\n"
"s_cmp_lg_u32 s73, 0\n"
"s_cbranch_scc1 .Lploop1_%=\n"
"s_mov_b32 s72, 1\n"
"s_branch .Lpok1_%=\n"
".Lpslow2_%=:\n"
"s_mov_b32 s73, 100000\n"
".Lploop2_%=:\n"
"s_cmp_lg_u32 s72, 0\n"
"s_cbranch_scc1 .Lpok2_%=\n"
"s_sleep 4\n"
"buffer_load_dwordx2 v[120:121], v31, s[36:39], s81 offen offset:0 sc1\n"
"s_waitcnt vmcnt(0)\n"
"v_cmp_ne_u64 vcc, s[70:71], v[120:121]\n"
"s_cbranch_vccz .Lpok2_%=\n"
"s_sub_u32 s73, s73, 1\n"
"s_cmp_lg_u32 s73, 0\n"
"s_cbranch_scc1 .Lploop2_%=\n"
"s_mov_b32 s72, 1\n"
"s_branch .Lpok2_%=\n"
".Lpslow3_%=:\n"
"s_mov_b32 s73, 100000\n"
".Lploop3_%=:\n"
"s_cmp_lg_u32 s72, 0\n"
"s_cbranch_scc1 .Lpok3_%=\n"
"s_sleep 4\n"
"buffer_load_dwordx2 v[122:123], v30, s[36:39], s69 offen offset:8 sc1\n"
"s_waitcnt vmcnt(0)\n"
"v_cmp_ne_u64 vcc, s[70:71], v[122:123]\n"
"s_cbranch_vccz .Lpok3_%=\n"
"s_sub_u32 s73, s73, 1\n"
"s_cmp_lg_u32 s73, 0\n"
"s_cbranch_scc1 .Lploop3_%=\n"
"s_mov_b32 s72, 1\n"
"s_branch .Lpok3_%=\n"
".Lpslow4_%=:\n"
"s_mov_b32 s73, 100000\n"
".Lploop4_%=:\n"
"s_cmp_lg_u32 s72, 0\n"
"s_cbranch_scc1 .Lpok4_%=\n"
"s_sleep 4\n"
"buffer_load_dwordx2 v[126:127], v30, s[36:39], s69 offen offset:24 sc1\n"
"s_waitcnt vmcnt(0)\n"
"v_cmp_ne_u64 vcc, s[70:71], v[126:127]\n"
"s_cbranch_vccz .Lpok4_%=\n"
"s_sub_u32 s73, s73, 1\n"
"s_cmp_lg_u32 s73, 0\n"
"s_cbranch_scc1 .Lploop4_%=\n"
"s_mov_b32 s72, 1\n"
"s_branch .Lpok4_%=\n"
".Lpslow5_%=:\n"
"s_mov_b32 s73, 100000\n"
".Lploop5_%=:\n"
"s_cmp_lg_u32 s72, 0\n"
"s_cbranch_scc1 .Lpok5_%=\n"
"s_sleep 4\n"
"buffer_load_dwordx2 v[124:125], v30, s[36:39], s69 offen offset:16 sc1\n"
"s_waitcnt vmcnt(0)\n"
"v_cmp_ne_u64 vcc, s[70:71], v[124:125]\n"
"s_cbranch_vccz .Lpok5_%=\n"
"s_sub_u32 s73, s73, 1\n"
"s_cmp_lg_u32 s73, 0\n"
"s_cbranch_scc1 .Lploop5_%=\n"
"s_mov_b32 s72, 1\n"
"s_branch .Lpok5_%=\n"
".Lpslow6_%=:\n"
"s_mov_b32 s73, 100000\n"
".Lploop6_%=:\n"
"s_cmp_lg_u32 s72, 0\n"
"s_cbranch_scc1 .Lpok6_%=\n"
"s_sleep 4\n"
"buffer_load_dwordx2 v[26:27], v31, s[36:39], s86 offen offset:0 sc1\n"
"s_waitcnt vmcnt(0)\n"
"v_cmp_ne_u64 vcc, s[70:71], v[26:27]\n"
"s_cbranch_vccz .Lpok6_%=\n"
"s_sub_u32 s73, s73, 1\n"
"s_cmp_lg_u32 s73, 0\n"
"s_cbranch_scc1 .Lploop6_%=\n"
"s_mov_b32 s72, 1\n"
"s_branch .Lpok6_%=\n"
".Lpslow7_%=:\n"
"s_mov_b32 s73, 100000\n"
".Lploop7_%=:\n"
"s_cmp_lg_u32 s72, 0\n"
"s_cbranch_scc1 .Lpok7_%=\n"
"s_sleep 4\n"
"buffer_load_dwordx2 v[120:121], v25, s[36:39], s96 offen offset:0 sc1\n"
"s_waitcnt vmcnt(0)\n"
"v_cmp_ne_u64 vcc, s[70:71], v[120:121]\n"
"s_cbranch_vccz .Lpok7_%=\n"
"s_sub_u32 s73, s73, 1\n"
"s_cmp_lg_u32 s73, 0\n"
"s_cbranch_scc1 .Lploop7_%=\n"
"s_mov_b32 s72, 1\n"
"s_branch .Lpok7_%=\n"
".Lpslow8_%=:\n"
"s_mov_b32 s73, 100000\n"
".Lploop8_%=:\n"
"s_cmp_lg_u32 s72, 0\n"
"s_cbranch_scc1 .Lpok8_%=\n"
"s_sleep 4\n"
"buffer_load_dwordx2 v[26:27], v31, s[36:39], s86 offen offset:0 sc1\n"
"s_waitcnt vmcnt(0)\n"
"v_cmp_ne_u64 vcc, s[70:71], v[26:27]\n"
"s_cbranch_vccz .Lpok8_%=\n"
"s_sub_u32 s73, s73, 1\n"
"s_cmp_lg_u32 s73, 0\n"
"s_cbranch_scc1 .Lploop8_%=\n"
"s_mov_b32 s72, 1\n"
"s_branch .Lpok8_%=\n"
".Lpslow9_%=:\n"
"s_mov_b32 s73, 100000\n"
".Lploop9_%=:\n"
"s_cmp_lg_u32 s72, 0\n"
"s_cbranch_scc1 .Lpok9_%=\n"
"s_sleep 4\n"
"buffer_load_dwordx2 v[26:27], v31, s[36:39], s86 offen offset:0 sc1\n"
"s_waitcnt vmcnt(0)\n"
"v_cmp_ne_u64 vcc, s[70:71], v[26:27]\n"
"s_cbranch_vccz .Lpok9_%=\n"
"s_sub_u32 s73, s73, 1\n"
"s_cmp_lg_u32 s73, 0\n"
"s_cbranch_scc1 .Lploop9_%=\n"
"s_mov_b32 s72, 1\n"
"s_branch .Lpok9_%=\n"
".Lpslow10_%=:\n"
"s_mov_b32 s73, 100000\n"
".Lploop10_%=:\n"
"s_cmp_lg_u32 s72, 0\n"
"s_cbranch_scc1 .Lpok10_%=\n"
"s_sleep 4\n"
"buffer_load_dwordx2 v[90:91], v30, s[36:39], s69 offen offset:8 sc1\n"
"s_waitcnt vmcnt(0)\n"
"v_cmp_ne_u64 vcc, s[70:71], v[90:91]\n"
"s_cbranch_vccz .Lpok10_%=\n"
"s_sub_u32 s73, s73, 1\n"
"s_cmp_lg_u32 s73, 0\n"
"s_cbranch_scc1 .Lploop10_%=\n"
"s_mov_b32 s72, 1\n"
"s_branch .Lpok10_%=\n"
".Lpslow11_%=:\n"
"s_mov_b32 s73, 100000\n"
".Lploop11_%=:\n"
"s_cmp_lg_u32 s72, 0\n"
"s_cbranch_scc1 .Lpok11_%=\n"
"s_sleep 4\n"
"buffer_load_dwordx2 v[92:93], v30, s[36:39], s69 offen offset:16 sc1\n"
"s_waitcnt vmcnt(0)\n"
"v_cmp_ne_u64 vcc, s[70:71], v[92:93]\n"
"s_cbranch_vccz .Lpok11_%=\n"
"s_sub_u32 s73, s73, 1\n"
"s_cmp_lg_u32 s73, 0\n"
"s_cbranch_scc1 .Lploop11_%=\n"
"s_mov_b32 s72, 1\n"
"s_branch .Lpok11_%=\n"
".Lpslow12_%=:\n"
"s_mov_b32 s73, 100000\n"
".Lploop12_%=:\n"
"s_cmp_lg_u32 s72, 0\n"
"s_cbranch_scc1 .Lpok12_%=\n"
"s_sleep 4\n"
"buffer_load_dwordx2 v[94:95], v30, s[36:39], s69 offen offset:24 sc1\n"
"s_waitcnt vmcnt(0)\n"
"v_cmp_ne_u64 vcc, s[70:71], v[94:95]\n"
"s_cbranch_vccz .Lpok12_%=\n"
"s_sub_u32 s73, s73, 1\n"
"s_cmp_lg_u32 s73, 0\n"
"s_cbranch_scc1 .Lploop12_%=\n"
"s_mov_b32 s72, 1\n"
"s_branch .Lpok12_%=\n"
".Lcode_end_%=:\n"
"s_endpgm\n"
".p2align 12\n"
    :: "s"(__builtin_amdgcn_kernarg_segment_ptr()), "s"(blockIdx.x), "v"(threadIdx.x)
    : "v1", "v2", "v3", "v4", "v5", "v6", "v7", "v8", "v9", "v10", "v11", "v12", "v13", "v14", "v15", "v16", "v17", "v18", "v19", "v20", "v21", "v22", "v23", "v24", "v25", "v26", "v27", "v28", "v29", "v30", "v31", "v32", "v33", "v34", "v35", "v36", "v37", "v38", "v39", "v40", "v41", "v42", "v43", "v44", "v45", "v46", "v47", "v48", "v49", "v50", "v51", "v52", "v53", "v54", "v55", "v56", "v57", "v58", "v59", "v60", "v61", "v62", "v63", "v64", "v65", "v66", "v67", "v68", "v69", "v70", "v71", "v72", "v73", "v74", "v75", "v76", "v77", "v78", "v79", "v80", "v81", "v82", "v83", "v84", "v85", "v86", "v87", "v88", "v89", "v90", "v91", "v92", "v93", "v94", "v95", "v96", "v97", "v98", "v99", "v100", "v101", "v102", "v103", "v104", "v105", "v106", "v107", "v108", "v109", "v110", "v111", "v112", "v113", "v114", "v115", "v116", "v117", "v118", "v119", "v120", "v121", "v122", "v123", "v124", "v125", "v126", "v127", "v128", "v129", "v130", "v131", "v132", "v133", "v134", "v135", "v136", "v137", "v138", "v139", "v140", "v141", "v142", "v143", "v144", "v145", "v146", "v147", "v148", "v149", "v150", "v151", "v152", "v153", "v154", "v155", "v156", "v157", "v158", "v159", "v160", "v161", "v162", "v163", "v164", "v165", "v166", "v167", "v168", "v169", "v170", "v171", "v172", "v173", "v174", "v175", "v176", "v177", "v178", "v179", "v180", "v181", "v182", "v183", "v184", "v185", "v186", "v187", "v188", "v189", "v190", "v191", "v192", "v193", "v194", "v195", "v196", "v197", "v198", "v199", "v200", "v201", "v202", "v203", "v204", "v205", "v206", "v207", "v208", "v209", "v210", "v211", "v212", "v213", "v214", "v215", "v216", "v217", "v218", "v219", "v220", "v221", "v222", "v223", "v224", "v225", "v226", "v227", "v228", "v229", "v230", "v231", "v232", "v233", "v234", "v235", "v236", "v237", "v238", "v239", "v240", "v241", "v242", "v243", "v244", "v245", "v246", "v247", "v248", "v249", "v250", "v251", "v252", "v253", "v254", "v255", "a0", "a1", "a2", "a3", "a4", "a5", "a6", "a7", "a8", "a9", "a10", "a11", "a12", "a13", "a14", "a15", "a16", "a17", "a18", "a19", "a20", "a21", "a22", "a23", "a24", "a25", "a26", "a27", "a28", "a29", "a30", "a31", "a32", "a33", "a34", "a35", "a36", "a37", "a38", "a39", "a40", "a41", "a42", "a43", "a44", "a45", "a46", "a47", "a48", "a49", "a50", "a51", "a52", "a53", "a54", "a55", "a56", "a57", "a58", "a59", "a60", "a61", "a62", "a63", "a64", "a65", "a66", "a67", "a68", "a69", "a70", "a71", "a72", "a73", "a74", "a75", "a76", "a77", "a78", "a79", "a80", "a81", "a82", "a83", "a84", "a85", "a86", "a87", "a88", "a89", "a90", "a91", "a92", "a93", "a94", "a95", "a96", "a97", "a98", "a99", "a100", "a101", "a102", "a103", "a104", "a105", "a106", "a107", "a108", "a109", "a110", "a111", "a112", "a113", "a114", "a115", "a116", "a117", "a118", "a119", "a120", "a121", "a122", "a123", "a124", "a125", "a126", "a127", "a128", "a129", "a130", "a131", "a132", "a133", "a134", "a135", "a136", "a137", "a138", "a139", "a140", "a141", "a142", "a143", "a144", "a145", "a146", "a147", "a148", "a149", "a150", "a151", "a152", "a153", "a154", "a155", "a156", "a157", "a158", "a159", "a160", "a161", "a162", "a163", "a164", "a165", "a166", "a167", "a168", "a169", "a170", "a171", "a172", "a173", "a174", "a175", "a176", "a177", "a178", "a179", "a180", "a181", "a182", "a183", "a184", "a185", "a186", "a187", "a188", "a189", "a190", "a191", "a192", "a193", "a194", "a195", "a196", "a197", "a198", "a199", "a200", "a201", "a202", "a203", "a204", "a205", "a206", "a207", "a208", "a209", "a210", "a211", "a212", "a213", "a214", "a215", "a216", "a217", "a218", "a219", "a220", "a221", "a222", "a223", "a224", "a225", "a226", "a227", "a228", "a229", "a230", "a231", "a232", "a233", "a234", "a235", "a236", "a237", "a238", "a239", "a240", "a241", "a242", "a243", "a244", "a245", "a246", "a247", "a248", "a249", "a250", "a251", "a252", "a253", "a254", "a255", "s4", "s5", "s6", "s7", "s8", "s9", "s10", "s11", "s12", "s13", "s14", "s15", "s16", "s17", "s18", "s19", "s20", "s21", "s22", "s23", "s24", "s25", "s26", "s27", "s28", "s29", "s30", "s31", "s34", "s35", "s36", "s37", "s38", "s39", "s40", "s41", "s42", "s43", "s44", "s45", "s46", "s47", "s48", "s49", "s50", "s51", "s52", "s53", "s54", "s55", "s56", "s57", "s58", "s59", "s60", "s61", "s62", "s63", "s64", "s65", "s66", "s67", "s68", "s69", "s70", "s71", "s72", "s73", "s74", "s75", "s76", "s77", "s78", "s79", "s80", "s81", "s82", "s83", "s84", "s85", "s86", "s87", "s88", "s89", "s90", "s91", "s92", "s93", "s94", "s95", "s96", "s97", "s98", "s99", "vcc", "scc", "memory");
}

extern "C" void kernel_launch(void* const* d_in, const int* in_sizes, int n_in, void* d_out, int out_size, void* d_ws, size_t ws_size,
                              hipStream_t stream) {
  (void)in_sizes; (void)n_in; (void)out_size; (void)ws_size;
  (void)hipFuncSetAttribute((const void*)fnet_mlp_fused, hipFuncAttributeMaxDynamicSharedMemorySize, 163840);
  hipLaunchKernelGGL(fnet_mlp_fused, dim3(256), dim3(256), 163840, stream, d_in[0], d_in[1], d_in[2], d_in[3], d_in[4], d_in[6], d_in[7], d_in[8], d_in[9],
                     d_out, d_ws);
}
```
